```python
import torch

def _softmax(x, axis=-1):
    return torch.softmax(x, dim=axis)

def _gelu(x):
    return torch.nn.functional.gelu(x, approximate='tanh')

def _silu(x):
    return torch.nn.functional.silu(x)

def _sigmoid(x):
    return torch.sigmoid(x)

def _layernorm(x, axis=-1, eps=1e-05):
    m = x.mean(dim=axis, keepdim=True)
    v = x.var(dim=axis, keepdim=True, unbiased=False)
    return (x - m) / torch.sqrt(v + eps)

def _scatter_add(tgt, idx, src, axis=0):
    return tgt.clone().index_add_(axis, idx, src)

def _segment_sum(src, seg, n):
    out = torch.zeros((n,) + src.shape[1:], dtype=src.dtype)
    return out.index_add_(0, seg, src)

def _pad(x, pw):
    flat = tuple((v for pair in reversed(pw) for v in pair))
    return torch.nn.functional.pad(x, flat)
kernel_name = 'concat_heads__memory_aug_recurrent_mem_tr__spectral_gfnet__prompt'

def setup_inputs():

    _w = lambda a: (lambda t: t.to(torch.bfloat16) if t.is_floating_point() else t)(torch.as_tensor(a))

    def _A_setup():
        B, S, Dm = (1, 2048, 1024)
        H, Dh = (32, 128)
        M, seg = (8, 32)
        seg = min(seg, S)
        S = S // seg * seg
        return {'x': _w((lambda *s: torch.randn(*s, dtype=torch.bfloat16) * 0.05)(B, S, Dm)), 'mem0': _w((lambda *s: torch.randn(*s, dtype=torch.bfloat16) * 0.4)(B, M, Dm)), 'Wq': _w((lambda *s: torch.randn(*s, dtype=torch.bfloat16) * 6.4)(Dm, H, Dh) * Dm ** (-0.5)), 'Wk': _w((lambda *s: torch.randn(*s, dtype=torch.bfloat16) * 6.4)(Dm, H, Dh) * Dm ** (-0.5)), 'Wv': _w((lambda *s: torch.randn(*s, dtype=torch.bfloat16) * 0.4)(Dm, H, Dh) * Dm ** (-0.5)), 'Wo': _w((lambda *s: torch.randn(*s, dtype=torch.bfloat16) * 0.4)(H, Dh, Dm) * (H * Dh) ** (-0.5)), 'seg_len': seg}
    _A_in = _A_setup()

    def _B_setup():
        B, S, D = (1, 2048, 1024)
        Df = D // 2 + 1
        return {'x': _w((lambda *s: torch.randn(*s, dtype=torch.bfloat16) * 0.05)(B, S, D)), 'w_r': _w((lambda *s: torch.randn(*s, dtype=torch.bfloat16) * 3.2)(S, Df) * 0.02), 'w_i': _w((lambda *s: torch.randn(*s, dtype=torch.bfloat16) * 3.2)(S, Df) * 0.02)}
    _B_in = _B_setup()
    return {'A_x': _A_in['x'], 'A_mem0': _A_in['mem0'], 'A_Wq': _A_in['Wq'], 'A_Wk': _A_in['Wk'], 'A_Wv': _A_in['Wv'], 'A_Wo': _A_in['Wo'], 'A_seg_len': _A_in['seg_len'], 'B_x': _B_in['x'], 'B_w_r': _B_in['w_r'], 'B_w_i': _B_in['w_i']}

def reference(A_x, A_mem0, A_Wq, A_Wk, A_Wv, A_Wo, A_seg_len, B_x, B_w_r, B_w_i):

    def _A_ref(x, mem0, Wq, Wk, Wv, Wo, seg_len):
        B, S, Dm = x.shape
        H, Dh = (Wq.shape[1], Wq.shape[2])
        M = mem0.shape[1]
        scale = Dh ** (-0.5)
        n_seg = S // seg_len
        L = M + seg_len
        idx = torch.arange(L)
        mask = (idx[:, None] < M) | (idx[:, None] >= idx[None, :])
        mem = mem0
        outs = []
        for s in range(n_seg):
            xs = x[:, s * seg_len:(s + 1) * seg_len, :].clone()
            h = torch.concatenate([mem, xs], axis=1)
            q = torch.einsum('btd,dhe->bhte', h, Wq)
            k = torch.einsum('btd,dhe->bhte', h, Wk)
            v = torch.einsum('btd,dhe->bhte', h, Wv)
            scores = torch.einsum('bhqe,bhke->bhqk', q, k) * scale
            scores = torch.where(mask[None, None], scores, -1000000000.0)
            p = _softmax(scores, axis=-1)
            o = torch.einsum('bhqk,bhke->bhqe', p, v)
            o = torch.einsum('bhte,hed->btd', o, Wo)
            mem = o[:, :M, :].clone()
            outs.append(o[:, M:, :])
        return torch.concatenate(outs, axis=1)

    def _B_ref(x, w_r, w_i):
        B, S, D = x.shape
        X = (lambda x, axes=(-2, -1): torch.fft.rfft2(x.float(), dim=axes))(x, axes=(-2, -1))
        W = w_r + 1j * w_i
        X = X * W
        y = (lambda x, s, axes=(-2, -1): torch.fft.irfft2(x, s=s, dim=axes).to(torch.bfloat16))(X, (S, D), axes=(-2, -1))
        return y
    a = _A_ref(A_x, A_mem0, A_Wq, A_Wk, A_Wv, A_Wo, A_seg_len)
    b = _B_ref(B_x, B_w_r, B_w_i)
    return torch.concatenate([a, b], axis=-1)
```

```cpp
#if defined(__HIP_DEVICE_COMPILE__)
#pragma clang attribute push (__attribute__((target("no-packed-fp32-ops"))), apply_to = function)
#endif
#include <hip/hip_runtime.h>
#include <hip/hip_bf16.h>
#include <stdint.h>
#include <type_traits>

typedef unsigned short u16;
#define TIDX ((int)__builtin_amdgcn_workitem_id_x())
#define BIDX ((int)__builtin_amdgcn_workgroup_id_x())
__device__ __forceinline__ int opaque_tid() { int t = TIDX; asm volatile("" : "+v"(t)); return t; }
__device__ __forceinline__ int lane_now() { int t; asm volatile("v_mbcnt_lo_u32_b32 %0, -1, 0\n\tv_mbcnt_hi_u32_b32 %0, -1, %0" : "=v"(t)); return t; }
typedef __attribute__((ext_vector_type(8))) short bf16x8;
typedef __attribute__((ext_vector_type(4))) float f32x4;

constexpr int S_LEN = 2048, DM = 1024, NH = 32, DH = 128, MM = 8, SEG = 32;
constexpr int NSEG = S_LEN / SEG;
constexpr int HD = NH * DH;
constexpr int OUTW = 2048;

typedef unsigned v4u __attribute__((ext_vector_type(4)));
typedef unsigned v2u __attribute__((ext_vector_type(2)));
typedef short v4i16 __attribute__((ext_vector_type(4)));
typedef __amdgpu_buffer_rsrc_t rsrc_t;
#define AUX_SC1 16
__device__ __forceinline__ v4u ld_sc1s(rsrc_t rs, int voff, int soff) { return __builtin_amdgcn_raw_buffer_load_b128(rs, voff, soff, AUX_SC1); }
__device__ __forceinline__ void st_sc1s(rsrc_t rs, int voff, int soff, v4u v) { __builtin_amdgcn_raw_buffer_store_b128(v, rs, voff, soff, AUX_SC1); asm volatile("s_nop 1" :: "v"(v) : "memory"); }
__device__ __forceinline__ void st8_sc1s(rsrc_t rs, int voff, int soff, v2u v) { __builtin_amdgcn_raw_buffer_store_b64(v, rs, voff, soff, AUX_SC1); }
__device__ __forceinline__ void st_sc1(rsrc_t rs, int off, v4u v) { __builtin_amdgcn_raw_buffer_store_b128(v, rs, off, 0, AUX_SC1); }
__device__ __forceinline__ void out8(void* p, v2u v) { asm volatile("global_store_dwordx2 %0, %1, off sc1" :: "v"(p), "v"(v) : "memory"); }
typedef __bf16 bf16x2_t __attribute__((ext_vector_type(2)));
typedef float f32x2_t __attribute__((ext_vector_type(2)));
__device__ __forceinline__ unsigned cvt_pk_bf16(float lo, float hi) {
    f32x2_t f = {lo, hi};
    return __builtin_bit_cast(unsigned, __builtin_convertvector(f, bf16x2_t));
}
#define SENT 0xFFBFFFBF
__device__ __forceinline__ unsigned umax3(unsigned a, unsigned b, unsigned c) { return max(max(a, b), c); }
__device__ __forceinline__ unsigned mx4(unsigned acc, v4u v) { return umax3(umax3(acc, v[0], v[1]), v[2], v[3]); }
__device__ __forceinline__ bf16x8 as_frag(v4u v) { return __builtin_bit_cast(bf16x8, v); }
__device__ __forceinline__ float xor16_sum(float x) {
    auto r = __builtin_amdgcn_permlane16_swap(__float_as_uint(x), __float_as_uint(x), false, false);
    return __uint_as_float(r[0]) + __uint_as_float(r[1]);
}
__device__ __forceinline__ float xor32_sum(float x) {
    auto r = __builtin_amdgcn_permlane32_swap(__float_as_uint(x), __float_as_uint(x), false, false);
    return __uint_as_float(r[0]) + __uint_as_float(r[1]);
}
__device__ __forceinline__ float xor16_max(float x) {
    auto r = __builtin_amdgcn_permlane16_swap(__float_as_uint(x), __float_as_uint(x), false, false);
    return fmaxf(__uint_as_float(r[0]), __uint_as_float(r[1]));
}
__device__ __forceinline__ float xor32_max(float x) {
    auto r = __builtin_amdgcn_permlane32_swap(__float_as_uint(x), __float_as_uint(x), false, false);
    return fmaxf(__uint_as_float(r[0]), __uint_as_float(r[1]));
}
constexpr int SPIN_LIMIT = 400000;
constexpr int OOB = 0x7FFFFF00;
#define PV1 2
#define PV2 11
#define LW 0
#define PVA 8
#define PL 20
#define PF 48
#define CHS 0
#define DLY_U 4
#define SWZ 0
#define FROT 4224
#define WROT 384
#define FNEAR 1u
#define FST 0
#define PB 14
#define TD2 65
#define SL0 44
#define PS 52
#define TE0 10
#define TD 65
#define PV 4
#define PC 31
#define PP 15
#define PK0 34
#define PKP 1
#define PKM 1
#define PKA 42
#define PXD 1
constexpr int NPROBE = 2, DLY_L = 7, DLY_C = 5;
constexpr int M_OFF = 32 * 16384, F_OFF = M_OFF + 2 * 16384, C_OFF = F_OFF + 2 * 16384, REC = C_OFF + 16 * 16384, PERWG = REC / 128, SLOT_BYTES = 34 * 2097152;
static_assert(PERWG == 6656, "arming shares");
__device__ __forceinline__ int rec_base(int s) { return (s >> 1) * 2097152 + (s & 1) * REC; }
__device__ __forceinline__ char* arena(char* slots) { return (char*)(((uintptr_t)slots + 2097151u) & ~(uintptr_t)2097151u); }
constexpr int KX_OFF = SLOT_BYTES, VX_OFF = KX_OFF + (32 << 20), FX_OFF = VX_OFF + (32 << 20), WT_OFF = FX_OFF + (16 << 20), ARENA_BYTES = WT_OFF + (10 << 20);
__device__ __forceinline__ rsrc_t slot_rsrc(char* slots) { return __builtin_amdgcn_make_buffer_rsrc((void*)arena(slots), 0, FX_OFF, 0x00020000); }
__device__ __forceinline__ int blk_off(int seg, int h, unsigned tau) { return seg * 524288 + (((h & 3) >> 1) * 16 + ((h >> 2) ^ (seg & SWZ)) * 2 + (h & 1)) * 16384 + (int)((tau ^ (unsigned)(seg >> 2)) & 1u) * 8192; }
constexpr int TM_OFF = 124 * 16384;
constexpr int CAL_OFF = 32 * 2097152;
__device__ __forceinline__ unsigned half_bits(int s, unsigned tau) { return ((tau ^ (unsigned)(s >> 1)) & 1u) ? 0xFFu : 0u; }
__device__ __forceinline__ int chunk_off(int s, int g, int j, unsigned tau) { return rec_base(s) + (j < 2 ? (g & 3) * 2 + j : 6 + 3 * g + j) * 16384 + (int)(half_bits(s, tau) & 1u) * 8192; }
__device__ __forceinline__ int x_off(int s, int h, unsigned tau) { return chunk_off(s, h >> 2, 2, tau) + (h & 3) * 2048; }
__device__ __forceinline__ int km_off(int s, int h, unsigned tau) { return chunk_off(s, h >> 2, 3, tau) + (h & 3) * 2048; }
__device__ __forceinline__ int m_off(int s, int h, unsigned tau) { return rec_base(s) + M_OFF + (h >> 4) * 16384 + (int)((half_bits(s, tau) >> (h >> 2)) & 1u) * 8192 + (h & 15) * 512; }
__device__ __forceinline__ int v_off(int s, int h, unsigned tau) { return chunk_off(s, h >> 2, 4, tau) + (h & 3) * 2048; }
__device__ __forceinline__ int f_off(int s, int g, unsigned tau) { return rec_base(s) + F_OFF + (g >> 2) * 16384 + (int)((half_bits(s, tau) >> g) & 1u) * 8192 + (g & 3) * 2048; }
__device__ __forceinline__ int hcol(int h) { return ((h & 3) * 8 + (h >> 2)) * 128; }
constexpr int VP = 144;

struct ChainArgs {
    u16* out;
    const u16* mem0; const u16* Wq; const u16* Wk; const u16* Wv; const u16* Wo;
    char* slots;
    unsigned* tmo;
    unsigned long long* stamps;
    unsigned* sync;
    unsigned long long* boot;
};
constexpr int SY_KV = 0;
constexpr int SY_Q = 64;
constexpr int SY_PROG = 128;
constexpr int SY_OX = 136;
constexpr int SY_DONE = 264;
constexpr int SY_OXS = 472;
constexpr int SY_KV0 = 408;
#define TCH 0
#define TXT 10
#define XPR 2
#define F0E 1
#define KVS 8
constexpr int SY_FLAGS = 664;
constexpr int SY_WORDS = SY_FLAGS + SY_FLAGS / 8;
#define XA_FAST 6
#define XA_D 12
#define SQ_FAST 49
#define SQ_SF 4
#define SSEGV 48
constexpr int SSEG = SSEGV;
typedef __attribute__((address_space(1))) unsigned gu32;
typedef __attribute__((address_space(1))) unsigned long long gu64;
__device__ __forceinline__ unsigned ld_flag(const unsigned* p) { return __hip_atomic_load((gu32*)p, __ATOMIC_RELAXED, __HIP_MEMORY_SCOPE_AGENT); }
__device__ __forceinline__ unsigned long long boot_tag(const unsigned long long* boot) {
    return __hip_atomic_load((gu64*)(boot + 144), __ATOMIC_RELAXED, __HIP_MEMORY_SCOPE_AGENT) + 1ull;
}
__device__ __forceinline__ unsigned long long boot_wait(const unsigned long long* boot) {
    const int l = TIDX & 63;
    unsigned long long tag = 0, x = 0, y = 0;
#pragma nounroll
    for (int spin = 0; spin < 100000; ++spin) {
        asm volatile("" ::: "memory");
        x = __hip_atomic_load((gu64*)(boot + l), __ATOMIC_RELAXED, __HIP_MEMORY_SCOPE_AGENT);
        y = __hip_atomic_load((gu64*)(boot + 64 + l), __ATOMIC_RELAXED, __HIP_MEMORY_SCOPE_AGENT);
        tag = boot_tag(boot);
        if (__all((x | 3u) == ((tag << 2) | 3u) && (y | 3u) == ((tag << 2) | 3u))) break;
        __builtin_amdgcn_s_sleep(16);
    }
    asm volatile("" ::: "memory");
    const unsigned long long side = __ballot(x & 1u);
    const unsigned sd = (unsigned)(side & 1u) | (unsigned)((side >> 15) & 2u) | (unsigned)((side >> 30) & 4u) | (unsigned)((side >> 45) & 8u);
    const unsigned m = (sd ^ (__popcll(__ballot(x & 2u)) + __popcll(__ballot(y & 2u)) > 64 ? 15u : 0u)) & 15u;
    return (tag << 8) | m | ((~m & 15u) << 4);
}
template <int SL>
__device__ __forceinline__ void wait_ge(const unsigned* p, unsigned need, unsigned* tmo, unsigned code) {
#pragma nounroll
    for (int spin = 0;; ++spin) {
        asm volatile("" ::: "memory");
        if (__all(ld_flag(p) >= need)) break;
        if (spin > 400000 / SL) { __hip_atomic_store((gu32*)tmo, code, __ATOMIC_RELAXED, __HIP_MEMORY_SCOPE_AGENT); break; }
        __builtin_amdgcn_s_sleep(SL);
    }
    asm volatile("" ::: "memory");
}
template <int SL>
__device__ __forceinline__ void wg_wait_acquire(const unsigned* p, unsigned* tmo, unsigned code) {
    if (TIDX < 64) {
        wait_ge<SL>(p, 1u, tmo, code);
        __builtin_amdgcn_fence(__ATOMIC_ACQUIRE, "agent");
        asm volatile("s_waitcnt vmcnt(0)" ::: "memory");
    }
    __syncthreads();
}
__device__ __forceinline__ void arrive(unsigned* sync, const int f, const unsigned need, const int n) {
    const int l = lane_now();
    unsigned old = 0;
    if (l == 0) old = __hip_atomic_fetch_add((gu32*)(sync + (SY_FLAGS + (f >> 3)) * 32), 1u, __ATOMIC_RELAXED, __HIP_MEMORY_SCOPE_AGENT);
    if (__builtin_amdgcn_readfirstlane(old) == need - 1u && l < n) __hip_atomic_store((gu32*)(sync + (f + l) * 32), 16u, __ATOMIC_RELAXED, __HIP_MEMORY_SCOPE_AGENT);
}
__device__ __forceinline__ void wg_publish(unsigned* sync, const int f, const unsigned need, const int n) {
    asm volatile("s_waitcnt vmcnt(0)" ::: "memory");
    __syncthreads();
    if (TIDX < 64) arrive(sync, f, need, n);
}
#define TMO_SET(code) { dead = true; if (l == 0) __hip_atomic_store(a.tmo, (code) + s, __ATOMIC_RELAXED, __HIP_MEMORY_SCOPE_AGENT); }

__device__ __forceinline__ void glds16h(unsigned voff, const void* sbase, unsigned lds_dst) {
    unsigned keep;
    asm volatile("s_mov_b32 %0, m0\n\ts_mov_b32 m0, %3\n\ts_nop 0\n\tglobal_load_lds_dwordx4 %1, %2 sc1\n\ts_mov_b32 m0, %0" : "=&s"(keep) : "v"(voff), "s"(sbase), "s"(lds_dst) : "memory");
}
__device__ __forceinline__ void touch(const void* p, unsigned lds_dst) {
    unsigned keep;
    asm volatile("s_mov_b32 %0, m0\n\ts_mov_b32 m0, %2\n\ts_nop 0\n\tglobal_load_lds_dword %1, off\n\ts_mov_b32 m0, %0" : "=&s"(keep) : "v"(p), "s"(lds_dst) : "memory");
}
__device__ __forceinline__ void touch_sc1(const void* p, unsigned lds_dst) {
    unsigned keep;
    asm volatile("s_mov_b32 %0, m0\n\ts_mov_b32 m0, %2\n\ts_nop 0\n\tglobal_load_lds_dword %1, off sc1\n\ts_mov_b32 m0, %0" : "=&s"(keep) : "v"(p), "s"(lds_dst) : "memory");
}
#define WEV(code)
#define WEVP
#define WEVA
constexpr int C2_K = 32768;
constexpr int C2_V = 65536;
constexpr int C2_R1 = 0, R1S = 3200;
constexpr int C2_R3 = C2_R1 + 4 * R1S;
constexpr int C2_MB = 24576;
#define PST(n)
#define LT(n)
#define LTA(n)
#define LTOUT0
#define LTOUT
#define CN(n)
#define CNS
#define CNOUT
#define CLOB16(a, b, c, d, e, f, g, h, i, j, k, l, m, n, o, p) "v" #a, "v" #b, "v" #c, "v" #d, "v" #e, "v" #f, "v" #g, "v" #h, "v" #i, "v" #j, "v" #k, "v" #l, "v" #m, "v" #n, "v" #o, "v" #p
#define LDS3(p) ((__attribute__((address_space(3))) v4i16*)(p))
__device__ __forceinline__ bf16x8 tr_frag(const void* p0, const void* p1) {
    v4i16 lo = __builtin_amdgcn_ds_read_tr16_b64_v4i16(LDS3(p0));
    v4i16 hi = __builtin_amdgcn_ds_read_tr16_b64_v4i16(LDS3(p1));
    return bf16x8{lo[0], lo[1], lo[2], lo[3], hi[0], hi[1], hi[2], hi[3]};
}
typedef __bf16 bf16x8v __attribute__((ext_vector_type(8)));
typedef __bf16 bf16x16v __attribute__((ext_vector_type(16)));
typedef unsigned v8u __attribute__((ext_vector_type(8)));
typedef unsigned v32u __attribute__((ext_vector_type(32)));
#define PUT8(v, o, x) { const v8u x_ = (x); _Pragma("unroll") for (int e_ = 0; e_ < 8; ++e_) (v)[(o) + e_] = x_[e_]; }
#define SUB4(v, o) v4u{(v)[(o)], (v)[(o) + 1], (v)[(o) + 2], (v)[(o) + 3]}
#define PUT4(v, o, x) { const v4u x_ = (x); (v)[(o)] = x_[0]; (v)[(o) + 1] = x_[1]; (v)[(o) + 2] = x_[2]; (v)[(o) + 3] = x_[3]; }
__device__ __forceinline__ v2u tr4(const u16* p) { return __builtin_bit_cast(v2u, __builtin_amdgcn_ds_read_tr16_b64_v4i16(LDS3(p))); }
__device__ __forceinline__ f32x4 smm(v4u a, v8u b, f32x4 c, int idx) {
    return __builtin_amdgcn_smfmac_f32_16x16x64_bf16(__builtin_bit_cast(bf16x8v, a), __builtin_bit_cast(bf16x16v, b), c, idx, 0, 0);
}
__device__ __forceinline__ v8u tr_frag64(const u16* p, const int pitch) {
    const v2u r0 = tr4(p), r1 = tr4(p + 8 * pitch), r2 = tr4(p + 32 * pitch), r3 = tr4(p + 40 * pitch);
    return v8u{r0[0], r1[0], r0[1], r1[1], r2[0], r3[0], r2[1], r3[1]};
}
#define MM(a_, b_, c_) __builtin_amdgcn_mfma_f32_16x16x32_bf16(as_frag(a_), as_frag(b_), c_, 0, 0, 0)
#define STR2(x) #x
#define STR(x) STR2(x)
#define PAUSE(n) ".rept (" STR(n) ") / 4\n\ts_nop 15\n\t.endr\n\t.rept (" STR(n) ") %% 4\n\ts_nop 3\n\t.endr\n\t"
#define ZERO4 f32x4{0.f, 0.f, 0.f, 0.f}
constexpr int XB_OFF = CAL_OFF + 262144, XP = 68;
__device__ __forceinline__ void last_rows(const char* chsm, rsrc_t rsS, const int wu, const int grp, const int c, const unsigned tau) {
    const int l = lane_now();
    __builtin_amdgcn_s_barrier();
    asm volatile("" ::: "memory");
    const float* rp = (const float*)(chsm + C2_K) + (4 * wu + (l >> 4)) * XP + 4 * (l & 15);
    const f32x4 s0 = (*(const f32x4*)rp + *(const f32x4*)(rp + 32 * XP)) + (*(const f32x4*)(rp + 64 * XP) + *(const f32x4*)(rp + 96 * XP));
    v2u o;
    o[0] = cvt_pk_bf16(s0[0], s0[1]); o[1] = cvt_pk_bf16(s0[2], s0[3]);
    st8_sc1s(rsS, l * 8, XB_OFF + ((c >> 1) * 8 + wu) * 16384 + (int)(tau & 1u) * 8192 + (c & 1) * 4096 + grp * 512, o);
}
__device__ __forceinline__ void chain_body(const ChainArgs& a, const int grp, const int c, char* chsm) {
    const int wu = __builtin_amdgcn_readfirstlane(TIDX >> 6);
    const int widx = grp * 16 + c;
    const int hs = 4 * grp + (c >> 2), jq = c & 3;
    const rsrc_t rsS = slot_rsrc(a.slots);
    const unsigned l0 = (unsigned)(uintptr_t)(__attribute__((address_space(3))) char*)chsm;
    bool dead = false;

    const int cal_off = CAL_OFF + grp * 32768 + (c >> 3) * 16384 + (c & 7) * 1024;
        if (wu == 0) {
            const int l = lane_now();
            const v4u ff = {SENT, SENT, SENT, SENT};
#pragma unroll
            for (int e = 0; e < 13; ++e) st_sc1s(rsS, l * 16, widx * 2 * PERWG + min(e * 1024, 2 * PERWG - 1024), ff);
            const rsrc_t rsY = __builtin_amdgcn_make_buffer_rsrc((void*)a.sync, 0, 131072, 0x00020000);
            st_sc1s(rsY, l * 16, widx * 1024, v4u{0u, 0u, 0u, 0u});
            __builtin_amdgcn_raw_buffer_store_b32(0u, rsS, (l & 3) * 256 + ((l >> 2) & 1) * 8192, cal_off, AUX_SC1);
            if (c < 2) __builtin_amdgcn_raw_buffer_store_b32(0u, rsS, (l >> 5) * 8192 + (l & 31) * 4, TM_OFF + c * 16384 + grp * 256, AUX_SC1);
        }
    asm volatile("" ::: "memory");
    if (wu >= 4) {
        __builtin_amdgcn_s_sleep(DLY_U);
        const int l = opaque_tid() & 63, i = l & 15, g = l >> 4;
        const int sidx = (i & 1) ? 0xEEEE : 0x4444;
        const int u = wu - 4;
        const int k0 = 256 * u;
        u16* stg = (u16*)(chsm + 40960 + u * 20480);
        v4u af[4];
        v32u wS[6];
        {
            v4u raw[3][16];
            {
                const size_t wo = (size_t)k0 * HD + hcol(hs) + 32 * jq;
                const rsrc_t rq = __builtin_amdgcn_make_buffer_rsrc((void*)(a.Wq + wo), 0, 256 * HD * 2, 0x00020000);
                const rsrc_t rk = __builtin_amdgcn_make_buffer_rsrc((void*)(a.Wk + wo), 0, 256 * HD * 2, 0x00020000);
                const rsrc_t rv = __builtin_amdgcn_make_buffer_rsrc((void*)(a.Wv + wo), 0, 256 * HD * 2, 0x00020000);
                const int vw = (l >> 2) * HD * 2 + (l & 3) * 16;
#pragma unroll
                for (int p = 0; p < 4; ++p) af[p] = *(const v4u*)(a.mem0 + (size_t)(i >> 1) * DM + k0 + 64 * p + 16 * g + 8 * (i & 1));
#pragma unroll
                for (int e = 0; e < 16; ++e) raw[0][e] = __builtin_amdgcn_raw_buffer_load_b128(rq, vw, e * 16 * HD * 2, 0);
#pragma unroll
                for (int e = 0; e < 16; ++e) raw[1][e] = __builtin_amdgcn_raw_buffer_load_b128(rk, vw, e * 16 * HD * 2, 0);
#pragma unroll
                for (int e = 0; e < 12; ++e) raw[2][e] = __builtin_amdgcn_raw_buffer_load_b128(rv, vw, e * 16 * HD * 2, 0);
                __builtin_amdgcn_sched_barrier(0);
#pragma unroll
                for (int e = 0; e < 16; ++e) *(v4u*)(stg + (16 * e + (l >> 2)) * 40 + 8 * (l & 3)) = raw[0][e];
                __builtin_amdgcn_sched_barrier(0);
#pragma unroll
                for (int e = 12; e < 16; ++e) raw[2][e] = __builtin_amdgcn_raw_buffer_load_b128(rv, vw, e * 16 * HD * 2, 0);
            }
            const int fr = 16 * (g >> 1) + 4 * (g & 1) + (i >> 2);
            __builtin_amdgcn_sched_barrier(0);
#pragma unroll
            for (int hx = 0; hx < 3; ++hx) {
                if (hx) {
#pragma unroll
                    for (int e = 0; e < 16; ++e) *(v4u*)(stg + (16 * e + (l >> 2)) * 40 + 8 * (l & 3)) = raw[hx][e];
                }
                __builtin_amdgcn_s_waitcnt(0xC07F);
#pragma unroll
                for (int jj = 0; jj < 2; ++jj)
#pragma unroll
                    for (int p = 0; p < 4; ++p) PUT8(wS[2 * hx + jj], 8 * p, tr_frag64(stg + (64 * p + fr) * 40 + 16 * jj + 4 * (i & 3), 40))
                __builtin_amdgcn_s_waitcnt(0xC07F);
                __builtin_amdgcn_sched_barrier(0);
            }
        }
        PST(u)
        if (u == 0) { PST(4) }
        const unsigned r1w = l0 + C2_R1 + u * R1S + (2 * g * 16 + i) * 4, r1v = l0 + C2_R1 + u * R1S + 2176 + (2 * g * 16 + ((i & 9) | ((i & 2) << 1) | ((i & 4) >> 1))) * 4;
#define LD4(d, va, so, o) "buffer_load_dwordx4 v[" d "], " va ", %[rs], " so " offen offset:" o " sc1\n\t"
#define CHK(a, b, c, d) "v_max3_u32 v244, v244, v" a ", v" b "\n\tv_max3_u32 v244, v244, v" c ", v" d "\n\t"
#define CHK0(a, b, c, d) "v_max_u32 v244, v" a ", v" b "\n\tv_max3_u32 v244, v244, v" c ", v" d "\n\t"
#define SMF(d, a, w) "v_smfmac_f32_16x16x64_bf16 v[" d "], v[" a "], v[" w "], %[sx]\n\t"
#define ADD(d, a, b) "v_add_f32 v" d ", v" a ", v" b "\n\t"
#define Z2(r) "v_mov_b64 v[" r "], 0\n\t"
#define DW2(ad, x, y, o0, o1) "ds_write2_b32 " ad ", v" x ", v" y " offset0:" o0 " offset1:" o1 "\n\t"
#define A0 "224:227"
#define A1 "228:231"
#define A2 "232:235"
#define A3 "236:239"
#define Q0 "200:203"
#define Q1 "204:207"
#define Q2 "208:211"
#define Q3 "212:215"
#define V0 "216:219"
#define V1 "220:223"
#define S1_HEAD \
            Z2("216:217") Z2("218:219") Z2("220:221") Z2("222:223") \
            "s_waitcnt vmcnt(4)\n\t" ZQ \
            "s_waitcnt vmcnt(3)\n\t" SMF(V0, A0, "136:143") CHK0("224", "225", "226", "227") SMF(V1, A0, "168:175") \
            "s_waitcnt vmcnt(2)\n\t" SMF(V0, A1, "144:151") CHK("228", "229", "230", "231") SMF(V1, A1, "176:183") \
            "s_waitcnt vmcnt(1)\n\t" SMF(V0, A2, "152:159") CHK("232", "233", "234", "235") SMF(V1, A2, "184:191") \
            "s_waitcnt vmcnt(0)\n\t" SMF(V0, A3, "160:167") CHK("236", "237", "238", "239") SMF(V1, A3, "192:199") \
            "v_cmp_eq_u32 vcc, " STR(SENT) ", v244\n\t"
#define S1_TAIL(B1, B2) \
            SMF(Q0, A0, "8:15") SMF(Q0, A1, "16:23") SMF(Q0, A2, "24:31") \
            ADD("240", "216", "217") ADD("241", "218", "219") SMF(Q0, A3, "32:39") DW2("%[rv]", "240", "241", "0", "16") \
            ADD("242", "220", "221") ADD("243", "222", "223") SMF(Q1, A0, "40:47") DW2("%[rv]", "242", "243", "128", "144") \
            SMF(Q1, A1, "48:55") SMF(Q1, A2, "56:63") SMF(Q1, A3, "64:71") \
            ADD("240", "200", "201") ADD("241", "202", "203") SMF(Q2, A0, "72:79") DW2("%[rw]", "240", "241", "0", "16") \
            SMF(Q2, A1, "80:87") SMF(Q2, A2, "88:95") "s_nop 1\n\t" \
            ADD("242", "204", "205") ADD("243", "206", "207") SMF(Q2, A3, "96:103") DW2("%[rw]", "242", "243", "136", "152") \
            SMF(Q3, A0, "104:111") SMF(Q3, A1, "112:119") \
            B1        \
            LTA(0) \
            SMF(Q3, A2, "120:127") SMF(Q3, A3, "128:135") "s_nop 0\n\t" \
            ADD("240", "208", "209") ADD("241", "210", "211") DW2("v246", "240", "241", "0", "16") \
            "s_nop 3\n\t" \
            ADD("242", "212", "213") ADD("243", "214", "215") DW2("v246", "242", "243", "136", "152") \
            "s_waitcnt lgkmcnt(0)\n\t" B2 "s_barrier\n\t"
#define ZQ Z2("200:201") Z2("202:203") Z2("204:205") Z2("206:207") Z2("208:209") Z2("210:211") Z2("212:213") Z2("214:215")
#define U_W "{v[8:39]}"(wS[0]), "{v[40:71]}"(wS[1]), "{v[72:103]}"(wS[2]), "{v[104:135]}"(wS[3]), "{v[136:167]}"(wS[4]), "{v[168:199]}"(wS[5])
#define U_CLOB CLOB16(200, 201, 202, 203, 204, 205, 206, 207, 208, 209, 210, 211, 212, 213, 214, 215), CLOB16(216, 217, 218, 219, 220, 221, 222, 223, 224, 225, 226, 227, 228, 229, 230, 231), \
            CLOB16(232, 233, 234, 235, 236, 237, 238, 239, 240, 241, 242, 243, 244, 245, 246, 247)
        __builtin_amdgcn_s_setprio(2);
        {
            asm volatile(
                "v_add_u32 v246, 1088, %[rw]\n\t"
                S1_HEAD "s_nop 0\n\t" S1_TAIL("s_nop 1\n\t", "s_barrier\n\t")
                : LTOUT0
                : U_W, "{v[224:227]}"(af[0]), "{v[228:231]}"(af[1]), "{v[232:235]}"(af[2]), "{v[236:239]}"(af[3]), [sx] "v"(sidx), [rv] "v"(r1v), [rw] "v"(r1w)
                : "memory", "vcc", CLOB16(200, 201, 202, 203, 204, 205, 206, 207, 208, 209, 210, 211, 212, 213, 214, 215), CLOB16(216, 217, 218, 219, 220, 221, 222, 223, 240, 241, 242, 243, 244, 245, 246, 247));
        }
        const unsigned tau = __builtin_amdgcn_readfirstlane(*(volatile unsigned*)(chsm + C2_MB + 8));
        const unsigned long long wmask = u == 3 ? (c == 0 ? ~0ull : 0ull) : u == 1 ? 0x0020202020202000ull : u == 2 ? 1ull << (NSEG - 3) : 0ull;
        const int pvv = u == 3 ? 2 : -1, pvm = u == 3 ? -1 : 0;
        const unsigned* pfl = a.sync + (u == 3 ? SY_PROG + grp : u == 1 ? SY_KV + 16 + grp : SY_Q + 7 * 8 + grp) * 32;
        int s_first = 0;
        asm volatile("s_branch 1f\n\t.p2align 8\n1:" : "+s"(s_first));
        for (int s = s_first; s < NSEG - 1; ++s) {
            __builtin_amdgcn_s_setprio(0);
            LT(1)
            const int pv = (wmask >> s) & 1ull ? pvv + (s & pvm) : 0;
            const unsigned* pa = pfl;
            if (pv < 0) pfl += 256;
            const int cb = grp & 3;
            int soffc = rec_base(s) + C_OFF + c * 16384 + (int)((half_bits(s, tau) >> grp) & 1u) * 8192 + cb * 2048;
            int o_as = rec_base(s + 1) + ((c >> 3) + (grp >> 2) * 4) * 16384 + (int)((half_bits(s + 1, tau) >> grp) & 1u) * 8192 + ((4 * c + cb) & 31) * 256;
            int soff = chunk_off(s + 1, grp, u >> 1, tau) + (u & 1) * 4096;
            const int o_tm = (s >> 1) * 2097152 + TM_OFF + (s & 1) * 16384;
            int lim = dead ? 0 : SPIN_LIMIT, bad = 0;
            const bool rcd = u > 0 || widx < 32;
            const int pc = u > 0 ? 3 * widx + u - 1 : rcd ? 384 + widx : s * 96 + widx - 32;
            const int o_arm = (rcd ? s + 2 < NSEG : pc < 512) ? (rcd ? rec_base(s + 2) : XB_OFF) + (pc >> 3) * 16384 + (int)(half_bits(rcd ? s + 2 : 0, tau) & 1u) * 8192 + (pc & 7) * 1024 : OOB;
            asm volatile("" : "+s"(soffc), "+s"(o_as), "+s"(soff), "+s"(lim));
#define GL(b, o) "global_load_lds_dwordx4 %[l16], " b " offset:" o " sc1\n\t"
#define PK PAUSE(PKP)
#define PK4 PAUSE(PKM)
#define PKE PAUSE(PKA)
#define PKS PAUSE(PK0)
#define C_TRY(x, x0, x1, x2, x3, y, y0, y1, y2, y3) \
                "s_waitcnt vmcnt(3)\n\t" \
                "v_mfma_f32_16x16x32_bf16 v[240:243], v[216:219], v[" x "], 0\n\t" CHK0(x0, x1, x2, x3) \
                "s_waitcnt vmcnt(2)\n\t" \
                "v_mfma_f32_16x16x32_bf16 v[240:243], v[216:219], v[" y "], v[240:243]\n\t" CHK(y0, y1, y2, y3) \
                "v_cmp_eq_u32 vcc, " STR(SENT) ", v244\n\t" \
                "s_cbranch_vccz 3b\n\t" CN(1) \
                LD4(x, "%[l16]", "%[sc]", "0") LD4(y, "%[l16]", "%[sc]", "1024")
            asm volatile(
                "v_and_b32 v221, 16, %[l16]\n\t"
                "v_mov_b32 v222, 0x3f80\n\t"
                "v_bfe_u32 v223, %[l16], 5, 2\n\t"
                "v_lshlrev_b32 v221, v221, v222\n\t"
                "v_cmp_eq_u32 vcc, 0, v223\n\tv_and_b32 v220, 0xf0, %[l16]\n\tv_bfe_u32 v222, %[l16], 8, 1\n\tv_cndmask_b32 v216, 0, v221, vcc\n\t"
                "v_cmp_eq_u32 vcc, 1, v223\n\tv_lshl_add_u32 v220, v222, 3, v220\n\tv_lshrrev_b32 v222, 9, %[l16]\n\tv_cndmask_b32 v217, 0, v221, vcc\n\t"
                "v_cmp_eq_u32 vcc, 2, v223\n\tv_lshl_add_u32 v220, v222, 30, v220\n\ts_nop 0\n\tv_cndmask_b32 v218, 0, v221, vcc\n\t"
                "v_cmp_eq_u32 vcc, 3, v223\n\ts_nop 1\n\tv_cndmask_b32 v219, 0, v221, vcc\n\t"
                "v_mul_u32_u24 v222, 0x8000, v222\n\t"
                "v_and_b32 v221, 0xff, v220\n\t"
                "v_add_u32 v221, v221, v222\n\t"
                "v_lshrrev_b32 v247, 2, %[l16]\n\t"
                PKE
                "v_mov_b32 v240, " STR(SENT) "\n\tv_mov_b32 v241, v240\n\tv_mov_b32 v242, v240\n\tv_mov_b32 v243, v240\n\t"
                "buffer_store_dwordx4 v[240:243], %[l16], %[rs], %[arm] offen sc1\n\t"
                LTA(2)
                "s_setprio 2\n\t"
                "s_barrier\n\t"
                LTA(3)
                "v_add_u32 v246, 1088, %[rw]\n\t"
                "s_cmp_lt_u32 %[uu], 1\n\t"
                "s_cbranch_scc1 7f\n\t"
                "s_cmp_eq_u32 %[uu], 3\n\t"
                "s_cbranch_scc1 20f\n\t"
                "s_memrealtime s[84:85]\n\t"
                "v_lshrrev_b32 v241, 9, %[l16]\n\t"
                "v_and_b32 v242, 0x7c, v247\n\t"
                "v_lshl_add_u32 v241, v241, 13, v242\n\t"
                "s_waitcnt lgkmcnt(0)\n\t"
                "v_mov_b32 v240, s84\n\t"
                "buffer_store_dword v240, v241, %[rs], %[ts] offen sc1\n\t"
                "s_cmp_lt_i32 %[pv], 0\n\t"
                "s_cbranch_scc0 8f\n\t"
                "v_and_b32 v241, 12, v247\n\t"
                "10:\n\t"
                "global_load_dword v240, v241, %[pa] sc1\n\t"
                "s_waitcnt vmcnt(0)\n\t"
                "v_cmp_gt_u32 vcc, 16, v240\n\t"
                "s_cbranch_vccz 8f\n\t"
                "s_sleep 6\n\t"
                "s_sub_u32 %[lim], %[lim], 1\n\t"
                "s_cbranch_scc0 10b\n\t"
                "s_mov_b32 %[bad], 3000\n\t"
                "s_mov_b32 %[lim], 0\n\t"
                "s_branch 8f\n\t"
                "20:\n\t"
                PAUSE(PS)
                "s_memrealtime s[84:85]\n\t"
                "s_load_dword s86, %[tb], 0\n\ts_load_dword s87, %[tb], 0x100\n\ts_load_dword s88, %[tb], 0x200\n\ts_load_dword s89, %[tb], 0x300\n\t"
                "s_load_dword s90, %[tb], 0x400\n\ts_load_dword s91, %[tb], 0x500\n\ts_load_dword s92, %[tb], 0x600\n\ts_load_dword s93, %[tb], 0x700\n\t"
                "s_waitcnt lgkmcnt(0)\n\t"
                "s_sub_u32 s86, s84, s86\n\ts_sub_u32 s87, s84, s87\n\ts_sub_u32 s88, s84, s88\n\ts_sub_u32 s89, s84, s89\n\t"
                "s_sub_u32 s90, s84, s90\n\ts_sub_u32 s91, s84, s91\n\ts_sub_u32 s92, s84, s92\n\ts_sub_u32 s93, s84, s93\n\t"
                "s_or_b32 s85, s86, s87\n\ts_min_u32 s86, s86, s87\n\ts_or_b32 s85, s85, s88\n\ts_min_u32 s86, s86, s88\n\ts_or_b32 s85, s85, s89\n\ts_min_u32 s86, s86, s89\n\t"
                "s_memrealtime s[88:89]\n\t"
                "s_or_b32 s85, s85, s90\n\ts_min_u32 s86, s86, s90\n\ts_or_b32 s85, s85, s91\n\ts_min_u32 s86, s86, s91\n\ts_or_b32 s85, s85, s92\n\ts_min_u32 s86, s86, s92\n\t"
                "s_or_b32 s85, s85, s93\n\ts_min_u32 s86, s86, s93\n\t"
                "s_cmp_gt_u32 s85, 127\n\t"
                CNS
                "s_cselect_b32 s85, " STR(TE0) ", 127\n\t"
                "s_min_u32 s86, s86, s85\n\t"
                "s_sub_u32 s84, s84, s86\n\t"
                "s_add_u32 s84, s84, %[td]\n\t"
                "s_cmp_lt_i32 %[pv], 1\n\t"
                "s_cbranch_scc1 4f\n\t"
                "v_mov_b32 v247, %[pv]\n\t"
                "s_mov_b64 exec, 1\n\t"
                "global_store_dword %[l16], v247, %[pa] sc1\n\t"
                "s_mov_b64 exec, -1\n\t"
                "4:\n\t"
                "s_waitcnt lgkmcnt(0)\n\t"
                "s_sub_i32 s84, s84, s88\n\t"
                "s_max_i32 s84, s84, 0\n\t"
                "s_min_i32 s84, s84, 40\n\t"
                "21:\n\t"
                "s_sub_u32 s84, s84, 1\n\t"
                "s_cbranch_scc0 21b\n\t"
                "s_branch 8f\n\t"
                "7:\n\t"
                PAUSE(PC)
                LD4(A0, "%[l16]", "%[sc]", "0") LD4(A1, "%[l16]", "%[sc]", "1024")
                PAUSE(PP)
                LD4(A2, "%[l16]", "%[sc]", "0") LD4(A3, "%[l16]", "%[sc]", "1024")
                "s_waitcnt vmcnt(3)\n\t"
                "v_mfma_f32_16x16x32_bf16 v[240:243], v[216:219], v[224:227], 0\n\t" CHK0("224", "225", "226", "227")
                "s_waitcnt vmcnt(2)\n\t"
                "v_mfma_f32_16x16x32_bf16 v[240:243], v[216:219], v[228:231], v[240:243]\n\t" CHK("228", "229", "230", "231")
                "v_cmp_eq_u32 vcc, " STR(SENT) ", v244\n\t"
                "s_cbranch_vccnz 30f\n\t"
                "3:\n\t"
                "s_nop 3\n\t"
                "v_cvt_pk_bf16_f32 v244, v240, v241\n\t"
                "v_cvt_pk_bf16_f32 v245, v242, v243\n\t"
                "buffer_store_dwordx2 v[244:245], v221, %[rs], %[oas] offen sc1\n\t"
                PAUSE(PB)
                "8:\n\t"
                LTA(4)
                "s_barrier\n\t"
                PAUSE(1)
                "2:\n\t"
                LD4(A0, "%[l16]", "%[sa]", "0") LD4(A1, "%[l16]", "%[sa]", "1024") LD4(A2, "%[l16]", "%[sa]", "2048") LD4(A3, "%[l16]", "%[sa]", "3072")
                S1_HEAD
                "s_cbranch_vccnz 6f\n\t"
                "5:\n\t"
                LTA(5)
                S1_TAIL("s_waitcnt lgkmcnt(0)\n\ts_barrier\n\t", "")
                "s_setprio 0\n\t"
                "s_branch 9f\n\t"
                "30:\n\t" CN(1)
                LD4(A0, "%[l16]", "%[sc]", "0") LD4(A1, "%[l16]", "%[sc]", "1024")
                "1:\n\t"
                C_TRY(A2, "232", "233", "234", "235", A3, "236", "237", "238", "239")
                C_TRY(A0, "224", "225", "226", "227", A1, "228", "229", "230", "231")
                "s_sub_u32 %[lim], %[lim], 1\n\t"
                "s_cbranch_scc0 1b\n\t"
                "s_mov_b32 %[bad], 2000\n\t"
                "s_mov_b32 %[lim], 0\n\t"
                "s_branch 3b\n\t"
                "6:\n\t" CN(65536)
                "v_mov_b32 v240, %[tg]\n\t"
                "v_mov_b32 v241, %[lf]\n\t"
                "ds_write_b32 v241, v240\n\t"
                "s_nop 0\n\t"
                "s_sub_u32 %[lim], %[lim], 1\n\t"
                "s_cbranch_scc0 2b\n\t"
                "s_mov_b32 %[bad], 1\n\t"
                "s_branch 5b\n\t"
                "9:\n\t"
                : [lim] "+s"(lim), [bad] "+s"(bad), [oas] "+s"(o_as) LTOUT CNOUT
                : U_W, [l16] "v"(l * 16), [rs] "s"(rsS), [sx] "v"(sidx), [rv] "v"(r1v), [rw] "v"(r1w),
                  [sc] "s"(soffc), [sa] "s"(soff), [uu] "s"(u), [pv] "s"(pv), [pa] "s"(pa), [td] "s"(s >= SL0 ? TD2 : TD), [ts] "s"(c == 0 && (u == 2 || s < NSEG - 3) ? o_tm + grp * 256 + (2 - u) * 2097152 : OOB), [tb] "s"(arena(a.slots) + o_tm + ((half_bits(s, tau) >> grp) & 1u) * 8192),

                  [arm] "s"(o_arm)
,
                  [tg] "s"(s + 1), [lf] "s"(l0 + C2_MB + 16)
                : "memory", "vcc", "scc", "s84", "s85", "s86", "s87", "s88", "s89", "s90", "s91", "s92", "s93", U_CLOB);
            if (__builtin_expect(bad != 0, 0)) TMO_SET((unsigned)bad)
        }
        last_rows(chsm, rsS, wu, grp, c, tau);
        if (u == 3 && widx == 0 && l == 0) __hip_atomic_store((gu64*)(a.boot + 144), *(volatile unsigned long long*)(chsm + C2_MB), __ATOMIC_RELAXED, __HIP_MEMORY_SCOPE_AGENT);
        return;
    }
    const int l = opaque_tid() & 63, i = l & 15, g = l >> 4;
    const int sidx = (i & 1) ? 0xEEEE : 0x4444;
    const int ha = 4 * grp + wu;
    unsigned long long tag;
    v32u wO[2];
    {
        u16* stg = (u16*)(chsm + 122880 + wu * 10240);
        tag = boot_tag(a.boot);
        asm volatile("" ::: "memory");
        v4u rawo[16];
        const u16* src = a.Wo + (size_t)(hcol(ha) + (l >> 3)) * DM + 64 * c + 8 * (l & 7);
#pragma unroll
        for (int e = 0; e < 16; ++e) rawo[e] = *(const v4u*)(src + (size_t)8 * e * DM);
        __builtin_amdgcn_sched_barrier(0);
        if (wu == 0) {
            asm volatile("s_waitcnt vmcnt(16)" ::: "memory");
            unsigned tm[2] = {~0u, ~0u};
            const char* pb = arena(a.slots) + cal_off;
#pragma unroll
            for (int k = 0; k < 2 * NPROBE; ++k) {
                unsigned long long c0, c1; unsigned d;
                asm volatile("s_memtime %0\n\ts_waitcnt lgkmcnt(0)\n\ts_load_dword %2, %3, %4\n\ts_waitcnt lgkmcnt(0)\n\ts_memtime %1\n\ts_waitcnt lgkmcnt(0)"
                             : "=&s"(c0), "=&s"(c1), "=&s"(d) : "s"(pb), "i"((k >> 1) * 256 + (k & 1) * 8192) : "memory");
                tm[k & 1] = min(tm[k & 1], (unsigned)(c1 - c0));
            }
            const unsigned side = (__builtin_amdgcn_s_getreg(20 | (3 << 11)) >> 2) & 1u;
            if (l == 0) __hip_atomic_store((gu64*)(a.boot + widx), (tag << 2) | (((tm[0] > tm[1] ? 1u : 0u) ^ side) << 1) | side, __ATOMIC_RELAXED, __HIP_MEMORY_SCOPE_AGENT);
            PST(7)
        }
#pragma unroll
        for (int m = 0; m < 2; ++m) {
#pragma unroll
            for (int e = 0; e < 8; ++e) *(v4u*)(stg + (8 * e + (l >> 3)) * 72 + 8 * (l & 7)) = rawo[8 * m + e];
            __builtin_amdgcn_s_waitcnt(0xC07F);
#pragma unroll
            for (int ct = 0; ct < 4; ++ct) {
                const u16* p = stg + (8 * g + (i >> 2)) * 72 + 16 * ct + 4 * (i & 3);
                const v2u r0 = tr4(p), r1 = tr4(p + 4 * 72), r2 = tr4(p + 32 * 72), r3 = tr4(p + 36 * 72);
                PUT4(wO[ct >> 1], (ct & 1) * 16 + m * 8, (v4u{r0[0], r0[1], r1[0], r1[1]})) PUT4(wO[ct >> 1], (ct & 1) * 16 + m * 8 + 4, (v4u{r2[0], r2[1], r3[0], r3[1]}))
            }
            __builtin_amdgcn_s_waitcnt(0xC07F);
        }
        if (wu == 0) { PST(5) }
        if (widx == 0 && wu == 0) *(volatile unsigned long long*)(chsm + C2_MB) = tag;
    }
    if (wu >= 2) touch_sc1(arena(a.slots) + (size_t)min(l + (wu - 2) * 64, FX_OFF / 2097152 - 1) * 2097152 + 2097024 - 128, l0 + C2_MB + 256);
    unsigned long long fw = 0;
#pragma nounroll
    for (int spin = 0; spin < 100000; ++spin) {
        asm volatile("" ::: "memory");
        fw = __hip_atomic_load((gu64*)(a.boot + 256 + 16 * grp + (l & 7)), __ATOMIC_RELAXED, __HIP_MEMORY_SCOPE_AGENT);
        if (__all((fw | 255u) == ((tag << 8) | 255u))) break;
        __builtin_amdgcn_s_sleep(4);
    }
    asm volatile("" ::: "memory");
    const unsigned tau = __builtin_amdgcn_readfirstlane((unsigned)fw & 255u);
    if (wu == 3) { *(volatile unsigned*)(chsm + C2_MB + 8) = tau; *(volatile unsigned*)(chsm + C2_MB + 16) = ~0u; __builtin_amdgcn_s_waitcnt(0xC07F); }
    if (wu == 1) { PST(9) }

    const unsigned r3a = l0 + C2_R3 + wu * 2048 + g * 128 + i * 4;
    v32u yb;
    v8u kx;
    v8u sel; v2u sel3;
#pragma unroll
    for (int d = 0; d < 8; ++d) sel[d] = (i >> 1) == d ? (0x3F80u << ((i & 1) * 16)) : 0u;
#pragma unroll
    for (int d = 0; d < 2; ++d) sel3[d] = (i & 3) == 0 && (i >> 3) == d ? (0x3F80u << (((i >> 2) & 1) * 16)) : 0u;
#define MF(d, a, b, c) "v_mfma_f32_16x16x32_bf16 v[" d "], v[" a "], v[" b "], " c "\n\t"
#define PX4(d0, d1, d2, d3, a, w0, w1, w2, w3) MF(d0, a, w0, "v[" d0 "]") MF(d1, a, w1, "v[" d1 "]") MF(d2, a, w2, "v[" d2 "]") MF(d3, a, w3, "v[" d3 "]")
#define PXT(d0, d1, d2, d3, a0, a1, a2, a3) MF(d0, a0, "32:35", "0") MF(d1, a0, "48:51", "0") MF(d2, a0, "64:67", "0") MF(d3, a0, "80:83", "0") \
        PX4(d0, d1, d2, d3, a1, "36:39", "52:55", "68:71", "84:87") PX4(d0, d1, d2, d3, a2, "40:43", "56:59", "72:75", "88:91") PX4(d0, d1, d2, d3, a3, "44:47", "60:63", "76:79", "92:95")
#define CVP(d, a, b) "v_cvt_pk_bf16_f32 v" d ", v" a ", v" b "\n\t"
#define PROJ_X(cnt) \
        "s_waitcnt " cnt "(8)\n\t" PXT("192:195", "196:199", "200:203", "204:207", "160:163", "164:167", "168:171", "172:175") \
        "s_waitcnt " cnt "(4)\n\t" PXT("208:211", "212:215", "216:219", "220:223", "176:179", "180:183", "184:187", "188:191") \
        CVP("128", "192", "193") CVP("129", "194", "195") CVP("136", "196", "197") CVP("137", "198", "199") CVP("144", "200", "201") CVP("145", "202", "203") CVP("152", "204", "205") CVP("153", "206", "207") \
        CVP("132", "208", "209") CVP("133", "210", "211") CVP("140", "212", "213") CVP("141", "214", "215") CVP("148", "216", "217") CVP("149", "218", "219") CVP("156", "220", "221") CVP("157", "222", "223") \
        "s_waitcnt " cnt "(0)\n\t"
#define CLOB_PX CLOB16(160, 161, 162, 163, 164, 165, 166, 167, 168, 169, 170, 171, 172, 173, 174, 175), CLOB16(176, 177, 178, 179, 180, 181, 182, 183, 184, 185, 186, 187, 188, 189, 190, 191), \
        CLOB16(192, 193, 194, 195, 196, 197, 198, 199, 200, 201, 202, 203, 204, 205, 206, 207), CLOB16(208, 209, 210, 211, 212, 213, 214, 215, 216, 217, 218, 219, 220, 221, 222, 223)
#define LDX(d, va, so, o) "buffer_load_dwordx4 v[" d "], " va ", %[rs], " so " offen offset:" o " sc1\n\t"
#define DSV(d, o) "ds_read_b128 v[" d "], %[lv] offset:" o "\n\t"
    const int voffX = ((i >> 1) & 1) * 4096 + g * 256 + (2 * (i >> 2) + (i & 1)) * 16;
    const int pk = (((i >> 1) & 3) << 1) | (i >> 3);
    const unsigned hv = wu >= 2 ? l0 + C2_R1 + 2176 + ((wu & 1) * 128 + ((l >> 4) + 4 * ((l >> 3) & 1)) * 16 + 8 * ((l >> 1) & 1) + 4 * ((l >> 2) & 1) + 2 * (l & 1)) * 4
                                : l0 + C2_R1 + (i & 1) * 544 + (i >> 1) * 64 + g * 16;
    const unsigned hk = l0 + C2_R1 + 1088 + (i & 1) * 544 + pk * 64 + g * 16;
    const int vo1 = wu >= 2 ? l * 4 : wu == 1 ? (g >> 1) * 1024 + i * 16 + (g & 1) * 8 : (i & 1) ? 1 << 30 : (i >> 1) * 16 + 4 * g;
    const int vo2 = (2 * (i & 1) + (g >> 1)) * 128 + pk * 16 + (g & 1) * 8;
    const int voffK = (g >> 1) * 256 + i * 16 + (g & 1) * 8;
#define RD4(w, ad, a, b, c, d) "ds_read_" w " v[" a "], " ad "\n\tds_read_" w " v[" b "], " ad " offset:3200\n\tds_read_" w " v[" c "], " ad " offset:6400\n\tds_read_" w " v[" d "], " ad " offset:9600\n\t"
#define AD4(d0, d1, d2, d3, a0, a1, a2, a3) ADD(d0, d0, a0) ADD(d1, d1, a1) ADD(d2, d2, a2) ADD(d3, d3, a3)
#define QSC(r) "v_mul_f32 v" r ", 0x3e0293ee, v" r "\n\t"
#define FLD "buffer_load_dword v114, %[vf], %[rs], %[fl] offen sc1\n\t"
#define DUM "buffer_store_dword v168, off, %[rs], %[ob] sc1\n\t"
#define QHEAD "s_barrier\n\t" RD4("b128", "%[hv]", "160:163", "164:167", "168:171", "172:175") FLD \
        "s_waitcnt lgkmcnt(2)\n\t" AD4("160", "161", "162", "163", "164", "165", "166", "167") \
        "s_waitcnt lgkmcnt(0)\n\t" AD4("168", "169", "170", "171", "172", "173", "174", "175") \
        "s_barrier\n\t"
#define QTAIL AD4("160", "161", "162", "163", "168", "169", "170", "171") QSC("160") QSC("161") QSC("162") QSC("163")
#define SWA(d, a, b) "v_add_f32_dpp v" d ", v" a ", v" b " quad_perm:[1,0,3,2] row_mask:0xf bank_mask:0xf\n\t"
#define KLD(so) "buffer_load_dwordx2 v[96:97], %[vk], %[rs], " so " offen sc1\n\tbuffer_load_dwordx2 v[102:103], %[vk], %[rs], " so " offen offset:512 sc1\n\t" \
        "s_add_u32 " so ", " so ", 4096\n\t" \
        "buffer_load_dwordx2 v[100:101], %[vk], %[rs], " so " offen sc1\n\tbuffer_load_dwordx2 v[98:99], %[vk], %[rs], " so " offen offset:512 sc1\n\t"
    auto hand_over = [&](const int o_1, const int o_2, const int o_f, const int tg) {
        int dmy = 0;
        asm volatile(
            "s_cmp_eq_u32 %[wu], 0\n\t"
            "s_cbranch_scc1 2f\n\t"
            "s_cmp_eq_u32 %[wu], 1\n\t"
            "s_cbranch_scc1 1f\n\t"
            "s_barrier\n\t"
            RD4("b64", "%[hv]", "160:161", "162:163", "164:165", "166:167") FLD
            "s_waitcnt lgkmcnt(2)\n\t" ADD("160", "160", "162") ADD("161", "161", "163")
            "s_waitcnt lgkmcnt(0)\n\t" ADD("164", "164", "166") ADD("165", "165", "167") ADD("160", "160", "164") ADD("161", "161", "165")
            CVP("168", "160", "161")
            "buffer_store_dword v168, %[v1], %[rs], %[o1] offen sc1\n\t"
            "s_barrier\n\t"
            DUM PAUSE(PV2)
            "s_branch 3f\n\t"
            "5:\n\t"
            "v_mov_b32 v115, %[lf]\n\t"
            "ds_read_b32 v115, v115\n\t"
            "s_waitcnt lgkmcnt(0)\n\t"
            "v_cmp_eq_u32 vcc, %[tg], v115\n\t"
            "s_cbranch_vccnz 4f\n\t" CN(256)
            PAUSE(PL)
            "s_branch 4f\n\t"
            "1:\n\t"
            Z2("176:177") Z2("178:179")
            "s_waitcnt vmcnt(0)\n\t"
            QHEAD QTAIL
            "s_mov_b64 exec, %[em]\n\t"
            CVP("176", "160", "161") CVP("177", "162", "163")
            "s_not_b64 exec, exec\n\t"
            CVP("178", "160", "161") CVP("179", "162", "163")
            "s_mov_b64 exec, -1\n\t"
            "s_nop 0\n\t"
            MF("180:183", "96:99", "176:179", "0") MF("184:187", "100:103", "176:179", "0")
            "s_nop 7\n\t"
            SWA("180", "184", "180") SWA("181", "185", "181") SWA("182", "186", "182") SWA("183", "187", "183")
            CVP("188", "180", "181") CVP("189", "182", "183")
            "buffer_store_dwordx2 v[188:189], %[v1], %[rs], %[o1] offen sc1\n\t"
            DUM PAUSE(PV1)
            "s_branch 3f\n\t"
            "2:\n\t"
            QHEAD
            RD4("b128", "%[hk]", "176:179", "180:183", "184:187", "188:191")
            QTAIL CVP("164", "160", "161") CVP("165", "162", "163")
            "s_waitcnt lgkmcnt(2)\n\t" AD4("176", "177", "178", "179", "180", "181", "182", "183")
            "s_waitcnt lgkmcnt(0)\n\t" AD4("184", "185", "186", "187", "188", "189", "190", "191") AD4("176", "177", "178", "179", "184", "185", "186", "187")
            CVP("166", "176", "177") CVP("167", "178", "179")
            "s_nop 1\n\t"
            "v_mfma_f32_16x16x16_bf16 v[168:171], v[166:167], v[164:165], 0\n\t"
            "buffer_store_dwordx2 v[166:167], %[v2], %[rs], %[o2] offen sc1\n\t"
            "s_nop 6\n\t"
            SWA("172", "169", "168") SWA("173", "171", "170")
            CVP("174", "172", "173")
            "buffer_store_dword v174, %[v1], %[rs], %[o1] offen sc1\n\t"
            "3:\n\t"
            PAUSE(PVA)
            "s_waitcnt vmcnt(2)\n\t"
            "v_cmp_ne_u32 vcc, " STR(SENT) ", v114\n\t"
            "s_cbranch_vccnz 5b\n\t"
            "4:\n\t"
            : [dm] "+s"(dmy) CNOUT : "{v[96:103]}"(kx), [fl] "s"(o_f), [vf] "v"(i * 128), [ob] "s"(1 << 30), [tg] "s"(tg), [lf] "s"(l0 + C2_MB + 16), [wu] "s"(wu), [hv] "v"(hv), [hk] "v"(hk), [v1] "v"(vo1), [v2] "v"(vo2), [rs] "s"(rsS), [o1] "s"(o_1), [o2] "s"(o_2), [em] "s"(0x5555555555555555ull)
            : "memory", "scc", "vcc", "v114", "v115", CLOB16(160, 161, 162, 163, 164, 165, 166, 167, 168, 169, 170, 171, 172, 173, 174, 175), CLOB16(176, 177, 178, 179, 180, 181, 182, 183, 184, 185, 186, 187, 188, 189, 190, 191));
    };
    auto o1_of = [&](const int s) { return wu >= 2 ? v_off(s, hs, tau) + jq * 512 + (wu & 1) * 256 : wu == 1 ? x_off(s, hs, tau) + jq * 256 : m_off(s, hs, tau) + jq * 128; };

    __builtin_amdgcn_s_setprio(1);
    int o_f = f_off(0, grp, tau);
    int o_cs = rec_base(0) + C_OFF + c * 16384 + (int)(half_bits(0, tau) & 1u) * 8192 + wu * 2048 + grp * 256;
    int o_1 = o1_of(0), o_2 = km_off(0, hs, tau) + jq * 512, bq = x_off(0, ha, tau), bm = m_off(0, ha, tau), bb = v_off(0, ha, tau);
    {
            const int b0 = blk_off(0, ha, tau);
            int k0 = KX_OFF + blk_off(0, hs, tau) + jq * 1024;
            asm volatile(
                LDX("160:163", "%[vx]", "%[vb]", "0") LDX("164:167", "%[vx]", "%[vb]", "1024") LDX("168:171", "%[vx]", "%[vb]", "2048") LDX("172:175", "%[vx]", "%[vb]", "3072")
                LDX("176:179", "%[vx]", "%[vb]", "128") LDX("180:183", "%[vx]", "%[vb]", "1152") LDX("184:187", "%[vx]", "%[vb]", "2176") LDX("188:191", "%[vx]", "%[vb]", "3200")
                KLD("%[k0]")
                "v_mov_b32 v131, 0\n\tv_mov_b32 v135, 0\n\tv_mov_b32 v139, 0\n\tv_mov_b32 v143, 0\n\tv_mov_b32 v147, 0\n\tv_mov_b32 v151, 0\n\tv_mov_b32 v155, 0\n\tv_mov_b32 v159, 0\n\t"
                PROJ_X("vmcnt")
                : "=&{v[128:159]}"(yb), "=&{v[96:103]}"(kx), [k0] "+s"(k0)
                : "{v[32:63]}"(wO[0]), "{v[64:95]}"(wO[1]), [vx] "v"(voffX), [vk] "v"(voffK), [rs] "s"(rsS), [vb] "s"(VX_OFF + b0)
                : "memory", "scc", CLOB_PX);
        if (wu == 1) { PST(10) }
    }
    bool ahead = false;
    int s_first = 0;
    asm volatile("s_branch 1f\n\t.p2align 8\n1:" : "+s"(s_first));
    for (int s = s_first; s < NSEG - 1; ++s) {
        unsigned long long ok = 0;
        const int sq = s + 1 + ((l >> 3) & 1);
        const unsigned* fp = a.sync + (sq < KVS ? (SY_KV0 + sq * 8 + grp) * 32 + (l & 7) : (SY_KV + 8 + grp) * 32 + (l & 3));
        if (__builtin_expect(s <= KVS && ahead, 0)) ok = __ballot(ld_flag(fp) != 0u);
        hand_over(o_1, o_2, o_f, s);
        if (s == 0 && wu == 1) { PST(8) }
        LT(0)
#define LDPA "buffer_load_dwordx2 v[160:161], v254, %[rs], %[bm] offen sc1\n\t" LDX("164:167", "%[l16]", "%[bq]", "0") LDX("168:171", "%[l16]", "%[bq]", "1024")
#define SM(d, a, b, x) "v_smfmac_f32_16x16x64_bf16 v[" d "], v[" a "], v[" b "], " x "\n\t"
#define MX2(a, b) "v_max_u32 v244, v" a ", v" b "\n\t"
#define MX3(a, b) "v_max3_u32 v244, v244, v" a ", v" b "\n\t"
#define ZAM Z2("208:209") Z2("210:211") Z2("212:213") Z2("214:215") Z2("216:217") Z2("218:219") Z2("220:221") Z2("222:223")
#define MUL(d, a, b) "v_mul_f32 v" d ", v" a ", v" b "\n\t"
#define CV0(d, a) "v_cvt_pk_bf16_f32 v" d ", v" a ", 0\n\t"
#define RDC "ds_read_b64 v[224:225], %[rc]\n\tds_read_b64 v[226:227], %[rc] offset:2048\n\tds_read_b64 v[228:229], %[rc] offset:4096\n\tds_read_b64 v[230:231], %[rc] offset:6144\n\t"
#define EXD(d, r) "v_exp_f32 v" d ", v" r "\n\t"
#define SUBM(r) "v_sub_f32 v" r ", v" r ", v250\n\t"
#define PCVM CVP("241", "180", "180") CVP("243", "180", "180")
#define PCVX CVP("242", "178", "179") CVP("240", "176", "177")
#define OUT(t0, t1, a0, a1, a2, a3, ad, o0, o1) ADD(t0, a0, a1) ADD(t1, a2, a3) MUL(t0, t0, "250") MUL(t1, t1, "251") DW2(ad, t0, t1, o0, o1)
#define TS(n)
#define TSOUT
        int lim = dead ? 0 : SPIN_LIMIT, bad = 0;
        asm volatile(
            LDX("192:195", "%[l16]", "%[bb]", "0") LDX("196:199", "%[l16]", "%[bb]", "1024")
            ZAM Z2("224:225") Z2("226:227")
            Z2("16:17") Z2("18:19") Z2("20:21") Z2("22:23") Z2("24:25") Z2("26:27") Z2("28:29") Z2("30:31")
            "v_mov_b32 v200, 0x3f803f80\n\tv_mov_b32 v201, 0x3f803f80\n\tv_mov_b32 v202, 0x3f80\n\tv_mov_b32 v203, 0\n\t"
            "v_mov_b64 v[204:205], v[200:201]\n\tv_mov_b64 v[206:207], v[202:203]\n\t"
            "v_xor_b32 v252, 0xa000, %[sx]\n\tv_add_u32 v253, 1024, %[ra]\n\tv_lshrrev_b32 v254, 1, %[l16]\n\t"
            PAUSE(PV)
            TS(8)
            LDPA
            "4:\n\t"
            "s_waitcnt vmcnt(4)\n\t"
            SM("208:211", "192:195", "32:39", "%[sx]") MX2("192", "193") SM("212:215", "192:195", "48:55", "%[sx]") MX3("194", "195")
            SM("216:219", "192:195", "64:71", "%[sx]") SM("220:223", "192:195", "80:87", "%[sx]")
            "s_waitcnt vmcnt(3)\n\t"
            SM("208:211", "196:199", "40:47", "%[sx]") MX3("196", "197") SM("212:215", "196:199", "56:63", "%[sx]") MX3("198", "199")
            SM("216:219", "196:199", "72:79", "%[sx]") "v_cmp_eq_u32 vcc, " STR(SENT) ", v244\n\t" SM("220:223", "196:199", "88:95", "%[sx]")
            "s_cbranch_vccnz 5f\n\t"
            "1:\n\t" TS(11)
            "s_waitcnt vmcnt(2)\n\t"
            "v_mfma_f32_16x16x16_bf16 v[236:239], v[112:113], v[160:161], 0\n\t"
            MX2("160", "161") ADD("245", "208", "209") ADD("246", "210", "211") CV0("130", "245") CV0("134", "246")
            ADD("245", "212", "213") ADD("246", "214", "215") CV0("138", "245") CV0("142", "246")
            EXD("180", "236")
            "s_waitcnt vmcnt(1)\n\t"
            MF("228:231", "104:107", "164:167", "0")
            MX3("164", "165") MX3("166", "167") ADD("245", "216", "217") ADD("246", "218", "219") CV0("146", "245") CV0("150", "246")
            ADD("245", "220", "221") ADD("246", "222", "223") CV0("154", "245") CV0("158", "246") PCVM
            "s_waitcnt vmcnt(0)\n\t"
            MF("228:231", "108:111", "168:171", "v[228:231]")
            MX3("168", "169") MX3("170", "171")
            "v_cmp_eq_u32 vcc, " STR(SENT) ", v244\n\t"
            "s_cbranch_vccnz 7f\n\t"
            "2:\n\t" TS(9)
            "s_nop 3\n\t"
            EXD("179", "231") EXD("178", "230") EXD("177", "229") EXD("176", "228")
            PCVX
            "v_max3_f32 v250, |v236|, |v231|, |v230|\n\t"
            "v_max3_f32 v250, v250, |v229|, |v228|\n\t"
            SM("224:227", "240:243", "200:207", "v252")
            "v_cmp_nge_f32 vcc, 0x41c00000, v250\n\t"
            "s_nop 0\n\t"
            SM("16:19", "240:243", "128:135", "v252")
            "s_cbranch_vccnz 10f\n\t"
            "13:\n\t"
            "s_nop 0\n\t"
            SM("20:23", "240:243", "136:143", "v252") "s_nop 1\n\t"
            ADD("250", "224", "225") ADD("251", "226", "227")
            SM("24:27", "240:243", "144:151", "v252")
            "v_rcp_f32 v250, v250\n\t"
            SM("28:31", "240:243", "152:159", "v252")
            "v_rcp_f32 v251, v251\n\t"
            OUT("245", "246", "16", "17", "18", "19", "%[ra]", "0", "16")
            OUT("247", "248", "20", "21", "22", "23", "%[ra]", "128", "144")
            OUT("245", "246", "24", "25", "26", "27", "v253", "0", "16")
            OUT("247", "248", "28", "29", "30", "31", "v253", "128", "144")
            TS(10)
            "s_waitcnt lgkmcnt(0)\n\t"
            "s_barrier\n\t"
            RDC
            "v_lshrrev_b32 v247, 2, %[l16]\n\t"
            "s_waitcnt lgkmcnt(2)\n\t"
            ADD("224", "224", "226") ADD("225", "225", "227")
            "s_waitcnt lgkmcnt(0)\n\t"
            ADD("228", "228", "230") ADD("229", "229", "231") ADD("224", "224", "228") ADD("225", "225", "229")
            "v_cvt_pk_bf16_f32 v245, v224, v225\n\t"
            "buffer_store_dword v245, v247, %[rs], %[cs] offen sc1\n\t"
            "s_branch 9f\n\t"
            "10:\n\t"
            "v_max3_f32 v250, v236, v231, v230\n\t"
            "v_max3_f32 v250, v250, v229, v228\n\t"
            "s_nop 1\n\t"
            "v_max_f32_dpp v251, v250, v250 quad_perm:[1,0,3,2] row_mask:0xf bank_mask:0xf\n\t"
            "v_max_f32_dpp v244, v250, v250 quad_perm:[1,0,3,2] row_mask:0xf bank_mask:0xf\n\t"
            "s_nop 1\n\t"
            "v_permlane16_swap_b32 v251, v244\n\t"
            "v_max_f32 v250, v251, v244\n\t"
            "v_max_f32 v251, v251, v244\n\t"
            Z2("224:225") Z2("226:227")
            "v_permlane32_swap_b32 v250, v251\n\t"
            "v_max_f32 v250, v250, v251\n\t"
            Z2("16:17") Z2("18:19")
            SUBM("228") SUBM("229") SUBM("230") SUBM("231") SUBM("236")
            EXD("180", "236") EXD("179", "231") EXD("178", "230") EXD("177", "229") EXD("176", "228")
            PCVM PCVX
            "s_nop 1\n\t"
            SM("224:227", "240:243", "200:207", "v252") "s_nop 1\n\t"
            SM("16:19", "240:243", "128:135", "v252")
            "s_branch 13b\n\t"
            "5:\n\t" CN(1)
            LDX("192:195", "%[l16]", "%[bb]", "0") LDX("196:199", "%[l16]", "%[bb]", "1024")
            LDPA
            "s_sub_u32 %[lim], %[lim], 1\n\t"
            "s_nop 1\n\t"
            ZAM
            "s_cbranch_scc0 4b\n\t"
            "s_mov_b32 %[bad], 1500\n\t"
            "s_mov_b32 %[lim], 0\n\t"
            "s_branch 1b\n\t"
            "7:\n\t" CN(65536)
            LDPA
            "s_sub_u32 %[lim], %[lim], 1\n\t"
            "s_cbranch_scc0 1b\n\t"
            "s_mov_b32 %[bad], 1000\n\t"
            "s_branch 2b\n\t"
            "9:\n\t"
            : [lim] "+s"(lim), [bad] "+s"(bad), "+{v[128:159]}"(yb) TSOUT CNOUT
            : "{v[32:63]}"(wO[0]), "{v[64:95]}"(wO[1]), "{v[104:111]}"(sel), "{v[112:113]}"(sel3), [sx] "v"(sidx), [l16] "v"(l * 16),
              [ra] "v"(r3a), [rs] "s"(rsS), [bb] "s"(bb), [bq] "s"(bq), [bm] "s"(bm), [cs] "s"(o_cs), [rc] "v"(l0 + C2_R3 + wu * 512 + l * 8)
            : "memory", "vcc", "scc", CLOB_PX, CLOB16(16, 17, 18, 19, 20, 21, 22, 23, 24, 25, 26, 27, 28, 29, 30, 31),
              CLOB16(224, 225, 226, 227, 228, 229, 230, 231, 232, 233, 234, 235, 236, 237, 238, 239), CLOB16(240, 241, 242, 243, 244, 245, 246, 247, 248, 249, 250, 251, 252, 253, 254, 254));
        if (__builtin_expect(bad != 0, 0)) TMO_SET((unsigned)bad)
        __builtin_amdgcn_s_setprio(0);
        if (s == 0 && wu == 1) { PST(6) }

        bool skip = s + 2 >= NSEG;
        if (__builtin_expect(s <= KVS, 0)) {
#pragma nounroll
            for (int spin = 0; !ahead || (s == KVS && (ok & 0xFF00u) != 0xFF00u); ++spin) {
                asm volatile("" ::: "memory");
                ok = __ballot(ld_flag(fp) != 0u);
                if ((ok & 255u) == 255u) break;
                if (spin > 100000) { TMO_SET(3001u) break; }
                __builtin_amdgcn_s_sleep(6);
            }
            asm volatile("" ::: "memory");
            if (!ahead) {
                const char* kp = arena(a.slots) + VX_OFF + blk_off(s + 1, ha, tau);
#pragma unroll
                for (int e = 0; e < 8; ++e) glds16h((unsigned)l * 16u, kp + e * 1024, l0 + C2_V + wu * 8192 + e * 1024);
                asm volatile("s_waitcnt vmcnt(0)" ::: "memory");
            }
            ahead = (ok & 0xFF00u) == 0xFF00u;
            skip = !ahead;
        }
        {
            const char* vb = arena(a.slots) + (s + 2 >= NSEG ? KX_OFF : VX_OFF) + (blk_off(skip ? s + 1 : s + 2, ha, tau) ^ (s + 2 >= NSEG ? 8192 : 0));
            unsigned keep;
            int k0 = wu == 1 ? KX_OFF + blk_off(s + 1, hs, tau) + jq * 1024 : 1 << 30;
#define GV(b, o) "global_load_lds_dwordx4 %[l16], " b " offset:" o " sc1\n\t"
#define PXG(d0, d1, d2, d3, a0, a1, a2, a3, b) MF(d0, a0, "32:35", "0") MF(d1, a0, "48:51", "0") MF(d2, a0, "64:67", "0") MF(d3, a0, "80:83", "0") GV(b, "0") \
        PX4(d0, d1, d2, d3, a1, "36:39", "52:55", "68:71", "84:87") GV(b, "1024") PX4(d0, d1, d2, d3, a2, "40:43", "56:59", "72:75", "88:91") GV(b, "2048") PX4(d0, d1, d2, d3, a3, "44:47", "60:63", "76:79", "92:95") GV(b, "3072")
            asm volatile(
                KLD("%[k0]")
                PAUSE(PXD)
                DSV("160:163", "0") DSV("164:167", "1024") DSV("168:171", "2048") DSV("172:175", "3072") DSV("176:179", "128") DSV("180:183", "1152") DSV("184:187", "2176") DSV("188:191", "3200")
                "s_mov_b32 %[kp], m0\n\t"
                "s_mov_b32 m0, %[ld]\n\t"
                "s_waitcnt lgkmcnt(0)\n\t"
                PXG("192:195", "196:199", "200:203", "204:207", "160:163", "164:167", "168:171", "172:175", "%[vb]")
                "s_add_u32 m0, %[ld], 4096\n\t"
                PXG("208:211", "212:215", "216:219", "220:223", "176:179", "180:183", "184:187", "188:191", "%[vc]")
                "s_mov_b32 m0, %[kp]\n\t"
                CVP("128", "192", "193") CVP("129", "194", "195") CVP("136", "196", "197") CVP("137", "198", "199") CVP("144", "200", "201") CVP("145", "202", "203") CVP("152", "204", "205") CVP("153", "206", "207")
                CVP("132", "208", "209") CVP("133", "210", "211") CVP("140", "212", "213") CVP("141", "214", "215") CVP("148", "216", "217") CVP("149", "218", "219") CVP("156", "220", "221") CVP("157", "222", "223")
                : "+{v[128:159]}"(yb), [kp] "=&s"(keep), "=&{v[96:103]}"(kx), [k0] "+s"(k0)
                : "{v[32:63]}"(wO[0]), "{v[64:95]}"(wO[1]), [lv] "v"(l0 + C2_V + wu * 8192 + voffX), [l16] "v"(l * 16), [vk] "v"(voffK), [rs] "s"(rsS), [vb] "s"(vb), [vc] "s"(vb + 4096),
                  [ld] "s"(l0 + (skip ? C2_V + 32768 : C2_V) + wu * 8192)
                : "memory", "scc", CLOB_PX);
        }
        __builtin_amdgcn_sched_barrier(0);
        o_1 = o1_of(s + 1); o_2 = km_off(s + 1, hs, tau) + jq * 512;
        bq = x_off(s + 1, ha, tau); bm = m_off(s + 1, ha, tau); bb = v_off(s + 1, ha, tau);
        o_f = f_off(s + 1, grp, tau);
        o_cs = rec_base(s + 1) + C_OFF + c * 16384 + (int)(half_bits(s + 1, tau) & 1u) * 8192 + wu * 2048 + grp * 256;
        asm volatile("" : "+s"(o_cs), "+s"(o_f), "+s"(o_1), "+s"(o_2), "+s"(bq), "+s"(bm), "+s"(bb));
        __builtin_amdgcn_s_barrier();
        asm volatile(
            "s_cmp_lg_u32 %[wu], 3\n\t"
            "s_cbranch_scc1 1f\n\t"
            "v_mov_b32 v160, %[lf]\n\t"
            PAUSE(PF)
            "ds_read_b32 v160, v160\n\t"
            "s_waitcnt lgkmcnt(0)\n\t"
            "v_cmp_eq_u32 vcc, %[tg], v160\n\t"
            "s_cbranch_vccz 1f\n\t"
            "s_mov_b32 exec_hi, 0\n\t"
            "buffer_store_dword v160, %[l4], %[rs], %[fo] offen sc1\n\t"
            "s_mov_b64 exec, -1\n\t"
            "1:\n\t"
            :: [wu] "s"(wu), [lf] "s"(l0 + C2_MB + 16), [tg] "s"(s + 1), [l4] "v"(l * 4), [rs] "s"(rsS), [fo] "s"(o_f + c * 128)
            : "memory", "scc", "vcc", "v160");
        __builtin_amdgcn_s_setprio(1);
    }
    {
        constexpr int s = NSEG - 1;
        const int l = lane_now(), i = l & 15, g = l >> 4;
        v4u qf[8], kf[8], pv[2], px[4];
        asm volatile("s_waitcnt vmcnt(0)" ::: "memory");
        __builtin_amdgcn_s_sleep(4);
#pragma unroll
        for (int e = 0; e < 8; ++e) kf[e] = ld_sc1s(rsS, l * 16, KX_OFF + blk_off(s, ha, tau) + e * 1024);
#pragma unroll
        for (int e = 0; e < 8; ++e) qf[e] = *(const v4u*)(chsm + C2_V + 32768 + wu * 8192 + e * 1024 + l * 16);
        f32x4 sx[3] = {ZERO4, ZERO4, ZERO4};
        float mxx[2] = {-3.0e38f, -3.0e38f};
#pragma unroll
        for (int ks = 0; ks < 4; ++ks) {
            sx[0] = MM(kf[ks], qf[ks], sx[0]);
            sx[1] = MM(kf[ks], qf[4 + ks], sx[1]);
            sx[2] = MM(kf[4 + ks], qf[4 + ks], sx[2]);
        }
#pragma unroll
        for (int r = 0; r < 4; ++r) {
            if (4 * g + r > i) { sx[0][r] = -3.0e38f; sx[2][r] = -3.0e38f; }
            mxx[0] = fmaxf(mxx[0], sx[0][r]);
            mxx[1] = fmaxf(mxx[1], fmaxf(sx[1][r], sx[2][r]));
        }
        asm volatile("" : "+v"(sx[0]), "+v"(sx[1]), "+v"(sx[2]), "+v"(mxx[0]), "+v"(mxx[1]));
        hand_over(o_1, o_2, o_f, s);
        const int bk = km_off(s, ha, tau);
        __builtin_amdgcn_s_sleep(DLY_L);
#pragma nounroll
        for (int spin = 0;; ++spin) {
            asm volatile("" ::: "memory");
            pv[0] = ld_sc1s(rsS, l * 16, bb); pv[1] = ld_sc1s(rsS, l * 16, bb + 1024);
#pragma unroll
            for (int ks = 0; ks < 4; ++ks) px[ks] = ld_sc1s(rsS, g * 128 + (i >> 1) * 16, bk + ks * 512);
            if (__all(mx4(mx4(mx4(mx4(mx4(mx4(0u, pv[0]), pv[1]), px[0]), px[1]), px[2]), px[3]) != SENT)) break;
            if (dead) break;
            if (spin > SPIN_LIMIT) { TMO_SET(1700u) break; }
        }
#define SUB8(v, o) v8u{(v)[(o)], (v)[(o) + 1], (v)[(o) + 2], (v)[(o) + 3], (v)[(o) + 4], (v)[(o) + 5], (v)[(o) + 6], (v)[(o) + 7]}
#pragma unroll
        for (int ct = 0; ct < 4; ++ct) {
            f32x4 am = smm(pv[0], SUB8(wO[ct >> 1], (ct & 1) * 16), ZERO4, sidx);
            am = smm(pv[1], SUB8(wO[ct >> 1], (ct & 1) * 16 + 8), am, sidx);
            yb[8 * ct + 2] = cvt_pk_bf16(am[0] + am[1], 0.f); yb[8 * ct + 6] = cvt_pk_bf16(am[2] + am[3], 0.f);
        }
        const v8u one = {0x3F803F80u, 0x3F803F80u, 0x3F80u, 0u, 0x3F803F80u, 0x3F803F80u, 0x3F80u, 0u};
        const float SCL2E = 0.08838834764831845f * 1.4426950408889634f;
        float* xo = (float*)(chsm + C2_K) + (wu * 32 + 4 * g) * XP + i;
#pragma unroll
        for (int qt = 0; qt < 2; ++qt) {
            f32x4 sc[3] = {sx[qt], sx[2], ZERO4};
#pragma unroll
            for (int ks = 0; ks < 4; ++ks) sc[2] = MM(px[ks], qf[4 * qt + ks], sc[2]);
            float mx = fmaxf(mxx[qt], fmaxf(sc[2][1], sc[2][3]));
            mx = xor32_max(xor16_max(mx));
            const float nmx = -mx * SCL2E;
#define PE(x) __builtin_amdgcn_exp2f(fmaf(x, SCL2E, nmx))
            const float m0 = PE(sc[2][1]), m1 = PE(sc[2][3]);
            const v4u ae = {cvt_pk_bf16(PE(sc[0][0]), PE(sc[0][1])), cvt_pk_bf16(m0, m0), cvt_pk_bf16(PE(sc[0][2]), PE(sc[0][3])), 0u};
            const v4u ao = {qt ? cvt_pk_bf16(PE(sc[1][0]), PE(sc[1][1])) : 0u, 0u, qt ? cvt_pk_bf16(PE(sc[1][2]), PE(sc[1][3])) : 0u, cvt_pk_bf16(m1, m1)};
            const f32x4 sum = smm(ao, one, smm(ae, one, ZERO4, 0xE444), 0x4EEE);
#pragma unroll
            for (int ct = 0; ct < 4; ++ct) {
                const f32x4 o = smm(ao, SUB8(yb, 8 * ct), smm(ae, SUB8(yb, 8 * ct), ZERO4, 0xE444), 0x4EEE);
#pragma unroll
                for (int r = 0; r < 4; ++r) xo[(16 * qt + r) * XP + 16 * ct] = o[r] * __builtin_amdgcn_rcpf(sum[r]);
            }
        }
        __builtin_amdgcn_s_waitcnt(0xC07F);
        last_rows(chsm, rsS, wu, grp, c, tau);
        if (wu < 2) {
            v4u sel, p0, p1;
#pragma unroll
            for (int d = 0; d < 4; ++d) sel[d] = (l & 6) == 2 * d ? (0x3F80u << ((l & 1) * 16)) : 0u;
            const int xb = XB_OFF + ((c >> 1) * 8 + grp) * 16384 + (int)(tau & 1u) * 8192 + (c & 1) * 4096 + wu * 256;
            __builtin_amdgcn_s_sleep(DLY_C);
#pragma nounroll
            for (int spin = 0;; ++spin) {
                asm volatile("" ::: "memory");
                p0 = ld_sc1s(rsS, g * 512 + i * 16, xb); p1 = ld_sc1s(rsS, g * 512 + i * 16, xb + 2048);
                if (__all(mx4(mx4(0u, p0), p1) != SENT)) break;
                if (dead) break;
                if (spin > SPIN_LIMIT) { TMO_SET(1800u) break; }
            }
            const f32x4 d = MM(sel, p1, MM(sel, p0, ZERO4));
            if (g < 2) out8(a.out + (size_t)(s * SEG + 4 * grp + 2 * wu + (i >> 3)) * OUTW + 64 * c + 8 * (i & 7) + 4 * g, v2u{cvt_pk_bf16(d[0], d[1]), cvt_pk_bf16(d[2], d[3])});
        }
    }
}

struct cpx { float x, y; };
__device__ __forceinline__ cpx cmul(cpx a, cpx b) { return {a.x * b.x - a.y * b.y, a.x * b.y + a.y * b.x}; }
__device__ __forceinline__ cpx cadd(cpx a, cpx b) { return {a.x + b.x, a.y + b.y}; }
__device__ __forceinline__ cpx csub(cpx a, cpx b) { return {a.x - b.x, a.y - b.y}; }
template <int DIR> __device__ __forceinline__ cpx mul_i(cpx a) {
    return (DIR < 0) ? cpx{a.y, -a.x} : cpx{-a.y, a.x};
}
template <int DIR> __device__ __forceinline__ void r4(cpx& a, cpx& b, cpx& c, cpx& d) {
    cpx t0 = cadd(a, c), t1 = csub(a, c), t2 = cadd(b, d), t3 = mul_i<DIR>(csub(b, d));
    a = cadd(t0, t2); b = cadd(t1, t3); c = csub(t0, t2); d = csub(t1, t3);
}
template <int DIR> __device__ __forceinline__ cpx twc(float c, float s) { return cpx{c, (float)DIR * s}; }
template <int DIR> __device__ __forceinline__ void dft16(cpx (&v)[16]) {
#pragma unroll
    for (int a = 0; a < 4; ++a) r4<DIR>(v[a], v[a + 4], v[a + 8], v[a + 12]);
    const float C1 = 0.92387953251128674f, S1 = 0.38268343236508977f, R2 = 0.70710678118654752f;
    v[1 + 4] = cmul(v[1 + 4], twc<DIR>(C1, S1));
    v[1 + 8] = cmul(v[1 + 8], twc<DIR>(R2, R2));
    v[1 + 12] = cmul(v[1 + 12], twc<DIR>(S1, C1));
    v[2 + 4] = cmul(v[2 + 4], twc<DIR>(R2, R2));
    v[2 + 8] = mul_i<DIR>(v[2 + 8]);
    v[2 + 12] = cmul(v[2 + 12], twc<DIR>(-R2, R2));
    v[3 + 4] = cmul(v[3 + 4], twc<DIR>(S1, C1));
    v[3 + 8] = cmul(v[3 + 8], twc<DIR>(-R2, R2));
    v[3 + 12] = cmul(v[3 + 12], twc<DIR>(-C1, -S1));
#pragma unroll
    for (int d = 0; d < 4; ++d) r4<DIR>(v[4 * d], v[4 * d + 1], v[4 * d + 2], v[4 * d + 3]);
}
__device__ __forceinline__ constexpr int x16(int q) { return 4 * (q & 3) + (q >> 2); }
template <int DIR> __device__ __forceinline__ void dft8(cpx (&v)[8]) {
    r4<DIR>(v[0], v[2], v[4], v[6]);
    r4<DIR>(v[1], v[3], v[5], v[7]);
    const float R2 = 0.70710678118654752f;
    v[3] = cmul(v[3], twc<DIR>(R2, R2));
    v[5] = mul_i<DIR>(v[5]);
    v[7] = cmul(v[7], twc<DIR>(-R2, R2));
#pragma unroll
    for (int d = 0; d < 4; ++d) { cpx p = v[2 * d], q = v[2 * d + 1]; v[2 * d] = cadd(p, q); v[2 * d + 1] = csub(p, q); }
}
__device__ __forceinline__ int pad(int i) { return i + (i >> 4); }
__device__ __forceinline__ cpx expi(float x) { float s, c; sincospif(x, &s, &c); return cpx{c, s}; }
template <int R, int DIR> __device__ __forceinline__ void twiddle(cpx* v, cpx w1) {
    cpx w[R];
    w[1] = w1;
#pragma unroll
    for (int r = 2; r < R; ++r) w[r] = (r & 1) ? cmul(w[r - 1], w1) : cmul(w[r / 2], w[r / 2]);
#pragma unroll
    for (int r = 1; r < R; ++r) v[r] = DIR < 0 ? cmul(v[r], w[r]) : cpx{v[r].x * w[r].x + v[r].y * w[r].y, v[r].y * w[r].x - v[r].x * w[r].y};
}
struct TwBase { cpx b; cpx c[4]; };
template <int N> __device__ __forceinline__ TwBase make_tw(int t) {
    TwBase tb;
    tb.b = expi(-(float)(t & 15) * (1.0f / 128.0f));
    constexpr int NQ = (N == 1024) ? 4 : 2, T = N / 16;
#pragma unroll
    for (int q = 0; q < NQ; ++q) tb.c[q] = expi(-(float)(t + T * q) * (2.0f / (float)N));
    return tb;
}
template <int N> __device__ __forceinline__ void fft_sync() {
    if constexpr (N == 1024) { __builtin_amdgcn_fence(__ATOMIC_ACQ_REL, "wavefront"); __builtin_amdgcn_wave_barrier(); } else __syncthreads();
}
template <int N, int DIR, bool RIN, bool ROUT>
__device__ __forceinline__ void fft_reg(cpx* buf, const TwBase& tb, int t, cpx (&v)[16]) {
    constexpr int T = N / 16;
    if constexpr (!RIN) {
#pragma unroll
        for (int r = 0; r < 16; ++r) v[r] = buf[pad(t + T * r)];
        fft_sync<N>();
    }
    dft16<DIR>(v);
#pragma unroll
    for (int q = 0; q < 16; ++q) buf[pad(16 * t + x16(q))] = v[q];
    fft_sync<N>();
    {
        const int k = t & 15;
#pragma unroll
        for (int r = 0; r < 16; ++r) v[r] = buf[pad(t + T * r)];
        fft_sync<N>();
        twiddle<16, DIR>(v, tb.b);
        dft16<DIR>(v);
        const int o = (t >> 4) * 256 + k;
#pragma unroll
        for (int q = 0; q < 16; ++q) buf[pad(o + 16 * x16(q))] = v[q];
        fft_sync<N>();
    }
    if constexpr (N == 1024) {
#pragma unroll
        for (int q = 0; q < 4; ++q)
#pragma unroll
            for (int r = 0; r < 4; ++r) v[4 * q + r] = buf[pad(t + 64 * q + 256 * r)];
#pragma unroll
        for (int q = 0; q < 4; ++q) {
            twiddle<4, DIR>(&v[4 * q], tb.c[q]);
            r4<DIR>(v[4 * q], v[4 * q + 1], v[4 * q + 2], v[4 * q + 3]);
#pragma unroll
            for (int r = 0; r < 4; ++r) buf[pad(t + 64 * q + 256 * r)] = v[4 * q + r];
        }
        fft_sync<N>();
    } else {
        cpx u[2][8];
#pragma unroll
        for (int q = 0; q < 2; ++q)
#pragma unroll
            for (int r = 0; r < 8; ++r) u[q][r] = buf[pad(t + 128 * q + 256 * r)];
        if constexpr (ROUT) fft_sync<N>();
#pragma unroll
        for (int q = 0; q < 2; ++q) {
            twiddle<8, DIR>(u[q], tb.c[q]);
            dft8<DIR>(u[q]);
#pragma unroll
            for (int d = 0; d < 4; ++d) {
                if constexpr (ROUT) { v[q + 2 * d] = u[q][2 * d]; v[q + 2 * d + 8] = u[q][2 * d + 1]; }
                else { buf[pad(t + 128 * q + 256 * d)] = u[q][2 * d]; buf[pad(t + 128 * q + 256 * (d + 4))] = u[q][2 * d + 1]; }
            }
        }
        if constexpr (!ROUT) fft_sync<N>();
    }
}
__device__ __forceinline__ int f_pos(int rb, int x, unsigned tw) { x = (x + rb * FROT) & 32767; return rb * 65536 + x + (x & ~8191) + (int)((tw ^ (unsigned)(rb >> 5)) & 1u) * 8192; }
__device__ __forceinline__ int w_pos(int k, int n, unsigned tw) { return k * 16384 + ((n * 4 + k * WROT) & 8191) + (int)((tw ^ (unsigned)(k >> 7)) & 1u) * 8192; }
constexpr int PB1 = 1024 + 64, PB2 = 2048 + 128;
typedef float f32x4v __attribute__((ext_vector_type(4)));
typedef unsigned u32x4v __attribute__((ext_vector_type(4)));

__device__ __forceinline__ void fft_rows_fwd(const u16* __restrict__ x, rsrc_t rsF, const unsigned tw, int rb, char* smem, int tid) {
    cpx (*buf)[PB1] = (cpx (*)[PB1])smem;
    int f = tid >> 6, t = tid & 63;
    const u16* p0 = x + (size_t)(rb * 8 + 2 * f) * 1024 + 16 * t;
    u32x4v a0 = *(const u32x4v*)p0, a1 = *(const u32x4v*)(p0 + 8), b0 = *(const u32x4v*)(p0 + 1024), b1 = *(const u32x4v*)(p0 + 1032);
    const TwBase tb = make_tw<1024>(t);
    for (int it = 0; it < 4; ++it, rb += 64) {
        asm volatile("" : "+v"(tid));
        f = tid >> 6; t = tid & 63;
        __syncthreads();
        cpx* d = &buf[f][pad(16 * t)];
#pragma unroll
        for (int e = 0; e < 4; ++e) {
            d[2 * e] = cpx{__uint_as_float(a0[e] << 16), __uint_as_float(b0[e] << 16)};
            d[2 * e + 1] = cpx{__uint_as_float(a0[e] & 0xFFFF0000u), __uint_as_float(b0[e] & 0xFFFF0000u)};
            d[8 + 2 * e] = cpx{__uint_as_float(a1[e] << 16), __uint_as_float(b1[e] << 16)};
            d[8 + 2 * e + 1] = cpx{__uint_as_float(a1[e] & 0xFFFF0000u), __uint_as_float(b1[e] & 0xFFFF0000u)};
        }
        if (it < 3) p0 += 512 * 1024;
        a0 = *(const u32x4v*)p0; a1 = *(const u32x4v*)(p0 + 8); b0 = *(const u32x4v*)(p0 + 1024); b1 = *(const u32x4v*)(p0 + 1032);
        fft_sync<1024>();
        cpx z[16];
        fft_reg<1024, -1, false, false>(buf[f], tb, t, z);
        __syncthreads();
#pragma unroll
        for (int m = 0; m < 8; ++m) {
            const int idx = tid + 256 * m, k = idx >> 2, ff = idx & 3;
            const cpx zk = buf[ff][pad(k)], zn = buf[ff][pad(k ? 1024 - k : 512)];
            f32x4v o = {0.5f * (zk.x + zn.x), 0.5f * (zk.y - zn.y), 0.5f * (zk.y + zn.y), -0.5f * (zk.x - zn.x)};
            if (m == 0 && k == 0) o = f32x4v{zk.x, zn.x, zk.y, zn.y};
            st_sc1(rsF, f_pos(rb, idx * 16, tw), __builtin_bit_cast(v4u, o));
        }
    }
}
__device__ __forceinline__ void filt_transpose(const u16* __restrict__ wr, const u16* __restrict__ wi, rsrc_t rsW, const unsigned tw, const int tb, char* smem, const int tid) {
    const v4u* s0 = (const v4u*)(wr + (size_t)tb * 32 * 513);
    const v4u* s1 = (const v4u*)(wi + (size_t)tb * 32 * 513);
    v4u r0[5], r1[5];
#pragma unroll
    for (int e = 0; e < 5; ++e) {
        const int idx = tid + 512 * e, ic = idx < 2052 ? idx : 2051;
        r0[e] = s0[ic]; r1[e] = s1[ic];
    }
#pragma unroll
    for (int e = 0; e < 5; ++e) {
        const int idx = tid + 512 * e;
        if (idx < 2052) { *(v4u*)(smem + idx * 16) = r0[e]; *(v4u*)(smem + 32832 + idx * 16) = r1[e]; }
    }
    __syncthreads();
    const u16* a0 = (const u16*)smem;
    const u16* a1 = (const u16*)(smem + 32832);
    for (int idx = tid; idx < 513 * 8; idx += 512) {
        const int k = idx >> 3, n4 = (idx & 7) * 4;
        v4u v;
#pragma unroll
        for (int j = 0; j < 4; ++j) v[j] = (unsigned)a0[(n4 + j) * 513 + k] | ((unsigned)a1[(n4 + j) * 513 + k] << 16);
        st_sc1(rsW, w_pos(k, tb * 32 + n4, tw), v);
    }
}

__device__ __forceinline__ void fft_cols(rsrc_t rsF, rsrc_t rsW, const unsigned tw, int blk, const int n, char* smem, int tid) {
    cpx (*buf)[PB2] = (cpx (*)[PB2])smem;
    int c = tid >> 7, t = tid & 127;
    f32x4v v[8];
    unsigned wv[16], wn[16];
    auto wld = [&](const int k, const int n) { return __builtin_amdgcn_raw_buffer_load_b32(rsW, w_pos(k, n, tw), 0, 0); };
    auto fetch = [&](const int bk) {
        const int k0 = bk * 2, kc = k0 + c;
#pragma unroll
        for (int m = 0; m < 8; ++m) { const int idx = tid + 256 * m; v[m] = __builtin_bit_cast(f32x4v, __builtin_amdgcn_raw_buffer_load_b128(rsF, f_pos(idx >> 3, k0 * 64 + (idx & 7) * 16, tw), 0, 0)); }
#pragma unroll
        for (int m = 0; m < 16; ++m) wn[m] = wld(kc, t + 128 * m);
    };
    fetch(blk);
    const TwBase tb = make_tw<2048>(t);
    for (int it = 0; it < n; ++it, blk += 64) {
        asm volatile("" : "+v"(tid));
        c = tid >> 7; t = tid & 127;
        __syncthreads();
#pragma unroll
        for (int m = 0; m < 8; ++m) {
            const int idx = tid + 256 * m, rb = idx >> 3, pc = idx & 7;
            cpx* d = &buf[pc >> 2][pad(rb * 8 + 2 * (pc & 3))];
            d[0] = cpx{v[m][0], v[m][1]}; d[1] = cpx{v[m][2], v[m][3]};
        }
#pragma unroll
        for (int m = 0; m < 16; ++m) wv[m] = wn[m];
        fetch(it + 1 < n ? blk + 64 : blk);
        __syncthreads();
        cpx z[16];
        fft_reg<2048, -1, false, true>(buf[c], tb, t, z);
        auto wcx = [](const unsigned w) { return cpx{__uint_as_float(w << 16), __uint_as_float(w & 0xFFFF0000u)}; };
        if (blk < 2) {
#pragma unroll
            for (int m = 0; m < 16; ++m) buf[c][pad(t + 128 * m)] = z[m];
            __syncthreads();
            if (blk == 0 && c == 0) {
#pragma unroll
                for (int m = 0; m < 16; ++m) {
                    const int n = t + 128 * m, nn = (2048 - n) & 2047;
                    const cpx zn = buf[0][pad(nn)], a = wcx(wv[m]), b = wcx(wld(0, nn)), e = wcx(wld(512, n)), f = wcx(wld(512, nn));
                    const cpx h0 = cmul(cpx{0.5f * (z[m].x + zn.x), 0.5f * (z[m].y - zn.y)}, cpx{0.5f * (a.x + b.x), 0.5f * (a.y - b.y)});
                    const cpx h5 = cmul(cpx{0.5f * (z[m].y + zn.y), -0.5f * (z[m].x - zn.x)}, cpx{0.5f * (e.x + f.x), 0.5f * (e.y - f.y)});
                    z[m] = cpx{h0.x - h5.y, h0.y + h5.x};
                    wv[m] = 0x3F80u;
                }
            }
            __syncthreads();
        }
#pragma unroll
        for (int m = 0; m < 16; ++m) z[m] = cmul(z[m], wcx(wv[m]));
        fft_reg<2048, 1, true, false>(buf[c], tb, t, z);
        const float sc = 1.0f / 2048.0f;
#pragma unroll
        for (int m = 0; m < 8; ++m) {
            const int idx = tid + 256 * m, rb = idx >> 3, pc = idx & 7;
            const cpx* d = &buf[pc >> 2][pad(rb * 8 + 2 * (pc & 3))];
            st_sc1(rsF, f_pos(rb, blk * 128 + pc * 16, tw), __builtin_bit_cast(v4u, f32x4v{d[0].x * sc, d[0].y * sc, d[1].x * sc, d[1].y * sc}));
        }
    }
}

__device__ __forceinline__ void fft_rows_inv(rsrc_t rsF, const unsigned tw, u16* __restrict__ out, int rb, char* smem, int tid) {
    cpx (*buf)[PB1] = (cpx (*)[PB1])smem;
    int f = tid >> 6, t = tid & 63;
    f32x4v v[8];
    auto fetch = [&](const int r) {
#pragma unroll
        for (int m = 0; m < 8; ++m) v[m] = __builtin_bit_cast(f32x4v, __builtin_amdgcn_raw_buffer_load_b128(rsF, f_pos(r, (tid + 256 * m) * 16, tw), 0, 0));
    };
    fetch(rb);
    const TwBase tb = make_tw<1024>(t);
    for (int it = 0; it < 4; ++it, rb += 64) {
        asm volatile("" : "+v"(tid));
        f = tid >> 6; t = tid & 63;
        __syncthreads();
        cpx* bf = buf[tid & 3];
#pragma unroll
        for (int m = 0; m < 8; ++m) {
            const int k = (tid >> 2) + 64 * m;
            if (m == 0 && k == 0) { bf[0] = cpx{v[0][0], v[0][2]}; bf[pad(512)] = cpx{v[0][1], v[0][3]}; }
            else { bf[pad(k)] = cpx{v[m][0] - v[m][3], v[m][1] + v[m][2]}; bf[pad(1024 - k)] = cpx{v[m][0] + v[m][3], v[m][2] - v[m][1]}; }
        }
        fetch(it < 3 ? rb + 64 : rb);
        __syncthreads();
        cpx z[16];
        fft_reg<1024, 1, false, false>(buf[f], tb, t, z);
        const float sc = 1.0f / 1024.0f;
        const cpx* d = &buf[f][pad(16 * t)];
        u32x4v a[2], b[2];
#pragma unroll
        for (int h = 0; h < 2; ++h)
#pragma unroll
            for (int e = 0; e < 4; ++e) {
                cpx p = d[8 * h + 2 * e], q = d[8 * h + 2 * e + 1];
                a[h][e] = cvt_pk_bf16(p.x * sc, q.x * sc);
                b[h][e] = cvt_pk_bf16(p.y * sc, q.y * sc);
            }
        u16* o0 = out + (size_t)(rb * 8 + 2 * f) * OUTW + 1024 + 16 * t;
        *(u32x4v*)o0 = a[0]; *(u32x4v*)(o0 + 8) = a[1];
        *(u32x4v*)(o0 + OUTW) = b[0]; *(u32x4v*)(o0 + OUTW + 8) = b[1];
    }
}

__device__ __forceinline__ void glds16s(unsigned voff, const void* sbase, unsigned lds_dst) {
    unsigned keep;
    asm volatile("s_mov_b32 %0, m0\n\ts_mov_b32 m0, %3\n\ts_nop 0\n\tglobal_load_lds_dwordx4 %1, %2\n\ts_mov_b32 m0, %0" : "=&s"(keep) : "v"(voff), "s"(sbase), "s"(lds_dst) : "memory");
}
#define K8_BAR do { asm volatile("" ::: "memory"); __builtin_amdgcn_s_barrier(); asm volatile("" ::: "memory"); } while (0)
#define K8_WV(n) asm volatile("s_waitcnt vmcnt(" #n ")" ::: "memory")
#define K8_WL(n) asm volatile("s_waitcnt lgkmcnt(" #n ")" ::: "memory")
#define K8_SCHED __builtin_amdgcn_sched_barrier(0)
template <int BN>
__device__ __forceinline__ int bswz2(int row) {
    if (BN >= 128) return (row & 3) | (((row >> 3) & 1) << 2);
    else return ((row >> 1) & 1) | (((row >> 3) & 1) << 1);
}
template <int BM>
__device__ __forceinline__ void gemm_tile(u16* sm, const char* A, const int s0, const unsigned tau, const u16* B, u16* C) {
    constexpr int K = HD, LDB = DM, LDC = OUTW;
    constexpr int BN = 128, BK = 64, ST = BM == 128 ? 4 : 3, WN = BM == 128 ? 4 : 2;
    constexpr int ATILE = BM * BK, BTILE = BK * BN;
    constexpr int NT = 8 / WN, GA = BM / 64, GB = 2, GS = GA + GB;
    const int tid = opaque_tid(), w = tid >> 6, l = tid & 63, i = l & 15, g = l >> 4;
    const int wr = w / WN, wc = w % WN;
    const unsigned wu = __builtin_amdgcn_readfirstlane(w);
    unsigned vb;
    { const int row = l >> 4, c = l & 15; vb = (unsigned)(row * LDB + 8 * (c ^ ((row | ((wu & 1) << 2)) << 1))) * 2u; }
    const int sg = s0 + (BM == 128 ? wu >> 1 : wu);
    const char* ab = A + (BM == 128 ? (wu & 1) * 4096 : 0);
    const char* bb = (const char*)(B + (size_t)wu * 8 * LDB);
    const unsigned lds0 = (unsigned)(uintptr_t)(__attribute__((address_space(3))) u16*)sm;
    const unsigned va = (unsigned)l * 16u, va2 = va + 2048u, vb2 = vb + 4u * LDB * 2u - 1024u;
    auto req = [&](const int buf, const int kt) {
        const unsigned d = lds0 + buf * (ATILE + BTILE) * 2;
        const int wh = kt >> 1;
        const char* pa = ab + ((blk_off(sg, (wh & 7) * 4 + (wh >> 3), tau) ^ 8192) + (kt & 1) * 2048);
        const char* pb = bb + (size_t)kt * BK * LDB * 2;
        unsigned keep;
        if (BM == 128) asm volatile("s_mov_b32 %0, m0\n\ts_mov_b32 m0, %1\n\ts_nop 0\n\tglobal_load_lds_dwordx4 %3, %5\n\tglobal_load_lds_dwordx4 %3, %5 offset:1024\n\t"
                     "s_mov_b32 m0, %2\n\ts_nop 0\n\tglobal_load_lds_dwordx4 %6, %8\n\tglobal_load_lds_dwordx4 %7, %8 offset:1024\n\ts_mov_b32 m0, %0"
                     : "=&s"(keep) : "s"(d + wu * GA * 1024), "s"(d + ATILE * 2 + wu * GB * 1024), "v"(va), "v"(va2), "s"(pa), "v"(vb), "v"(vb2), "s"(pb) : "memory");
        else asm volatile("s_mov_b32 %0, m0\n\ts_mov_b32 m0, %1\n\ts_nop 0\n\tglobal_load_lds_dwordx4 %3, %5\n\tglobal_load_lds_dwordx4 %3, %5 offset:1024\n\t"
                     "global_load_lds_dwordx4 %4, %5 offset:2048\n\tglobal_load_lds_dwordx4 %4, %5 offset:3072\n\t"
                     "s_mov_b32 m0, %2\n\ts_nop 0\n\tglobal_load_lds_dwordx4 %6, %8\n\tglobal_load_lds_dwordx4 %7, %8 offset:1024\n\ts_mov_b32 m0, %0"
                     : "=&s"(keep) : "s"(d + wu * GA * 1024), "s"(d + ATILE * 2 + wu * GB * 1024), "v"(va), "v"(va2), "s"(pa), "v"(vb), "v"(vb2), "s"(pb) : "memory");
    };
    f32x4 acc[4][NT] = {};
    constexpr int nt = K / BK;
#pragma unroll
    for (int s = 0; s < ST - 1; ++s) req(s, s);
    int boff[NT];
    const int aoff = wr * 4096 + l * 8;
#pragma unroll
    for (int n = 0; n < NT; ++n) { const int row = 8 * g + (i >> 2); boff[n] = row * BN + 16 * ((wc * NT + n) ^ bswz2<BN>(row)) + 4 * (i & 3); }
    asm volatile("s_waitcnt vmcnt(%0)" :: "i"((ST - 2) * GS) : "memory");
    K8_BAR;
    if (wu >= 4) K8_BAR;
    for (int t0 = 0; t0 < nt + ST - 1 - (nt + ST - 1) % ST; t0 += ST) {
#pragma unroll
        for (int rb = 0; rb < ST; ++rb) {
            const int t = t0 + rb;
            if (nt % ST != 0 && t >= nt) break;
            const int kk = min(t + ST - 1, nt - 1);
            const u16* as = sm + rb * (ATILE + BTILE);
            const u16* bs = as + ATILE;
            bf16x8 af[2][4], bf[2][NT];
#pragma unroll
            for (int ks = 0; ks < 2; ++ks) {
#pragma unroll
                for (int n = 0; n < NT; ++n) bf[ks][n] = tr_frag(bs + boff[n] + ks * 32 * BN, bs + boff[n] + ks * 32 * BN + 4 * BN);
#pragma unroll
                for (int m = 0; m < 4; ++m) af[ks][m] = *(const bf16x8*)(as + aoff + m * 1024 + ks * 512);
            }
            K8_SCHED;
            req((rb + ST - 1) % ST, kk);
            K8_WL(0);
            if (wu >= 4) asm volatile("s_waitcnt vmcnt(%0)" :: "i"((ST - 2) * GS) : "memory");
            K8_BAR;
            __builtin_amdgcn_s_setprio(1);
#pragma unroll
            for (int q = 0; q < 8; ++q)
#pragma unroll
                for (int n = 0; n < NT; ++n) acc[q & 3][n] = __builtin_amdgcn_mfma_f32_16x16x32_bf16(bf[q >> 2][n], af[q >> 2][q & 3], acc[q & 3][n], 0, 0, 0);
            __builtin_amdgcn_s_setprio(0);
            K8_SCHED;
            asm volatile("s_waitcnt vmcnt(%0)" :: "i"((ST - 2) * GS) : "memory");
            K8_BAR;
        }
    }
    if (wu < 4) K8_BAR;
    asm volatile("s_waitcnt vmcnt(0)" ::: "memory");
#pragma unroll
    for (int m = 0; m < 4; ++m)
#pragma unroll
        for (int n = 0; n < NT; n += 2) {
            auto r0 = __builtin_amdgcn_permlane16_swap(cvt_pk_bf16(acc[m][n][0], acc[m][n][1]), cvt_pk_bf16(acc[m][n + 1][0], acc[m][n + 1][1]), false, false);
            auto r1 = __builtin_amdgcn_permlane16_swap(cvt_pk_bf16(acc[m][n][2], acc[m][n][3]), cvt_pk_bf16(acc[m][n + 1][2], acc[m][n + 1][3]), false, false);
            *(v4u*)(C + (wr * 64 + m * 16 + i) * LDC + (wc * NT + n + (g & 1)) * 16 + 8 * (g >> 1)) = v4u{r0[0], r1[0], r0[1], r1[1]};
        }
}

__device__ __forceinline__ void xattn_unit(const ChainArgs& a, rsrc_t rsQ, rsrc_t rsK, rsrc_t rsV, rsrc_t rsS, const int s, const int sn, const int h, u16 (*Vs)[VP], unsigned* mbox, const unsigned tau, int& pend, bool& ready) {
    const int l = opaque_tid() & 63, i = l & 15, g = l >> 4;
    const unsigned known = __builtin_amdgcn_readfirstlane(*(volatile unsigned*)mbox);
    const bool early = known > (unsigned)(s + 1);
    const int bq = km_off(s, h, tau), bb = v_off(s, h, tau);
    auto count = [&](const int t) { arrive(a.sync, t < SSEG ? SY_OX + (t >> 2) * 8 : SY_OXS + (t - SSEG) * 8, t < SSEG ? 128u : 32u, 8); };
    const int xk = blk_off(s, h, tau), xq = xk ^ 8192;
    v4u vv[8], vm[2], k0f[4], k1f[4], kmf[4], q0f[4], q1f[4];
#pragma unroll
    for (int e = 0; e < 8; ++e) vv[e] = ld_sc1s(rsV, l * 16 + e * 1024, xk);
#pragma unroll
    for (int ks = 0; ks < 4; ++ks) {
        k0f[ks] = ld_sc1s(rsK, l * 16 + ks * 1024, xk);
        k1f[ks] = ld_sc1s(rsK, l * 16 + ks * 1024, xk + 4096);
        q0f[ks] = ld_sc1s(rsQ, l * 16 + ks * 1024, xq);
        q1f[ks] = ld_sc1s(rsQ, l * 16 + ks * 1024, xq + 4096);
    }
#pragma unroll
    for (int e = 0; e < 2; ++e) vm[e] = ld_sc1s(rsS, l * 16 + e * 1024, early ? bb : 1 << 30);
#pragma unroll
    for (int ks = 0; ks < 4; ++ks) kmf[ks] = ld_sc1s(rsS, ks * 512 + g * 128 + (i & 7) * 16, early ? bq : 1 << 30);
    const unsigned fl = ld_flag(a.sync + (l >= 8 ? SY_PROG * 32 : (((l & 4) ? SY_Q : SY_KV) + (min(sn, NSEG - 1) >> 3) * 8) * 32 + (l & 3)) + (h >> 2) * 32);
    if (pend >= 0) { asm volatile("s_waitcnt vmcnt(31)" ::: "memory"); count(pend); }
#pragma unroll
    for (int e = 0; e < 8; ++e) *(v4u*)&Vs[16 * (e >> 2) + i][32 * (e & 3) + 8 * g] = vv[e];
    const float scale = 0.08838834764831845f;
    const float L2E = 1.4426950408889634f;
    auto round2 = [&](const f32x4 c, float* o) {
#pragma unroll
        for (int p = 0; p < 2; ++p) {
            unsigned u = cvt_pk_bf16(c[2 * p], c[2 * p + 1]);
            float lo = __uint_as_float(u << 16) * scale, hi = __uint_as_float(u & 0xFFFF0000u) * scale;
            unsigned u2 = cvt_pk_bf16(lo, hi);
            o[2 * p] = __uint_as_float(u2 << 16);
            o[2 * p + 1] = __uint_as_float(u2 & 0xFFFF0000u);
        }
    };
    float sv[2][12], mxx[2];
    {
        f32x4 sx[3] = {};
#pragma unroll
        for (int ks = 0; ks < 4; ++ks) {
            sx[0] = __builtin_amdgcn_mfma_f32_16x16x32_bf16(as_frag(k0f[ks]), as_frag(q0f[ks]), sx[0], 0, 0, 0);
            sx[1] = __builtin_amdgcn_mfma_f32_16x16x32_bf16(as_frag(k0f[ks]), as_frag(q1f[ks]), sx[1], 0, 0, 0);
            sx[2] = __builtin_amdgcn_mfma_f32_16x16x32_bf16(as_frag(k1f[ks]), as_frag(q1f[ks]), sx[2], 0, 0, 0);
        }
        round2(sx[0], &sv[0][0]); round2(sx[1], &sv[1][0]); round2(sx[2], &sv[1][4]);
        mxx[0] = mxx[1] = -3.0e38f;
#pragma unroll
        for (int r = 0; r < 4; ++r) {
            if (4 * g + r > i) { sv[0][r] = -3.0e38f; sv[1][4 + r] = -3.0e38f; }
            mxx[0] = fmaxf(mxx[0], sv[0][r]);
            mxx[1] = fmaxf(mxx[1], fmaxf(sv[1][r], sv[1][4 + r]));
        }
    }
    const u16* vb = &Vs[4 * g + (i >> 2)][4 * (i & 3)];
    bf16x8 vfx[8];
#pragma unroll
    for (int t = 0; t < 8; ++t) vfx[t] = tr_frag(vb + 16 * t, vb + 16 * VP + 16 * t);
#pragma unroll
    for (int t = 0; t < 8; ++t) asm volatile("" : "+v"(vfx[t]));
#pragma unroll
    for (int r = 0; r < 4; ++r) asm volatile("" : "+v"(sv[0][r]), "+v"(sv[1][r]), "+v"(sv[1][4 + r]));
    asm volatile("" : "+v"(mxx[0]), "+v"(mxx[1]));
    bool have = false;
    unsigned seen = __builtin_amdgcn_readlane(fl, 8);
    ready = (sn >> 3) == (s >> 3) || (__ballot(fl != 0u) & 255ull) == 255ull;
    if (early) {
        if ((h & 3) == 0 && l == 0) atomicMax(mbox, seen);
        unsigned m = mx4(mx4(0u, vm[0]), vm[1]);
#pragma unroll
        for (int ks = 0; ks < 4; ++ks) m = mx4(m, kmf[ks]);
        have = __all(m != SENT);
    } else if ((h & 3) == 0) {
        const unsigned* pw = a.sync + (SY_PROG + (h >> 2)) * 32;
        unsigned v = seen;
        if (v <= (unsigned)(s + 1)) {
            if (s < NSEG - XA_FAST) { wait_ge<32>(pw, (unsigned)(s + 1), a.tmo, 4000u); __builtin_amdgcn_s_sleep(24); }
            else { wait_ge<6>(pw, (unsigned)(s + 1), a.tmo, 4000u); __builtin_amdgcn_s_sleep(XA_D); }
            v = (unsigned)(s + 1);
        }
        if (l == 0) atomicMax(mbox, v);
    } else {
#pragma nounroll
        for (int spin = 0; spin < 4000000 && *(volatile unsigned*)mbox < (unsigned)(s + 1); ++spin) __builtin_amdgcn_s_sleep(2);
        asm volatile("" ::: "memory");
    }
#pragma nounroll
    for (int spin = 0; !have; ++spin) {
        asm volatile("" ::: "memory");
#pragma unroll
        for (int e = 0; e < 2; ++e) vm[e] = ld_sc1s(rsS, l * 16 + e * 1024, bb);
#pragma unroll
        for (int ks = 0; ks < 4; ++ks) kmf[ks] = ld_sc1s(rsS, ks * 512 + g * 128 + (i & 7) * 16, bq);
        unsigned m = mx4(mx4(0u, vm[0]), vm[1]);
#pragma unroll
        for (int ks = 0; ks < 4; ++ks) m = mx4(m, kmf[ks]);
        if (__all(m != SENT) || spin > 20000) break;
        __builtin_amdgcn_s_sleep(2);
    }
#pragma unroll
    for (int e = 0; e < 8; ++e) *(unsigned*)&Vs[32 + (i >> 2) + 4 * ((i >> 1) & 1)][64 * (e >> 2) + 16 * g + 4 * (e & 3) + 2 * (i & 1)] = vm[e >> 2][e & 3];
    f32x4 sm_[2] = {};
#pragma unroll
    for (int ks = 0; ks < 4; ++ks) {
        sm_[0] = __builtin_amdgcn_mfma_f32_16x16x32_bf16(as_frag(kmf[ks]), as_frag(q0f[ks]), sm_[0], 0, 0, 0);
        sm_[1] = __builtin_amdgcn_mfma_f32_16x16x32_bf16(as_frag(kmf[ks]), as_frag(q1f[ks]), sm_[1], 0, 0, 0);
    }
    v4u pf[2][2];
#pragma unroll
    for (int qt = 0; qt < 2; ++qt) {
        round2(sm_[qt], &sv[qt][8]);
        float mx = fmaxf(fmaxf(mxx[qt], fmaxf(sv[qt][8], sv[qt][9])), fmaxf(sv[qt][10], sv[qt][11]));
        mx = xor16_max(mx);
        mx = xor32_max(mx);
        const float nmx = -mx * L2E;
        float sum = 0.f;
#pragma unroll
        for (int e = 0; e < 12; ++e) {
            if (qt == 0 && e >= 4 && e < 8) { sv[qt][e] = 0.f; continue; }
            float ev = __builtin_amdgcn_exp2f(fmaf(sv[qt][e], L2E, nmx));
            if (e >= 8) ev = g < 2 ? ev : 0.f;
            sv[qt][e] = ev; sum += ev;
        }
        sum = xor16_sum(sum);
        sum = xor32_sum(sum);
        const float inv = 1.0f / sum;
        pf[qt][0][0] = cvt_pk_bf16(sv[qt][0] * inv, sv[qt][1] * inv);
        pf[qt][0][1] = cvt_pk_bf16(sv[qt][2] * inv, sv[qt][3] * inv);
        pf[qt][0][2] = qt ? cvt_pk_bf16(sv[qt][4] * inv, sv[qt][5] * inv) : 0u;
        pf[qt][0][3] = qt ? cvt_pk_bf16(sv[qt][6] * inv, sv[qt][7] * inv) : 0u;
        pf[qt][1][0] = cvt_pk_bf16(sv[qt][8] * inv, sv[qt][9] * inv);
        pf[qt][1][1] = cvt_pk_bf16(sv[qt][10] * inv, sv[qt][11] * inv);
        pf[qt][1][2] = 0u; pf[qt][1][3] = 0u;
    }
    v2u vmf[8];
#pragma unroll
    for (int t = 0; t < 8; ++t) vmf[t] = tr4(&Vs[32 + 4 * (g & 1) + (i >> 2)][16 * t + 4 * (i & 3)]);
#pragma unroll
    for (int t = 0; t < 8; t += 2) {
        unsigned pk[2][2][2];
#pragma unroll
        for (int tt = 0; tt < 2; ++tt) {
            const bf16x8 vf0 = vfx[t + tt];
            const bf16x8 vf1 = as_frag(v4u{vmf[t + tt][0], vmf[t + tt][1], 0u, 0u});
#pragma unroll
            for (int qt = 0; qt < 2; ++qt) {
                f32x4 o = {};
                o = __builtin_amdgcn_mfma_f32_16x16x32_bf16(vf0, as_frag(pf[qt][0]), o, 0, 0, 0);
                o = __builtin_amdgcn_mfma_f32_16x16x32_bf16(vf1, as_frag(pf[qt][1]), o, 0, 0, 0);
                pk[qt][tt][0] = cvt_pk_bf16(o[0], o[1]);
                pk[qt][tt][1] = cvt_pk_bf16(o[2], o[3]);
            }
        }
#pragma unroll
        for (int qt = 0; qt < 2; ++qt) {
            auto r0 = __builtin_amdgcn_permlane16_swap(pk[qt][0][0], pk[qt][1][0], false, false);
            auto r1 = __builtin_amdgcn_permlane16_swap(pk[qt][0][1], pk[qt][1][1], false, false);
            v4u o = {r0[0], r1[0], r0[1], r1[1]};
            st_sc1s(rsV, (t >> 1) * 1024 + ((g & 1) * 2 + (g >> 1)) * 256 + i * 16, xq + qt * 4096, o);
        }
    }
    pend = seen > (unsigned)(sn + 1) && ready ? s : -1;
    if (pend < 0) { asm volatile("s_waitcnt vmcnt(0)" ::: "memory"); count(s); }
}

__device__ __forceinline__ void tile_stream(const ChainArgs& a, const u16* X, unsigned long long todo, const int xc, unsigned& tau, const bool early, const int nw, u16* sm8 WEVP) {
    constexpr int K = DM, LDB = HD;
    const int tid = opaque_tid(), w = tid >> 6, l = tid & 63, i = l & 15, g = l >> 4;
    const int wr = w >> 2, wc = w & 3;
    const unsigned lds0 = (unsigned)(uintptr_t)(__attribute__((address_space(3))) u16*)sm8;
    const unsigned wu = __builtin_amdgcn_readfirstlane(w);
    const unsigned ldsw = lds0 + wu * 1024;
    unsigned voffA, voffB;
    { int row = tid >> 3, c = tid & 7; voffA = (unsigned)(row * K + 8 * (c ^ (row & 7))) * 2u; }
    { int row = tid >> 4, c = tid & 15; voffB = (unsigned)(row * LDB + 8 * (c ^ (bswz2<128>(row) << 1))) * 2u; }
    auto stA = [&](int b, int h, const char* p) {
        const char* s = p + (size_t)(h * 128) * K * 2;
        glds16s(voffA, s, ldsw + (2 * b + h) * 16384);
        glds16s(voffA, s + (size_t)64 * K * 2, ldsw + (2 * b + h) * 16384 + 8192);
    };
    auto stB = [&](int b, int h, const char* p) {
        const char* s = p + h * 2048;
        glds16s(voffB, s, ldsw + 65536 + (2 * b + h) * 16384);
        glds16s(voffB, s + (size_t)32 * LDB * 2, ldsw + 65536 + (2 * b + h) * 16384 + 8192);
    };
    constexpr int SA = 128, SB = 64 * LDB * 2;
    auto a_of = [&](const unsigned t) { return (const char*)(X + (size_t)((t >> 4) & 7) * 256 * DM); };
    auto b_of = [&](const unsigned t) { const int kind = t & 7; return (const char*)((kind < 2 ? a.Wk : (kind < 4 ? a.Wv : a.Wq)) + hcol(4 * xc + (kind & 1) * 2)); };
    auto c_of = [&](const unsigned t) { const int kind = t & 7; return ((kind & 6) == 2 ? VX_OFF - KX_OFF : 0) + (int)((t >> 4) & 7) * 4194304 + blk_off(0, 4 * xc + (kind & 1) * 2, kind < 4 ? tau : tau ^ 1u) + (int)((t >> 3) & 1u); };
    auto publish = [&](const unsigned t) {
        const int kind = t & 7, ln = lane_now();
        if (wu == 0 && ln < (kind < 4 ? 1 : 2))
            __hip_atomic_store((gu32*)(a.sync + ((kind < 4 ? SY_KV : SY_Q) + ((t >> 4) & 7) * 8 + xc) * 32 + (kind < 4 ? kind : 2 * kind - 8 + ln)), 16u, __ATOMIC_RELAXED, __HIP_MEMORY_SCOPE_AGENT);
    };
    const char* smc = (const char*)sm8;
    int aoff[2], boff[2];
#pragma unroll
    for (int ks = 0; ks < 2; ++ks) aoff[ks] = (wr * 64 + i) * 128 + (((ks * 4 + g) ^ (i & 7)) << 4);
#pragma unroll
    for (int n = 0; n < 2; ++n) { int row = 8 * g + (i >> 2); boff[n] = 65536 + row * 256 + (((wc * 2 + n) ^ bswz2<128>(row)) << 5) + 8 * (i & 3); }
    f32x4 acc[2][2][4][2] = {};
    bf16x8 At[4][2], B0[2][2], B1[2][2];
#define K8_LDA(b, h) _Pragma("unroll") for (int m = 0; m < 4; ++m) _Pragma("unroll") for (int ks = 0; ks < 2; ++ks) \
        At[m][ks] = *(const bf16x8*)(smc + aoff[ks] + (2 * (b) + (h)) * 16384 + m * 2048)
#define K8_LDB(dst, b, h) _Pragma("unroll") for (int n = 0; n < 2; ++n) _Pragma("unroll") for (int ks = 0; ks < 2; ++ks) { \
        const char* p_ = smc + boff[n] + (2 * (b) + (h)) * 16384 + ks * 8192; \
        v4i16 lo_ = __builtin_amdgcn_ds_read_tr16_b64_v4i16((__attribute__((address_space(3))) v4i16*)p_); \
        v4i16 hi_ = __builtin_amdgcn_ds_read_tr16_b64_v4i16((__attribute__((address_space(3))) v4i16*)(p_ + 1024)); \
        dst[n][ks] = bf16x8{lo_[0], lo_[1], lo_[2], lo_[3], hi_[0], hi_[1], hi_[2], hi_[3]}; }
#define K8_MMA(ai, bj, Bt) do { __builtin_amdgcn_s_setprio(1); \
        _Pragma("unroll") for (int m = 0; m < 4; ++m) _Pragma("unroll") for (int n = 0; n < 2; ++n) _Pragma("unroll") for (int ks = 0; ks < 2; ++ks) \
            acc[ai][bj][m][n] = __builtin_amdgcn_mfma_f32_16x16x32_bf16(Bt[n][ks], At[m][ks], acc[ai][bj][m][n], 0, 0, 0); \
        __builtin_amdgcn_s_setprio(0); } while (0)
    const rsrc_t rsC = __builtin_amdgcn_make_buffer_rsrc((void*)(arena(a.slots) + KX_OFF), 0, FX_OFF - KX_OFF, 0x00020000);
#define K8_EPI(ai, bj, cb) do { const int ln_ = lane_now(); \
        const int vo_ = (int)(wu & 3) * 1024 + (((ln_ >> 4) & 1) * 2 + (ln_ >> 5)) * 256 + (ln_ & 15) * 16; \
        _Pragma("unroll") for (int m = 0; m < 4; ++m) { \
            const f32x4 c0 = acc[ai][bj][m][0], c1 = acc[ai][bj][m][1]; \
            auto r0 = __builtin_amdgcn_permlane16_swap(cvt_pk_bf16(c0[0], c0[1]), cvt_pk_bf16(c1[0], c1[1]), false, false); \
            auto r1 = __builtin_amdgcn_permlane16_swap(cvt_pk_bf16(c0[2], c0[3]), cvt_pk_bf16(c1[2], c1[3]), false, false); \
            const int sg_ = 4 * ai + 2 * (int)(wu >> 2) + (m >> 1); \
            st_sc1s(rsC, vo_, ((ai ? ((cb) & ~1) ^ 8192 : (cb) & 1 ? 1 << 30 : (cb)) ^ ((sg_ & SWZ) << 15)) + sg_ * 524288 + bj * 16384 + (m & 1) * 4096, v4u{r0[0], r1[0], r0[1], r1[1]}); \
            acc[ai][bj][m][0] = ZERO4; acc[ai][bj][m][1] = ZERO4; } } while (0)
    constexpr int nt = K / 64;
    unsigned cur = (unsigned)todo & 255u, prev = 0u;
    const char* pA = a_of(cur); const char* pB = b_of(cur);
    stB(0, 0, pB); stA(0, 0, pA); stB(0, 1, pB); stA(0, 1, pA);
    if (wu >= 4) K8_BAR;
    K8_WV(4); K8_BAR;
    stB(1, 0, pB + SB); stA(1, 0, pA + SA); stB(1, 1, pB + SB);
    K8_WV(6); K8_BAR;
    int cbp = 0;
    for (;;) {
        todo >>= 8;
        const unsigned nxt = todo != 0ull ? (unsigned)todo & 255u : cur;
        int cb = c_of(cur);
#pragma nounroll
        for (int t = 0; t < nt; t += 2) {
            if (__builtin_expect(early && prev == 0u && t == TXT, 0))
                touch((const char*)X + 3 * 524288 + (size_t)(nw * 320 + min(TIDX, 319)) * 128, lds0 + 163840 - 256);
            if (__builtin_expect(early && prev == 0u && t == nt - 4, 0)) {
                tau = __builtin_amdgcn_readfirstlane((unsigned)boot_wait(a.boot) & 255u);
                cb = c_of(cur);
            }
            const bool first = t == 0 && prev != 0u, last = t == nt - 2;
            const bool soon = prev < 0x20u;
            if (t == 2 && !soon) { publish(prev); WEV(3) }
            K8_LDB(B0, 0, 0); K8_SCHED; K8_LDA(0, 0); stA(1, 1, pA + SA);
            if (first) K8_EPI(1, 1, cbp);
            pA = last ? a_of(nxt) : pA + 2 * SA; pB = last ? b_of(nxt) : pB + 2 * SB;
            K8_WL(8); K8_BAR; K8_WL(0); K8_MMA(0, 0, B0); K8_BAR; K8_SCHED;
            K8_LDB(B1, 0, 1); stB(0, 0, pB);
            K8_BAR; K8_WL(0); K8_MMA(0, 1, B1); K8_BAR;
            K8_LDA(0, 1); stA(0, 0, pA);
            K8_BAR; K8_WL(0); K8_MMA(1, 0, B0); K8_BAR; K8_SCHED;
            stB(0, 1, pB);
            if (first && !soon) K8_WV(10); else K8_WV(6);
            K8_BAR; K8_MMA(1, 1, B1); K8_BAR;
            if (first && soon) { publish(prev); WEV(3) }
            K8_LDB(B0, 1, 0); K8_SCHED; K8_LDA(1, 0); stA(0, 1, pA);
            K8_WL(8); K8_BAR; K8_WL(0); K8_MMA(0, 0, B0); K8_BAR; K8_SCHED;
            K8_LDB(B1, 1, 1); stB(1, 0, pB + SB);
            if (last) K8_EPI(0, 0, cb);
            K8_BAR; K8_WL(0); K8_MMA(0, 1, B1); K8_BAR;
            K8_LDA(1, 1); stA(1, 0, pA + SA);
            if (last) K8_EPI(0, 1, cb);
            K8_BAR; K8_WL(0); K8_MMA(1, 0, B0); K8_BAR; K8_SCHED;
            stB(1, 1, pB + SB);
            if (last) { K8_EPI(1, 0, cb); K8_WV(18); } else K8_WV(6);
            K8_BAR; K8_MMA(1, 1, B1); K8_BAR;
        }
        WEV(2)
        prev = cur; cbp = cb; cur = nxt;
        if (todo == 0ull) break;
    }
    K8_EPI(1, 1, cbp);
    K8_WV(0);
    if (wu < 4) K8_BAR;
    K8_BAR;
    publish(prev);
    WEV(3)
}

__device__ __forceinline__ void oproj_squad(const ChainArgs& a, u16* out, const int ct, const int rh, const int xc, const unsigned tau, char* smem) {
    const int tid = opaque_tid(), w = tid >> 6, l = tid & 63, i = l & 15, g = l >> 4;
    const int wu = __builtin_amdgcn_readfirstlane(w);
    bf16x8 wf[2][16];
    {
        u16* stg = (u16*)(smem + wu * 10240);
#pragma unroll
        for (int ch = 0; ch < 4; ++ch) {
            const u16* src = a.Wo + (size_t)hcol(4 * wu + ch) * DM + 32 * ct;
            v4u raw[8];
#pragma unroll
            for (int e = 0; e < 8; ++e) { int idx = l + 64 * e, r = idx >> 2, q4 = idx & 3; raw[e] = *(const v4u*)(src + (size_t)r * DM + 8 * q4); }
#pragma unroll
            for (int e = 0; e < 8; ++e) { int idx = l + 64 * e, r = idx >> 2, q4 = idx & 3; *(v4u*)(stg + r * 40 + 8 * q4) = raw[e]; }
            __builtin_amdgcn_s_waitcnt(0xC07F);
#pragma unroll
            for (int jj = 0; jj < 2; ++jj)
#pragma unroll
                for (int ks = 0; ks < 4; ++ks) {
                    const u16* p = stg + (32 * ks + 8 * g + (i >> 2)) * 40 + 16 * jj + 4 * (i & 3);
                    wf[jj][4 * ch + ks] = tr_frag(p, p + 4 * 40);
                }
            __builtin_amdgcn_s_waitcnt(0xC07F);
        }
    }
    const rsrc_t rsO = __builtin_amdgcn_make_buffer_rsrc((void*)(arena(a.slots) + VX_OFF), 0, 32 << 20, 0x00020000);
    float* red = (float*)(smem + 81920);
    const int voff = rh * 4096 + l * 16;
    volatile unsigned* rdy = (volatile unsigned*)(smem + 81920 + 2 * 8 * 16 * 36 * 4);
    bool ready = false;
    for (int s = SSEG; s < NSEG - 1; ++s) {
        if (!ready) {
            if (wu == 0) {
                const unsigned* q = a.sync + (SY_OXS + (s - SSEG) * 8 + xc) * 32;
#pragma nounroll
                for (int spin = 0;; ++spin) {
                    asm volatile("" ::: "memory");
                    if (ld_flag(q) != 0u) break;
                    if (spin > 20000) { __hip_atomic_store((gu32*)a.tmo, 7000u, __ATOMIC_RELAXED, __HIP_MEMORY_SCOPE_AGENT); break; }
                    if (s == SSEG) __builtin_amdgcn_s_sleep(64); else if (s < NSEG - 5) __builtin_amdgcn_s_sleep(16); else __builtin_amdgcn_s_sleep(SQ_SF);
                }
            }
            asm volatile("" ::: "memory");
            __builtin_amdgcn_s_barrier();
            asm volatile("" ::: "memory");
        }
        v4u xf[16];
#pragma unroll
        for (int ks = 0; ks < 16; ++ks) xf[ks] = ld_sc1s(rsO, voff + (ks & 3) * 1024, blk_off(s, 4 * wu + (ks >> 2), tau) ^ 8192);
        unsigned nf = 0;
        if (wu == 0 && s + 1 >= SQ_FAST) nf = ld_flag(a.sync + (SY_OXS + (s + 1 - SSEG) * 8 + xc) * 32);
        f32x4 acc[2] = {};
#pragma unroll
        for (int ks = 0; ks < 16; ++ks)
#pragma unroll
            for (int jj = 0; jj < 2; ++jj) acc[jj] = __builtin_amdgcn_mfma_f32_16x16x32_bf16(wf[jj][ks], as_frag(xf[ks]), acc[jj], 0, 0, 0);
        float* rp = red + (s & 1) * (8 * 16 * 36);
#pragma unroll
        for (int jj = 0; jj < 2; ++jj) *(f32x4*)&rp[(wu * 16 + i) * 36 + 16 * jj + 4 * g] = acc[jj];
        if (wu == 0) rdy[s & 1] = nf;
        __syncthreads();
        ready = __builtin_amdgcn_readfirstlane(rdy[s & 1]) != 0u;
        if (tid >= 64 && tid < 192) {
            const int t = tid - 64, row = t >> 3, q4 = t & 7;
            f32x4 sv_[8];
#pragma unroll
            for (int e = 0; e < 8; ++e) sv_[e] = *(const f32x4*)&rp[(e * 16 + row) * 36 + 4 * q4];
            const f32x4 s0 = ((sv_[0] + sv_[1]) + (sv_[2] + sv_[3])) + ((sv_[4] + sv_[5]) + (sv_[6] + sv_[7]));
            v2u o; o[0] = cvt_pk_bf16(s0[0], s0[1]); o[1] = cvt_pk_bf16(s0[2], s0[3]);
            out8(out + (size_t)(s * SEG + 16 * rh + row) * OUTW + 32 * ct + 4 * q4, o);
        }
    }
}

__device__ __forceinline__ void kv_stream(const ChainArgs& a, const u16* __restrict__ X, const u16* __restrict__ B, char* C, const int head, const int xc, const int wk, unsigned& tau, char* smem) {
    const int tid = opaque_tid(), w = tid >> 6, l = tid & 63, i = l & 15, g = l >> 4;
    const int wu = __builtin_amdgcn_readfirstlane(w), kh = wu >> 2, cq = wu & 3;
    const unsigned lds0 = (unsigned)(uintptr_t)(__attribute__((address_space(3))) char*)smem;
    unsigned voA, voB;
    { const int row = (wu & 1) * 8 + (l >> 3), c = l & 7; voA = (unsigned)(row * DM + 8 * (c ^ (row & 7))) * 2u + (wu >> 1) * 512; }
    { const int row = tid >> 4, c = tid & 15; voB = (unsigned)(row * HD + 8 * (c ^ (bswz2<128>(row) << 1))) * 2u; }
    auto stB = [&](int ch, int slot) {
#pragma unroll
        for (int e = 0; e < 4; ++e) glds16s(voB, (const char*)B + (size_t)(4 * ch + e) * 32 * HD * 2, lds0 + 65536 + slot * 32768 + e * 8192 + wu * 1024);
    };
    const unsigned ldA = lds0 + (wu >> 1) * 8192 + (wu & 1) * 1024;
    auto stP = [&](const char* p, const unsigned d) {
        unsigned keep;
        asm volatile("s_mov_b32 %0, m0\n\ts_mov_b32 m0, %1\n\ts_nop 0\n\tglobal_load_lds_dwordx4 %3, %2 offset:-4096\n\tglobal_load_lds_dwordx4 %4, %2 offset:-2048\n\t"
                     "global_load_lds_dwordx4 %5, %2\n\tglobal_load_lds_dwordx4 %6, %2 offset:2048\n\ts_mov_b32 m0, %0"
                     : "=&s"(keep) : "s"(d), "s"(p), "v"(voA + 8192u), "v"(voA + 6272u), "v"(voA + 4352u), "v"(voA + 2432u) : "memory");
    };
    auto srcA = [&](int h) { return (const char*)X + (size_t)min(h, 2 * KVS - 1) * 32768 - 4096; };
    auto stA = [&](int h) { stP(srcA(h), ldA + (h & 3) * 32768 + 4096); };
    stB(0, 0); stA(0); stA(1); stB(1, 1);
    touch((const char*)X + 65536 + min(wk * 512 + tid, (KVS - 1) * 512 - 1) * 128, lds0 + 131072 + wu * 1024);
    if (KVS > 9) touch((const char*)X + 65536 + min(4096 + wk * 512 + tid, (KVS - 1) * 512 - 1) * 128, lds0 + 131072 + wu * 1024);
    stB(2, 2);
    bf16x8 bf[16][2], af[8];
    f32x4 acc[2][2] = {}, keep[2];
    const int rb = 64 * kh + 8 * g + (i >> 2), sw = bswz2<128>(rb);
    const char* bp = smem + 65536 + rb * 256 + 8 * (i & 3);
    const char* sa0 = smem + kh * 2048 + i * 128 + 16 * (g ^ (i & 7));
    const char* sa1 = smem + kh * 2048 + i * 128 + 16 * ((4 + g) ^ (i & 7));
#define KV_AF(slot, q) *(const bf16x8*)((((q) & 1) ? sa1 : sa0) + (slot) * 32768 + ((q) >> 1) * 4096)
#pragma unroll
    for (int ch = 0; ch < 8; ++ch) {
        if (ch == 0) { if (KVS > 9) K8_WV(10); else K8_WV(9); } else if (ch < 6) K8_WV(8); else if (ch == 6) K8_WV(4); else K8_WV(0);
        K8_BAR;
#pragma unroll
        for (int e = 0; e < 4; ++e) { const char* p_ = bp + (ch % 3) * 32768 + (e >> 1) * 8192 + 32 * ((2 * cq + (e & 1)) ^ sw); bf[2 * ch + (e >> 1)][e & 1] = tr_frag(p_, p_ + 1024); }
#pragma unroll
        for (int e = 0; e < 4; ++e) af[e] = KV_AF(e >> 1, 2 * ch + (e & 1));
        K8_WL(0); K8_SCHED; K8_BAR;
        if (ch < 5) stB(ch + 3, ch % 3);
        if (ch == 7) { stA(2); stA(3); }
#pragma unroll
        for (int e = 0; e < 8; ++e) acc[e >> 2][e & 1] = __builtin_amdgcn_mfma_f32_16x16x32_bf16(bf[2 * ch + ((e >> 1) & 1)][e & 1], af[e >> 1], acc[e >> 2][e & 1], 0, 0, 0);
    }
    float* red = (float*)(smem + 131072) + l * 4;
#pragma unroll
    for (int p = 0; p < 2; ++p) { *(f32x4*)(red + (p * 8 + wu) * 256) = kh ? acc[p][0] : acc[p][1]; keep[p] = kh ? acc[p][1] : acc[p][0]; }
    unsigned long long tag = 0;
    if (wu == 4) { tag = boot_wait(a.boot); *(volatile unsigned*)(smem + 163840 - 512) = (unsigned)tag & 255u; }
    const rsrc_t rc = __builtin_amdgcn_make_buffer_rsrc((void*)C, 0, KVS * 524288, 0x00020000);
    const int vo = cq * 1024 + (2 * kh + (g >> 1)) * 256 + i * 16 + (g & 1) * 8;
    auto put = [&](const f32x4 o, const int so) {
        const v2u pk = {cvt_pk_bf16(o[0], o[1]), cvt_pk_bf16(o[2], o[3])};
        asm volatile("buffer_store_dwordx2 %0, %1, %2, %3 offen sc1" :: "v"(pk), "v"(vo), "s"(rc), "s"(so) : "memory");
    };
    K8_WL(0); K8_WV(0); K8_BAR;
    tau = __builtin_amdgcn_readfirstlane(*(volatile unsigned*)(smem + 163840 - 512));
#pragma unroll
    for (int p = 0; p < 2; ++p) put(keep[p] + *(const f32x4*)(red + (p * 8 + (wu ^ 4)) * 256), blk_off(0, head, tau) + p * 4096);
    stA(4);
    K8_WV(4); K8_BAR;
    if (tid == 256) __hip_atomic_store((gu64*)(a.boot + 256 + 16 * xc + wk), tag, __ATOMIC_RELAXED, __HIP_MEMORY_SCOPE_AGENT);
    auto stages = [&](auto kc) {
        constexpr int KH = decltype(kc)::value;
        if (KH) K8_BAR;
        f32x4 ac[2] = {ZERO4, ZERO4}, kp = ZERO4;
        int h_first = 2, so = OOB;
        asm volatile("" : "+s"(h_first));
        const char* pn = srcA(5);
        unsigned dn = ldA + 32768 + 4096;
        int r0 = (int)(sa0 - smem) + 2 * 32768, r1 = (int)(sa1 - smem) + 2 * 32768;
        for (int h = h_first; h < 2 * KVS + 4; ++h) {
            const f32x4 oth = *(const f32x4*)(red + (wu ^ 4) * 256);
            *(f32x4*)(red + wu * 256) = ac[KH ^ 1];
            bf16x8 ax[16];
#pragma unroll
            for (int q = 0; q < 16; ++q) ax[q] = *(const bf16x8*)(smem + ((q & 1) ? r1 : r0) + (q >> 1) * 4096);
            K8_SCHED;
            stP(pn, dn);
            put((KH ? ac[1] : kp) + oth, so);
            kp = ac[0];
            __builtin_amdgcn_s_waitcnt(0xC07F);
            K8_WV(10);
            K8_BAR;
            if (KH && (h & 1) && h > 5 && tid == 256) __hip_atomic_store((gu32*)(a.sync + (SY_KV0 + (h / 2 - 2) * 8 + xc) * 32 + wk), 16u, __ATOMIC_RELAXED, __HIP_MEMORY_SCOPE_AGENT);
            if (__builtin_expect(h < 2 * KVS, 1)) {
                __builtin_amdgcn_s_setprio(1);
#pragma unroll
                for (int q = 0; q < 16; ++q)
#pragma unroll
                    for (int n = 0; n < 2; ++n) ac[n] = __builtin_amdgcn_mfma_f32_16x16x32_bf16(bf[q][n], ax[q], q ? ac[n] : ZERO4, 0, 0, 0);
                __builtin_amdgcn_s_setprio(0);
            }
            {
                const int hs = h - 1 + KH;
                so = blk_off(hs >> 1, head, tau) + (hs & 1) * 4096;
                asm volatile("" : "+s"(so));
                so = hs > 1 && hs < 2 * KVS ? so : OOB;
                pn = srcA(h + 4); dn = ldA + (h & 3) * 32768 + 4096;
                r0 = (r0 + 32768) & 131071; r1 = (r1 + 32768) & 131071;
                asm volatile("" : "+s"(so), "+s"(pn), "+s"(dn), "+v"(r0), "+v"(r1));
            }
            K8_BAR;
        }
        if (!KH) K8_BAR;
    };
    if (kh) stages(std::integral_constant<int, 1>{}); else stages(std::integral_constant<int, 0>{});
    K8_WV(0);
}

struct MegaArgs {
    ChainArgs ch;
    const u16* x; u16* out;
    const u16* bx; const u16* wr; const u16* wi;
};
constexpr int MEGA_LDS = 163840;
#define WSTAMP(n)
__global__ __launch_bounds__(512, 2) void mega_kernel(MegaArgs m) {
    extern __shared__ __attribute__((aligned(256))) char chsm[];
    const int b = BIDX, xc = (b & 3) + 4 * (b >> 7), sl = (b >> 3) & 15;
    if ((b & 4) == CHS) { chain_body(m.ch, xc, sl, chsm); return; }
    const ChainArgs& a = m.ch;
    const int wk = sl, tid = TIDX;
    char* ar = arena(a.slots);
    unsigned tauw = 0;
    WSTAMP(0)
    if (wk < 8) kv_stream(a, m.x, (wk < 4 ? a.Wk : a.Wv) + hcol(4 * xc + (wk & 3)), ar + (wk < 4 ? KX_OFF : VX_OFF), 4 * xc + (wk & 3), xc, wk, tauw, chsm);
    if (TCH && wk < 8) {
        const int t = opaque_tid(), n = xc * 8 + wk;
        const unsigned sp = (unsigned)(uintptr_t)(__attribute__((address_space(3))) char*)chsm + 163840 - 256;
        touch((const char*)m.x + 3 * 524288 + (n * 320 + min(t, 319)) * 128, sp);
        touch((const char*)m.bx + (n * 512 + t) * 128, sp);
        touch((const char*)(t < 256 ? m.wr : m.wi) + (n * 256 + (t & 255)) * 128, sp);
    }
    WSTAMP(8) WEV(1)
    {
        const int p = wk & 1, q = wk & 3;
        const unsigned long long todo = wk < 2 ? 0x84u + p : wk < 4 ? 0x74544240u + 0x01010101u * p : wk < 8 ? 0ull
                                      : wk < 12 ? (KVS > 8 ? 0x7060503018ull : 0x7060503010ull) + 0x0101010101ull * q : wk < 14 ? 0x341420u + 0x010101u * p : 0x64442422u + 0x01010101u * p;
        __syncthreads();
        if (todo != 0ull) tile_stream(a, m.x, todo, xc, tauw, wk >= 8, xc * 8 + (wk & 7), (u16*)chsm WEVA);
    }
    WSTAMP(2)
    if (wk < 2) {
        unsigned* mbox = (unsigned*)(chsm + 131072);
        if (tid < 5) mbox[tid] = tid ? 2u : 0u;
        __syncthreads();
        const int w = __builtin_amdgcn_readfirstlane(tid >> 6), h = 4 * xc + (w & 3);
        const rsrc_t rsQ = __builtin_amdgcn_make_buffer_rsrc((void*)(ar + KX_OFF), 0, S_LEN * HD * 4, 0x00020000);
        const rsrc_t rsK = __builtin_amdgcn_make_buffer_rsrc((void*)(ar + KX_OFF), 0, S_LEN * HD * 4, 0x00020000);
        const rsrc_t rsV = __builtin_amdgcn_make_buffer_rsrc((void*)(ar + VX_OFF), 0, S_LEN * HD * 4, 0x00020000);
        const rsrc_t rsS = slot_rsrc(a.slots);
        u16 (*Vs)[VP] = (u16 (*)[VP])(chsm + w * (40 * VP * 2));
        const unsigned tau = tauw;
        int pend = -1;
        bool ready = false;
        for (int s = 2 * wk + (w >> 2), sn; s < NSEG - 1; s = sn) {
            const int band = s >> 3;
            unsigned n = 0;
            if (lane_now() == 0) n = atomicAdd(mbox + 1 + (w & 3), 1u);
            n = __builtin_amdgcn_readfirstlane(n);
            sn = 2 * wk + (int)(n & 1u) + 4 * (int)(n >> 1);
            if (sn - s > 4) __builtin_amdgcn_s_setprio(2); else __builtin_amdgcn_s_setprio(0);
            if (s < 4) wait_ge<32>(a.sync + (SY_KV0 + (KVS - 1) * 8 + xc) * 32 + (tid & 7), 16u, a.tmo, 4100u);
            if (!ready) {
                if (band > 0) wait_ge<32>(a.sync + (SY_KV + band * 8 + xc) * 32 + (tid & 3), 1u, a.tmo, 4100u);
                wait_ge<32>(a.sync + (SY_Q + band * 8 + xc) * 32 + (tid & 3), 1u, a.tmo, 4200u);
            }
            xattn_unit(a, rsQ, rsK, rsV, rsS, s, sn, h, Vs, mbox, tau, pend, ready);
        }
        return;
    }
    if (wk >= 4 && wk < 8) {
        const int fw = (wk - 4) * 8 + xc;
        const rsrc_t rsF = __builtin_amdgcn_make_buffer_rsrc((void*)(ar + FX_OFF), 0, 16 << 20, 0x00020000);
        const rsrc_t rsW = __builtin_amdgcn_make_buffer_rsrc((void*)(ar + WT_OFF), 0, 10 << 20, 0x00020000);
        const unsigned tw = tauw ^ FNEAR;
        {
            const int tq = opaque_tid();
            fft_rows_fwd(m.bx, rsF, tw, 2 * fw + (tq >> 8), chsm + (tq >> 8) * 36864, tq & 255);
        }
        for (int t = fw; t < 64; t += 32) {
            __syncthreads();
            filt_transpose(m.wr, m.wi, rsW, tw, t, chsm, opaque_tid());
        }
        wg_publish(a.sync, SY_DONE, 32u, 8);
        WSTAMP(3) WEV(4)
        wg_wait_acquire<16>(a.sync + (SY_DONE + xc) * 32, a.tmo, 6000u);
        {
            const int tq = opaque_tid();
            for (int z = 0; z < fw; ++z) __builtin_amdgcn_s_sleep(FST);
            fft_cols(rsF, rsW, tw, 2 * fw + (tq >> 8), 4, chsm + (tq >> 8) * 36864, tq & 255);
        }
        wg_publish(a.sync, SY_DONE + 8, 32u, 8);
        WSTAMP(4) WEV(5)
        wg_wait_acquire<16>(a.sync + (SY_DONE + 8 + xc) * 32, a.tmo, 6001u);
        {
            const int tq = opaque_tid();
            fft_rows_inv(rsF, tw, m.out, 2 * fw + (tq >> 8), chsm + (tq >> 8) * 36864, tq & 255);
        }
        WSTAMP(5) WEV(6)
    }
    if (wk >= 12) {
        const int r = 2 * (wk - 12);
        wg_wait_acquire<16>(a.sync + (SY_OX + r * 8 + xc) * 32, a.tmo, 5000u);
        wg_wait_acquire<16>(a.sync + (SY_OX + (r + 1) * 8 + xc) * 32, a.tmo, 5000u);
        WEV(7)
        gemm_tile<256>((u16*)chsm, ar + VX_OFF, 4 * r, tauw, a.Wo + xc * 128, m.out + (size_t)r * 128 * OUTW + xc * 128);
        WEV(8)
    }
    if (wk < 4 || (wk >> 1) == 5) {
        const int r = wk < 4 ? wk + 6 : wk;
        wg_wait_acquire<16>(a.sync + (SY_OX + r * 8 + xc) * 32, a.tmo, 5000u);
        WEV(7)
        gemm_tile<128>((u16*)chsm, ar + VX_OFF, 4 * r, tauw, a.Wo + xc * 128, m.out + (size_t)r * 128 * OUTW + xc * 128);
        WEV(8)
        WSTAMP(9)
    }
    if (wk >= 4 && wk < 14 && (wk >> 1) != 5) {
        __syncthreads();
        oproj_squad(a, m.out, (wk < 10 ? wk - 4 : wk - 6) * 4 + (xc & 3), xc >> 2, xc, tauw, chsm);
        WSTAMP(7)
    }
}

#if defined(__HIP_DEVICE_COMPILE__)
#pragma clang attribute pop
#endif
extern "C" void kernel_launch(void* const* d_in, const int* in_sizes, int n_in,
                              void* d_out, int out_size, void* d_ws, size_t ws_size,
                              hipStream_t stream) {
    char* ws = (char*)d_ws;
    size_t off = 0;
    auto alloc = [&](size_t bytes) { void* p = ws + off; off += (bytes + 255) & ~(size_t)255; return p; };
    char* slots = (char*)alloc((size_t)ARENA_BYTES + (2u << 20));
    static_assert((SY_WORDS + 2) * 128 <= 131072, "sync block");
    unsigned* sync = (unsigned*)alloc(131072);
    unsigned long long* boot = (unsigned long long*)alloc(4096);

    MegaArgs m;
    m.ch.mem0 = (const u16*)d_in[1]; m.ch.Wq = (const u16*)d_in[2]; m.ch.Wk = (const u16*)d_in[3]; m.ch.Wv = (const u16*)d_in[4]; m.ch.Wo = (const u16*)d_in[5];
    m.ch.out = (u16*)d_out; m.ch.slots = slots; m.ch.tmo = sync + SY_WORDS * 32;
    m.ch.stamps = (unsigned long long*)(ws + (240ull << 20));
    m.ch.sync = sync; m.ch.boot = boot;
    m.x = (const u16*)d_in[0]; m.out = (u16*)d_out;
    m.bx = (const u16*)d_in[7]; m.wr = (const u16*)d_in[8]; m.wi = (const u16*)d_in[9];
    (void)hipFuncSetAttribute((const void*)mega_kernel, hipFuncAttributeMaxDynamicSharedMemorySize, MEGA_LDS);
    mega_kernel<<<256, 512, MEGA_LDS, stream>>>(m);
}
```

```cpp
#if defined(__HIP_DEVICE_COMPILE__)
#pragma clang attribute push (__attribute__((target("no-packed-fp32-ops"))), apply_to = function)
#endif
#include <hip/hip_runtime.h>
#include <hip/hip_bf16.h>
#include <stdint.h>
#include <type_traits>

typedef unsigned short u16;
#define TIDX ((int)__builtin_amdgcn_workitem_id_x())
#define BIDX ((int)__builtin_amdgcn_workgroup_id_x())
__device__ __forceinline__ int opaque_tid() { int t = TIDX; asm volatile("" : "+v"(t)); return t; }
__device__ __forceinline__ int lane_now() { int t; asm volatile("v_mbcnt_lo_u32_b32 %0, -1, 0\n\tv_mbcnt_hi_u32_b32 %0, -1, %0" : "=v"(t)); return t; }
typedef __attribute__((ext_vector_type(8))) short bf16x8;
typedef __attribute__((ext_vector_type(4))) float f32x4;

constexpr int S_LEN = 2048, DM = 1024, NH = 32, DH = 128, MM = 8, SEG = 32;
constexpr int NSEG = S_LEN / SEG;
constexpr int HD = NH * DH;
constexpr int OUTW = 2048;

typedef unsigned v4u __attribute__((ext_vector_type(4)));
typedef unsigned v2u __attribute__((ext_vector_type(2)));
typedef short v4i16 __attribute__((ext_vector_type(4)));
typedef __amdgpu_buffer_rsrc_t rsrc_t;
#define AUX_SC1 16
__device__ __forceinline__ v4u ld_sc1s(rsrc_t rs, int voff, int soff) { return __builtin_amdgcn_raw_buffer_load_b128(rs, voff, soff, AUX_SC1); }
__device__ __forceinline__ void st_sc1s(rsrc_t rs, int voff, int soff, v4u v) { __builtin_amdgcn_raw_buffer_store_b128(v, rs, voff, soff, AUX_SC1); asm volatile("s_nop 1" :: "v"(v) : "memory"); }
__device__ __forceinline__ void st8_sc1s(rsrc_t rs, int voff, int soff, v2u v) { __builtin_amdgcn_raw_buffer_store_b64(v, rs, voff, soff, AUX_SC1); }
__device__ __forceinline__ void st_sc1(rsrc_t rs, int off, v4u v) { __builtin_amdgcn_raw_buffer_store_b128(v, rs, off, 0, AUX_SC1); }
__device__ __forceinline__ void out8(void* p, v2u v) { asm volatile("global_store_dwordx2 %0, %1, off sc1" :: "v"(p), "v"(v) : "memory"); }
typedef __bf16 bf16x2_t __attribute__((ext_vector_type(2)));
typedef float f32x2_t __attribute__((ext_vector_type(2)));
__device__ __forceinline__ unsigned cvt_pk_bf16(float lo, float hi) {
    f32x2_t f = {lo, hi};
    return __builtin_bit_cast(unsigned, __builtin_convertvector(f, bf16x2_t));
}
#define SENT 0xFFBFFFBF
__device__ __forceinline__ unsigned umax3(unsigned a, unsigned b, unsigned c) { return max(max(a, b), c); }
__device__ __forceinline__ unsigned mx4(unsigned acc, v4u v) { return umax3(umax3(acc, v[0], v[1]), v[2], v[3]); }
__device__ __forceinline__ bf16x8 as_frag(v4u v) { return __builtin_bit_cast(bf16x8, v); }
__device__ __forceinline__ float xor16_sum(float x) {
    auto r = __builtin_amdgcn_permlane16_swap(__float_as_uint(x), __float_as_uint(x), false, false);
    return __uint_as_float(r[0]) + __uint_as_float(r[1]);
}
__device__ __forceinline__ float xor32_sum(float x) {
    auto r = __builtin_amdgcn_permlane32_swap(__float_as_uint(x), __float_as_uint(x), false, false);
    return __uint_as_float(r[0]) + __uint_as_float(r[1]);
}
__device__ __forceinline__ float xor16_max(float x) {
    auto r = __builtin_amdgcn_permlane16_swap(__float_as_uint(x), __float_as_uint(x), false, false);
    return fmaxf(__uint_as_float(r[0]), __uint_as_float(r[1]));
}
__device__ __forceinline__ float xor32_max(float x) {
    auto r = __builtin_amdgcn_permlane32_swap(__float_as_uint(x), __float_as_uint(x), false, false);
    return fmaxf(__uint_as_float(r[0]), __uint_as_float(r[1]));
}
constexpr int SPIN_LIMIT = 400000;
constexpr int OOB = 0x7FFFFF00;
#define PV1 2
#define PV2 11
#define LW 0
#define PVA 8
#define PL 20
#define PF 48
#define CHS 0
#define DLY_U 4
#define SWZ 0
#define FROT 4224
#define WROT 384
#define FNEAR 1u
#define FST 0
#define PG0 2
#define PG1 32
#define PGP 0
#define FTR 1
#define FSG 0
#define FSR 0
#define PB 14
#define TD2 60
#define SL0 16
#define PS 46
#define TE0 10
#define TD 70
#define PV 1
#define PC 31
#define PP 15
#define PK0 34
#define PKP 1
#define PKM 1
#define PKA 28
#define PXD 1
constexpr int NPROBE = 2, DLY_L = 7, DLY_C = 5;
constexpr int M_OFF = 32 * 16384, F_OFF = M_OFF + 2 * 16384, C_OFF = F_OFF + 2 * 16384, REC = C_OFF + 16 * 16384, PERWG = REC / 128, SLOT_BYTES = 34 * 2097152;
static_assert(PERWG == 6656, "arming shares");
__device__ __forceinline__ int rec_base(int s) { return (s >> 1) * 2097152 + (s & 1) * REC; }
__device__ __forceinline__ char* arena(char* slots) { return (char*)(((uintptr_t)slots + 2097151u) & ~(uintptr_t)2097151u); }
constexpr int KX_OFF = SLOT_BYTES, VX_OFF = KX_OFF + (32 << 20), FX_OFF = VX_OFF + (32 << 20), WT_OFF = FX_OFF + (16 << 20), ARENA_BYTES = WT_OFF + (10 << 20);
__device__ __forceinline__ rsrc_t slot_rsrc(char* slots) { return __builtin_amdgcn_make_buffer_rsrc((void*)arena(slots), 0, FX_OFF, 0x00020000); }
__device__ __forceinline__ int blk_off(int seg, int h, unsigned tau) { return seg * 524288 + (((h & 3) >> 1) * 16 + ((h >> 2) ^ (seg & SWZ)) * 2 + (h & 1)) * 16384 + (int)((tau ^ (unsigned)(seg >> 2)) & 1u) * 8192; }
constexpr int TM_OFF = 124 * 16384;
constexpr int CAL_OFF = 32 * 2097152;
__device__ __forceinline__ unsigned half_bits(int s, unsigned tau) { return ((tau ^ (unsigned)(s >> 1)) & 1u) ? 0xFFu : 0u; }
__device__ __forceinline__ int chunk_off(int s, int g, int j, unsigned tau) { return rec_base(s) + (j < 2 ? (g & 3) * 2 + j : 6 + 3 * g + j) * 16384 + (int)(half_bits(s, tau) & 1u) * 8192; }
__device__ __forceinline__ int x_off(int s, int h, unsigned tau) { return chunk_off(s, h >> 2, 2, tau) + (h & 3) * 2048; }
__device__ __forceinline__ int km_off(int s, int h, unsigned tau) { return chunk_off(s, h >> 2, 3, tau) + (h & 3) * 2048; }
__device__ __forceinline__ int m_off(int s, int h, unsigned tau) { return rec_base(s) + M_OFF + (h >> 4) * 16384 + (int)((half_bits(s, tau) >> (h >> 2)) & 1u) * 8192 + (h & 15) * 512; }
__device__ __forceinline__ int v_off(int s, int h, unsigned tau) { return chunk_off(s, h >> 2, 4, tau) + (h & 3) * 2048; }
__device__ __forceinline__ int f_off(int s, int g, unsigned tau) { return rec_base(s) + F_OFF + (g >> 2) * 16384 + (int)((half_bits(s, tau) >> g) & 1u) * 8192 + (g & 3) * 2048; }
__device__ __forceinline__ int hcol(int h) { return ((h & 3) * 8 + (h >> 2)) * 128; }
constexpr int VP = 144;

struct ChainArgs {
    u16* out;
    const u16* mem0; const u16* Wq; const u16* Wk; const u16* Wv; const u16* Wo;
    char* slots;
    unsigned* tmo;
    unsigned long long* stamps;
    unsigned* sync;
    unsigned long long* boot;
};
constexpr int SY_KV = 0;
constexpr int SY_Q = 64;
constexpr int SY_PROG = 128;
constexpr int SY_OX = 136;
constexpr int SY_DONE = 264;
constexpr int SY_OXS = 472;
constexpr int SY_KV0 = 408;
#define TCH 0
#define TXT 10
#define XPR 2
#define F0E 1
#define STRICT 0x20u
#define DLY_T 0
#define TC0 0
#define KVS 8
constexpr int SY_FLAGS = 664;
constexpr int SY_WORDS = SY_FLAGS + SY_FLAGS / 8;
#define XA_FAST 6
#define XA_D 12
#define SQ_FAST 49
#define SQ_SF 4
#define SSEGV 48
constexpr int SSEG = SSEGV;
typedef __attribute__((address_space(1))) unsigned gu32;
typedef __attribute__((address_space(1))) unsigned long long gu64;
__device__ __forceinline__ unsigned ld_flag(const unsigned* p) { return __hip_atomic_load((gu32*)p, __ATOMIC_RELAXED, __HIP_MEMORY_SCOPE_AGENT); }
__device__ __forceinline__ unsigned long long boot_tag(const unsigned long long* boot) {
    return __hip_atomic_load((gu64*)(boot + 144), __ATOMIC_RELAXED, __HIP_MEMORY_SCOPE_AGENT) + 1ull;
}
__device__ __forceinline__ unsigned long long boot_wait(const unsigned long long* boot) {
    const int l = TIDX & 63;
    unsigned long long tag = 0, x = 0, y = 0;
#pragma nounroll
    for (int spin = 0; spin < 100000; ++spin) {
        asm volatile("" ::: "memory");
        x = __hip_atomic_load((gu64*)(boot + l), __ATOMIC_RELAXED, __HIP_MEMORY_SCOPE_AGENT);
        y = __hip_atomic_load((gu64*)(boot + 64 + l), __ATOMIC_RELAXED, __HIP_MEMORY_SCOPE_AGENT);
        tag = boot_tag(boot);
        if (__all((x | 3u) == ((tag << 2) | 3u) && (y | 3u) == ((tag << 2) | 3u))) break;
        __builtin_amdgcn_s_sleep(16);
    }
    asm volatile("" ::: "memory");
    const unsigned long long side = __ballot(x & 1u);
    const unsigned sd = (unsigned)(side & 1u) | (unsigned)((side >> 15) & 2u) | (unsigned)((side >> 30) & 4u) | (unsigned)((side >> 45) & 8u);
    const unsigned m = (sd ^ (__popcll(__ballot(x & 2u)) + __popcll(__ballot(y & 2u)) > 64 ? 15u : 0u)) & 15u;
    return (tag << 8) | m | ((~m & 15u) << 4);
}
template <int SL>
__device__ __forceinline__ void wait_ge(const unsigned* p, unsigned need, unsigned* tmo, unsigned code) {
#pragma nounroll
    for (int spin = 0;; ++spin) {
        asm volatile("" ::: "memory");
        if (__all(ld_flag(p) >= need)) break;
        if (spin > 400000 / SL) { __hip_atomic_store((gu32*)tmo, code, __ATOMIC_RELAXED, __HIP_MEMORY_SCOPE_AGENT); break; }
        __builtin_amdgcn_s_sleep(SL);
    }
    asm volatile("" ::: "memory");
}
template <int SL>
__device__ __forceinline__ void wg_wait_acquire(const unsigned* p, unsigned* tmo, unsigned code) {
    if (TIDX < 64) {
        wait_ge<SL>(p, 1u, tmo, code);
        __builtin_amdgcn_fence(__ATOMIC_ACQUIRE, "agent");
        asm volatile("s_waitcnt vmcnt(0)" ::: "memory");
    }
    __syncthreads();
}
__device__ __forceinline__ void arrive(unsigned* sync, const int f, const unsigned need, const int n) {
    const int l = lane_now();
    unsigned old = 0;
    if (l == 0) old = __hip_atomic_fetch_add((gu32*)(sync + (SY_FLAGS + (f >> 3)) * 32), 1u, __ATOMIC_RELAXED, __HIP_MEMORY_SCOPE_AGENT);
    if (__builtin_amdgcn_readfirstlane(old) == need - 1u && l < n) __hip_atomic_store((gu32*)(sync + (f + l) * 32), 16u, __ATOMIC_RELAXED, __HIP_MEMORY_SCOPE_AGENT);
}
__device__ __forceinline__ void wg_publish(unsigned* sync, const int f, const unsigned need, const int n) {
    asm volatile("s_waitcnt vmcnt(0)" ::: "memory");
    __syncthreads();
    if (TIDX < 64) arrive(sync, f, need, n);
}
#define TMO_SET(code) { dead = true; if (l == 0) __hip_atomic_store(a.tmo, (code) + s, __ATOMIC_RELAXED, __HIP_MEMORY_SCOPE_AGENT); }

__device__ __forceinline__ void glds16h(unsigned voff, const void* sbase, unsigned lds_dst) {
    unsigned keep;
    asm volatile("s_mov_b32 %0, m0\n\ts_mov_b32 m0, %3\n\ts_nop 0\n\tglobal_load_lds_dwordx4 %1, %2 sc1\n\ts_mov_b32 m0, %0" : "=&s"(keep) : "v"(voff), "s"(sbase), "s"(lds_dst) : "memory");
}
__device__ __forceinline__ void touch(const void* p, unsigned lds_dst) {
    unsigned keep;
    asm volatile("s_mov_b32 %0, m0\n\ts_mov_b32 m0, %2\n\ts_nop 0\n\tglobal_load_lds_dword %1, off\n\ts_mov_b32 m0, %0" : "=&s"(keep) : "v"(p), "s"(lds_dst) : "memory");
}
__device__ __forceinline__ void touch_sc1(const void* p, unsigned lds_dst) {
    unsigned keep;
    asm volatile("s_mov_b32 %0, m0\n\ts_mov_b32 m0, %2\n\ts_nop 0\n\tglobal_load_lds_dword %1, off sc1\n\ts_mov_b32 m0, %0" : "=&s"(keep) : "v"(p), "s"(lds_dst) : "memory");
}
#define WEV(code)
#define WEVP
#define WEVA
constexpr int C2_K = 32768;
constexpr int C2_V = 65536;
constexpr int C2_R1 = 0, R1S = 3200;
constexpr int C2_R3 = C2_R1 + 4 * R1S;
constexpr int C2_MB = 24576;
#define PST(n)
#define LT(n)
#define LTA(n)
#define LTOUT0
#define LTOUT
#define CN(n)
#define CNS
#define CNOUT
#define CLOB16(a, b, c, d, e, f, g, h, i, j, k, l, m, n, o, p) "v" #a, "v" #b, "v" #c, "v" #d, "v" #e, "v" #f, "v" #g, "v" #h, "v" #i, "v" #j, "v" #k, "v" #l, "v" #m, "v" #n, "v" #o, "v" #p
#define LDS3(p) ((__attribute__((address_space(3))) v4i16*)(p))
__device__ __forceinline__ bf16x8 tr_frag(const void* p0, const void* p1) {
    v4i16 lo = __builtin_amdgcn_ds_read_tr16_b64_v4i16(LDS3(p0));
    v4i16 hi = __builtin_amdgcn_ds_read_tr16_b64_v4i16(LDS3(p1));
    return bf16x8{lo[0], lo[1], lo[2], lo[3], hi[0], hi[1], hi[2], hi[3]};
}
typedef __bf16 bf16x8v __attribute__((ext_vector_type(8)));
typedef __bf16 bf16x16v __attribute__((ext_vector_type(16)));
typedef unsigned v8u __attribute__((ext_vector_type(8)));
typedef unsigned v32u __attribute__((ext_vector_type(32)));
#define PUT8(v, o, x) { const v8u x_ = (x); _Pragma("unroll") for (int e_ = 0; e_ < 8; ++e_) (v)[(o) + e_] = x_[e_]; }
#define SUB4(v, o) v4u{(v)[(o)], (v)[(o) + 1], (v)[(o) + 2], (v)[(o) + 3]}
#define PUT4(v, o, x) { const v4u x_ = (x); (v)[(o)] = x_[0]; (v)[(o) + 1] = x_[1]; (v)[(o) + 2] = x_[2]; (v)[(o) + 3] = x_[3]; }
__device__ __forceinline__ v2u tr4(const u16* p) { return __builtin_bit_cast(v2u, __builtin_amdgcn_ds_read_tr16_b64_v4i16(LDS3(p))); }
__device__ __forceinline__ f32x4 smm(v4u a, v8u b, f32x4 c, int idx) {
    return __builtin_amdgcn_smfmac_f32_16x16x64_bf16(__builtin_bit_cast(bf16x8v, a), __builtin_bit_cast(bf16x16v, b), c, idx, 0, 0);
}
__device__ __forceinline__ v8u tr_frag64(const u16* p, const int pitch) {
    const v2u r0 = tr4(p), r1 = tr4(p + 8 * pitch), r2 = tr4(p + 32 * pitch), r3 = tr4(p + 40 * pitch);
    return v8u{r0[0], r1[0], r0[1], r1[1], r2[0], r3[0], r2[1], r3[1]};
}
#define MM(a_, b_, c_) __builtin_amdgcn_mfma_f32_16x16x32_bf16(as_frag(a_), as_frag(b_), c_, 0, 0, 0)
#define STR2(x) #x
#define STR(x) STR2(x)
#define PAUSE(n) ".rept (" STR(n) ") / 4\n\ts_nop 15\n\t.endr\n\t.rept (" STR(n) ") %% 4\n\ts_nop 3\n\t.endr\n\t"
#define ZERO4 f32x4{0.f, 0.f, 0.f, 0.f}
constexpr int XB_OFF = CAL_OFF + 262144, XP = 68;
__device__ __forceinline__ void last_rows(const char* chsm, rsrc_t rsS, const int wu, const int grp, const int c, const unsigned tau) {
    const int l = lane_now();
    __builtin_amdgcn_s_barrier();
    asm volatile("" ::: "memory");
    const float* rp = (const float*)(chsm + C2_K) + (4 * wu + (l >> 4)) * XP + 4 * (l & 15);
    const f32x4 s0 = (*(const f32x4*)rp + *(const f32x4*)(rp + 32 * XP)) + (*(const f32x4*)(rp + 64 * XP) + *(const f32x4*)(rp + 96 * XP));
    v2u o;
    o[0] = cvt_pk_bf16(s0[0], s0[1]); o[1] = cvt_pk_bf16(s0[2], s0[3]);
    st8_sc1s(rsS, l * 8, XB_OFF + ((c >> 1) * 8 + wu) * 16384 + (int)(tau & 1u) * 8192 + (c & 1) * 4096 + grp * 512, o);
}
__device__ __forceinline__ const unsigned* kv_flag(const unsigned* sync, const int sq, const int grp, const int l) { return sync + (sq < KVS ? (SY_KV0 + sq * 8 + grp) * 32 + (l & 7) : (SY_KV + 8 + grp) * 32 + (l & 3)); }
__device__ __forceinline__ void chain_body(const ChainArgs& a, const int grp, const int c, char* chsm) {
    const int wu = __builtin_amdgcn_readfirstlane(TIDX >> 6);
    const int widx = grp * 16 + c;
    const int hs = 4 * grp + (c >> 2), jq = c & 3;
    const rsrc_t rsS = slot_rsrc(a.slots);
    const unsigned l0 = (unsigned)(uintptr_t)(__attribute__((address_space(3))) char*)chsm;
    bool dead = false;

    const int cal_off = CAL_OFF + grp * 32768 + (c >> 3) * 16384 + (c & 7) * 1024;
        if (wu == 0) {
            const int l = lane_now();
            const v4u ff = {SENT, SENT, SENT, SENT};
#pragma unroll
            for (int e = 0; e < 13; ++e) st_sc1s(rsS, l * 16, widx * 2 * PERWG + min(e * 1024, 2 * PERWG - 1024), ff);
            const rsrc_t rsY = __builtin_amdgcn_make_buffer_rsrc((void*)a.sync, 0, 131072, 0x00020000);
            st_sc1s(rsY, l * 16, widx * 1024, v4u{0u, 0u, 0u, 0u});
            __builtin_amdgcn_raw_buffer_store_b32(0u, rsS, (l & 3) * 256 + ((l >> 2) & 1) * 8192, cal_off, AUX_SC1);
            if (c < 2) __builtin_amdgcn_raw_buffer_store_b32(0u, rsS, (l >> 5) * 8192 + (l & 31) * 4, TM_OFF + c * 16384 + grp * 256, AUX_SC1);
        }
    asm volatile("" ::: "memory");
    if (wu >= 4) {
        __builtin_amdgcn_s_sleep(DLY_U);
        const int l = opaque_tid() & 63, i = l & 15, g = l >> 4;
        const int sidx = (i & 1) ? 0xEEEE : 0x4444;
        const int u = wu - 4;
        const int k0 = 256 * u;
        u16* stg = (u16*)(chsm + 40960 + u * 20480);
        v4u af[4];
        v32u wS[6];
        {
            v4u raw[3][16];
            {
                const size_t wo = (size_t)k0 * HD + hcol(hs) + 32 * jq;
                const rsrc_t rq = __builtin_amdgcn_make_buffer_rsrc((void*)(a.Wq + wo), 0, 256 * HD * 2, 0x00020000);
                const rsrc_t rk = __builtin_amdgcn_make_buffer_rsrc((void*)(a.Wk + wo), 0, 256 * HD * 2, 0x00020000);
                const rsrc_t rv = __builtin_amdgcn_make_buffer_rsrc((void*)(a.Wv + wo), 0, 256 * HD * 2, 0x00020000);
                const int vw = (l >> 2) * HD * 2 + (l & 3) * 16;
#pragma unroll
                for (int p = 0; p < 4; ++p) af[p] = *(const v4u*)(a.mem0 + (size_t)(i >> 1) * DM + k0 + 64 * p + 16 * g + 8 * (i & 1));
#pragma unroll
                for (int e = 0; e < 16; ++e) raw[0][e] = __builtin_amdgcn_raw_buffer_load_b128(rq, vw, e * 16 * HD * 2, 0);
#pragma unroll
                for (int e = 0; e < 16; ++e) raw[1][e] = __builtin_amdgcn_raw_buffer_load_b128(rk, vw, e * 16 * HD * 2, 0);
#pragma unroll
                for (int e = 0; e < 12; ++e) raw[2][e] = __builtin_amdgcn_raw_buffer_load_b128(rv, vw, e * 16 * HD * 2, 0);
                __builtin_amdgcn_sched_barrier(0);
#pragma unroll
                for (int e = 0; e < 16; ++e) *(v4u*)(stg + (16 * e + (l >> 2)) * 40 + 8 * (l & 3)) = raw[0][e];
                __builtin_amdgcn_sched_barrier(0);
#pragma unroll
                for (int e = 12; e < 16; ++e) raw[2][e] = __builtin_amdgcn_raw_buffer_load_b128(rv, vw, e * 16 * HD * 2, 0);
            }
            const int fr = 16 * (g >> 1) + 4 * (g & 1) + (i >> 2);
            __builtin_amdgcn_sched_barrier(0);
#pragma unroll
            for (int hx = 0; hx < 3; ++hx) {
                if (hx) {
#pragma unroll
                    for (int e = 0; e < 16; ++e) *(v4u*)(stg + (16 * e + (l >> 2)) * 40 + 8 * (l & 3)) = raw[hx][e];
                }
                __builtin_amdgcn_s_waitcnt(0xC07F);
#pragma unroll
                for (int jj = 0; jj < 2; ++jj)
#pragma unroll
                    for (int p = 0; p < 4; ++p) PUT8(wS[2 * hx + jj], 8 * p, tr_frag64(stg + (64 * p + fr) * 40 + 16 * jj + 4 * (i & 3), 40))
                __builtin_amdgcn_s_waitcnt(0xC07F);
                __builtin_amdgcn_sched_barrier(0);
            }
        }
        PST(u)
        if (u == 0) { PST(4) }
        const unsigned r1w = l0 + C2_R1 + u * R1S + (2 * g * 16 + i) * 4, r1v = l0 + C2_R1 + u * R1S + 2176 + (2 * g * 16 + ((i & 9) | ((i & 2) << 1) | ((i & 4) >> 1))) * 4;
#define LD4(d, va, so, o) "buffer_load_dwordx4 v[" d "], " va ", %[rs], " so " offen offset:" o " sc1\n\t"
#define CHK(a, b, c, d) "v_max3_u32 v244, v244, v" a ", v" b "\n\tv_max3_u32 v244, v244, v" c ", v" d "\n\t"
#define CHK0(a, b, c, d) "v_max_u32 v244, v" a ", v" b "\n\tv_max3_u32 v244, v244, v" c ", v" d "\n\t"
#define SMF(d, a, w) "v_smfmac_f32_16x16x64_bf16 v[" d "], v[" a "], v[" w "], %[sx]\n\t"
#define ADD(d, a, b) "v_add_f32 v" d ", v" a ", v" b "\n\t"
#define Z2(r) "v_mov_b64 v[" r "], 0\n\t"
#define DW2(ad, x, y, o0, o1) "ds_write2_b32 " ad ", v" x ", v" y " offset0:" o0 " offset1:" o1 "\n\t"
#define A0 "224:227"
#define A1 "228:231"
#define A2 "232:235"
#define A3 "236:239"
#define Q0 "200:203"
#define Q1 "204:207"
#define Q2 "208:211"
#define Q3 "212:215"
#define V0 "216:219"
#define V1 "220:223"
#define S1_HEAD \
            Z2("216:217") Z2("218:219") Z2("220:221") Z2("222:223") \
            "s_waitcnt vmcnt(4)\n\t" ZQ \
            "s_waitcnt vmcnt(3)\n\t" SMF(V0, A0, "136:143") CHK0("224", "225", "226", "227") SMF(V1, A0, "168:175") \
            "s_waitcnt vmcnt(2)\n\t" SMF(V0, A1, "144:151") CHK("228", "229", "230", "231") SMF(V1, A1, "176:183") \
            "s_waitcnt vmcnt(1)\n\t" SMF(V0, A2, "152:159") CHK("232", "233", "234", "235") SMF(V1, A2, "184:191") \
            "s_waitcnt vmcnt(0)\n\t" SMF(V0, A3, "160:167") CHK("236", "237", "238", "239") SMF(V1, A3, "192:199") \
            "v_cmp_eq_u32 vcc, " STR(SENT) ", v244\n\t"
#define S1_TAIL(B1, B2) \
            SMF(Q0, A0, "8:15") SMF(Q0, A1, "16:23") SMF(Q0, A2, "24:31") \
            ADD("240", "216", "217") ADD("241", "218", "219") SMF(Q0, A3, "32:39") DW2("%[rv]", "240", "241", "0", "16") \
            ADD("242", "220", "221") ADD("243", "222", "223") SMF(Q1, A0, "40:47") DW2("%[rv]", "242", "243", "128", "144") \
            SMF(Q1, A1, "48:55") SMF(Q1, A2, "56:63") SMF(Q1, A3, "64:71") \
            ADD("240", "200", "201") ADD("241", "202", "203") SMF(Q2, A0, "72:79") DW2("%[rw]", "240", "241", "0", "16") \
            SMF(Q2, A1, "80:87") SMF(Q2, A2, "88:95") "s_nop 1\n\t" \
            ADD("242", "204", "205") ADD("243", "206", "207") SMF(Q2, A3, "96:103") DW2("%[rw]", "242", "243", "136", "152") \
            SMF(Q3, A0, "104:111") SMF(Q3, A1, "112:119") \
            B1        \
            LTA(0) \
            SMF(Q3, A2, "120:127") SMF(Q3, A3, "128:135") "s_nop 0\n\t" \
            ADD("240", "208", "209") ADD("241", "210", "211") DW2("v246", "240", "241", "0", "16") \
            "s_nop 3\n\t" \
            ADD("242", "212", "213") ADD("243", "214", "215") DW2("v246", "242", "243", "136", "152") \
            "s_waitcnt lgkmcnt(0)\n\t" B2 "s_barrier\n\t"
#define ZQ Z2("200:201") Z2("202:203") Z2("204:205") Z2("206:207") Z2("208:209") Z2("210:211") Z2("212:213") Z2("214:215")
#define U_W "{v[8:39]}"(wS[0]), "{v[40:71]}"(wS[1]), "{v[72:103]}"(wS[2]), "{v[104:135]}"(wS[3]), "{v[136:167]}"(wS[4]), "{v[168:199]}"(wS[5])
#define U_CLOB CLOB16(200, 201, 202, 203, 204, 205, 206, 207, 208, 209, 210, 211, 212, 213, 214, 215), CLOB16(216, 217, 218, 219, 220, 221, 222, 223, 224, 225, 226, 227, 228, 229, 230, 231), \
            CLOB16(232, 233, 234, 235, 236, 237, 238, 239, 240, 241, 242, 243, 244, 245, 246, 247)
        __builtin_amdgcn_s_setprio(2);
        {
            asm volatile(
                "v_add_u32 v246, 1088, %[rw]\n\t"
                S1_HEAD "s_nop 0\n\t" S1_TAIL("s_nop 1\n\t", "s_barrier\n\t")
                : LTOUT0
                : U_W, "{v[224:227]}"(af[0]), "{v[228:231]}"(af[1]), "{v[232:235]}"(af[2]), "{v[236:239]}"(af[3]), [sx] "v"(sidx), [rv] "v"(r1v), [rw] "v"(r1w)
                : "memory", "vcc", CLOB16(200, 201, 202, 203, 204, 205, 206, 207, 208, 209, 210, 211, 212, 213, 214, 215), CLOB16(216, 217, 218, 219, 220, 221, 222, 223, 240, 241, 242, 243, 244, 245, 246, 247));
        }
        const unsigned tau = __builtin_amdgcn_readfirstlane(*(volatile unsigned*)(chsm + C2_MB + 8));
        const unsigned long long wmask = u == 3 ? (c == 0 ? ~0ull : 0ull) : u == 1 ? 0x0020202020202000ull : u == 2 ? 1ull << (NSEG - 3) : 0ull;
        const int pvv = u == 3 ? 2 : -1, pvm = u == 3 ? -1 : 0;
        const unsigned* pfl = a.sync + (u == 3 ? SY_PROG + grp : u == 1 ? SY_KV + 16 + grp : SY_Q + 7 * 8 + grp) * 32;
        int s_first = 0;
        asm volatile("s_branch 1f\n\t.p2align 8\n1:" : "+s"(s_first));
        for (int s = s_first; s < NSEG - 1; ++s) {
            __builtin_amdgcn_s_setprio(0);
            LT(1)
            const int pv = (wmask >> s) & 1ull ? pvv + (s & pvm) : 0;
            const unsigned* pa = pfl;
            if (pv < 0) pfl += 256;
            const int cb = grp & 3;
            int soffc = rec_base(s) + C_OFF + c * 16384 + (int)((half_bits(s, tau) >> grp) & 1u) * 8192 + cb * 2048;
            int o_as = rec_base(s + 1) + ((c >> 3) + (grp >> 2) * 4) * 16384 + (int)((half_bits(s + 1, tau) >> grp) & 1u) * 8192 + ((4 * c + cb) & 31) * 256;
            int soff = chunk_off(s + 1, grp, u >> 1, tau) + (u & 1) * 4096;
            const int o_tm = (s >> 1) * 2097152 + TM_OFF + (s & 1) * 16384;
            int lim = dead ? 0 : SPIN_LIMIT, bad = 0;
            const bool rcd = u > 0 || widx < 32;
            const int pc = u > 0 ? 3 * widx + u - 1 : rcd ? 384 + widx : s * 96 + widx - 32;
            const int o_arm = (rcd ? s + 2 < NSEG : pc < 512) ? (rcd ? rec_base(s + 2) : XB_OFF) + (pc >> 3) * 16384 + (int)(half_bits(rcd ? s + 2 : 0, tau) & 1u) * 8192 + (pc & 7) * 1024 : OOB;
            bool there = true;
            if (__builtin_expect(s <= KVS, 0)) {
                there = __all(ld_flag(kv_flag(a.sync, s + 2, grp, l)) != 0u);
                *(volatile unsigned*)(chsm + C2_MB + 32 + (s & 1) * 16 + u * 4) = there ? 1u : 0u;
                __builtin_amdgcn_s_waitcnt(0xC07F);
            }
            const char* vb = !there ? nullptr : arena(a.slots) + (s + 2 >= NSEG ? KX_OFF : VX_OFF) + (blk_off(min(s + 2, NSEG - 1), 4 * grp + 2 * u - 2, tau) ^ (s + 2 >= NSEG ? 8192 : 0));
            asm volatile("" : "+s"(soffc), "+s"(o_as), "+s"(soff), "+s"(lim), "+s"(vb));
#define GL(o) "global_load_lds_dwordx4 %[l16], s[84:85] offset:" o " sc1\n\t" PAUSE(PGP)
#define GL4 GL("0") GL("1024") GL("2048") GL("3072")
#define GLN(d) "s_add_u32 m0, m0, 4096\n\ts_add_u32 s84, s84, " d "\n\ts_addc_u32 s85, s85, 0\n\t"
#define PKE PAUSE(PKA)
#define C_TRY(x, x0, x1, x2, x3, y, y0, y1, y2, y3) \
                "s_waitcnt vmcnt(3)\n\t" \
                "v_mfma_f32_16x16x32_bf16 v[240:243], v[216:219], v[" x "], 0\n\t" CHK0(x0, x1, x2, x3) \
                "s_waitcnt vmcnt(2)\n\t" \
                "v_mfma_f32_16x16x32_bf16 v[240:243], v[216:219], v[" y "], v[240:243]\n\t" CHK(y0, y1, y2, y3) \
                "v_cmp_eq_u32 vcc, " STR(SENT) ", v244\n\t" \
                "s_cbranch_vccz 3b\n\t" CN(1) \
                LD4(x, "%[l16]", "%[sc]", "0") LD4(y, "%[l16]", "%[sc]", "1024")
            asm volatile(
                "v_and_b32 v221, 16, %[l16]\n\t"
                "v_mov_b32 v222, 0x3f80\n\t"
                "v_bfe_u32 v223, %[l16], 5, 2\n\t"
                "v_lshlrev_b32 v221, v221, v222\n\t"
                "v_cmp_eq_u32 vcc, 0, v223\n\tv_and_b32 v220, 0xf0, %[l16]\n\tv_bfe_u32 v222, %[l16], 8, 1\n\tv_cndmask_b32 v216, 0, v221, vcc\n\t"
                "v_cmp_eq_u32 vcc, 1, v223\n\tv_lshl_add_u32 v220, v222, 3, v220\n\tv_lshrrev_b32 v222, 9, %[l16]\n\tv_cndmask_b32 v217, 0, v221, vcc\n\t"
                "v_cmp_eq_u32 vcc, 2, v223\n\tv_lshl_add_u32 v220, v222, 30, v220\n\ts_nop 0\n\tv_cndmask_b32 v218, 0, v221, vcc\n\t"
                "v_cmp_eq_u32 vcc, 3, v223\n\ts_nop 1\n\tv_cndmask_b32 v219, 0, v221, vcc\n\t"
                "v_mul_u32_u24 v222, 0x8000, v222\n\t"
                "v_and_b32 v221, 0xff, v220\n\t"
                "v_add_u32 v221, v221, v222\n\t"
                "v_lshrrev_b32 v247, 2, %[l16]\n\t"
                PKE
                "v_mov_b32 v240, " STR(SENT) "\n\tv_mov_b32 v241, v240\n\tv_mov_b32 v242, v240\n\tv_mov_b32 v243, v240\n\t"
                "buffer_store_dwordx4 v[240:243], %[l16], %[rs], %[arm] offen sc1\n\t"
                LTA(2)
                "s_setprio 2\n\t"
                "s_barrier\n\t"
                LTA(3)
                "v_add_u32 v246, 1088, %[rw]\n\t"
                "s_cmp_lt_u32 %[uu], 1\n\t"
                "s_cbranch_scc1 7f\n\t"
                "s_cmp_eq_u32 %[uu], 3\n\t"
                "s_cbranch_scc1 20f\n\t"
                "s_memrealtime s[84:85]\n\t"
                "v_lshrrev_b32 v241, 9, %[l16]\n\t"
                "v_and_b32 v242, 0x7c, v247\n\t"
                "v_lshl_add_u32 v241, v241, 13, v242\n\t"
                "s_waitcnt lgkmcnt(0)\n\t"
                "v_mov_b32 v240, s84\n\t"
                "buffer_store_dword v240, v241, %[rs], %[ts] offen sc1\n\t"
                "s_cmp_eq_u64 %[vb], 0\n\t"
                "s_cbranch_scc1 11f\n\t"
                "s_mov_b32 s86, m0\n\t"
                "s_mov_b64 s[84:85], %[vb]\n\t"
                "s_mov_b32 m0, %[lv]\n\t"
                PAUSE(PG0)
                "s_cmp_eq_u32 %[uu], 1\n\t"
                "s_cbranch_scc1 12f\n\t"
                PAUSE(PG1)
                "12:\n\t"
                GL4 GLN("4096") GL4 GLN("12288") GL4 GLN("4096") GL4
                "s_mov_b32 m0, s86\n\t"
                "11:\n\t"
                "s_cmp_lt_i32 %[pv], 0\n\t"
                "s_cbranch_scc0 8f\n\t"
                "v_and_b32 v241, 12, v247\n\t"
                "10:\n\t"
                "global_load_dword v240, v241, %[pa] sc1\n\t"
                "s_waitcnt vmcnt(0)\n\t"
                "v_cmp_gt_u32 vcc, 16, v240\n\t"
                "s_cbranch_vccz 8f\n\t"
                "s_sleep 6\n\t"
                "s_sub_u32 %[lim], %[lim], 1\n\t"
                "s_cbranch_scc0 10b\n\t"
                "s_mov_b32 %[bad], 3000\n\t"
                "s_mov_b32 %[lim], 0\n\t"
                "s_branch 8f\n\t"
                "20:\n\t"
                PAUSE(PS)
                "s_memrealtime s[84:85]\n\t"
                "s_load_dword s86, %[tb], 0\n\ts_load_dword s87, %[tb], 0x100\n\ts_load_dword s88, %[tb], 0x200\n\ts_load_dword s89, %[tb], 0x300\n\t"
                "s_load_dword s90, %[tb], 0x400\n\ts_load_dword s91, %[tb], 0x500\n\ts_load_dword s92, %[tb], 0x600\n\ts_load_dword s93, %[tb], 0x700\n\t"
                "s_waitcnt lgkmcnt(0)\n\t"
                "s_sub_u32 s86, s84, s86\n\ts_sub_u32 s87, s84, s87\n\ts_sub_u32 s88, s84, s88\n\ts_sub_u32 s89, s84, s89\n\t"
                "s_sub_u32 s90, s84, s90\n\ts_sub_u32 s91, s84, s91\n\ts_sub_u32 s92, s84, s92\n\ts_sub_u32 s93, s84, s93\n\t"
                "s_or_b32 s85, s86, s87\n\ts_min_u32 s86, s86, s87\n\ts_or_b32 s85, s85, s88\n\ts_min_u32 s86, s86, s88\n\ts_or_b32 s85, s85, s89\n\ts_min_u32 s86, s86, s89\n\t"
                "s_memrealtime s[88:89]\n\t"
                "s_or_b32 s85, s85, s90\n\ts_min_u32 s86, s86, s90\n\ts_or_b32 s85, s85, s91\n\ts_min_u32 s86, s86, s91\n\ts_or_b32 s85, s85, s92\n\ts_min_u32 s86, s86, s92\n\t"
                "s_or_b32 s85, s85, s93\n\ts_min_u32 s86, s86, s93\n\t"
                "s_cmp_gt_u32 s85, 127\n\t"
                CNS
                "s_cselect_b32 s85, " STR(TE0) ", 127\n\t"
                "s_min_u32 s86, s86, s85\n\t"
                "s_sub_u32 s84, s84, s86\n\t"
                "s_add_u32 s84, s84, %[td]\n\t"
                "s_cmp_lt_i32 %[pv], 1\n\t"
                "s_cbranch_scc1 4f\n\t"
                "v_mov_b32 v247, %[pv]\n\t"
                "s_mov_b64 exec, 1\n\t"
                "global_store_dword %[l16], v247, %[pa] sc1\n\t"
                "s_mov_b64 exec, -1\n\t"
                "4:\n\t"
                "s_waitcnt lgkmcnt(0)\n\t"
                "s_sub_i32 s84, s84, s88\n\t"
                "s_max_i32 s84, s84, 0\n\t"
                "s_min_i32 s84, s84, 40\n\t"
                "21:\n\t"
                "s_sub_u32 s84, s84, 1\n\t"
                "s_cbranch_scc0 21b\n\t"
                "s_branch 8f\n\t"
                "7:\n\t"
                PAUSE(PC)
                LD4(A0, "%[l16]", "%[sc]", "0") LD4(A1, "%[l16]", "%[sc]", "1024")
                PAUSE(PP)
                LD4(A2, "%[l16]", "%[sc]", "0") LD4(A3, "%[l16]", "%[sc]", "1024")
                "s_waitcnt vmcnt(3)\n\t"
                "v_mfma_f32_16x16x32_bf16 v[240:243], v[216:219], v[224:227], 0\n\t" CHK0("224", "225", "226", "227")
                "s_waitcnt vmcnt(2)\n\t"
                "v_mfma_f32_16x16x32_bf16 v[240:243], v[216:219], v[228:231], v[240:243]\n\t" CHK("228", "229", "230", "231")
                "v_cmp_eq_u32 vcc, " STR(SENT) ", v244\n\t"
                "s_cbranch_vccnz 30f\n\t"
                "3:\n\t"
                "s_nop 3\n\t"
                "v_cvt_pk_bf16_f32 v244, v240, v241\n\t"
                "v_cvt_pk_bf16_f32 v245, v242, v243\n\t"
                "buffer_store_dwordx2 v[244:245], v221, %[rs], %[oas] offen sc1\n\t"
                PAUSE(PB)
                "8:\n\t"
                LTA(4)
                "s_barrier\n\t"
                PAUSE(1)
                "2:\n\t"
                LD4(A0, "%[l16]", "%[sa]", "0") LD4(A1, "%[l16]", "%[sa]", "1024") LD4(A2, "%[l16]", "%[sa]", "2048") LD4(A3, "%[l16]", "%[sa]", "3072")
                S1_HEAD
                "s_cbranch_vccnz 6f\n\t"
                "5:\n\t"
                LTA(5)
                S1_TAIL("s_waitcnt lgkmcnt(0)\n\ts_barrier\n\t", "")
                "s_setprio 0\n\t"
                "s_branch 9f\n\t"
                "30:\n\t" CN(1)
                LD4(A0, "%[l16]", "%[sc]", "0") LD4(A1, "%[l16]", "%[sc]", "1024")
                "1:\n\t"
                C_TRY(A2, "232", "233", "234", "235", A3, "236", "237", "238", "239")
                C_TRY(A0, "224", "225", "226", "227", A1, "228", "229", "230", "231")
                "s_sub_u32 %[lim], %[lim], 1\n\t"
                "s_cbranch_scc0 1b\n\t"
                "s_mov_b32 %[bad], 2000\n\t"
                "s_mov_b32 %[lim], 0\n\t"
                "s_branch 3b\n\t"
                "6:\n\t" CN(65536)
                "v_mov_b32 v240, %[tg]\n\t"
                "v_mov_b32 v241, %[lf]\n\t"
                "ds_write_b32 v241, v240\n\t"
                "s_nop 0\n\t"
                "s_sub_u32 %[lim], %[lim], 1\n\t"
                "s_cbranch_scc0 2b\n\t"
                "s_mov_b32 %[bad], 1\n\t"
                "s_branch 5b\n\t"
                "9:\n\t"
                : [lim] "+s"(lim), [bad] "+s"(bad), [oas] "+s"(o_as) LTOUT CNOUT
                : U_W, [l16] "v"(l * 16), [rs] "s"(rsS), [sx] "v"(sidx), [rv] "v"(r1v), [rw] "v"(r1w),
                  [sc] "s"(soffc), [sa] "s"(soff), [uu] "s"(u), [pv] "s"(pv), [pa] "s"(pa), [vb] "s"(vb), [lv] "s"(l0 + (s + 2 >= NSEG ? C2_V + 32768 : s & 1 ? C2_K : C2_V) + (u - 1) * 16384), [td] "s"(s >= SL0 ? TD2 : TD), [ts] "s"(c == 0 && (u == 2 || s < NSEG - 3) ? o_tm + grp * 256 + (2 - u) * 2097152 : OOB), [tb] "s"(arena(a.slots) + o_tm + ((half_bits(s, tau) >> grp) & 1u) * 8192),

                  [arm] "s"(o_arm)
,
                  [tg] "s"(s + 1), [lf] "s"(l0 + C2_MB + 16)
                : "memory", "vcc", "scc", "s84", "s85", "s86", "s87", "s88", "s89", "s90", "s91", "s92", "s93", U_CLOB);
            if (__builtin_expect(bad != 0, 0)) TMO_SET((unsigned)bad)
        }
        last_rows(chsm, rsS, wu, grp, c, tau);
        if (u == 3 && widx == 0 && l == 0) __hip_atomic_store((gu64*)(a.boot + 144), *(volatile unsigned long long*)(chsm + C2_MB), __ATOMIC_RELAXED, __HIP_MEMORY_SCOPE_AGENT);
        return;
    }
    const int l = opaque_tid() & 63, i = l & 15, g = l >> 4;
    const int sidx = (i & 1) ? 0xEEEE : 0x4444;
    const int ha = 4 * grp + wu;
    unsigned long long tag;
    v32u wO[2];
    {
        u16* stg = (u16*)(chsm + 122880 + wu * 10240);
        tag = boot_tag(a.boot);
        asm volatile("" ::: "memory");
        v4u rawo[16];
        const u16* src = a.Wo + (size_t)(hcol(ha) + (l >> 3)) * DM + 64 * c + 8 * (l & 7);
#pragma unroll
        for (int e = 0; e < 16; ++e) rawo[e] = *(const v4u*)(src + (size_t)8 * e * DM);
        __builtin_amdgcn_sched_barrier(0);
        if (wu == 0) {
            asm volatile("s_waitcnt vmcnt(16)" ::: "memory");
            unsigned tm[2] = {~0u, ~0u};
            const char* pb = arena(a.slots) + cal_off;
#pragma unroll
            for (int k = 0; k < 2 * NPROBE; ++k) {
                unsigned long long c0, c1; unsigned d;
                asm volatile("s_memtime %0\n\ts_waitcnt lgkmcnt(0)\n\ts_load_dword %2, %3, %4\n\ts_waitcnt lgkmcnt(0)\n\ts_memtime %1\n\ts_waitcnt lgkmcnt(0)"
                             : "=&s"(c0), "=&s"(c1), "=&s"(d) : "s"(pb), "i"((k >> 1) * 256 + (k & 1) * 8192) : "memory");
                tm[k & 1] = min(tm[k & 1], (unsigned)(c1 - c0));
            }
            const unsigned side = (__builtin_amdgcn_s_getreg(20 | (3 << 11)) >> 2) & 1u;
            if (l == 0) __hip_atomic_store((gu64*)(a.boot + widx), (tag << 2) | (((tm[0] > tm[1] ? 1u : 0u) ^ side) << 1) | side, __ATOMIC_RELAXED, __HIP_MEMORY_SCOPE_AGENT);
            PST(7)
        }
#pragma unroll
        for (int m = 0; m < 2; ++m) {
#pragma unroll
            for (int e = 0; e < 8; ++e) *(v4u*)(stg + (8 * e + (l >> 3)) * 72 + 8 * (l & 7)) = rawo[8 * m + e];
            __builtin_amdgcn_s_waitcnt(0xC07F);
#pragma unroll
            for (int ct = 0; ct < 4; ++ct) {
                const u16* p = stg + (8 * g + (i >> 2)) * 72 + 16 * ct + 4 * (i & 3);
                const v2u r0 = tr4(p), r1 = tr4(p + 4 * 72), r2 = tr4(p + 32 * 72), r3 = tr4(p + 36 * 72);
                PUT4(wO[ct >> 1], (ct & 1) * 16 + m * 8, (v4u{r0[0], r0[1], r1[0], r1[1]})) PUT4(wO[ct >> 1], (ct & 1) * 16 + m * 8 + 4, (v4u{r2[0], r2[1], r3[0], r3[1]}))
            }
            __builtin_amdgcn_s_waitcnt(0xC07F);
        }
        if (wu == 0) { PST(5) }
        if (widx == 0 && wu == 0) *(volatile unsigned long long*)(chsm + C2_MB) = tag;
    }
    if (wu >= 2) touch_sc1(arena(a.slots) + (size_t)min(l + (wu - 2) * 64, FX_OFF / 2097152 - 1) * 2097152 + 2097024 - 128, l0 + C2_MB + 256);
    unsigned long long fw = 0;
#pragma nounroll
    for (int spin = 0; spin < 100000; ++spin) {
        asm volatile("" ::: "memory");
        fw = __hip_atomic_load((gu64*)(a.boot + 256 + 16 * grp + (l & 7)), __ATOMIC_RELAXED, __HIP_MEMORY_SCOPE_AGENT);
        if (__all((fw | 255u) == ((tag << 8) | 255u))) break;
        __builtin_amdgcn_s_sleep(4);
    }
    asm volatile("" ::: "memory");
    const unsigned tau = __builtin_amdgcn_readfirstlane((unsigned)fw & 255u);
    if (wu == 3) { *(volatile unsigned*)(chsm + C2_MB + 8) = tau; *(volatile unsigned*)(chsm + C2_MB + 16) = ~0u; __builtin_amdgcn_s_waitcnt(0xC07F); }
    if (wu == 1) { PST(9) }

    const unsigned r3a = l0 + C2_R3 + wu * 2048 + g * 128 + i * 4;
    v32u yb;
    v8u kx;
    v8u sel; v2u sel3;
#pragma unroll
    for (int d = 0; d < 8; ++d) sel[d] = (i >> 1) == d ? (0x3F80u << ((i & 1) * 16)) : 0u;
#pragma unroll
    for (int d = 0; d < 2; ++d) sel3[d] = (i & 3) == 0 && (i >> 3) == d ? (0x3F80u << (((i >> 2) & 1) * 16)) : 0u;
#define MF(d, a, b, c) "v_mfma_f32_16x16x32_bf16 v[" d "], v[" a "], v[" b "], " c "\n\t"
#define PX4(d0, d1, d2, d3, a, w0, w1, w2, w3) MF(d0, a, w0, "v[" d0 "]") MF(d1, a, w1, "v[" d1 "]") MF(d2, a, w2, "v[" d2 "]") MF(d3, a, w3, "v[" d3 "]")
#define PXT(d0, d1, d2, d3, a0, a1, a2, a3) MF(d0, a0, "32:35", "0") MF(d1, a0, "48:51", "0") MF(d2, a0, "64:67", "0") MF(d3, a0, "80:83", "0") \
        PX4(d0, d1, d2, d3, a1, "36:39", "52:55", "68:71", "84:87") PX4(d0, d1, d2, d3, a2, "40:43", "56:59", "72:75", "88:91") PX4(d0, d1, d2, d3, a3, "44:47", "60:63", "76:79", "92:95")
#define CVP(d, a, b) "v_cvt_pk_bf16_f32 v" d ", v" a ", v" b "\n\t"
#define PROJ_X(cnt) \
        "s_waitcnt " cnt "(8)\n\t" PXT("192:195", "196:199", "200:203", "204:207", "160:163", "164:167", "168:171", "172:175") \
        "s_waitcnt " cnt "(4)\n\t" PXT("208:211", "212:215", "216:219", "220:223", "176:179", "180:183", "184:187", "188:191") \
        CVP("128", "192", "193") CVP("129", "194", "195") CVP("136", "196", "197") CVP("137", "198", "199") CVP("144", "200", "201") CVP("145", "202", "203") CVP("152", "204", "205") CVP("153", "206", "207") \
        CVP("132", "208", "209") CVP("133", "210", "211") CVP("140", "212", "213") CVP("141", "214", "215") CVP("148", "216", "217") CVP("149", "218", "219") CVP("156", "220", "221") CVP("157", "222", "223") \
        "s_waitcnt " cnt "(0)\n\t"
#define CLOB_PX CLOB16(160, 161, 162, 163, 164, 165, 166, 167, 168, 169, 170, 171, 172, 173, 174, 175), CLOB16(176, 177, 178, 179, 180, 181, 182, 183, 184, 185, 186, 187, 188, 189, 190, 191), \
        CLOB16(192, 193, 194, 195, 196, 197, 198, 199, 200, 201, 202, 203, 204, 205, 206, 207), CLOB16(208, 209, 210, 211, 212, 213, 214, 215, 216, 217, 218, 219, 220, 221, 222, 223)
#define LDX(d, va, so, o) "buffer_load_dwordx4 v[" d "], " va ", %[rs], " so " offen offset:" o " sc1\n\t"
#define DSV(d, o) "ds_read_b128 v[" d "], %[lv] offset:" o "\n\t"
    const int voffX = ((i >> 1) & 1) * 4096 + g * 256 + (2 * (i >> 2) + (i & 1)) * 16;
    const int pk = (((i >> 1) & 3) << 1) | (i >> 3);
    const unsigned hv = wu >= 2 ? l0 + C2_R1 + 2176 + ((wu & 1) * 128 + ((l >> 4) + 4 * ((l >> 3) & 1)) * 16 + 8 * ((l >> 1) & 1) + 4 * ((l >> 2) & 1) + 2 * (l & 1)) * 4
                                : l0 + C2_R1 + (i & 1) * 544 + (i >> 1) * 64 + g * 16;
    const unsigned hk = l0 + C2_R1 + 1088 + (i & 1) * 544 + pk * 64 + g * 16;
    const int vo1 = wu >= 2 ? l * 4 : wu == 1 ? (g >> 1) * 1024 + i * 16 + (g & 1) * 8 : (i & 1) ? 1 << 30 : (i >> 1) * 16 + 4 * g;
    const int vo2 = (2 * (i & 1) + (g >> 1)) * 128 + pk * 16 + (g & 1) * 8;
    const int voffK = (g >> 1) * 256 + i * 16 + (g & 1) * 8;
#define RD4(w, ad, a, b, c, d) "ds_read_" w " v[" a "], " ad "\n\tds_read_" w " v[" b "], " ad " offset:3200\n\tds_read_" w " v[" c "], " ad " offset:6400\n\tds_read_" w " v[" d "], " ad " offset:9600\n\t"
#define AD4(d0, d1, d2, d3, a0, a1, a2, a3) ADD(d0, d0, a0) ADD(d1, d1, a1) ADD(d2, d2, a2) ADD(d3, d3, a3)
#define QSC(r) "v_mul_f32 v" r ", 0x3e0293ee, v" r "\n\t"
#define FLD "buffer_load_dword v114, %[vf], %[rs], %[fl] offen sc1\n\t"
#define DUM "buffer_store_dword v168, off, %[rs], %[ob] sc1\n\t"
#define QHEAD "s_barrier\n\t" RD4("b128", "%[hv]", "160:163", "164:167", "168:171", "172:175") FLD \
        "s_waitcnt lgkmcnt(2)\n\t" AD4("160", "161", "162", "163", "164", "165", "166", "167") \
        "s_waitcnt lgkmcnt(0)\n\t" AD4("168", "169", "170", "171", "172", "173", "174", "175") \
        "s_barrier\n\t"
#define QTAIL AD4("160", "161", "162", "163", "168", "169", "170", "171") QSC("160") QSC("161") QSC("162") QSC("163")
#define SWA(d, a, b) "v_add_f32_dpp v" d ", v" a ", v" b " quad_perm:[1,0,3,2] row_mask:0xf bank_mask:0xf\n\t"
#define KLD(so) "buffer_load_dwordx2 v[96:97], %[vk], %[rs], " so " offen sc1\n\tbuffer_load_dwordx2 v[102:103], %[vk], %[rs], " so " offen offset:512 sc1\n\t" \
        "s_add_u32 " so ", " so ", 4096\n\t" \
        "buffer_load_dwordx2 v[100:101], %[vk], %[rs], " so " offen sc1\n\tbuffer_load_dwordx2 v[98:99], %[vk], %[rs], " so " offen offset:512 sc1\n\t"
    auto hand_over = [&](const int o_1, const int o_2, const int o_f, const int tg) {
        int dmy = 0;
        asm volatile(
            "s_cmp_eq_u32 %[wu], 0\n\t"
            "s_cbranch_scc1 2f\n\t"
            "s_cmp_eq_u32 %[wu], 1\n\t"
            "s_cbranch_scc1 1f\n\t"
            "s_barrier\n\t"
            RD4("b64", "%[hv]", "160:161", "162:163", "164:165", "166:167") FLD
            "s_waitcnt lgkmcnt(2)\n\t" ADD("160", "160", "162") ADD("161", "161", "163")
            "s_waitcnt lgkmcnt(0)\n\t" ADD("164", "164", "166") ADD("165", "165", "167") ADD("160", "160", "164") ADD("161", "161", "165")
            CVP("168", "160", "161")
            "buffer_store_dword v168, %[v1], %[rs], %[o1] offen sc1\n\t"
            "s_barrier\n\t"
            DUM PAUSE(PV2)
            "s_branch 3f\n\t"
            "5:\n\t"
            "v_mov_b32 v115, %[lf]\n\t"
            "ds_read_b32 v115, v115\n\t"
            "s_waitcnt lgkmcnt(0)\n\t"
            "v_cmp_eq_u32 vcc, %[tg], v115\n\t"
            "s_cbranch_vccnz 4f\n\t" CN(256)
            PAUSE(PL)
            "s_branch 4f\n\t"
            "1:\n\t"
            Z2("176:177") Z2("178:179")
            "s_waitcnt vmcnt(0)\n\t"
            QHEAD QTAIL
            "s_mov_b64 exec, %[em]\n\t"
            CVP("176", "160", "161") CVP("177", "162", "163")
            "s_not_b64 exec, exec\n\t"
            CVP("178", "160", "161") CVP("179", "162", "163")
            "s_mov_b64 exec, -1\n\t"
            "s_nop 0\n\t"
            MF("180:183", "96:99", "176:179", "0") MF("184:187", "100:103", "176:179", "0")
            "s_nop 7\n\t"
            SWA("180", "184", "180") SWA("181", "185", "181") SWA("182", "186", "182") SWA("183", "187", "183")
            CVP("188", "180", "181") CVP("189", "182", "183")
            "buffer_store_dwordx2 v[188:189], %[v1], %[rs], %[o1] offen sc1\n\t"
            DUM PAUSE(PV1)
            "s_branch 3f\n\t"
            "2:\n\t"
            QHEAD
            RD4("b128", "%[hk]", "176:179", "180:183", "184:187", "188:191")
            QTAIL CVP("164", "160", "161") CVP("165", "162", "163")
            "s_waitcnt lgkmcnt(2)\n\t" AD4("176", "177", "178", "179", "180", "181", "182", "183")
            "s_waitcnt lgkmcnt(0)\n\t" AD4("184", "185", "186", "187", "188", "189", "190", "191") AD4("176", "177", "178", "179", "184", "185", "186", "187")
            CVP("166", "176", "177") CVP("167", "178", "179")
            "s_nop 1\n\t"
            "v_mfma_f32_16x16x16_bf16 v[168:171], v[166:167], v[164:165], 0\n\t"
            "buffer_store_dwordx2 v[166:167], %[v2], %[rs], %[o2] offen sc1\n\t"
            "s_nop 6\n\t"
            SWA("172", "169", "168") SWA("173", "171", "170")
            CVP("174", "172", "173")
            "buffer_store_dword v174, %[v1], %[rs], %[o1] offen sc1\n\t"
            "3:\n\t"
            PAUSE(PVA)
            "s_waitcnt vmcnt(2)\n\t"
            "v_cmp_ne_u32 vcc, " STR(SENT) ", v114\n\t"
            "s_cbranch_vccnz 5b\n\t"
            "4:\n\t"
            : [dm] "+s"(dmy) CNOUT : "{v[96:103]}"(kx), [fl] "s"(o_f), [vf] "v"(i * 128), [ob] "s"(1 << 30), [tg] "s"(tg), [lf] "s"(l0 + C2_MB + 16), [wu] "s"(wu), [hv] "v"(hv), [hk] "v"(hk), [v1] "v"(vo1), [v2] "v"(vo2), [rs] "s"(rsS), [o1] "s"(o_1), [o2] "s"(o_2), [em] "s"(0x5555555555555555ull)
            : "memory", "scc", "vcc", "v114", "v115", CLOB16(160, 161, 162, 163, 164, 165, 166, 167, 168, 169, 170, 171, 172, 173, 174, 175), CLOB16(176, 177, 178, 179, 180, 181, 182, 183, 184, 185, 186, 187, 188, 189, 190, 191));
    };
    auto o1_of = [&](const int s) { return wu >= 2 ? v_off(s, hs, tau) + jq * 512 + (wu & 1) * 256 : wu == 1 ? x_off(s, hs, tau) + jq * 256 : m_off(s, hs, tau) + jq * 128; };

    __builtin_amdgcn_s_setprio(1);
    int o_f = f_off(0, grp, tau);
    int o_cs = rec_base(0) + C_OFF + c * 16384 + (int)(half_bits(0, tau) & 1u) * 8192 + wu * 2048 + grp * 256;
    int o_1 = o1_of(0), o_2 = km_off(0, hs, tau) + jq * 512, bq = x_off(0, ha, tau), bm = m_off(0, ha, tau), bb = v_off(0, ha, tau);
    {
            const int b0 = blk_off(0, ha, tau);
            int k0 = KX_OFF + blk_off(0, hs, tau) + jq * 1024;
            asm volatile(
                LDX("160:163", "%[vx]", "%[vb]", "0") LDX("164:167", "%[vx]", "%[vb]", "1024") LDX("168:171", "%[vx]", "%[vb]", "2048") LDX("172:175", "%[vx]", "%[vb]", "3072")
                LDX("176:179", "%[vx]", "%[vb]", "128") LDX("180:183", "%[vx]", "%[vb]", "1152") LDX("184:187", "%[vx]", "%[vb]", "2176") LDX("188:191", "%[vx]", "%[vb]", "3200")
                KLD("%[k0]")
                "v_mov_b32 v131, 0\n\tv_mov_b32 v135, 0\n\tv_mov_b32 v139, 0\n\tv_mov_b32 v143, 0\n\tv_mov_b32 v147, 0\n\tv_mov_b32 v151, 0\n\tv_mov_b32 v155, 0\n\tv_mov_b32 v159, 0\n\t"
                PROJ_X("vmcnt")
                : "=&{v[128:159]}"(yb), "=&{v[96:103]}"(kx), [k0] "+s"(k0)
                : "{v[32:63]}"(wO[0]), "{v[64:95]}"(wO[1]), [vx] "v"(voffX), [vk] "v"(voffK), [rs] "s"(rsS), [vb] "s"(VX_OFF + b0)
                : "memory", "scc", CLOB_PX);
        if (wu == 1) { PST(10) }
    }
    int s_first = 0;
    asm volatile("s_branch 1f\n\t.p2align 8\n1:" : "+s"(s_first));
    for (int s = s_first; s < NSEG - 1; ++s) {
        hand_over(o_1, o_2, o_f, s);
        if (s == 0 && wu == 1) { PST(8) }
        LT(0)
#define LDPA "buffer_load_dwordx2 v[160:161], v254, %[rs], %[bm] offen sc1\n\t" LDX("164:167", "%[l16]", "%[bq]", "0") LDX("168:171", "%[l16]", "%[bq]", "1024")
#define SM(d, a, b, x) "v_smfmac_f32_16x16x64_bf16 v[" d "], v[" a "], v[" b "], " x "\n\t"
#define MX2(a, b) "v_max_u32 v244, v" a ", v" b "\n\t"
#define MX3(a, b) "v_max3_u32 v244, v244, v" a ", v" b "\n\t"
#define ZAM Z2("208:209") Z2("210:211") Z2("212:213") Z2("214:215") Z2("216:217") Z2("218:219") Z2("220:221") Z2("222:223")
#define MUL(d, a, b) "v_mul_f32 v" d ", v" a ", v" b "\n\t"
#define CV0(d, a) "v_cvt_pk_bf16_f32 v" d ", v" a ", 0\n\t"
#define RDC "ds_read_b64 v[224:225], %[rc]\n\tds_read_b64 v[226:227], %[rc] offset:2048\n\tds_read_b64 v[228:229], %[rc] offset:4096\n\tds_read_b64 v[230:231], %[rc] offset:6144\n\t"
#define EXD(d, r) "v_exp_f32 v" d ", v" r "\n\t"
#define SUBM(r) "v_sub_f32 v" r ", v" r ", v250\n\t"
#define PCVM CVP("241", "180", "180") CVP("243", "180", "180")
#define PCVX CVP("242", "178", "179") CVP("240", "176", "177")
#define OUT(t0, t1, a0, a1, a2, a3, ad, o0, o1) ADD(t0, a0, a1) ADD(t1, a2, a3) MUL(t0, t0, "250") MUL(t1, t1, "251") DW2(ad, t0, t1, o0, o1)
#define TS(n)
#define TSOUT
        int lim = dead ? 0 : SPIN_LIMIT, bad = 0;
        asm volatile(
            LDX("192:195", "%[l16]", "%[bb]", "0") LDX("196:199", "%[l16]", "%[bb]", "1024")
            ZAM Z2("224:225") Z2("226:227")
            Z2("16:17") Z2("18:19") Z2("20:21") Z2("22:23") Z2("24:25") Z2("26:27") Z2("28:29") Z2("30:31")
            "v_mov_b32 v200, 0x3f803f80\n\tv_mov_b32 v201, 0x3f803f80\n\tv_mov_b32 v202, 0x3f80\n\tv_mov_b32 v203, 0\n\t"
            "v_mov_b64 v[204:205], v[200:201]\n\tv_mov_b64 v[206:207], v[202:203]\n\t"
            "v_xor_b32 v252, 0xa000, %[sx]\n\tv_add_u32 v253, 1024, %[ra]\n\tv_lshrrev_b32 v254, 1, %[l16]\n\t"
            PAUSE(PV)
            TS(8)
            LDPA
            "4:\n\t"
            "s_waitcnt vmcnt(4)\n\t"
            SM("208:211", "192:195", "32:39", "%[sx]") MX2("192", "193") SM("212:215", "192:195", "48:55", "%[sx]") MX3("194", "195")
            SM("216:219", "192:195", "64:71", "%[sx]") SM("220:223", "192:195", "80:87", "%[sx]")
            "s_waitcnt vmcnt(3)\n\t"
            SM("208:211", "196:199", "40:47", "%[sx]") MX3("196", "197") SM("212:215", "196:199", "56:63", "%[sx]") MX3("198", "199")
            SM("216:219", "196:199", "72:79", "%[sx]") "v_cmp_eq_u32 vcc, " STR(SENT) ", v244\n\t" SM("220:223", "196:199", "88:95", "%[sx]")
            "s_cbranch_vccnz 5f\n\t"
            "1:\n\t" TS(11)
            "s_waitcnt vmcnt(2)\n\t"
            "v_mfma_f32_16x16x16_bf16 v[236:239], v[112:113], v[160:161], 0\n\t"
            MX2("160", "161") ADD("245", "208", "209") ADD("246", "210", "211") CV0("130", "245") CV0("134", "246")
            ADD("245", "212", "213") ADD("246", "214", "215") CV0("138", "245") CV0("142", "246")
            EXD("180", "236")
            "s_waitcnt vmcnt(1)\n\t"
            MF("228:231", "104:107", "164:167", "0")
            MX3("164", "165") MX3("166", "167") ADD("245", "216", "217") ADD("246", "218", "219") CV0("146", "245") CV0("150", "246")
            ADD("245", "220", "221") ADD("246", "222", "223") CV0("154", "245") CV0("158", "246") PCVM
            "s_waitcnt vmcnt(0)\n\t"
            MF("228:231", "108:111", "168:171", "v[228:231]")
            MX3("168", "169") MX3("170", "171")
            "v_cmp_eq_u32 vcc, " STR(SENT) ", v244\n\t"
            "s_cbranch_vccnz 7f\n\t"
            "2:\n\t" TS(9)
            "s_nop 3\n\t"
            EXD("179", "231") EXD("178", "230") EXD("177", "229") EXD("176", "228")
            PCVX
            "v_max3_f32 v250, |v236|, |v231|, |v230|\n\t"
            "v_max3_f32 v250, v250, |v229|, |v228|\n\t"
            SM("224:227", "240:243", "200:207", "v252")
            "v_cmp_nge_f32 vcc, 0x41c00000, v250\n\t"
            "s_nop 0\n\t"
            SM("16:19", "240:243", "128:135", "v252")
            "s_cbranch_vccnz 10f\n\t"
            "13:\n\t"
            "s_nop 0\n\t"
            SM("20:23", "240:243", "136:143", "v252") "s_nop 1\n\t"
            ADD("250", "224", "225") ADD("251", "226", "227")
            SM("24:27", "240:243", "144:151", "v252")
            "v_rcp_f32 v250, v250\n\t"
            SM("28:31", "240:243", "152:159", "v252")
            "v_rcp_f32 v251, v251\n\t"
            OUT("245", "246", "16", "17", "18", "19", "%[ra]", "0", "16")
            OUT("247", "248", "20", "21", "22", "23", "%[ra]", "128", "144")
            OUT("245", "246", "24", "25", "26", "27", "v253", "0", "16")
            OUT("247", "248", "28", "29", "30", "31", "v253", "128", "144")
            TS(10)
            "s_waitcnt lgkmcnt(0)\n\t"
            "s_barrier\n\t"
            RDC
            "v_lshrrev_b32 v247, 2, %[l16]\n\t"
            "s_waitcnt lgkmcnt(2)\n\t"
            ADD("224", "224", "226") ADD("225", "225", "227")
            "s_waitcnt lgkmcnt(0)\n\t"
            ADD("228", "228", "230") ADD("229", "229", "231") ADD("224", "224", "228") ADD("225", "225", "229")
            "v_cvt_pk_bf16_f32 v245, v224, v225\n\t"
            "buffer_store_dword v245, v247, %[rs], %[cs] offen sc1\n\t"
            "s_branch 9f\n\t"
            "10:\n\t"
            "v_max3_f32 v250, v236, v231, v230\n\t"
            "v_max3_f32 v250, v250, v229, v228\n\t"
            "s_nop 1\n\t"
            "v_max_f32_dpp v251, v250, v250 quad_perm:[1,0,3,2] row_mask:0xf bank_mask:0xf\n\t"
            "v_max_f32_dpp v244, v250, v250 quad_perm:[1,0,3,2] row_mask:0xf bank_mask:0xf\n\t"
            "s_nop 1\n\t"
            "v_permlane16_swap_b32 v251, v244\n\t"
            "v_max_f32 v250, v251, v244\n\t"
            "v_max_f32 v251, v251, v244\n\t"
            Z2("224:225") Z2("226:227")
            "v_permlane32_swap_b32 v250, v251\n\t"
            "v_max_f32 v250, v250, v251\n\t"
            Z2("16:17") Z2("18:19")
            SUBM("228") SUBM("229") SUBM("230") SUBM("231") SUBM("236")
            EXD("180", "236") EXD("179", "231") EXD("178", "230") EXD("177", "229") EXD("176", "228")
            PCVM PCVX
            "s_nop 1\n\t"
            SM("224:227", "240:243", "200:207", "v252") "s_nop 1\n\t"
            SM("16:19", "240:243", "128:135", "v252")
            "s_branch 13b\n\t"
            "5:\n\t" CN(1)
            LDX("192:195", "%[l16]", "%[bb]", "0") LDX("196:199", "%[l16]", "%[bb]", "1024")
            LDPA
            "s_sub_u32 %[lim], %[lim], 1\n\t"
            "s_nop 1\n\t"
            ZAM
            "s_cbranch_scc0 4b\n\t"
            "s_mov_b32 %[bad], 1500\n\t"
            "s_mov_b32 %[lim], 0\n\t"
            "s_branch 1b\n\t"
            "7:\n\t" CN(65536)
            LDPA
            "s_sub_u32 %[lim], %[lim], 1\n\t"
            "s_cbranch_scc0 1b\n\t"
            "s_mov_b32 %[bad], 1000\n\t"
            "s_branch 2b\n\t"
            "9:\n\t"
            : [lim] "+s"(lim), [bad] "+s"(bad), "+{v[128:159]}"(yb) TSOUT CNOUT
            : "{v[32:63]}"(wO[0]), "{v[64:95]}"(wO[1]), "{v[104:111]}"(sel), "{v[112:113]}"(sel3), [sx] "v"(sidx), [l16] "v"(l * 16),
              [ra] "v"(r3a), [rs] "s"(rsS), [bb] "s"(bb), [bq] "s"(bq), [bm] "s"(bm), [cs] "s"(o_cs), [rc] "v"(l0 + C2_R3 + wu * 512 + l * 8)
            : "memory", "vcc", "scc", CLOB_PX, CLOB16(16, 17, 18, 19, 20, 21, 22, 23, 24, 25, 26, 27, 28, 29, 30, 31),
              CLOB16(224, 225, 226, 227, 228, 229, 230, 231, 232, 233, 234, 235, 236, 237, 238, 239), CLOB16(240, 241, 242, 243, 244, 245, 246, 247, 248, 249, 250, 251, 252, 253, 254, 254));
        if (__builtin_expect(bad != 0, 0)) TMO_SET((unsigned)bad)
        __builtin_amdgcn_s_setprio(0);
        if (s == 0 && wu == 1) { PST(6) }

        if (__builtin_expect(s <= KVS + 1, 0)) {
            if (s == 0 || __builtin_amdgcn_readfirstlane(*(volatile unsigned*)(chsm + C2_MB + 32 + ((s - 1) & 1) * 16 + 4 + (wu >> 1) * 4)) == 0u) {
                wait_ge<6>(kv_flag(a.sync, s + 1, grp, l), 1u, a.tmo, 3001u);
                const char* kp = arena(a.slots) + VX_OFF + blk_off(s + 1, ha, tau);
#pragma unroll
                for (int e = 0; e < 8; ++e) glds16h((unsigned)l * 16u, kp + e * 1024, l0 + (s & 1 ? C2_V : C2_K) + wu * 8192 + e * 1024);
                asm volatile("s_waitcnt vmcnt(0)" ::: "memory");
            }
        }
        {
            int k0 = KX_OFF + blk_off(s + 1, hs, tau) + jq * 1024;
            asm volatile(
                DSV("160:163", "0") DSV("164:167", "1024") DSV("168:171", "2048") DSV("172:175", "3072") DSV("176:179", "128") DSV("180:183", "1152") DSV("184:187", "2176") DSV("188:191", "3200")
                "s_cmp_lg_u32 %[wu], 1\n\t"
                "s_cbranch_scc1 1f\n\t"
                KLD("%[k0]")
                "1:\n\t"
                "s_waitcnt lgkmcnt(4)\n\t"
                PXT("192:195", "196:199", "200:203", "204:207", "160:163", "164:167", "168:171", "172:175")
                "s_waitcnt lgkmcnt(0)\n\t"
                PXT("208:211", "212:215", "216:219", "220:223", "176:179", "180:183", "184:187", "188:191")
                CVP("128", "192", "193") CVP("129", "194", "195") CVP("136", "196", "197") CVP("137", "198", "199") CVP("144", "200", "201") CVP("145", "202", "203") CVP("152", "204", "205") CVP("153", "206", "207")
                CVP("132", "208", "209") CVP("133", "210", "211") CVP("140", "212", "213") CVP("141", "214", "215") CVP("148", "216", "217") CVP("149", "218", "219") CVP("156", "220", "221") CVP("157", "222", "223")
                : "+{v[128:159]}"(yb), "+{v[96:103]}"(kx), [k0] "+s"(k0)
                : "{v[32:63]}"(wO[0]), "{v[64:95]}"(wO[1]), [lv] "v"(l0 + (s & 1 ? C2_V : C2_K) + wu * 8192 + voffX), [vk] "v"(voffK), [rs] "s"(rsS), [wu] "s"(wu)
                : "memory", "scc", CLOB_PX);
        }
        __builtin_amdgcn_sched_barrier(0);
        o_1 = o1_of(s + 1); o_2 = km_off(s + 1, hs, tau) + jq * 512;
        bq = x_off(s + 1, ha, tau); bm = m_off(s + 1, ha, tau); bb = v_off(s + 1, ha, tau);
        o_f = f_off(s + 1, grp, tau);
        o_cs = rec_base(s + 1) + C_OFF + c * 16384 + (int)(half_bits(s + 1, tau) & 1u) * 8192 + wu * 2048 + grp * 256;
        asm volatile("" : "+s"(o_cs), "+s"(o_f), "+s"(o_1), "+s"(o_2), "+s"(bq), "+s"(bm), "+s"(bb));
        __builtin_amdgcn_s_barrier();
        asm volatile(
            "s_cmp_lg_u32 %[wu], 3\n\t"
            "s_cbranch_scc1 1f\n\t"
            "v_mov_b32 v160, %[lf]\n\t"
            PAUSE(PF)
            "ds_read_b32 v160, v160\n\t"
            "s_waitcnt lgkmcnt(0)\n\t"
            "v_cmp_eq_u32 vcc, %[tg], v160\n\t"
            "s_cbranch_vccz 1f\n\t"
            "s_mov_b32 exec_hi, 0\n\t"
            "buffer_store_dword v160, %[l4], %[rs], %[fo] offen sc1\n\t"
            "s_mov_b64 exec, -1\n\t"
            "1:\n\t"
            :: [wu] "s"(wu), [lf] "s"(l0 + C2_MB + 16), [tg] "s"(s + 1), [l4] "v"(l * 4), [rs] "s"(rsS), [fo] "s"(o_f + c * 128)
            : "memory", "scc", "vcc", "v160");
        __builtin_amdgcn_s_setprio(1);
    }
    {
        constexpr int s = NSEG - 1;
        const int l = lane_now(), i = l & 15, g = l >> 4;
        v4u qf[8], kf[8], pv[2], px[4];
        asm volatile("s_waitcnt vmcnt(0)" ::: "memory");
        __builtin_amdgcn_s_sleep(4);
#pragma unroll
        for (int e = 0; e < 8; ++e) kf[e] = ld_sc1s(rsS, l * 16, KX_OFF + blk_off(s, ha, tau) + e * 1024);
#pragma unroll
        for (int e = 0; e < 8; ++e) qf[e] = *(const v4u*)(chsm + C2_V + 32768 + wu * 8192 + e * 1024 + l * 16);
        f32x4 sx[3] = {ZERO4, ZERO4, ZERO4};
        float mxx[2] = {-3.0e38f, -3.0e38f};
#pragma unroll
        for (int ks = 0; ks < 4; ++ks) {
            sx[0] = MM(kf[ks], qf[ks], sx[0]);
            sx[1] = MM(kf[ks], qf[4 + ks], sx[1]);
            sx[2] = MM(kf[4 + ks], qf[4 + ks], sx[2]);
        }
#pragma unroll
        for (int r = 0; r < 4; ++r) {
            if (4 * g + r > i) { sx[0][r] = -3.0e38f; sx[2][r] = -3.0e38f; }
            mxx[0] = fmaxf(mxx[0], sx[0][r]);
            mxx[1] = fmaxf(mxx[1], fmaxf(sx[1][r], sx[2][r]));
        }
        asm volatile("" : "+v"(sx[0]), "+v"(sx[1]), "+v"(sx[2]), "+v"(mxx[0]), "+v"(mxx[1]));
        hand_over(o_1, o_2, o_f, s);
        const int bk = km_off(s, ha, tau);
        __builtin_amdgcn_s_sleep(DLY_L);
#pragma nounroll
        for (int spin = 0;; ++spin) {
            asm volatile("" ::: "memory");
            pv[0] = ld_sc1s(rsS, l * 16, bb); pv[1] = ld_sc1s(rsS, l * 16, bb + 1024);
#pragma unroll
            for (int ks = 0; ks < 4; ++ks) px[ks] = ld_sc1s(rsS, g * 128 + (i >> 1) * 16, bk + ks * 512);
            if (__all(mx4(mx4(mx4(mx4(mx4(mx4(0u, pv[0]), pv[1]), px[0]), px[1]), px[2]), px[3]) != SENT)) break;
            if (dead) break;
            if (spin > SPIN_LIMIT) { TMO_SET(1700u) break; }
        }
#define SUB8(v, o) v8u{(v)[(o)], (v)[(o) + 1], (v)[(o) + 2], (v)[(o) + 3], (v)[(o) + 4], (v)[(o) + 5], (v)[(o) + 6], (v)[(o) + 7]}
#pragma unroll
        for (int ct = 0; ct < 4; ++ct) {
            f32x4 am = smm(pv[0], SUB8(wO[ct >> 1], (ct & 1) * 16), ZERO4, sidx);
            am = smm(pv[1], SUB8(wO[ct >> 1], (ct & 1) * 16 + 8), am, sidx);
            yb[8 * ct + 2] = cvt_pk_bf16(am[0] + am[1], 0.f); yb[8 * ct + 6] = cvt_pk_bf16(am[2] + am[3], 0.f);
        }
        const v8u one = {0x3F803F80u, 0x3F803F80u, 0x3F80u, 0u, 0x3F803F80u, 0x3F803F80u, 0x3F80u, 0u};
        const float SCL2E = 0.08838834764831845f * 1.4426950408889634f;
        float* xo = (float*)(chsm + C2_K) + (wu * 32 + 4 * g) * XP + i;
#pragma unroll
        for (int qt = 0; qt < 2; ++qt) {
            f32x4 sc[3] = {sx[qt], sx[2], ZERO4};
#pragma unroll
            for (int ks = 0; ks < 4; ++ks) sc[2] = MM(px[ks], qf[4 * qt + ks], sc[2]);
            float mx = fmaxf(mxx[qt], fmaxf(sc[2][1], sc[2][3]));
            mx = xor32_max(xor16_max(mx));
            const float nmx = -mx * SCL2E;
#define PE(x) __builtin_amdgcn_exp2f(fmaf(x, SCL2E, nmx))
            const float m0 = PE(sc[2][1]), m1 = PE(sc[2][3]);
            const v4u ae = {cvt_pk_bf16(PE(sc[0][0]), PE(sc[0][1])), cvt_pk_bf16(m0, m0), cvt_pk_bf16(PE(sc[0][2]), PE(sc[0][3])), 0u};
            const v4u ao = {qt ? cvt_pk_bf16(PE(sc[1][0]), PE(sc[1][1])) : 0u, 0u, qt ? cvt_pk_bf16(PE(sc[1][2]), PE(sc[1][3])) : 0u, cvt_pk_bf16(m1, m1)};
            const f32x4 sum = smm(ao, one, smm(ae, one, ZERO4, 0xE444), 0x4EEE);
#pragma unroll
            for (int ct = 0; ct < 4; ++ct) {
                const f32x4 o = smm(ao, SUB8(yb, 8 * ct), smm(ae, SUB8(yb, 8 * ct), ZERO4, 0xE444), 0x4EEE);
#pragma unroll
                for (int r = 0; r < 4; ++r) xo[(16 * qt + r) * XP + 16 * ct] = o[r] * __builtin_amdgcn_rcpf(sum[r]);
            }
        }
        __builtin_amdgcn_s_waitcnt(0xC07F);
        last_rows(chsm, rsS, wu, grp, c, tau);
        if (wu < 2) {
            v4u sel, p0, p1;
#pragma unroll
            for (int d = 0; d < 4; ++d) sel[d] = (l & 6) == 2 * d ? (0x3F80u << ((l & 1) * 16)) : 0u;
            const int xb = XB_OFF + ((c >> 1) * 8 + grp) * 16384 + (int)(tau & 1u) * 8192 + (c & 1) * 4096 + wu * 256;
            __builtin_amdgcn_s_sleep(DLY_C);
#pragma nounroll
            for (int spin = 0;; ++spin) {
                asm volatile("" ::: "memory");
                p0 = ld_sc1s(rsS, g * 512 + i * 16, xb); p1 = ld_sc1s(rsS, g * 512 + i * 16, xb + 2048);
                if (__all(mx4(mx4(0u, p0), p1) != SENT)) break;
                if (dead) break;
                if (spin > SPIN_LIMIT) { TMO_SET(1800u) break; }
            }
            const f32x4 d = MM(sel, p1, MM(sel, p0, ZERO4));
            if (g < 2) out8(a.out + (size_t)(s * SEG + 4 * grp + 2 * wu + (i >> 3)) * OUTW + 64 * c + 8 * (i & 7) + 4 * g, v2u{cvt_pk_bf16(d[0], d[1]), cvt_pk_bf16(d[2], d[3])});
        }
    }
}

struct cpx { float x, y; };
__device__ __forceinline__ cpx cmul(cpx a, cpx b) { return {a.x * b.x - a.y * b.y, a.x * b.y + a.y * b.x}; }
__device__ __forceinline__ cpx cadd(cpx a, cpx b) { return {a.x + b.x, a.y + b.y}; }
__device__ __forceinline__ cpx csub(cpx a, cpx b) { return {a.x - b.x, a.y - b.y}; }
template <int DIR> __device__ __forceinline__ cpx mul_i(cpx a) {
    return (DIR < 0) ? cpx{a.y, -a.x} : cpx{-a.y, a.x};
}
template <int DIR> __device__ __forceinline__ void r4(cpx& a, cpx& b, cpx& c, cpx& d) {
    cpx t0 = cadd(a, c), t1 = csub(a, c), t2 = cadd(b, d), t3 = mul_i<DIR>(csub(b, d));
    a = cadd(t0, t2); b = cadd(t1, t3); c = csub(t0, t2); d = csub(t1, t3);
}
template <int DIR> __device__ __forceinline__ cpx twc(float c, float s) { return cpx{c, (float)DIR * s}; }
template <int DIR> __device__ __forceinline__ void dft16(cpx (&v)[16]) {
#pragma unroll
    for (int a = 0; a < 4; ++a) r4<DIR>(v[a], v[a + 4], v[a + 8], v[a + 12]);
    const float C1 = 0.92387953251128674f, S1 = 0.38268343236508977f, R2 = 0.70710678118654752f;
    v[1 + 4] = cmul(v[1 + 4], twc<DIR>(C1, S1));
    v[1 + 8] = cmul(v[1 + 8], twc<DIR>(R2, R2));
    v[1 + 12] = cmul(v[1 + 12], twc<DIR>(S1, C1));
    v[2 + 4] = cmul(v[2 + 4], twc<DIR>(R2, R2));
    v[2 + 8] = mul_i<DIR>(v[2 + 8]);
    v[2 + 12] = cmul(v[2 + 12], twc<DIR>(-R2, R2));
    v[3 + 4] = cmul(v[3 + 4], twc<DIR>(S1, C1));
    v[3 + 8] = cmul(v[3 + 8], twc<DIR>(-R2, R2));
    v[3 + 12] = cmul(v[3 + 12], twc<DIR>(-C1, -S1));
#pragma unroll
    for (int d = 0; d < 4; ++d) r4<DIR>(v[4 * d], v[4 * d + 1], v[4 * d + 2], v[4 * d + 3]);
}
__device__ __forceinline__ constexpr int x16(int q) { return 4 * (q & 3) + (q >> 2); }
template <int DIR> __device__ __forceinline__ void dft8(cpx (&v)[8]) {
    r4<DIR>(v[0], v[2], v[4], v[6]);
    r4<DIR>(v[1], v[3], v[5], v[7]);
    const float R2 = 0.70710678118654752f;
    v[3] = cmul(v[3], twc<DIR>(R2, R2));
    v[5] = mul_i<DIR>(v[5]);
    v[7] = cmul(v[7], twc<DIR>(-R2, R2));
#pragma unroll
    for (int d = 0; d < 4; ++d) { cpx p = v[2 * d], q = v[2 * d + 1]; v[2 * d] = cadd(p, q); v[2 * d + 1] = csub(p, q); }
}
__device__ __forceinline__ int pad(int i) { return i + (i >> 4); }
__device__ __forceinline__ cpx expi(float x) { float s, c; sincospif(x, &s, &c); return cpx{c, s}; }
template <int R, int DIR> __device__ __forceinline__ void twiddle(cpx* v, cpx w1) {
    cpx w[R];
    w[1] = w1;
#pragma unroll
    for (int r = 2; r < R; ++r) w[r] = (r & 1) ? cmul(w[r - 1], w1) : cmul(w[r / 2], w[r / 2]);
#pragma unroll
    for (int r = 1; r < R; ++r) v[r] = DIR < 0 ? cmul(v[r], w[r]) : cpx{v[r].x * w[r].x + v[r].y * w[r].y, v[r].y * w[r].x - v[r].x * w[r].y};
}
struct TwBase { cpx b; cpx c[4]; };
template <int N> __device__ __forceinline__ TwBase make_tw(int t) {
    TwBase tb;
    tb.b = expi(-(float)(t & 15) * (1.0f / 128.0f));
    constexpr int NQ = (N == 1024) ? 4 : 2, T = N / 16;
#pragma unroll
    for (int q = 0; q < NQ; ++q) tb.c[q] = expi(-(float)(t + T * q) * (2.0f / (float)N));
    return tb;
}
template <int N> __device__ __forceinline__ void fft_sync() {
    if constexpr (N == 1024) { __builtin_amdgcn_fence(__ATOMIC_ACQ_REL, "wavefront"); __builtin_amdgcn_wave_barrier(); } else __syncthreads();
}
template <int N, int DIR, bool RIN, bool ROUT>
__device__ __forceinline__ void fft_reg(cpx* buf, const TwBase& tb, int t, cpx (&v)[16]) {
    constexpr int T = N / 16;
    if constexpr (!RIN) {
#pragma unroll
        for (int r = 0; r < 16; ++r) v[r] = buf[pad(t + T * r)];
        fft_sync<N>();
    }
    dft16<DIR>(v);
#pragma unroll
    for (int q = 0; q < 16; ++q) buf[pad(16 * t + x16(q))] = v[q];
    fft_sync<N>();
    {
        const int k = t & 15;
#pragma unroll
        for (int r = 0; r < 16; ++r) v[r] = buf[pad(t + T * r)];
        fft_sync<N>();
        twiddle<16, DIR>(v, tb.b);
        dft16<DIR>(v);
        const int o = (t >> 4) * 256 + k;
#pragma unroll
        for (int q = 0; q < 16; ++q) buf[pad(o + 16 * x16(q))] = v[q];
        fft_sync<N>();
    }
    if constexpr (N == 1024) {
#pragma unroll
        for (int q = 0; q < 4; ++q)
#pragma unroll
            for (int r = 0; r < 4; ++r) v[4 * q + r] = buf[pad(t + 64 * q + 256 * r)];
#pragma unroll
        for (int q = 0; q < 4; ++q) {
            twiddle<4, DIR>(&v[4 * q], tb.c[q]);
            r4<DIR>(v[4 * q], v[4 * q + 1], v[4 * q + 2], v[4 * q + 3]);
#pragma unroll
            for (int r = 0; r < 4; ++r) buf[pad(t + 64 * q + 256 * r)] = v[4 * q + r];
        }
        fft_sync<N>();
    } else {
        cpx u[2][8];
#pragma unroll
        for (int q = 0; q < 2; ++q)
#pragma unroll
            for (int r = 0; r < 8; ++r) u[q][r] = buf[pad(t + 128 * q + 256 * r)];
        if constexpr (ROUT) fft_sync<N>();
#pragma unroll
        for (int q = 0; q < 2; ++q) {
            twiddle<8, DIR>(u[q], tb.c[q]);
            dft8<DIR>(u[q]);
#pragma unroll
            for (int d = 0; d < 4; ++d) {
                if constexpr (ROUT) { v[q + 2 * d] = u[q][2 * d]; v[q + 2 * d + 8] = u[q][2 * d + 1]; }
                else { buf[pad(t + 128 * q + 256 * d)] = u[q][2 * d]; buf[pad(t + 128 * q + 256 * (d + 4))] = u[q][2 * d + 1]; }
            }
        }
        if constexpr (!ROUT) fft_sync<N>();
    }
}
__device__ __forceinline__ int f_pos(int rb, int x, unsigned tw) { x = (x + rb * FROT) & 32767; return rb * 65536 + x + (x & ~8191) + (int)((tw ^ (unsigned)(rb >> 5)) & 1u) * 8192; }
__device__ __forceinline__ int w_pos(int k, int n, unsigned tw) { return k * 16384 + ((n * 4 + k * WROT) & 8191) + (int)((tw ^ (unsigned)(k >> 7)) & 1u) * 8192; }
constexpr int PB1 = 1024 + 64, PB2 = 2048 + 128;
typedef float f32x4v __attribute__((ext_vector_type(4)));
typedef unsigned u32x4v __attribute__((ext_vector_type(4)));

__device__ __forceinline__ void fft_rows_fwd(const u16* __restrict__ x, rsrc_t rsF, const unsigned tw, int rb, char* smem, int tid, const char* wr, const char* wi, const unsigned spare) {
    cpx (*buf)[PB1] = (cpx (*)[PB1])smem;
    int f = tid >> 6, t = tid & 63;
    const u16* p0 = x + (size_t)(rb * 8 + 2 * f) * 1024 + 16 * t;
    u32x4v a0 = *(const u32x4v*)p0, a1 = *(const u32x4v*)(p0 + 8), b0 = *(const u32x4v*)(p0 + 1024), b1 = *(const u32x4v*)(p0 + 1032);
    const TwBase tb = make_tw<1024>(t);
    for (int it = 0; it < 4; ++it, rb += 64) {
        asm volatile("" : "+v"(tid));
        f = tid >> 6; t = tid & 63;
        __syncthreads();
        cpx* d = &buf[f][pad(16 * t)];
#pragma unroll
        for (int e = 0; e < 4; ++e) {
            d[2 * e] = cpx{__uint_as_float(a0[e] << 16), __uint_as_float(b0[e] << 16)};
            d[2 * e + 1] = cpx{__uint_as_float(a0[e] & 0xFFFF0000u), __uint_as_float(b0[e] & 0xFFFF0000u)};
            d[8 + 2 * e] = cpx{__uint_as_float(a1[e] << 16), __uint_as_float(b1[e] << 16)};
            d[8 + 2 * e + 1] = cpx{__uint_as_float(a1[e] & 0xFFFF0000u), __uint_as_float(b1[e] & 0xFFFF0000u)};
        }
        if (it < 3) p0 += 512 * 1024;
        if (FTR) touch(((it & 1) ? wi : wr) + (size_t)((rb >> 1) & 31) * 32832 + (it >> 1) * 32 * 32832 + ((rb & 1) * 128 + (tid >> 1)) * 128, spare);
        a0 = *(const u32x4v*)p0; a1 = *(const u32x4v*)(p0 + 8); b0 = *(const u32x4v*)(p0 + 1024); b1 = *(const u32x4v*)(p0 + 1032);
        fft_sync<1024>();
        cpx z[16];
        fft_reg<1024, -1, false, false>(buf[f], tb, t, z);
        __syncthreads();
#pragma unroll
        for (int m = 0; m < 8; ++m) {
            const int idx = tid + 256 * m, k = idx >> 2, ff = idx & 3;
            const cpx zk = buf[ff][pad(k)], zn = buf[ff][pad(k ? 1024 - k : 512)];
            f32x4v o = {0.5f * (zk.x + zn.x), 0.5f * (zk.y - zn.y), 0.5f * (zk.y + zn.y), -0.5f * (zk.x - zn.x)};
            if (m == 0 && k == 0) o = f32x4v{zk.x, zn.x, zk.y, zn.y};
            st_sc1(rsF, f_pos(rb, idx * 16, tw), __builtin_bit_cast(v4u, o));
        }
    }
}
__device__ __forceinline__ void filt_transpose(const u16* __restrict__ wr, const u16* __restrict__ wi, rsrc_t rsW, const unsigned tw, const int tb, char* smem, const int tid) {
    const v4u* s0 = (const v4u*)(wr + (size_t)tb * 32 * 513);
    const v4u* s1 = (const v4u*)(wi + (size_t)tb * 32 * 513);
    v4u r0[5], r1[5];
#pragma unroll
    for (int e = 0; e < 5; ++e) {
        const int idx = tid + 512 * e, ic = idx < 2052 ? idx : 2051;
        r0[e] = s0[ic]; r1[e] = s1[ic];
    }
#pragma unroll
    for (int e = 0; e < 5; ++e) {
        const int idx = tid + 512 * e;
        if (idx < 2052) { *(v4u*)(smem + idx * 16) = r0[e]; *(v4u*)(smem + 32832 + idx * 16) = r1[e]; }
    }
    __syncthreads();
    const u16* a0 = (const u16*)smem;
    const u16* a1 = (const u16*)(smem + 32832);
    for (int idx = tid; idx < 513 * 8; idx += 512) {
        const int k = idx >> 3, n4 = (idx & 7) * 4;
        v4u v;
#pragma unroll
        for (int j = 0; j < 4; ++j) v[j] = (unsigned)a0[(n4 + j) * 513 + k] | ((unsigned)a1[(n4 + j) * 513 + k] << 16);
        st_sc1(rsW, w_pos(k, tb * 32 + n4, tw), v);
    }
}

__device__ __forceinline__ void fft_cols(rsrc_t rsF, rsrc_t rsW, const unsigned tw, int blk, const int n, char* smem, int tid) {
    cpx (*buf)[PB2] = (cpx (*)[PB2])smem;
    int c = tid >> 7, t = tid & 127;
    f32x4v v[8];
    unsigned wv[16], wn[16];
    auto wld = [&](const int k, const int n) { return __builtin_amdgcn_raw_buffer_load_b32(rsW, w_pos(k, n, tw), 0, 0); };
    auto fetch = [&](const int bk) {
        const int k0 = bk * 2, kc = k0 + c;
#pragma unroll
        for (int m = 0; m < 8; ++m) { const int idx = tid + 256 * m; v[m] = __builtin_bit_cast(f32x4v, __builtin_amdgcn_raw_buffer_load_b128(rsF, f_pos(idx >> 3, k0 * 64 + (idx & 7) * 16, tw), 0, 0)); }
#pragma unroll
        for (int m = 0; m < 16; ++m) wn[m] = wld(kc, t + 128 * m);
    };
    fetch(blk);
    const TwBase tb = make_tw<2048>(t);
    for (int it = 0; it < n; ++it, blk += 64) {
        asm volatile("" : "+v"(tid));
        c = tid >> 7; t = tid & 127;
        __syncthreads();
#pragma unroll
        for (int m = 0; m < 8; ++m) {
            const int idx = tid + 256 * m, rb = idx >> 3, pc = idx & 7;
            cpx* d = &buf[pc >> 2][pad(rb * 8 + 2 * (pc & 3))];
            d[0] = cpx{v[m][0], v[m][1]}; d[1] = cpx{v[m][2], v[m][3]};
        }
#pragma unroll
        for (int m = 0; m < 16; ++m) wv[m] = wn[m];
        fetch(it + 1 < n ? blk + 64 : blk);
        __syncthreads();
        cpx z[16];
        fft_reg<2048, -1, false, true>(buf[c], tb, t, z);
        auto wcx = [](const unsigned w) { return cpx{__uint_as_float(w << 16), __uint_as_float(w & 0xFFFF0000u)}; };
        if (blk < 2) {
#pragma unroll
            for (int m = 0; m < 16; ++m) buf[c][pad(t + 128 * m)] = z[m];
            __syncthreads();
            if (blk == 0 && c == 0) {
#pragma unroll
                for (int m = 0; m < 16; ++m) {
                    const int n = t + 128 * m, nn = (2048 - n) & 2047;
                    const cpx zn = buf[0][pad(nn)], a = wcx(wv[m]), b = wcx(wld(0, nn)), e = wcx(wld(512, n)), f = wcx(wld(512, nn));
                    const cpx h0 = cmul(cpx{0.5f * (z[m].x + zn.x), 0.5f * (z[m].y - zn.y)}, cpx{0.5f * (a.x + b.x), 0.5f * (a.y - b.y)});
                    const cpx h5 = cmul(cpx{0.5f * (z[m].y + zn.y), -0.5f * (z[m].x - zn.x)}, cpx{0.5f * (e.x + f.x), 0.5f * (e.y - f.y)});
                    z[m] = cpx{h0.x - h5.y, h0.y + h5.x};
                    wv[m] = 0x3F80u;
                }
            }
            __syncthreads();
        }
#pragma unroll
        for (int m = 0; m < 16; ++m) z[m] = cmul(z[m], wcx(wv[m]));
        fft_reg<2048, 1, true, false>(buf[c], tb, t, z);
        const float sc = 1.0f / 2048.0f;
#pragma unroll
        for (int m = 0; m < 8; ++m) {
            const int idx = tid + 256 * m, rb = idx >> 3, pc = idx & 7;
            const cpx* d = &buf[pc >> 2][pad(rb * 8 + 2 * (pc & 3))];
            st_sc1(rsF, f_pos(rb, blk * 128 + pc * 16, tw), __builtin_bit_cast(v4u, f32x4v{d[0].x * sc, d[0].y * sc, d[1].x * sc, d[1].y * sc}));
        }
    }
}

__device__ __forceinline__ void fft_rows_inv(rsrc_t rsF, const unsigned tw, u16* __restrict__ out, int rb, char* smem, int tid) {
    cpx (*buf)[PB1] = (cpx (*)[PB1])smem;
    int f = tid >> 6, t = tid & 63;
    f32x4v v[8];
    auto fetch = [&](const int r) {
#pragma unroll
        for (int m = 0; m < 8; ++m) v[m] = __builtin_bit_cast(f32x4v, __builtin_amdgcn_raw_buffer_load_b128(rsF, f_pos(r, (tid + 256 * m) * 16, tw), 0, 0));
    };
    fetch(rb);
    const TwBase tb = make_tw<1024>(t);
    for (int it = 0; it < 4; ++it, rb += 64) {
        asm volatile("" : "+v"(tid));
        f = tid >> 6; t = tid & 63;
        __syncthreads();
        cpx* bf = buf[tid & 3];
#pragma unroll
        for (int m = 0; m < 8; ++m) {
            const int k = (tid >> 2) + 64 * m;
            if (m == 0 && k == 0) { bf[0] = cpx{v[0][0], v[0][2]}; bf[pad(512)] = cpx{v[0][1], v[0][3]}; }
            else { bf[pad(k)] = cpx{v[m][0] - v[m][3], v[m][1] + v[m][2]}; bf[pad(1024 - k)] = cpx{v[m][0] + v[m][3], v[m][2] - v[m][1]}; }
        }
        fetch(it < 3 ? rb + 64 : rb);
        __syncthreads();
        cpx z[16];
        fft_reg<1024, 1, false, false>(buf[f], tb, t, z);
        const float sc = 1.0f / 1024.0f;
        const cpx* d = &buf[f][pad(16 * t)];
        u32x4v a[2], b[2];
#pragma unroll
        for (int h = 0; h < 2; ++h)
#pragma unroll
            for (int e = 0; e < 4; ++e) {
                cpx p = d[8 * h + 2 * e], q = d[8 * h + 2 * e + 1];
                a[h][e] = cvt_pk_bf16(p.x * sc, q.x * sc);
                b[h][e] = cvt_pk_bf16(p.y * sc, q.y * sc);
            }
        u16* o0 = out + (size_t)(rb * 8 + 2 * f) * OUTW + 1024 + 16 * t;
        *(u32x4v*)o0 = a[0]; *(u32x4v*)(o0 + 8) = a[1];
        *(u32x4v*)(o0 + OUTW) = b[0]; *(u32x4v*)(o0 + OUTW + 8) = b[1];
    }
}

__device__ __forceinline__ void glds16s(unsigned voff, const void* sbase, unsigned lds_dst) {
    unsigned keep;
    asm volatile("s_mov_b32 %0, m0\n\ts_mov_b32 m0, %3\n\ts_nop 0\n\tglobal_load_lds_dwordx4 %1, %2\n\ts_mov_b32 m0, %0" : "=&s"(keep) : "v"(voff), "s"(sbase), "s"(lds_dst) : "memory");
}
#define K8_BAR do { asm volatile("" ::: "memory"); __builtin_amdgcn_s_barrier(); asm volatile("" ::: "memory"); } while (0)
#define K8_WV(n) asm volatile("s_waitcnt vmcnt(" #n ")" ::: "memory")
#define K8_WL(n) asm volatile("s_waitcnt lgkmcnt(" #n ")" ::: "memory")
#define K8_SCHED __builtin_amdgcn_sched_barrier(0)
template <int BN>
__device__ __forceinline__ int bswz2(int row) {
    if (BN >= 128) return (row & 3) | (((row >> 3) & 1) << 2);
    else return ((row >> 1) & 1) | (((row >> 3) & 1) << 1);
}
template <int BM>
__device__ __forceinline__ void gemm_tile(u16* sm, const char* A, const int s0, const unsigned tau, const u16* B, u16* C) {
    constexpr int K = HD, LDB = DM, LDC = OUTW;
    constexpr int BN = 128, BK = 64, ST = BM == 128 ? 4 : 3, WN = BM == 128 ? 4 : 2;
    constexpr int ATILE = BM * BK, BTILE = BK * BN;
    constexpr int NT = 8 / WN, GA = BM / 64, GB = 2, GS = GA + GB;
    const int tid = opaque_tid(), w = tid >> 6, l = tid & 63, i = l & 15, g = l >> 4;
    const int wr = w / WN, wc = w % WN;
    const unsigned wu = __builtin_amdgcn_readfirstlane(w);
    unsigned vb;
    { const int row = l >> 4, c = l & 15; vb = (unsigned)(row * LDB + 8 * (c ^ ((row | ((wu & 1) << 2)) << 1))) * 2u; }
    const int sg = s0 + (BM == 128 ? wu >> 1 : wu);
    const char* ab = A + (BM == 128 ? (wu & 1) * 4096 : 0);
    const char* bb = (const char*)(B + (size_t)wu * 8 * LDB);
    const unsigned lds0 = (unsigned)(uintptr_t)(__attribute__((address_space(3))) u16*)sm;
    const unsigned va = (unsigned)l * 16u, va2 = va + 2048u, vb2 = vb + 4u * LDB * 2u - 1024u;
    auto req = [&](const int buf, const int kt) {
        const unsigned d = lds0 + buf * (ATILE + BTILE) * 2;
        const int wh = kt >> 1;
        const char* pa = ab + ((blk_off(sg, (wh & 7) * 4 + (wh >> 3), tau) ^ 8192) + (kt & 1) * 2048);
        const char* pb = bb + (size_t)kt * BK * LDB * 2;
        unsigned keep;
        if (BM == 128) asm volatile("s_mov_b32 %0, m0\n\ts_mov_b32 m0, %1\n\ts_nop 0\n\tglobal_load_lds_dwordx4 %3, %5\n\tglobal_load_lds_dwordx4 %3, %5 offset:1024\n\t"
                     "s_mov_b32 m0, %2\n\ts_nop 0\n\tglobal_load_lds_dwordx4 %6, %8\n\tglobal_load_lds_dwordx4 %7, %8 offset:1024\n\ts_mov_b32 m0, %0"
                     : "=&s"(keep) : "s"(d + wu * GA * 1024), "s"(d + ATILE * 2 + wu * GB * 1024), "v"(va), "v"(va2), "s"(pa), "v"(vb), "v"(vb2), "s"(pb) : "memory");
        else asm volatile("s_mov_b32 %0, m0\n\ts_mov_b32 m0, %1\n\ts_nop 0\n\tglobal_load_lds_dwordx4 %3, %5\n\tglobal_load_lds_dwordx4 %3, %5 offset:1024\n\t"
                     "global_load_lds_dwordx4 %4, %5 offset:2048\n\tglobal_load_lds_dwordx4 %4, %5 offset:3072\n\t"
                     "s_mov_b32 m0, %2\n\ts_nop 0\n\tglobal_load_lds_dwordx4 %6, %8\n\tglobal_load_lds_dwordx4 %7, %8 offset:1024\n\ts_mov_b32 m0, %0"
                     : "=&s"(keep) : "s"(d + wu * GA * 1024), "s"(d + ATILE * 2 + wu * GB * 1024), "v"(va), "v"(va2), "s"(pa), "v"(vb), "v"(vb2), "s"(pb) : "memory");
    };
    f32x4 acc[4][NT] = {};
    constexpr int nt = K / BK;
#pragma unroll
    for (int s = 0; s < ST - 1; ++s) req(s, s);
    int boff[NT];
    const int aoff = wr * 4096 + l * 8;
#pragma unroll
    for (int n = 0; n < NT; ++n) { const int row = 8 * g + (i >> 2); boff[n] = row * BN + 16 * ((wc * NT + n) ^ bswz2<BN>(row)) + 4 * (i & 3); }
    asm volatile("s_waitcnt vmcnt(%0)" :: "i"((ST - 2) * GS) : "memory");
    K8_BAR;
    if (wu >= 4) K8_BAR;
    for (int t0 = 0; t0 < nt + ST - 1 - (nt + ST - 1) % ST; t0 += ST) {
#pragma unroll
        for (int rb = 0; rb < ST; ++rb) {
            const int t = t0 + rb;
            if (nt % ST != 0 && t >= nt) break;
            const int kk = min(t + ST - 1, nt - 1);
            const u16* as = sm + rb * (ATILE + BTILE);
            const u16* bs = as + ATILE;
            bf16x8 af[2][4], bf[2][NT];
#pragma unroll
            for (int ks = 0; ks < 2; ++ks) {
#pragma unroll
                for (int n = 0; n < NT; ++n) bf[ks][n] = tr_frag(bs + boff[n] + ks * 32 * BN, bs + boff[n] + ks * 32 * BN + 4 * BN);
#pragma unroll
                for (int m = 0; m < 4; ++m) af[ks][m] = *(const bf16x8*)(as + aoff + m * 1024 + ks * 512);
            }
            K8_SCHED;
            req((rb + ST - 1) % ST, kk);
            K8_WL(0);
            if (wu >= 4) asm volatile("s_waitcnt vmcnt(%0)" :: "i"((ST - 2) * GS) : "memory");
            K8_BAR;
            __builtin_amdgcn_s_setprio(1);
#pragma unroll
            for (int q = 0; q < 8; ++q)
#pragma unroll
                for (int n = 0; n < NT; ++n) acc[q & 3][n] = __builtin_amdgcn_mfma_f32_16x16x32_bf16(bf[q >> 2][n], af[q >> 2][q & 3], acc[q & 3][n], 0, 0, 0);
            __builtin_amdgcn_s_setprio(0);
            K8_SCHED;
            asm volatile("s_waitcnt vmcnt(%0)" :: "i"((ST - 2) * GS) : "memory");
            K8_BAR;
        }
    }
    if (wu < 4) K8_BAR;
    asm volatile("s_waitcnt vmcnt(0)" ::: "memory");
#pragma unroll
    for (int m = 0; m < 4; ++m)
#pragma unroll
        for (int n = 0; n < NT; n += 2) {
            auto r0 = __builtin_amdgcn_permlane16_swap(cvt_pk_bf16(acc[m][n][0], acc[m][n][1]), cvt_pk_bf16(acc[m][n + 1][0], acc[m][n + 1][1]), false, false);
            auto r1 = __builtin_amdgcn_permlane16_swap(cvt_pk_bf16(acc[m][n][2], acc[m][n][3]), cvt_pk_bf16(acc[m][n + 1][2], acc[m][n + 1][3]), false, false);
            *(v4u*)(C + (wr * 64 + m * 16 + i) * LDC + (wc * NT + n + (g & 1)) * 16 + 8 * (g >> 1)) = v4u{r0[0], r1[0], r0[1], r1[1]};
        }
}

__device__ __forceinline__ void xattn_unit(const ChainArgs& a, rsrc_t rsQ, rsrc_t rsK, rsrc_t rsV, rsrc_t rsS, const int s, const int sn, const int h, u16 (*Vs)[VP], unsigned* mbox, const unsigned tau, int& pend, bool& ready) {
    const int l = opaque_tid() & 63, i = l & 15, g = l >> 4;
    const unsigned known = __builtin_amdgcn_readfirstlane(*(volatile unsigned*)mbox);
    const bool early = known > (unsigned)(s + 1);
    const int bq = km_off(s, h, tau), bb = v_off(s, h, tau);
    auto count = [&](const int t) { arrive(a.sync, t < SSEG ? SY_OX + (t >> 2) * 8 : SY_OXS + (t - SSEG) * 8, t < SSEG ? 128u : 32u, 8); };
    const int xk = blk_off(s, h, tau), xq = xk ^ 8192;
    v4u vv[8], vm[2], k0f[4], k1f[4], kmf[4], q0f[4], q1f[4];
#pragma unroll
    for (int e = 0; e < 8; ++e) vv[e] = ld_sc1s(rsV, l * 16 + e * 1024, xk);
#pragma unroll
    for (int ks = 0; ks < 4; ++ks) {
        k0f[ks] = ld_sc1s(rsK, l * 16 + ks * 1024, xk);
        k1f[ks] = ld_sc1s(rsK, l * 16 + ks * 1024, xk + 4096);
        q0f[ks] = ld_sc1s(rsQ, l * 16 + ks * 1024, xq);
        q1f[ks] = ld_sc1s(rsQ, l * 16 + ks * 1024, xq + 4096);
    }
#pragma unroll
    for (int e = 0; e < 2; ++e) vm[e] = ld_sc1s(rsS, l * 16 + e * 1024, early ? bb : 1 << 30);
#pragma unroll
    for (int ks = 0; ks < 4; ++ks) kmf[ks] = ld_sc1s(rsS, ks * 512 + g * 128 + (i & 7) * 16, early ? bq : 1 << 30);
    const unsigned fl = ld_flag(a.sync + (l >= 8 ? SY_PROG * 32 : (((l & 4) ? SY_Q : SY_KV) + (min(sn, NSEG - 1) >> 3) * 8) * 32 + (l & 3)) + (h >> 2) * 32);
    if (pend >= 0) { asm volatile("s_waitcnt vmcnt(31)" ::: "memory"); count(pend); }
#pragma unroll
    for (int e = 0; e < 8; ++e) *(v4u*)&Vs[16 * (e >> 2) + i][32 * (e & 3) + 8 * g] = vv[e];
    const float scale = 0.08838834764831845f;
    const float L2E = 1.4426950408889634f;
    auto round2 = [&](const f32x4 c, float* o) {
#pragma unroll
        for (int p = 0; p < 2; ++p) {
            unsigned u = cvt_pk_bf16(c[2 * p], c[2 * p + 1]);
            float lo = __uint_as_float(u << 16) * scale, hi = __uint_as_float(u & 0xFFFF0000u) * scale;
            unsigned u2 = cvt_pk_bf16(lo, hi);
            o[2 * p] = __uint_as_float(u2 << 16);
            o[2 * p + 1] = __uint_as_float(u2 & 0xFFFF0000u);
        }
    };
    float sv[2][12], mxx[2];
    {
        f32x4 sx[3] = {};
#pragma unroll
        for (int ks = 0; ks < 4; ++ks) {
            sx[0] = __builtin_amdgcn_mfma_f32_16x16x32_bf16(as_frag(k0f[ks]), as_frag(q0f[ks]), sx[0], 0, 0, 0);
            sx[1] = __builtin_amdgcn_mfma_f32_16x16x32_bf16(as_frag(k0f[ks]), as_frag(q1f[ks]), sx[1], 0, 0, 0);
            sx[2] = __builtin_amdgcn_mfma_f32_16x16x32_bf16(as_frag(k1f[ks]), as_frag(q1f[ks]), sx[2], 0, 0, 0);
        }
        round2(sx[0], &sv[0][0]); round2(sx[1], &sv[1][0]); round2(sx[2], &sv[1][4]);
        mxx[0] = mxx[1] = -3.0e38f;
#pragma unroll
        for (int r = 0; r < 4; ++r) {
            if (4 * g + r > i) { sv[0][r] = -3.0e38f; sv[1][4 + r] = -3.0e38f; }
            mxx[0] = fmaxf(mxx[0], sv[0][r]);
            mxx[1] = fmaxf(mxx[1], fmaxf(sv[1][r], sv[1][4 + r]));
        }
    }
    const u16* vb = &Vs[4 * g + (i >> 2)][4 * (i & 3)];
    bf16x8 vfx[8];
#pragma unroll
    for (int t = 0; t < 8; ++t) vfx[t] = tr_frag(vb + 16 * t, vb + 16 * VP + 16 * t);
#pragma unroll
    for (int t = 0; t < 8; ++t) asm volatile("" : "+v"(vfx[t]));
#pragma unroll
    for (int r = 0; r < 4; ++r) asm volatile("" : "+v"(sv[0][r]), "+v"(sv[1][r]), "+v"(sv[1][4 + r]));
    asm volatile("" : "+v"(mxx[0]), "+v"(mxx[1]));
    bool have = false;
    unsigned seen = __builtin_amdgcn_readlane(fl, 8);
    ready = (sn >> 3) == (s >> 3) || (__ballot(fl != 0u) & 255ull) == 255ull;
    if (early) {
        if ((h & 3) == 0 && l == 0) atomicMax(mbox, seen);
        unsigned m = mx4(mx4(0u, vm[0]), vm[1]);
#pragma unroll
        for (int ks = 0; ks < 4; ++ks) m = mx4(m, kmf[ks]);
        have = __all(m != SENT);
    } else if ((h & 3) == 0) {
        const unsigned* pw = a.sync + (SY_PROG + (h >> 2)) * 32;
        unsigned v = seen;
        if (v <= (unsigned)(s + 1)) {
            if (s < NSEG - XA_FAST) { wait_ge<32>(pw, (unsigned)(s + 1), a.tmo, 4000u); __builtin_amdgcn_s_sleep(24); }
            else { wait_ge<6>(pw, (unsigned)(s + 1), a.tmo, 4000u); __builtin_amdgcn_s_sleep(XA_D); }
            v = (unsigned)(s + 1);
        }
        if (l == 0) atomicMax(mbox, v);
    } else {
#pragma nounroll
        for (int spin = 0; spin < 4000000 && *(volatile unsigned*)mbox < (unsigned)(s + 1); ++spin) __builtin_amdgcn_s_sleep(2);
        asm volatile("" ::: "memory");
    }
#pragma nounroll
    for (int spin = 0; !have; ++spin) {
        asm volatile("" ::: "memory");
#pragma unroll
        for (int e = 0; e < 2; ++e) vm[e] = ld_sc1s(rsS, l * 16 + e * 1024, bb);
#pragma unroll
        for (int ks = 0; ks < 4; ++ks) kmf[ks] = ld_sc1s(rsS, ks * 512 + g * 128 + (i & 7) * 16, bq);
        unsigned m = mx4(mx4(0u, vm[0]), vm[1]);
#pragma unroll
        for (int ks = 0; ks < 4; ++ks) m = mx4(m, kmf[ks]);
        if (__all(m != SENT) || spin > 20000) break;
        __builtin_amdgcn_s_sleep(2);
    }
#pragma unroll
    for (int e = 0; e < 8; ++e) *(unsigned*)&Vs[32 + (i >> 2) + 4 * ((i >> 1) & 1)][64 * (e >> 2) + 16 * g + 4 * (e & 3) + 2 * (i & 1)] = vm[e >> 2][e & 3];
    f32x4 sm_[2] = {};
#pragma unroll
    for (int ks = 0; ks < 4; ++ks) {
        sm_[0] = __builtin_amdgcn_mfma_f32_16x16x32_bf16(as_frag(kmf[ks]), as_frag(q0f[ks]), sm_[0], 0, 0, 0);
        sm_[1] = __builtin_amdgcn_mfma_f32_16x16x32_bf16(as_frag(kmf[ks]), as_frag(q1f[ks]), sm_[1], 0, 0, 0);
    }
    v4u pf[2][2];
#pragma unroll
    for (int qt = 0; qt < 2; ++qt) {
        round2(sm_[qt], &sv[qt][8]);
        float mx = fmaxf(fmaxf(mxx[qt], fmaxf(sv[qt][8], sv[qt][9])), fmaxf(sv[qt][10], sv[qt][11]));
        mx = xor16_max(mx);
        mx = xor32_max(mx);
        const float nmx = -mx * L2E;
        float sum = 0.f;
#pragma unroll
        for (int e = 0; e < 12; ++e) {
            if (qt == 0 && e >= 4 && e < 8) { sv[qt][e] = 0.f; continue; }
            float ev = __builtin_amdgcn_exp2f(fmaf(sv[qt][e], L2E, nmx));
            if (e >= 8) ev = g < 2 ? ev : 0.f;
            sv[qt][e] = ev; sum += ev;
        }
        sum = xor16_sum(sum);
        sum = xor32_sum(sum);
        const float inv = 1.0f / sum;
        pf[qt][0][0] = cvt_pk_bf16(sv[qt][0] * inv, sv[qt][1] * inv);
        pf[qt][0][1] = cvt_pk_bf16(sv[qt][2] * inv, sv[qt][3] * inv);
        pf[qt][0][2] = qt ? cvt_pk_bf16(sv[qt][4] * inv, sv[qt][5] * inv) : 0u;
        pf[qt][0][3] = qt ? cvt_pk_bf16(sv[qt][6] * inv, sv[qt][7] * inv) : 0u;
        pf[qt][1][0] = cvt_pk_bf16(sv[qt][8] * inv, sv[qt][9] * inv);
        pf[qt][1][1] = cvt_pk_bf16(sv[qt][10] * inv, sv[qt][11] * inv);
        pf[qt][1][2] = 0u; pf[qt][1][3] = 0u;
    }
    v2u vmf[8];
#pragma unroll
    for (int t = 0; t < 8; ++t) vmf[t] = tr4(&Vs[32 + 4 * (g & 1) + (i >> 2)][16 * t + 4 * (i & 3)]);
#pragma unroll
    for (int t = 0; t < 8; t += 2) {
        unsigned pk[2][2][2];
#pragma unroll
        for (int tt = 0; tt < 2; ++tt) {
            const bf16x8 vf0 = vfx[t + tt];
            const bf16x8 vf1 = as_frag(v4u{vmf[t + tt][0], vmf[t + tt][1], 0u, 0u});
#pragma unroll
            for (int qt = 0; qt < 2; ++qt) {
                f32x4 o = {};
                o = __builtin_amdgcn_mfma_f32_16x16x32_bf16(vf0, as_frag(pf[qt][0]), o, 0, 0, 0);
                o = __builtin_amdgcn_mfma_f32_16x16x32_bf16(vf1, as_frag(pf[qt][1]), o, 0, 0, 0);
                pk[qt][tt][0] = cvt_pk_bf16(o[0], o[1]);
                pk[qt][tt][1] = cvt_pk_bf16(o[2], o[3]);
            }
        }
#pragma unroll
        for (int qt = 0; qt < 2; ++qt) {
            auto r0 = __builtin_amdgcn_permlane16_swap(pk[qt][0][0], pk[qt][1][0], false, false);
            auto r1 = __builtin_amdgcn_permlane16_swap(pk[qt][0][1], pk[qt][1][1], false, false);
            v4u o = {r0[0], r1[0], r0[1], r1[1]};
            st_sc1s(rsV, (t >> 1) * 1024 + ((g & 1) * 2 + (g >> 1)) * 256 + i * 16, xq + qt * 4096, o);
        }
    }
    pend = seen > (unsigned)(sn + 1) && ready ? s : -1;
    if (pend < 0) { asm volatile("s_waitcnt vmcnt(0)" ::: "memory"); count(s); }
}

__device__ __forceinline__ void tile_stream(const ChainArgs& a, const u16* X, unsigned long long todo, const int xc, unsigned& tau, const bool early, const int nw, u16* sm8 WEVP) {
    constexpr int K = DM, LDB = HD;
    const int tid = opaque_tid(), w = tid >> 6, l = tid & 63, i = l & 15, g = l >> 4;
    const int wr = w >> 2, wc = w & 3;
    const unsigned lds0 = (unsigned)(uintptr_t)(__attribute__((address_space(3))) u16*)sm8;
    const unsigned wu = __builtin_amdgcn_readfirstlane(w);
    const unsigned ldsw = lds0 + wu * 2048;
    unsigned voffA, voffB;
    { int row = 16 * w + (l >> 3), c = l & 7; voffA = (unsigned)(row * K + 8 * (c ^ (row & 7))) * 2u; }
    { int row = 8 * w + g, c = i; voffB = (unsigned)(row * LDB + 8 * (c ^ (bswz2<128>(row) << 1))) * 2u; }
    const unsigned voffA2 = voffA + 8u * K * 2u - 1024u, voffB2 = voffB + 4u * LDB * 2u - 1024u;
    auto st2 = [&](const unsigned v0, const unsigned v1, const char* p, const unsigned d) {
        unsigned keep;
        asm volatile("s_mov_b32 %0, m0\n\ts_mov_b32 m0, %1\n\ts_nop 0\n\tglobal_load_lds_dwordx4 %2, %4\n\tglobal_load_lds_dwordx4 %3, %4 offset:1024\n\ts_mov_b32 m0, %0"
                     : "=&s"(keep) : "s"(d), "v"(v0), "v"(v1), "s"(p) : "memory");
    };
    auto stA = [&](int b, int h, const char* p) {
        st2(voffA, voffA2, p + (size_t)(h * 128) * K * 2, ldsw + (2 * b + h) * 16384);
    };
    auto stB = [&](int b, int h, const char* p) {
        st2(voffB, voffB2, p + h * 2048, ldsw + 65536 + (2 * b + h) * 16384);
    };
    constexpr int SA = 128, SB = 64 * LDB * 2;
    auto a_of = [&](const unsigned t) { return (const char*)(X + (size_t)((t >> 4) & 7) * 256 * DM); };
    auto b_of = [&](const unsigned t) { const int kind = t & 7; return (const char*)((kind < 2 ? a.Wk : (kind < 4 ? a.Wv : a.Wq)) + hcol(4 * xc + (kind & 1) * 2)); };
    auto c_of = [&](const unsigned t) { const int kind = t & 7; return ((kind & 6) == 2 ? VX_OFF - KX_OFF : 0) + (int)((t >> 4) & 7) * 4194304 + blk_off(0, 4 * xc + (kind & 1) * 2, kind < 4 ? tau : tau ^ 1u) + (int)((t >> 3) & 1u); };
    auto publish = [&](const unsigned t) {
        const int kind = t & 7, ln = lane_now();
        if (wu == 0 && ln < (kind < 4 ? 1 : 2))
            __hip_atomic_store((gu32*)(a.sync + ((kind < 4 ? SY_KV : SY_Q) + ((t >> 4) & 7) * 8 + xc) * 32 + (kind < 4 ? kind : 2 * kind - 8 + ln)), 16u, __ATOMIC_RELAXED, __HIP_MEMORY_SCOPE_AGENT);
    };
    const char* smc = (const char*)sm8;
    int aoff[2], boff[2];
#pragma unroll
    for (int ks = 0; ks < 2; ++ks) aoff[ks] = (wr * 64 + i) * 128 + (((ks * 4 + g) ^ (i & 7)) << 4);
#pragma unroll
    for (int n = 0; n < 2; ++n) { int row = 8 * g + (i >> 2); boff[n] = 65536 + row * 256 + (((wc * 2 + n) ^ bswz2<128>(row)) << 5) + 8 * (i & 3); }
    f32x4 acc[2][2][4][2] = {};
    bf16x8 At[4][2], B0[2][2], B1[2][2];
#define K8_LDA(b, h) _Pragma("unroll") for (int m = 0; m < 4; ++m) _Pragma("unroll") for (int ks = 0; ks < 2; ++ks) \
        At[m][ks] = *(const bf16x8*)(smc + aoff[ks] + (2 * (b) + (h)) * 16384 + m * 2048)
#define K8_LDB(dst, b, h) _Pragma("unroll") for (int n = 0; n < 2; ++n) _Pragma("unroll") for (int ks = 0; ks < 2; ++ks) { \
        const char* p_ = smc + boff[n] + (2 * (b) + (h)) * 16384 + ks * 8192; \
        v4i16 lo_ = __builtin_amdgcn_ds_read_tr16_b64_v4i16((__attribute__((address_space(3))) v4i16*)p_); \
        v4i16 hi_ = __builtin_amdgcn_ds_read_tr16_b64_v4i16((__attribute__((address_space(3))) v4i16*)(p_ + 1024)); \
        dst[n][ks] = bf16x8{lo_[0], lo_[1], lo_[2], lo_[3], hi_[0], hi_[1], hi_[2], hi_[3]}; }
#define K8_MMA(ai, bj, Bt) do { __builtin_amdgcn_s_setprio(1); \
        _Pragma("unroll") for (int m = 0; m < 4; ++m) _Pragma("unroll") for (int n = 0; n < 2; ++n) _Pragma("unroll") for (int ks = 0; ks < 2; ++ks) \
            acc[ai][bj][m][n] = __builtin_amdgcn_mfma_f32_16x16x32_bf16(Bt[n][ks], At[m][ks], acc[ai][bj][m][n], 0, 0, 0); \
        __builtin_amdgcn_s_setprio(0); } while (0)
    const rsrc_t rsC = __builtin_amdgcn_make_buffer_rsrc((void*)(arena(a.slots) + KX_OFF), 0, FX_OFF - KX_OFF, 0x00020000);
#define K8_EPI(ai, bj, cb) do { const int ln_ = lane_now(); \
        const int vo_ = (int)(wu & 3) * 1024 + (((ln_ >> 4) & 1) * 2 + (ln_ >> 5)) * 256 + (ln_ & 15) * 16; \
        _Pragma("unroll") for (int m = 0; m < 4; ++m) { \
            const f32x4 c0 = acc[ai][bj][m][0], c1 = acc[ai][bj][m][1]; \
            auto r0 = __builtin_amdgcn_permlane16_swap(cvt_pk_bf16(c0[0], c0[1]), cvt_pk_bf16(c1[0], c1[1]), false, false); \
            auto r1 = __builtin_amdgcn_permlane16_swap(cvt_pk_bf16(c0[2], c0[3]), cvt_pk_bf16(c1[2], c1[3]), false, false); \
            const int sg_ = 4 * ai + 2 * (int)(wu >> 2) + (m >> 1); \
            st_sc1s(rsC, vo_, ((ai ? ((cb) & ~1) ^ 8192 : (cb) & 1 ? 1 << 30 : (cb)) ^ ((sg_ & SWZ) << 15)) + sg_ * 524288 + bj * 16384 + (m & 1) * 4096, v4u{r0[0], r1[0], r0[1], r1[1]}); \
            acc[ai][bj][m][0] = ZERO4; acc[ai][bj][m][1] = ZERO4; } } while (0)
    constexpr int nt = K / 64;
    unsigned cur = (unsigned)todo & 255u, prev = 0u;
    const char* pA = a_of(cur); const char* pB = b_of(cur);
    stB(0, 0, pB); stA(0, 0, pA); stB(0, 1, pB); stA(0, 1, pA);
    if (wu >= 4) K8_BAR;
    K8_WV(4); K8_BAR;
    stB(1, 0, pB + SB); stA(1, 0, pA + SA); stB(1, 1, pB + SB);
    K8_WV(6); K8_BAR;
    int cbp = 0;
    for (;;) {
        todo >>= 8;
        const unsigned nxt = todo != 0ull ? (unsigned)todo & 255u : cur;
        int cb = c_of(cur);
#pragma nounroll
        for (int t = 0; t < nt; t += 2) {
            if (!(TC0 & 1) && __builtin_expect(early && prev == 0u && t == TXT, 0))
                touch((const char*)X + 3 * 524288 + (size_t)(nw * 320 + min(TIDX, 319)) * 128, lds0 + 163840 - 256);
            if (__builtin_expect(early && prev == 0u && t == nt - 4, 0)) {
                tau = __builtin_amdgcn_readfirstlane((unsigned)boot_wait(a.boot) & 255u);
                cb = c_of(cur);
            }
            const bool first = t == 0 && prev != 0u, last = t == nt - 2;
            const bool soon = prev < 0x20u;
            if (t == 2 && !soon) { publish(prev); WEV(3) }
            K8_LDB(B0, 0, 0); K8_SCHED; K8_LDA(0, 0); stA(1, 1, pA + SA);
            if (first) K8_EPI(1, 1, cbp);
            pA = last ? a_of(nxt) : pA + 2 * SA; pB = last ? b_of(nxt) : pB + 2 * SB;
            K8_WL(8); K8_BAR; K8_WL(0); K8_MMA(0, 0, B0); K8_BAR; K8_SCHED;
            K8_LDB(B1, 0, 1); stB(0, 0, pB);
            K8_BAR; K8_WL(0); K8_MMA(0, 1, B1); K8_BAR;
            K8_LDA(0, 1); stA(0, 0, pA);
            K8_BAR; K8_WL(0); K8_MMA(1, 0, B0); K8_BAR; K8_SCHED;
            stB(0, 1, pB);
            if (first && !soon) K8_WV(10); else K8_WV(6);
            K8_BAR; K8_MMA(1, 1, B1); K8_BAR;
            if (first && soon) { publish(prev); WEV(3) }
            K8_LDB(B0, 1, 0); K8_SCHED; K8_LDA(1, 0); stA(0, 1, pA);
            K8_WL(8); K8_BAR; K8_WL(0); K8_MMA(0, 0, B0); K8_BAR; K8_SCHED;
            K8_LDB(B1, 1, 1); stB(1, 0, pB + SB);
            if (last) K8_EPI(0, 0, cb);
            K8_BAR; K8_WL(0); K8_MMA(0, 1, B1); K8_BAR;
            K8_LDA(1, 1); stA(1, 0, pA + SA);
            if (last) K8_EPI(0, 1, cb);
            K8_BAR; K8_WL(0); K8_MMA(1, 0, B0); K8_BAR; K8_SCHED;
            stB(1, 1, pB + SB);
            if (last) { K8_EPI(1, 0, cb); K8_WV(18); } else K8_WV(6);
            K8_BAR; K8_MMA(1, 1, B1); K8_BAR;
        }
        WEV(2)
        prev = cur; cbp = cb; cur = nxt;
        if (todo == 0ull) break;
    }
    K8_EPI(1, 1, cbp);
    K8_WV(0);
    if (wu < 4) K8_BAR;
    K8_BAR;
    publish(prev);
    WEV(3)
}

__device__ __forceinline__ void oproj_squad(const ChainArgs& a, u16* out, const int ct, const int rh, const int xc, const unsigned tau, char* smem) {
    const int tid = opaque_tid(), w = tid >> 6, l = tid & 63, i = l & 15, g = l >> 4;
    const int wu = __builtin_amdgcn_readfirstlane(w);
    bf16x8 wf[2][16];
    {
        u16* stg = (u16*)(smem + wu * 10240);
#pragma unroll
        for (int ch = 0; ch < 4; ++ch) {
            const u16* src = a.Wo + (size_t)hcol(4 * wu + ch) * DM + 32 * ct;
            v4u raw[8];
#pragma unroll
            for (int e = 0; e < 8; ++e) { int idx = l + 64 * e, r = idx >> 2, q4 = idx & 3; raw[e] = *(const v4u*)(src + (size_t)r * DM + 8 * q4); }
#pragma unroll
            for (int e = 0; e < 8; ++e) { int idx = l + 64 * e, r = idx >> 2, q4 = idx & 3; *(v4u*)(stg + r * 40 + 8 * q4) = raw[e]; }
            __builtin_amdgcn_s_waitcnt(0xC07F);
#pragma unroll
            for (int jj = 0; jj < 2; ++jj)
#pragma unroll
                for (int ks = 0; ks < 4; ++ks) {
                    const u16* p = stg + (32 * ks + 8 * g + (i >> 2)) * 40 + 16 * jj + 4 * (i & 3);
                    wf[jj][4 * ch + ks] = tr_frag(p, p + 4 * 40);
                }
            __builtin_amdgcn_s_waitcnt(0xC07F);
        }
    }
    const rsrc_t rsO = __builtin_amdgcn_make_buffer_rsrc((void*)(arena(a.slots) + VX_OFF), 0, 32 << 20, 0x00020000);
    float* red = (float*)(smem + 81920);
    const int voff = rh * 4096 + l * 16;
    volatile unsigned* rdy = (volatile unsigned*)(smem + 81920 + 2 * 8 * 16 * 36 * 4);
    bool ready = false;
    for (int s = SSEG; s < NSEG - 1; ++s) {
        if (!ready) {
            if (wu == 0) {
                const unsigned* q = a.sync + (SY_OXS + (s - SSEG) * 8 + xc) * 32;
#pragma nounroll
                for (int spin = 0;; ++spin) {
                    asm volatile("" ::: "memory");
                    if (ld_flag(q) != 0u) break;
                    if (spin > 20000) { __hip_atomic_store((gu32*)a.tmo, 7000u, __ATOMIC_RELAXED, __HIP_MEMORY_SCOPE_AGENT); break; }
                    if (s == SSEG) __builtin_amdgcn_s_sleep(64); else if (s < NSEG - 5) __builtin_amdgcn_s_sleep(16); else __builtin_amdgcn_s_sleep(SQ_SF);
                }
            }
            asm volatile("" ::: "memory");
            __builtin_amdgcn_s_barrier();
            asm volatile("" ::: "memory");
        }
        v4u xf[16];
#pragma unroll
        for (int ks = 0; ks < 16; ++ks) xf[ks] = ld_sc1s(rsO, voff + (ks & 3) * 1024, blk_off(s, 4 * wu + (ks >> 2), tau) ^ 8192);
        unsigned nf = 0;
        if (wu == 0 && s + 1 >= SQ_FAST) nf = ld_flag(a.sync + (SY_OXS + (s + 1 - SSEG) * 8 + xc) * 32);
        f32x4 acc[2] = {};
#pragma unroll
        for (int ks = 0; ks < 16; ++ks)
#pragma unroll
            for (int jj = 0; jj < 2; ++jj) acc[jj] = __builtin_amdgcn_mfma_f32_16x16x32_bf16(wf[jj][ks], as_frag(xf[ks]), acc[jj], 0, 0, 0);
        float* rp = red + (s & 1) * (8 * 16 * 36);
#pragma unroll
        for (int jj = 0; jj < 2; ++jj) *(f32x4*)&rp[(wu * 16 + i) * 36 + 16 * jj + 4 * g] = acc[jj];
        if (wu == 0) rdy[s & 1] = nf;
        __syncthreads();
        ready = __builtin_amdgcn_readfirstlane(rdy[s & 1]) != 0u;
        if (tid >= 64 && tid < 192) {
            const int t = tid - 64, row = t >> 3, q4 = t & 7;
            f32x4 sv_[8];
#pragma unroll
            for (int e = 0; e < 8; ++e) sv_[e] = *(const f32x4*)&rp[(e * 16 + row) * 36 + 4 * q4];
            const f32x4 s0 = ((sv_[0] + sv_[1]) + (sv_[2] + sv_[3])) + ((sv_[4] + sv_[5]) + (sv_[6] + sv_[7]));
            v2u o; o[0] = cvt_pk_bf16(s0[0], s0[1]); o[1] = cvt_pk_bf16(s0[2], s0[3]);
            out8(out + (size_t)(s * SEG + 16 * rh + row) * OUTW + 32 * ct + 4 * q4, o);
        }
    }
}

__device__ __forceinline__ void kv_stream(const ChainArgs& a, const u16* __restrict__ X, const u16* __restrict__ B, char* C, const int head, const int xc, const int wk, unsigned& tau, char* smem) {
    const int tid = opaque_tid(), w = tid >> 6, l = tid & 63, i = l & 15, g = l >> 4;
    const int wu = __builtin_amdgcn_readfirstlane(w), kh = wu >> 2, cq = wu & 3;
    const unsigned lds0 = (unsigned)(uintptr_t)(__attribute__((address_space(3))) char*)smem;
    unsigned voA, voB;
    { const int row = (wu & 1) * 8 + (l >> 3), c = l & 7; voA = (unsigned)(row * DM + 8 * (c ^ (row & 7))) * 2u + (wu >> 1) * 512; }
    { const int row = tid >> 4, c = tid & 15; voB = (unsigned)(row * HD + 8 * (c ^ (bswz2<128>(row) << 1))) * 2u; }
    auto stB = [&](int ch, int slot) {
#pragma unroll
        for (int e = 0; e < 4; ++e) glds16s(voB, (const char*)B + (size_t)(4 * ch + e) * 32 * HD * 2, lds0 + 65536 + slot * 32768 + e * 8192 + wu * 1024);
    };
    const unsigned ldA = lds0 + (wu >> 1) * 8192 + (wu & 1) * 1024;
    auto stP = [&](const char* p, const unsigned d) {
        unsigned keep;
        asm volatile("s_mov_b32 %0, m0\n\ts_mov_b32 m0, %1\n\ts_nop 0\n\tglobal_load_lds_dwordx4 %3, %2 offset:-4096\n\tglobal_load_lds_dwordx4 %4, %2 offset:-2048\n\t"
                     "global_load_lds_dwordx4 %5, %2\n\tglobal_load_lds_dwordx4 %6, %2 offset:2048\n\ts_mov_b32 m0, %0"
                     : "=&s"(keep) : "s"(d), "s"(p), "v"(voA + 8192u), "v"(voA + 6272u), "v"(voA + 4352u), "v"(voA + 2432u) : "memory");
    };
    auto srcA = [&](int h) { return (const char*)X + (size_t)min(h, 2 * KVS - 1) * 32768 - 4096; };
    auto stA = [&](int h) { stP(srcA(h), ldA + (h & 3) * 32768 + 4096); };
    stB(0, 0); stA(0); stA(1); stB(1, 1);
    touch((const char*)X + 65536 + min(wk * 512 + tid, (KVS - 1) * 512 - 1) * 128, lds0 + 131072 + wu * 1024);
    if (KVS > 9) touch((const char*)X + 65536 + min(4096 + wk * 512 + tid, (KVS - 1) * 512 - 1) * 128, lds0 + 131072 + wu * 1024);
    stB(2, 2);
    bf16x8 bf[16][2], af[8];
    f32x4 acc[2][2] = {}, keep[2];
    const int rb = 64 * kh + 8 * g + (i >> 2), sw = bswz2<128>(rb);
    const char* bp = smem + 65536 + rb * 256 + 8 * (i & 3);
    const char* sa0 = smem + kh * 2048 + i * 128 + 16 * (g ^ (i & 7));
    const char* sa1 = smem + kh * 2048 + i * 128 + 16 * ((4 + g) ^ (i & 7));
#define KV_AF(slot, q) *(const bf16x8*)((((q) & 1) ? sa1 : sa0) + (slot) * 32768 + ((q) >> 1) * 4096)
#pragma unroll
    for (int ch = 0; ch < 8; ++ch) {
        if (ch == 0) { if (KVS > 9) K8_WV(10); else K8_WV(9); } else if (ch < 6) K8_WV(8); else if (ch == 6) K8_WV(4); else K8_WV(0);
        K8_BAR;
#pragma unroll
        for (int e = 0; e < 4; ++e) { const char* p_ = bp + (ch % 3) * 32768 + (e >> 1) * 8192 + 32 * ((2 * cq + (e & 1)) ^ sw); bf[2 * ch + (e >> 1)][e & 1] = tr_frag(p_, p_ + 1024); }
#pragma unroll
        for (int e = 0; e < 4; ++e) af[e] = KV_AF(e >> 1, 2 * ch + (e & 1));
        K8_WL(0); K8_SCHED; K8_BAR;
        if (ch < 5) stB(ch + 3, ch % 3);
        if (ch == 7) { stA(2); stA(3); }
#pragma unroll
        for (int e = 0; e < 8; ++e) acc[e >> 2][e & 1] = __builtin_amdgcn_mfma_f32_16x16x32_bf16(bf[2 * ch + ((e >> 1) & 1)][e & 1], af[e >> 1], acc[e >> 2][e & 1], 0, 0, 0);
    }
    float* red = (float*)(smem + 131072) + l * 4;
#pragma unroll
    for (int p = 0; p < 2; ++p) { *(f32x4*)(red + (p * 8 + wu) * 256) = kh ? acc[p][0] : acc[p][1]; keep[p] = kh ? acc[p][1] : acc[p][0]; }
    unsigned long long tag = 0;
    if (wu == 4) { tag = boot_wait(a.boot); *(volatile unsigned*)(smem + 163840 - 512) = (unsigned)tag & 255u; }
    const rsrc_t rc = __builtin_amdgcn_make_buffer_rsrc((void*)C, 0, KVS * 524288, 0x00020000);
    const int vo = cq * 1024 + (2 * kh + (g >> 1)) * 256 + i * 16 + (g & 1) * 8;
    auto put = [&](const f32x4 o, const int so) {
        const v2u pk = {cvt_pk_bf16(o[0], o[1]), cvt_pk_bf16(o[2], o[3])};
        asm volatile("buffer_store_dwordx2 %0, %1, %2, %3 offen sc1" :: "v"(pk), "v"(vo), "s"(rc), "s"(so) : "memory");
    };
    K8_WL(0); K8_WV(0); K8_BAR;
    tau = __builtin_amdgcn_readfirstlane(*(volatile unsigned*)(smem + 163840 - 512));
#pragma unroll
    for (int p = 0; p < 2; ++p) put(keep[p] + *(const f32x4*)(red + (p * 8 + (wu ^ 4)) * 256), blk_off(0, head, tau) + p * 4096);
    stA(4);
    K8_WV(4); K8_BAR;
    if (tid == 256) __hip_atomic_store((gu64*)(a.boot + 256 + 16 * xc + wk), tag, __ATOMIC_RELAXED, __HIP_MEMORY_SCOPE_AGENT);
    auto stages = [&](auto kc) {
        constexpr int KH = decltype(kc)::value;
        if (KH) K8_BAR;
        f32x4 ac[2] = {ZERO4, ZERO4}, kp = ZERO4;
        int h_first = 2, so = OOB;
        asm volatile("" : "+s"(h_first));
        const char* pn = srcA(5);
        unsigned dn = ldA + 32768 + 4096;
        int r0 = (int)(sa0 - smem) + 2 * 32768, r1 = (int)(sa1 - smem) + 2 * 32768;
        for (int h = h_first; h < 2 * KVS + 4; ++h) {
            const f32x4 oth = *(const f32x4*)(red + (wu ^ 4) * 256);
            *(f32x4*)(red + wu * 256) = ac[KH ^ 1];
            bf16x8 ax[16];
#pragma unroll
            for (int q = 0; q < 16; ++q) ax[q] = *(const bf16x8*)(smem + ((q & 1) ? r1 : r0) + (q >> 1) * 4096);
            K8_SCHED;
            stP(pn, dn);
            put((KH ? ac[1] : kp) + oth, so);
            kp = ac[0];
            __builtin_amdgcn_s_waitcnt(0xC07F);
            K8_WV(10);
            if (__builtin_expect((STRICT >> h) & 1u, 0)) K8_WV(0);
            K8_BAR;
            if (KH && ((STRICT | (0xAAA80u & ~(STRICT << 2))) >> h) & 1u && tid == 256)
                __hip_atomic_store((gu32*)(a.sync + (SY_KV0 + (h / 2 - ((STRICT >> h) & 1u ? 1 : 2)) * 8 + xc) * 32 + wk), 16u, __ATOMIC_RELAXED, __HIP_MEMORY_SCOPE_AGENT);
            if (__builtin_expect(h < 2 * KVS, 1)) {
                __builtin_amdgcn_s_setprio(1);
#pragma unroll
                for (int q = 0; q < 16; ++q)
#pragma unroll
                    for (int n = 0; n < 2; ++n) ac[n] = __builtin_amdgcn_mfma_f32_16x16x32_bf16(bf[q][n], ax[q], q ? ac[n] : ZERO4, 0, 0, 0);
                __builtin_amdgcn_s_setprio(0);
            }
            {
                const int hs = h - 1 + KH;
                so = blk_off(hs >> 1, head, tau) + (hs & 1) * 4096;
                asm volatile("" : "+s"(so));
                so = hs > 1 && hs < 2 * KVS ? so : OOB;
                pn = srcA(h + 4); dn = ldA + (h & 3) * 32768 + 4096;
                r0 = (r0 + 32768) & 131071; r1 = (r1 + 32768) & 131071;
                asm volatile("" : "+s"(so), "+s"(pn), "+s"(dn), "+v"(r0), "+v"(r1));
            }
            K8_BAR;
        }
        if (!KH) K8_BAR;
    };
    if (kh) stages(std::integral_constant<int, 1>{}); else stages(std::integral_constant<int, 0>{});
    K8_WV(0);
}

struct MegaArgs {
    ChainArgs ch;
    const u16* x; u16* out;
    const u16* bx; const u16* wr; const u16* wi;
};
constexpr int MEGA_LDS = 163840;
#define WSTAMP(n)
__global__ __launch_bounds__(512, 2) void mega_kernel(MegaArgs m) {
    extern __shared__ __attribute__((aligned(256))) char chsm[];
    const int b = BIDX, xc = (b & 3) + 4 * (b >> 7), sl = (b >> 3) & 15;
    if ((b & 4) == CHS) { chain_body(m.ch, xc, sl, chsm); return; }
    const ChainArgs& a = m.ch;
    const int wk = sl, tid = TIDX;
    char* ar = arena(a.slots);
    unsigned tauw = 0;
    WSTAMP(0)
    if (wk < 8) kv_stream(a, m.x, (wk < 4 ? a.Wk : a.Wv) + hcol(4 * xc + (wk & 3)), ar + (wk < 4 ? KX_OFF : VX_OFF), 4 * xc + (wk & 3), xc, wk, tauw, chsm);
    if (TCH && wk < 8) {
        const int t = opaque_tid(), n = xc * 8 + wk;
        const unsigned sp = (unsigned)(uintptr_t)(__attribute__((address_space(3))) char*)chsm + 163840 - 256;
        touch((const char*)m.x + 3 * 524288 + (n * 320 + min(t, 319)) * 128, sp);
        touch((const char*)m.bx + (n * 512 + t) * 128, sp);
        touch((const char*)(t < 256 ? m.wr : m.wi) + (n * 256 + (t & 255)) * 128, sp);
    }
    WSTAMP(8) WEV(1)
    {
        const int p = wk & 1, q = wk & 3;
        const unsigned long long todo = wk < 2 ? 0x84u + p : wk < 4 ? 0x74544240u + 0x01010101u * p : wk < 8 ? 0ull
                                      : wk < 12 ? (KVS > 8 ? 0x7060503018ull : 0x7060503010ull) + 0x0101010101ull * q : wk < 14 ? 0x341420u + 0x010101u * p : 0x64442422u + 0x01010101u * p;
        __syncthreads();
        if (wk >= 12) {
            const int n = (xc * 4 + q) * 512 + opaque_tid();
            const unsigned sp = (unsigned)(uintptr_t)(__attribute__((address_space(3))) char*)chsm + 163840 - 256;
            if (TC0 & 1) { touch((const char*)m.x + 3 * 524288 + n * 128, sp); touch((const char*)m.x + 3 * 524288 + min(16384 + n, 20479) * 128, sp); }
            if (TC0 & 2) { touch((const char*)m.bx + n * 128, sp); touch((const char*)m.bx + (16384 + n) * 128, sp); }
            if (TC0 & 4) { touch((const char*)m.wr + n * 128, sp); touch((const char*)m.wi + n * 128, sp); }
        }
        if (todo != 0ull) tile_stream(a, m.x, todo, xc, tauw, wk >= 8, xc * 8 + (wk & 7), (u16*)chsm WEVA);
    }
    WSTAMP(2)
    if (wk < 2) {
        unsigned* mbox = (unsigned*)(chsm + 131072);
        if (tid < 5) mbox[tid] = tid ? 2u : 0u;
        __syncthreads();
        const int w = __builtin_amdgcn_readfirstlane(tid >> 6), h = 4 * xc + (w & 3);
        const rsrc_t rsQ = __builtin_amdgcn_make_buffer_rsrc((void*)(ar + KX_OFF), 0, S_LEN * HD * 4, 0x00020000);
        const rsrc_t rsK = __builtin_amdgcn_make_buffer_rsrc((void*)(ar + KX_OFF), 0, S_LEN * HD * 4, 0x00020000);
        const rsrc_t rsV = __builtin_amdgcn_make_buffer_rsrc((void*)(ar + VX_OFF), 0, S_LEN * HD * 4, 0x00020000);
        const rsrc_t rsS = slot_rsrc(a.slots);
        u16 (*Vs)[VP] = (u16 (*)[VP])(chsm + w * (40 * VP * 2));
        const unsigned tau = tauw;
        int pend = -1;
        bool ready = false;
        for (int s = 2 * wk + (w >> 2), sn; s < NSEG - 1; s = sn) {
            const int band = s >> 3;
            unsigned n = 0;
            if (lane_now() == 0) n = atomicAdd(mbox + 1 + (w & 3), 1u);
            n = __builtin_amdgcn_readfirstlane(n);
            sn = 2 * wk + (int)(n & 1u) + 4 * (int)(n >> 1);
            if (sn - s > 4) __builtin_amdgcn_s_setprio(2); else __builtin_amdgcn_s_setprio(0);
            if (s < 4) wait_ge<32>(a.sync + (SY_KV0 + (KVS - 1) * 8 + xc) * 32 + (tid & 7), 16u, a.tmo, 4100u);
            if (!ready) {
                if (band > 0) wait_ge<32>(a.sync + (SY_KV + band * 8 + xc) * 32 + (tid & 3), 1u, a.tmo, 4100u);
                wait_ge<32>(a.sync + (SY_Q + band * 8 + xc) * 32 + (tid & 3), 1u, a.tmo, 4200u);
            }
            xattn_unit(a, rsQ, rsK, rsV, rsS, s, sn, h, Vs, mbox, tau, pend, ready);
        }
        return;
    }
    if (wk >= 4 && wk < 8) {
        const int fw = (wk - 4) * 8 + xc;
        const rsrc_t rsF = __builtin_amdgcn_make_buffer_rsrc((void*)(ar + FX_OFF), 0, 16 << 20, 0x00020000);
        const rsrc_t rsW = __builtin_amdgcn_make_buffer_rsrc((void*)(ar + WT_OFF), 0, 10 << 20, 0x00020000);
        const unsigned tw = tauw ^ FNEAR;
        {
            const int tq = opaque_tid();
            for (int z = 0; z < FSR; ++z) if (TIDX >= 256) __builtin_amdgcn_s_barrier();
            fft_rows_fwd(m.bx, rsF, tw, 2 * fw + (tq >> 8), chsm + (tq >> 8) * 36864, tq & 255, (const char*)m.wr, (const char*)m.wi, (unsigned)(uintptr_t)(__attribute__((address_space(3))) char*)chsm + 163840 - 256);
            for (int z = 0; z < FSR; ++z) if (TIDX < 256) __builtin_amdgcn_s_barrier();
        }
        for (int t = fw; t < 64; t += 32) {
            __syncthreads();
            filt_transpose(m.wr, m.wi, rsW, tw, t, chsm, opaque_tid());
        }
        wg_publish(a.sync, SY_DONE, 32u, 8);
        WSTAMP(3) WEV(4)
        wg_wait_acquire<16>(a.sync + (SY_DONE + xc) * 32, a.tmo, 6000u);
        {
            const int tq = opaque_tid();
            for (int z = 0; z < fw; ++z) __builtin_amdgcn_s_sleep(FST);
            for (int z = 0; z < FSG; ++z) if (TIDX >= 256) __builtin_amdgcn_s_barrier();
            fft_cols(rsF, rsW, tw, 2 * fw + (tq >> 8), 4, chsm + (tq >> 8) * 36864, tq & 255);
            for (int z = 0; z < FSG; ++z) if (TIDX < 256) __builtin_amdgcn_s_barrier();
        }
        wg_publish(a.sync, SY_DONE + 8, 32u, 8);
        WSTAMP(4) WEV(5)
        wg_wait_acquire<16>(a.sync + (SY_DONE + 8 + xc) * 32, a.tmo, 6001u);
        {
            const int tq = opaque_tid();
            for (int z = 0; z < FSR; ++z) if (TIDX >= 256) __builtin_amdgcn_s_barrier();
            fft_rows_inv(rsF, tw, m.out, 2 * fw + (tq >> 8), chsm + (tq >> 8) * 36864, tq & 255);
            for (int z = 0; z < FSR; ++z) if (TIDX < 256) __builtin_amdgcn_s_barrier();
        }
        WSTAMP(5) WEV(6)
    }
    if (wk >= 12) {
        const int r = 2 * (wk - 12);
        wg_wait_acquire<16>(a.sync + (SY_OX + r * 8 + xc) * 32, a.tmo, 5000u);
        wg_wait_acquire<16>(a.sync + (SY_OX + (r + 1) * 8 + xc) * 32, a.tmo, 5000u);
        WEV(7)
        gemm_tile<256>((u16*)chsm, ar + VX_OFF, 4 * r, tauw, a.Wo + xc * 128, m.out + (size_t)r * 128 * OUTW + xc * 128);
        WEV(8)
    }
    if (wk < 4 || (wk >> 1) == 5) {
        const int r = wk < 4 ? wk + 6 : wk;
        wg_wait_acquire<16>(a.sync + (SY_OX + r * 8 + xc) * 32, a.tmo, 5000u);
        WEV(7)
        gemm_tile<128>((u16*)chsm, ar + VX_OFF, 4 * r, tauw, a.Wo + xc * 128, m.out + (size_t)r * 128 * OUTW + xc * 128);
        WEV(8)
        WSTAMP(9)
    }
    if (wk >= 4 && wk < 14 && (wk >> 1) != 5) {
        __syncthreads();
        oproj_squad(a, m.out, (wk < 10 ? wk - 4 : wk - 6) * 4 + (xc & 3), xc >> 2, xc, tauw, chsm);
        WSTAMP(7)
    }
}

#if defined(__HIP_DEVICE_COMPILE__)
#pragma clang attribute pop
#endif
extern "C" void kernel_launch(void* const* d_in, const int* in_sizes, int n_in,
                              void* d_out, int out_size, void* d_ws, size_t ws_size,
                              hipStream_t stream) {
    char* ws = (char*)d_ws;
    size_t off = 0;
    auto alloc = [&](size_t bytes) { void* p = ws + off; off += (bytes + 255) & ~(size_t)255; return p; };
    char* slots = (char*)alloc((size_t)ARENA_BYTES + (2u << 20));
    static_assert((SY_WORDS + 2) * 128 <= 131072, "sync block");
    unsigned* sync = (unsigned*)alloc(131072);
    unsigned long long* boot = (unsigned long long*)alloc(4096);

    MegaArgs m;
    m.ch.mem0 = (const u16*)d_in[1]; m.ch.Wq = (const u16*)d_in[2]; m.ch.Wk = (const u16*)d_in[3]; m.ch.Wv = (const u16*)d_in[4]; m.ch.Wo = (const u16*)d_in[5];
    m.ch.out = (u16*)d_out; m.ch.slots = slots; m.ch.tmo = sync + SY_WORDS * 32;
    m.ch.stamps = (unsigned long long*)(ws + (240ull << 20));
    m.ch.sync = sync; m.ch.boot = boot;
    m.x = (const u16*)d_in[0]; m.out = (u16*)d_out;
    m.bx = (const u16*)d_in[7]; m.wr = (const u16*)d_in[8]; m.wi = (const u16*)d_in[9];
    (void)hipFuncSetAttribute((const void*)mega_kernel, hipFuncAttributeMaxDynamicSharedMemorySize, MEGA_LDS);
    mega_kernel<<<256, 512, MEGA_LDS, stream>>>(m);
}
```

```cpp
#if defined(__HIP_DEVICE_COMPILE__)
#pragma clang attribute push (__attribute__((target("no-packed-fp32-ops"))), apply_to = function)
#endif
#include <hip/hip_runtime.h>
#include <hip/hip_bf16.h>
#include <stdint.h>
#include <type_traits>

typedef unsigned short u16;
#define TIDX ((int)__builtin_amdgcn_workitem_id_x())
#define BIDX ((int)__builtin_amdgcn_workgroup_id_x())
__device__ __forceinline__ int opaque_tid() { int t = TIDX; asm volatile("" : "+v"(t)); return t; }
__device__ __forceinline__ int lane_now() { int t; asm volatile("v_mbcnt_lo_u32_b32 %0, -1, 0\n\tv_mbcnt_hi_u32_b32 %0, -1, %0" : "=v"(t)); return t; }
typedef __attribute__((ext_vector_type(8))) short bf16x8;
typedef __attribute__((ext_vector_type(4))) float f32x4;

constexpr int S_LEN = 2048, DM = 1024, NH = 32, DH = 128, MM = 8, SEG = 32;
constexpr int NSEG = S_LEN / SEG;
constexpr int HD = NH * DH;
constexpr int OUTW = 2048;

typedef unsigned v4u __attribute__((ext_vector_type(4)));
typedef unsigned v2u __attribute__((ext_vector_type(2)));
typedef short v4i16 __attribute__((ext_vector_type(4)));
typedef __amdgpu_buffer_rsrc_t rsrc_t;
#define AUX_SC1 16
__device__ __forceinline__ v4u ld_sc1s(rsrc_t rs, int voff, int soff) { return __builtin_amdgcn_raw_buffer_load_b128(rs, voff, soff, AUX_SC1); }
__device__ __forceinline__ void st_sc1s(rsrc_t rs, int voff, int soff, v4u v) { __builtin_amdgcn_raw_buffer_store_b128(v, rs, voff, soff, AUX_SC1); asm volatile("s_nop 1" :: "v"(v) : "memory"); }
__device__ __forceinline__ void st8_sc1s(rsrc_t rs, int voff, int soff, v2u v) { __builtin_amdgcn_raw_buffer_store_b64(v, rs, voff, soff, AUX_SC1); }
__device__ __forceinline__ void st_sc1(rsrc_t rs, int off, v4u v) { __builtin_amdgcn_raw_buffer_store_b128(v, rs, off, 0, AUX_SC1); }
__device__ __forceinline__ void out8(void* p, v2u v) { asm volatile("global_store_dwordx2 %0, %1, off sc1" :: "v"(p), "v"(v) : "memory"); }
typedef __bf16 bf16x2_t __attribute__((ext_vector_type(2)));
typedef float f32x2_t __attribute__((ext_vector_type(2)));
__device__ __forceinline__ unsigned cvt_pk_bf16(float lo, float hi) {
    f32x2_t f = {lo, hi};
    return __builtin_bit_cast(unsigned, __builtin_convertvector(f, bf16x2_t));
}
#define SENT 0xFFBFFFBF
__device__ __forceinline__ unsigned umax3(unsigned a, unsigned b, unsigned c) { return max(max(a, b), c); }
__device__ __forceinline__ unsigned mx4(unsigned acc, v4u v) { return umax3(umax3(acc, v[0], v[1]), v[2], v[3]); }
__device__ __forceinline__ bf16x8 as_frag(v4u v) { return __builtin_bit_cast(bf16x8, v); }
__device__ __forceinline__ float xor16_sum(float x) {
    auto r = __builtin_amdgcn_permlane16_swap(__float_as_uint(x), __float_as_uint(x), false, false);
    return __uint_as_float(r[0]) + __uint_as_float(r[1]);
}
__device__ __forceinline__ float xor32_sum(float x) {
    auto r = __builtin_amdgcn_permlane32_swap(__float_as_uint(x), __float_as_uint(x), false, false);
    return __uint_as_float(r[0]) + __uint_as_float(r[1]);
}
__device__ __forceinline__ float xor16_max(float x) {
    auto r = __builtin_amdgcn_permlane16_swap(__float_as_uint(x), __float_as_uint(x), false, false);
    return fmaxf(__uint_as_float(r[0]), __uint_as_float(r[1]));
}
__device__ __forceinline__ float xor32_max(float x) {
    auto r = __builtin_amdgcn_permlane32_swap(__float_as_uint(x), __float_as_uint(x), false, false);
    return fmaxf(__uint_as_float(r[0]), __uint_as_float(r[1]));
}
constexpr int SPIN_LIMIT = 400000;
constexpr int OOB = 0x7FFFFF00;
#define PV1 2
#define PV2 11
#define LW 0
#define PVA 8
#define PL 20
#define PF 48
#define CHS 0
#define DLY_U 4
#define SWZ 0
#define FROT 4224
#define WROT 384
#define FNEAR 1u
#define FST 0
#define PG0 2
#define PG1 32
#define PGP 0
#define FTR 1
#define FSG 0
#define FSR 0
#define PB 14
#define TD2 60
#define SL0 44
#define PS 46
#define TE0 10
#define TD 60
#define PV 1
#define PC 31
#define PP 15
#define PK0 34
#define PKP 1
#define PKM 1
#define PKA 28
#define PXD 1
constexpr int NPROBE = 2, DLY_L = 7, DLY_C = 5;
constexpr int M_OFF = 32 * 16384, F_OFF = M_OFF + 2 * 16384, C_OFF = F_OFF + 2 * 16384, REC = C_OFF + 16 * 16384, PERWG = REC / 128, SLOT_BYTES = 34 * 2097152;
static_assert(PERWG == 6656, "arming shares");
__device__ __forceinline__ int rec_base(int s) { return (s >> 1) * 2097152 + (s & 1) * REC; }
__device__ __forceinline__ char* arena(char* slots) { return (char*)(((uintptr_t)slots + 2097151u) & ~(uintptr_t)2097151u); }
constexpr int KX_OFF = SLOT_BYTES, VX_OFF = KX_OFF + (32 << 20), FX_OFF = VX_OFF + (32 << 20), WT_OFF = FX_OFF + (16 << 20), ARENA_BYTES = WT_OFF + (10 << 20);
__device__ __forceinline__ rsrc_t slot_rsrc(char* slots) { return __builtin_amdgcn_make_buffer_rsrc((void*)arena(slots), 0, FX_OFF, 0x00020000); }
__device__ __forceinline__ int blk_off(int seg, int h, unsigned tau) { return seg * 524288 + (((h & 3) >> 1) * 16 + ((h >> 2) ^ (seg & SWZ)) * 2 + (h & 1)) * 16384 + (int)((tau ^ (unsigned)(seg >> 2)) & 1u) * 8192; }
constexpr int TM_OFF = 124 * 16384;
constexpr int CAL_OFF = 32 * 2097152;
__device__ __forceinline__ unsigned half_bits(int s, unsigned tau) { return ((tau ^ (unsigned)(s >> 1)) & 1u) ? 0xFFu : 0u; }
__device__ __forceinline__ int chunk_off(int s, int g, int j, unsigned tau) { return rec_base(s) + (j < 2 ? (g & 3) * 2 + j : 6 + 3 * g + j) * 16384 + (int)(half_bits(s, tau) & 1u) * 8192; }
__device__ __forceinline__ int x_off(int s, int h, unsigned tau) { return chunk_off(s, h >> 2, 2, tau) + (h & 3) * 2048; }
__device__ __forceinline__ int km_off(int s, int h, unsigned tau) { return chunk_off(s, h >> 2, 3, tau) + (h & 3) * 2048; }
__device__ __forceinline__ int m_off(int s, int h, unsigned tau) { return rec_base(s) + M_OFF + (h >> 4) * 16384 + (int)((half_bits(s, tau) >> (h >> 2)) & 1u) * 8192 + (h & 15) * 512; }
__device__ __forceinline__ int v_off(int s, int h, unsigned tau) { return chunk_off(s, h >> 2, 4, tau) + (h & 3) * 2048; }
__device__ __forceinline__ int f_off(int s, int g, unsigned tau) { return rec_base(s) + F_OFF + (g >> 2) * 16384 + (int)((half_bits(s, tau) >> g) & 1u) * 8192 + (g & 3) * 2048; }
__device__ __forceinline__ int hcol(int h) { return ((h & 3) * 8 + (h >> 2)) * 128; }
constexpr int VP = 144;

struct ChainArgs {
    u16* out;
    const u16* mem0; const u16* Wq; const u16* Wk; const u16* Wv; const u16* Wo;
    char* slots;
    unsigned* tmo;
    unsigned long long* stamps;
    unsigned* sync;
    unsigned long long* boot;
};
constexpr int SY_KV = 0;
constexpr int SY_Q = 64;
constexpr int SY_PROG = 128;
constexpr int SY_OX = 136;
constexpr int SY_DONE = 264;
constexpr int SY_OXS = 472;
constexpr int SY_KV0 = 408;
#define TCH 0
#define TXT 10
#define XPR 2
#define F0E 1
#define STRICT 0x20u
#define DLY_T 0
#define TC0 0
#define PLAN 1
#define TD23 0x74704240u
#define TD89 0x60503010ull
#define TM89 0x01010101ull
#define KVS 8
constexpr int SY_FLAGS = 680;
constexpr int SY_WORDS = SY_FLAGS + SY_FLAGS / 8;
#define XA_FAST 6
#define XA_D 12
#define SQ_FAST 49
#define SQ_SF 4
#define SSEGV 40
constexpr int SSEG = SSEGV;
typedef __attribute__((address_space(1))) unsigned gu32;
typedef __attribute__((address_space(1))) unsigned long long gu64;
__device__ __forceinline__ unsigned ld_flag(const unsigned* p) { return __hip_atomic_load((gu32*)p, __ATOMIC_RELAXED, __HIP_MEMORY_SCOPE_AGENT); }
__device__ __forceinline__ unsigned long long boot_tag(const unsigned long long* boot) {
    return __hip_atomic_load((gu64*)(boot + 144), __ATOMIC_RELAXED, __HIP_MEMORY_SCOPE_AGENT) + 1ull;
}
__device__ __forceinline__ unsigned long long boot_wait(const unsigned long long* boot) {
    const int l = TIDX & 63;
    unsigned long long tag = 0, x = 0, y = 0;
#pragma nounroll
    for (int spin = 0; spin < 100000; ++spin) {
        asm volatile("" ::: "memory");
        x = __hip_atomic_load((gu64*)(boot + l), __ATOMIC_RELAXED, __HIP_MEMORY_SCOPE_AGENT);
        y = __hip_atomic_load((gu64*)(boot + 64 + l), __ATOMIC_RELAXED, __HIP_MEMORY_SCOPE_AGENT);
        tag = boot_tag(boot);
        if (__all((x | 3u) == ((tag << 2) | 3u) && (y | 3u) == ((tag << 2) | 3u))) break;
        __builtin_amdgcn_s_sleep(16);
    }
    asm volatile("" ::: "memory");
    const unsigned long long side = __ballot(x & 1u);
    const unsigned sd = (unsigned)(side & 1u) | (unsigned)((side >> 15) & 2u) | (unsigned)((side >> 30) & 4u) | (unsigned)((side >> 45) & 8u);
    const unsigned m = (sd ^ (__popcll(__ballot(x & 2u)) + __popcll(__ballot(y & 2u)) > 64 ? 15u : 0u)) & 15u;
    return (tag << 8) | m | ((~m & 15u) << 4);
}
template <int SL>
__device__ __forceinline__ void wait_ge(const unsigned* p, unsigned need, unsigned* tmo, unsigned code) {
#pragma nounroll
    for (int spin = 0;; ++spin) {
        asm volatile("" ::: "memory");
        if (__all(ld_flag(p) >= need)) break;
        if (spin > 400000 / SL) { __hip_atomic_store((gu32*)tmo, code, __ATOMIC_RELAXED, __HIP_MEMORY_SCOPE_AGENT); break; }
        __builtin_amdgcn_s_sleep(SL);
    }
    asm volatile("" ::: "memory");
}
template <int SL>
__device__ __forceinline__ void wg_wait_acquire(const unsigned* p, unsigned* tmo, unsigned code) {
    if (TIDX < 64) {
        wait_ge<SL>(p, 1u, tmo, code);
        __builtin_amdgcn_fence(__ATOMIC_ACQUIRE, "agent");
        asm volatile("s_waitcnt vmcnt(0)" ::: "memory");
    }
    __syncthreads();
}
__device__ __forceinline__ void arrive(unsigned* sync, const int f, const unsigned need, const int n) {
    const int l = lane_now();
    unsigned old = 0;
    if (l == 0) old = __hip_atomic_fetch_add((gu32*)(sync + (SY_FLAGS + (f >> 3)) * 32), 1u, __ATOMIC_RELAXED, __HIP_MEMORY_SCOPE_AGENT);
    if (__builtin_amdgcn_readfirstlane(old) == need - 1u && l < n) __hip_atomic_store((gu32*)(sync + (f + l) * 32), 16u, __ATOMIC_RELAXED, __HIP_MEMORY_SCOPE_AGENT);
}
__device__ __forceinline__ void wg_publish(unsigned* sync, const int f, const unsigned need, const int n) {
    asm volatile("s_waitcnt vmcnt(0)" ::: "memory");
    __syncthreads();
    if (TIDX < 64) arrive(sync, f, need, n);
}
#define TMO_SET(code) { dead = true; if (l == 0) __hip_atomic_store(a.tmo, (code) + s, __ATOMIC_RELAXED, __HIP_MEMORY_SCOPE_AGENT); }

__device__ __forceinline__ void glds16h(unsigned voff, const void* sbase, unsigned lds_dst) {
    unsigned keep;
    asm volatile("s_mov_b32 %0, m0\n\ts_mov_b32 m0, %3\n\ts_nop 0\n\tglobal_load_lds_dwordx4 %1, %2 sc1\n\ts_mov_b32 m0, %0" : "=&s"(keep) : "v"(voff), "s"(sbase), "s"(lds_dst) : "memory");
}
__device__ __forceinline__ void touch(const void* p, unsigned lds_dst) {
    unsigned keep;
    asm volatile("s_mov_b32 %0, m0\n\ts_mov_b32 m0, %2\n\ts_nop 0\n\tglobal_load_lds_dword %1, off\n\ts_mov_b32 m0, %0" : "=&s"(keep) : "v"(p), "s"(lds_dst) : "memory");
}
__device__ __forceinline__ void touch_sc1(const void* p, unsigned lds_dst) {
    unsigned keep;
    asm volatile("s_mov_b32 %0, m0\n\ts_mov_b32 m0, %2\n\ts_nop 0\n\tglobal_load_lds_dword %1, off sc1\n\ts_mov_b32 m0, %0" : "=&s"(keep) : "v"(p), "s"(lds_dst) : "memory");
}
#define WEV(code)
#define WEVP
#define WEVA
constexpr int C2_K = 32768;
constexpr int C2_V = 65536;
constexpr int C2_R1 = 0, R1S = 3200;
constexpr int C2_R3 = C2_R1 + 4 * R1S;
constexpr int C2_MB = 24576;
#define PST(n)
#define LT(n)
#define LTA(n)
#define LTOUT0
#define LTOUT
#define CN(n)
#define CNS
#define CNOUT
#define CLOB16(a, b, c, d, e, f, g, h, i, j, k, l, m, n, o, p) "v" #a, "v" #b, "v" #c, "v" #d, "v" #e, "v" #f, "v" #g, "v" #h, "v" #i, "v" #j, "v" #k, "v" #l, "v" #m, "v" #n, "v" #o, "v" #p
#define LDS3(p) ((__attribute__((address_space(3))) v4i16*)(p))
__device__ __forceinline__ bf16x8 tr_frag(const void* p0, const void* p1) {
    v4i16 lo = __builtin_amdgcn_ds_read_tr16_b64_v4i16(LDS3(p0));
    v4i16 hi = __builtin_amdgcn_ds_read_tr16_b64_v4i16(LDS3(p1));
    return bf16x8{lo[0], lo[1], lo[2], lo[3], hi[0], hi[1], hi[2], hi[3]};
}
typedef __bf16 bf16x8v __attribute__((ext_vector_type(8)));
typedef __bf16 bf16x16v __attribute__((ext_vector_type(16)));
typedef unsigned v8u __attribute__((ext_vector_type(8)));
typedef unsigned v32u __attribute__((ext_vector_type(32)));
#define PUT8(v, o, x) { const v8u x_ = (x); _Pragma("unroll") for (int e_ = 0; e_ < 8; ++e_) (v)[(o) + e_] = x_[e_]; }
#define SUB4(v, o) v4u{(v)[(o)], (v)[(o) + 1], (v)[(o) + 2], (v)[(o) + 3]}
#define PUT4(v, o, x) { const v4u x_ = (x); (v)[(o)] = x_[0]; (v)[(o) + 1] = x_[1]; (v)[(o) + 2] = x_[2]; (v)[(o) + 3] = x_[3]; }
__device__ __forceinline__ v2u tr4(const u16* p) { return __builtin_bit_cast(v2u, __builtin_amdgcn_ds_read_tr16_b64_v4i16(LDS3(p))); }
__device__ __forceinline__ f32x4 smm(v4u a, v8u b, f32x4 c, int idx) {
    return __builtin_amdgcn_smfmac_f32_16x16x64_bf16(__builtin_bit_cast(bf16x8v, a), __builtin_bit_cast(bf16x16v, b), c, idx, 0, 0);
}
__device__ __forceinline__ v8u tr_frag64(const u16* p, const int pitch) {
    const v2u r0 = tr4(p), r1 = tr4(p + 8 * pitch), r2 = tr4(p + 32 * pitch), r3 = tr4(p + 40 * pitch);
    return v8u{r0[0], r1[0], r0[1], r1[1], r2[0], r3[0], r2[1], r3[1]};
}
#define MM(a_, b_, c_) __builtin_amdgcn_mfma_f32_16x16x32_bf16(as_frag(a_), as_frag(b_), c_, 0, 0, 0)
#define STR2(x) #x
#define STR(x) STR2(x)
#define PAUSE(n) ".rept (" STR(n) ") / 4\n\ts_nop 15\n\t.endr\n\t.rept (" STR(n) ") %% 4\n\ts_nop 3\n\t.endr\n\t"
#define ZERO4 f32x4{0.f, 0.f, 0.f, 0.f}
constexpr int XB_OFF = CAL_OFF + 262144, XP = 68;
__device__ __forceinline__ void last_rows(const char* chsm, rsrc_t rsS, const int wu, const int grp, const int c, const unsigned tau) {
    const int l = lane_now();
    __builtin_amdgcn_s_barrier();
    asm volatile("" ::: "memory");
    const float* rp = (const float*)(chsm + C2_K) + (4 * wu + (l >> 4)) * XP + 4 * (l & 15);
    const f32x4 s0 = (*(const f32x4*)rp + *(const f32x4*)(rp + 32 * XP)) + (*(const f32x4*)(rp + 64 * XP) + *(const f32x4*)(rp + 96 * XP));
    v2u o;
    o[0] = cvt_pk_bf16(s0[0], s0[1]); o[1] = cvt_pk_bf16(s0[2], s0[3]);
    st8_sc1s(rsS, l * 8, XB_OFF + ((c >> 1) * 8 + wu) * 16384 + (int)(tau & 1u) * 8192 + (c & 1) * 4096 + grp * 512, o);
}
__device__ __forceinline__ const unsigned* kv_flag(const unsigned* sync, const int sq, const int grp, const int l) { return sync + (sq < KVS ? (SY_KV0 + sq * 8 + grp) * 32 + (l & 7) : (SY_KV + 8 + grp) * 32 + (l & 3)); }
__device__ __forceinline__ void chain_body(const ChainArgs& a, const int grp, const int c, char* chsm) {
    const int wu = __builtin_amdgcn_readfirstlane(TIDX >> 6);
    const int widx = grp * 16 + c;
    const int hs = 4 * grp + (c >> 2), jq = c & 3;
    const rsrc_t rsS = slot_rsrc(a.slots);
    const unsigned l0 = (unsigned)(uintptr_t)(__attribute__((address_space(3))) char*)chsm;
    bool dead = false;

    const int cal_off = CAL_OFF + grp * 32768 + (c >> 3) * 16384 + (c & 7) * 1024;
        if (wu == 0) {
            const int l = lane_now();
            const v4u ff = {SENT, SENT, SENT, SENT};
#pragma unroll
            for (int e = 0; e < 13; ++e) st_sc1s(rsS, l * 16, widx * 2 * PERWG + min(e * 1024, 2 * PERWG - 1024), ff);
            const rsrc_t rsY = __builtin_amdgcn_make_buffer_rsrc((void*)a.sync, 0, 131072, 0x00020000);
            st_sc1s(rsY, l * 16, widx * 1024, v4u{0u, 0u, 0u, 0u});
            __builtin_amdgcn_raw_buffer_store_b32(0u, rsS, (l & 3) * 256 + ((l >> 2) & 1) * 8192, cal_off, AUX_SC1);
            if (c < 2) __builtin_amdgcn_raw_buffer_store_b32(0u, rsS, (l >> 5) * 8192 + (l & 31) * 4, TM_OFF + c * 16384 + grp * 256, AUX_SC1);
        }
    asm volatile("" ::: "memory");
    if (wu >= 4) {
        __builtin_amdgcn_s_sleep(DLY_U);
        const int l = opaque_tid() & 63, i = l & 15, g = l >> 4;
        const int sidx = (i & 1) ? 0xEEEE : 0x4444;
        const int u = wu - 4;
        const int k0 = 256 * u;
        u16* stg = (u16*)(chsm + 40960 + u * 20480);
        v4u af[4];
        v32u wS[6];
        {
            v4u raw[3][16];
            {
                const size_t wo = (size_t)k0 * HD + hcol(hs) + 32 * jq;
                const rsrc_t rq = __builtin_amdgcn_make_buffer_rsrc((void*)(a.Wq + wo), 0, 256 * HD * 2, 0x00020000);
                const rsrc_t rk = __builtin_amdgcn_make_buffer_rsrc((void*)(a.Wk + wo), 0, 256 * HD * 2, 0x00020000);
                const rsrc_t rv = __builtin_amdgcn_make_buffer_rsrc((void*)(a.Wv + wo), 0, 256 * HD * 2, 0x00020000);
                const int vw = (l >> 2) * HD * 2 + (l & 3) * 16;
#pragma unroll
                for (int p = 0; p < 4; ++p) af[p] = *(const v4u*)(a.mem0 + (size_t)(i >> 1) * DM + k0 + 64 * p + 16 * g + 8 * (i & 1));
#pragma unroll
                for (int e = 0; e < 16; ++e) raw[0][e] = __builtin_amdgcn_raw_buffer_load_b128(rq, vw, e * 16 * HD * 2, 0);
#pragma unroll
                for (int e = 0; e < 16; ++e) raw[1][e] = __builtin_amdgcn_raw_buffer_load_b128(rk, vw, e * 16 * HD * 2, 0);
#pragma unroll
                for (int e = 0; e < 12; ++e) raw[2][e] = __builtin_amdgcn_raw_buffer_load_b128(rv, vw, e * 16 * HD * 2, 0);
                __builtin_amdgcn_sched_barrier(0);
#pragma unroll
                for (int e = 0; e < 16; ++e) *(v4u*)(stg + (16 * e + (l >> 2)) * 40 + 8 * (l & 3)) = raw[0][e];
                __builtin_amdgcn_sched_barrier(0);
#pragma unroll
                for (int e = 12; e < 16; ++e) raw[2][e] = __builtin_amdgcn_raw_buffer_load_b128(rv, vw, e * 16 * HD * 2, 0);
            }
            const int fr = 16 * (g >> 1) + 4 * (g & 1) + (i >> 2);
            __builtin_amdgcn_sched_barrier(0);
#pragma unroll
            for (int hx = 0; hx < 3; ++hx) {
                if (hx) {
#pragma unroll
                    for (int e = 0; e < 16; ++e) *(v4u*)(stg + (16 * e + (l >> 2)) * 40 + 8 * (l & 3)) = raw[hx][e];
                }
                __builtin_amdgcn_s_waitcnt(0xC07F);
#pragma unroll
                for (int jj = 0; jj < 2; ++jj)
#pragma unroll
                    for (int p = 0; p < 4; ++p) PUT8(wS[2 * hx + jj], 8 * p, tr_frag64(stg + (64 * p + fr) * 40 + 16 * jj + 4 * (i & 3), 40))
                __builtin_amdgcn_s_waitcnt(0xC07F);
                __builtin_amdgcn_sched_barrier(0);
            }
        }
        PST(u)
        if (u == 0) { PST(4) }
        const unsigned r1w = l0 + C2_R1 + u * R1S + (2 * g * 16 + i) * 4, r1v = l0 + C2_R1 + u * R1S + 2176 + (2 * g * 16 + ((i & 9) | ((i & 2) << 1) | ((i & 4) >> 1))) * 4;
#define LD4(d, va, so, o) "buffer_load_dwordx4 v[" d "], " va ", %[rs], " so " offen offset:" o " sc1\n\t"
#define CHK(a, b, c, d) "v_max3_u32 v244, v244, v" a ", v" b "\n\tv_max3_u32 v244, v244, v" c ", v" d "\n\t"
#define CHK0(a, b, c, d) "v_max_u32 v244, v" a ", v" b "\n\tv_max3_u32 v244, v244, v" c ", v" d "\n\t"
#define SMF(d, a, w) "v_smfmac_f32_16x16x64_bf16 v[" d "], v[" a "], v[" w "], %[sx]\n\t"
#define ADD(d, a, b) "v_add_f32 v" d ", v" a ", v" b "\n\t"
#define Z2(r) "v_mov_b64 v[" r "], 0\n\t"
#define DW2(ad, x, y, o0, o1) "ds_write2_b32 " ad ", v" x ", v" y " offset0:" o0 " offset1:" o1 "\n\t"
#define A0 "224:227"
#define A1 "228:231"
#define A2 "232:235"
#define A3 "236:239"
#define Q0 "200:203"
#define Q1 "204:207"
#define Q2 "208:211"
#define Q3 "212:215"
#define V0 "216:219"
#define V1 "220:223"
#define S1_HEAD \
            Z2("216:217") Z2("218:219") Z2("220:221") Z2("222:223") \
            "s_waitcnt vmcnt(4)\n\t" ZQ \
            "s_waitcnt vmcnt(3)\n\t" SMF(V0, A0, "136:143") CHK0("224", "225", "226", "227") SMF(V1, A0, "168:175") \
            "s_waitcnt vmcnt(2)\n\t" SMF(V0, A1, "144:151") CHK("228", "229", "230", "231") SMF(V1, A1, "176:183") \
            "s_waitcnt vmcnt(1)\n\t" SMF(V0, A2, "152:159") CHK("232", "233", "234", "235") SMF(V1, A2, "184:191") \
            "s_waitcnt vmcnt(0)\n\t" SMF(V0, A3, "160:167") CHK("236", "237", "238", "239") SMF(V1, A3, "192:199") \
            "v_cmp_eq_u32 vcc, " STR(SENT) ", v244\n\t"
#define S1_TAIL(B1, B2) \
            SMF(Q0, A0, "8:15") SMF(Q0, A1, "16:23") SMF(Q0, A2, "24:31") \
            ADD("240", "216", "217") ADD("241", "218", "219") SMF(Q0, A3, "32:39") DW2("%[rv]", "240", "241", "0", "16") \
            ADD("242", "220", "221") ADD("243", "222", "223") SMF(Q1, A0, "40:47") DW2("%[rv]", "242", "243", "128", "144") \
            SMF(Q1, A1, "48:55") SMF(Q1, A2, "56:63") SMF(Q1, A3, "64:71") \
            ADD("240", "200", "201") ADD("241", "202", "203") SMF(Q2, A0, "72:79") DW2("%[rw]", "240", "241", "0", "16") \
            SMF(Q2, A1, "80:87") SMF(Q2, A2, "88:95") "s_nop 1\n\t" \
            ADD("242", "204", "205") ADD("243", "206", "207") SMF(Q2, A3, "96:103") DW2("%[rw]", "242", "243", "136", "152") \
            SMF(Q3, A0, "104:111") SMF(Q3, A1, "112:119") \
            B1        \
            LTA(0) \
            SMF(Q3, A2, "120:127") SMF(Q3, A3, "128:135") "s_nop 0\n\t" \
            ADD("240", "208", "209") ADD("241", "210", "211") DW2("v246", "240", "241", "0", "16") \
            "s_nop 3\n\t" \
            ADD("242", "212", "213") ADD("243", "214", "215") DW2("v246", "242", "243", "136", "152") \
            "s_waitcnt lgkmcnt(0)\n\t" B2 "s_barrier\n\t"
#define ZQ Z2("200:201") Z2("202:203") Z2("204:205") Z2("206:207") Z2("208:209") Z2("210:211") Z2("212:213") Z2("214:215")
#define U_W "{v[8:39]}"(wS[0]), "{v[40:71]}"(wS[1]), "{v[72:103]}"(wS[2]), "{v[104:135]}"(wS[3]), "{v[136:167]}"(wS[4]), "{v[168:199]}"(wS[5])
#define U_CLOB CLOB16(200, 201, 202, 203, 204, 205, 206, 207, 208, 209, 210, 211, 212, 213, 214, 215), CLOB16(216, 217, 218, 219, 220, 221, 222, 223, 224, 225, 226, 227, 228, 229, 230, 231), \
            CLOB16(232, 233, 234, 235, 236, 237, 238, 239, 240, 241, 242, 243, 244, 245, 246, 247)
        __builtin_amdgcn_s_setprio(2);
        {
            asm volatile(
                "v_add_u32 v246, 1088, %[rw]\n\t"
                S1_HEAD "s_nop 0\n\t" S1_TAIL("s_nop 1\n\t", "s_barrier\n\t")
                : LTOUT0
                : U_W, "{v[224:227]}"(af[0]), "{v[228:231]}"(af[1]), "{v[232:235]}"(af[2]), "{v[236:239]}"(af[3]), [sx] "v"(sidx), [rv] "v"(r1v), [rw] "v"(r1w)
                : "memory", "vcc", CLOB16(200, 201, 202, 203, 204, 205, 206, 207, 208, 209, 210, 211, 212, 213, 214, 215), CLOB16(216, 217, 218, 219, 220, 221, 222, 223, 240, 241, 242, 243, 244, 245, 246, 247));
        }
        const unsigned tau = __builtin_amdgcn_readfirstlane(*(volatile unsigned*)(chsm + C2_MB + 8));
        const unsigned long long wmask = u == 3 ? (c == 0 ? ~0ull : 0ull) : u == 1 ? 0x0020202020202000ull : u == 2 ? 1ull << (NSEG - 3) : 0ull;
        const int pvv = u == 3 ? 2 : -1, pvm = u == 3 ? -1 : 0;
        const unsigned* pfl = a.sync + (u == 3 ? SY_PROG + grp : u == 1 ? SY_KV + 16 + grp : SY_Q + 7 * 8 + grp) * 32;
        int s_first = 0;
        asm volatile("s_branch 1f\n\t.p2align 8\n1:" : "+s"(s_first));
        for (int s = s_first; s < NSEG - 1; ++s) {
            __builtin_amdgcn_s_setprio(0);
            LT(1)
            const int pv = (wmask >> s) & 1ull ? pvv + (s & pvm) : 0;
            const unsigned* pa = pfl;
            if (pv < 0) pfl += 256;
            const int cb = grp & 3;
            int soffc = rec_base(s) + C_OFF + c * 16384 + (int)((half_bits(s, tau) >> grp) & 1u) * 8192 + cb * 2048;
            int o_as = rec_base(s + 1) + ((c >> 3) + (grp >> 2) * 4) * 16384 + (int)((half_bits(s + 1, tau) >> grp) & 1u) * 8192 + ((4 * c + cb) & 31) * 256;
            int soff = chunk_off(s + 1, grp, u >> 1, tau) + (u & 1) * 4096;
            const int o_tm = (s >> 1) * 2097152 + TM_OFF + (s & 1) * 16384;
            int lim = dead ? 0 : SPIN_LIMIT, bad = 0;
            const bool rcd = u > 0 || widx < 32;
            const int pc = u > 0 ? 3 * widx + u - 1 : rcd ? 384 + widx : s * 96 + widx - 32;
            const int o_arm = (rcd ? s + 2 < NSEG : pc < 512) ? (rcd ? rec_base(s + 2) : XB_OFF) + (pc >> 3) * 16384 + (int)(half_bits(rcd ? s + 2 : 0, tau) & 1u) * 8192 + (pc & 7) * 1024 : OOB;
            bool there = true;
            if (__builtin_expect(s <= KVS, 0)) {
                there = __all(ld_flag(kv_flag(a.sync, s + 2, grp, l)) != 0u);
                *(volatile unsigned*)(chsm + C2_MB + 32 + (s & 1) * 16 + u * 4) = there ? 1u : 0u;
                __builtin_amdgcn_s_waitcnt(0xC07F);
            }
            const char* vb = !there ? nullptr : arena(a.slots) + (s + 2 >= NSEG ? KX_OFF : VX_OFF) + (blk_off(min(s + 2, NSEG - 1), 4 * grp + 2 * u - 2, tau) ^ (s + 2 >= NSEG ? 8192 : 0));
            asm volatile("" : "+s"(soffc), "+s"(o_as), "+s"(soff), "+s"(lim), "+s"(vb));
#define GL(o) "global_load_lds_dwordx4 %[l16], s[84:85] offset:" o " sc1\n\t" PAUSE(PGP)
#define GL4 GL("0") GL("1024") GL("2048") GL("3072")
#define GLN(d) "s_add_u32 m0, m0, 4096\n\ts_add_u32 s84, s84, " d "\n\ts_addc_u32 s85, s85, 0\n\t"
#define PKE PAUSE(PKA)
#define C_TRY(x, x0, x1, x2, x3, y, y0, y1, y2, y3) \
                "s_waitcnt vmcnt(3)\n\t" \
                "v_mfma_f32_16x16x32_bf16 v[240:243], v[216:219], v[" x "], 0\n\t" CHK0(x0, x1, x2, x3) \
                "s_waitcnt vmcnt(2)\n\t" \
                "v_mfma_f32_16x16x32_bf16 v[240:243], v[216:219], v[" y "], v[240:243]\n\t" CHK(y0, y1, y2, y3) \
                "v_cmp_eq_u32 vcc, " STR(SENT) ", v244\n\t" \
                "s_cbranch_vccz 3b\n\t" CN(1) \
                LD4(x, "%[l16]", "%[sc]", "0") LD4(y, "%[l16]", "%[sc]", "1024")
            asm volatile(
                "v_and_b32 v221, 16, %[l16]\n\t"
                "v_mov_b32 v222, 0x3f80\n\t"
                "v_bfe_u32 v223, %[l16], 5, 2\n\t"
                "v_lshlrev_b32 v221, v221, v222\n\t"
                "v_cmp_eq_u32 vcc, 0, v223\n\tv_and_b32 v220, 0xf0, %[l16]\n\tv_bfe_u32 v222, %[l16], 8, 1\n\tv_cndmask_b32 v216, 0, v221, vcc\n\t"
                "v_cmp_eq_u32 vcc, 1, v223\n\tv_lshl_add_u32 v220, v222, 3, v220\n\tv_lshrrev_b32 v222, 9, %[l16]\n\tv_cndmask_b32 v217, 0, v221, vcc\n\t"
                "v_cmp_eq_u32 vcc, 2, v223\n\tv_lshl_add_u32 v220, v222, 30, v220\n\ts_nop 0\n\tv_cndmask_b32 v218, 0, v221, vcc\n\t"
                "v_cmp_eq_u32 vcc, 3, v223\n\ts_nop 1\n\tv_cndmask_b32 v219, 0, v221, vcc\n\t"
                "v_mul_u32_u24 v222, 0x8000, v222\n\t"
                "v_and_b32 v221, 0xff, v220\n\t"
                "v_add_u32 v221, v221, v222\n\t"
                "v_lshrrev_b32 v247, 2, %[l16]\n\t"
                PKE
                "v_mov_b32 v240, " STR(SENT) "\n\tv_mov_b32 v241, v240\n\tv_mov_b32 v242, v240\n\tv_mov_b32 v243, v240\n\t"
                "buffer_store_dwordx4 v[240:243], %[l16], %[rs], %[arm] offen sc1\n\t"
                LTA(2)
                "s_setprio 2\n\t"
                "s_barrier\n\t"
                LTA(3)
                "v_add_u32 v246, 1088, %[rw]\n\t"
                "s_cmp_lt_u32 %[uu], 1\n\t"
                "s_cbranch_scc1 7f\n\t"
                "s_cmp_eq_u32 %[uu], 3\n\t"
                "s_cbranch_scc1 20f\n\t"
                "s_memrealtime s[84:85]\n\t"
                "v_lshrrev_b32 v241, 9, %[l16]\n\t"
                "v_and_b32 v242, 0x7c, v247\n\t"
                "v_lshl_add_u32 v241, v241, 13, v242\n\t"
                "s_waitcnt lgkmcnt(0)\n\t"
                "v_mov_b32 v240, s84\n\t"
                "buffer_store_dword v240, v241, %[rs], %[ts] offen sc1\n\t"
                "s_cmp_eq_u64 %[vb], 0\n\t"
                "s_cbranch_scc1 11f\n\t"
                "s_mov_b32 s86, m0\n\t"
                "s_mov_b64 s[84:85], %[vb]\n\t"
                "s_mov_b32 m0, %[lv]\n\t"
                PAUSE(PG0)
                "s_cmp_eq_u32 %[uu], 1\n\t"
                "s_cbranch_scc1 12f\n\t"
                PAUSE(PG1)
                "12:\n\t"
                GL4 GLN("4096") GL4 GLN("12288") GL4 GLN("4096") GL4
                "s_mov_b32 m0, s86\n\t"
                "11:\n\t"
                "s_cmp_lt_i32 %[pv], 0\n\t"
                "s_cbranch_scc0 8f\n\t"
                "v_and_b32 v241, 12, v247\n\t"
                "10:\n\t"
                "global_load_dword v240, v241, %[pa] sc1\n\t"
                "s_waitcnt vmcnt(0)\n\t"
                "v_cmp_gt_u32 vcc, 16, v240\n\t"
                "s_cbranch_vccz 8f\n\t"
                "s_sleep 6\n\t"
                "s_sub_u32 %[lim], %[lim], 1\n\t"
                "s_cbranch_scc0 10b\n\t"
                "s_mov_b32 %[bad], 3000\n\t"
                "s_mov_b32 %[lim], 0\n\t"
                "s_branch 8f\n\t"
                "20:\n\t"
                PAUSE(PS)
                "s_memrealtime s[84:85]\n\t"
                "s_load_dword s86, %[tb], 0\n\ts_load_dword s87, %[tb], 0x100\n\ts_load_dword s88, %[tb], 0x200\n\ts_load_dword s89, %[tb], 0x300\n\t"
                "s_load_dword s90, %[tb], 0x400\n\ts_load_dword s91, %[tb], 0x500\n\ts_load_dword s92, %[tb], 0x600\n\ts_load_dword s93, %[tb], 0x700\n\t"
                "s_waitcnt lgkmcnt(0)\n\t"
                "s_sub_u32 s86, s84, s86\n\ts_sub_u32 s87, s84, s87\n\ts_sub_u32 s88, s84, s88\n\ts_sub_u32 s89, s84, s89\n\t"
                "s_sub_u32 s90, s84, s90\n\ts_sub_u32 s91, s84, s91\n\ts_sub_u32 s92, s84, s92\n\ts_sub_u32 s93, s84, s93\n\t"
                "s_or_b32 s85, s86, s87\n\ts_min_u32 s86, s86, s87\n\ts_or_b32 s85, s85, s88\n\ts_min_u32 s86, s86, s88\n\ts_or_b32 s85, s85, s89\n\ts_min_u32 s86, s86, s89\n\t"
                "s_memrealtime s[88:89]\n\t"
                "s_or_b32 s85, s85, s90\n\ts_min_u32 s86, s86, s90\n\ts_or_b32 s85, s85, s91\n\ts_min_u32 s86, s86, s91\n\ts_or_b32 s85, s85, s92\n\ts_min_u32 s86, s86, s92\n\t"
                "s_or_b32 s85, s85, s93\n\ts_min_u32 s86, s86, s93\n\t"
                "s_cmp_gt_u32 s85, 127\n\t"
                CNS
                "s_cselect_b32 s85, " STR(TE0) ", 127\n\t"
                "s_min_u32 s86, s86, s85\n\t"
                "s_sub_u32 s84, s84, s86\n\t"
                "s_add_u32 s84, s84, %[td]\n\t"
                "s_cmp_lt_i32 %[pv], 1\n\t"
                "s_cbranch_scc1 4f\n\t"
                "v_mov_b32 v247, %[pv]\n\t"
                "s_mov_b64 exec, 1\n\t"
                "global_store_dword %[l16], v247, %[pa] sc1\n\t"
                "s_mov_b64 exec, -1\n\t"
                "4:\n\t"
                "s_waitcnt lgkmcnt(0)\n\t"
                "s_sub_i32 s84, s84, s88\n\t"
                "s_max_i32 s84, s84, 0\n\t"
                "s_min_i32 s84, s84, 40\n\t"
                "21:\n\t"
                "s_sub_u32 s84, s84, 1\n\t"
                "s_cbranch_scc0 21b\n\t"
                "s_branch 8f\n\t"
                "7:\n\t"
                PAUSE(PC)
                LD4(A0, "%[l16]", "%[sc]", "0") LD4(A1, "%[l16]", "%[sc]", "1024")
                PAUSE(PP)
                LD4(A2, "%[l16]", "%[sc]", "0") LD4(A3, "%[l16]", "%[sc]", "1024")
                "s_waitcnt vmcnt(3)\n\t"
                "v_mfma_f32_16x16x32_bf16 v[240:243], v[216:219], v[224:227], 0\n\t" CHK0("224", "225", "226", "227")
                "s_waitcnt vmcnt(2)\n\t"
                "v_mfma_f32_16x16x32_bf16 v[240:243], v[216:219], v[228:231], v[240:243]\n\t" CHK("228", "229", "230", "231")
                "v_cmp_eq_u32 vcc, " STR(SENT) ", v244\n\t"
                "s_cbranch_vccnz 30f\n\t"
                "3:\n\t"
                "s_nop 3\n\t"
                "v_cvt_pk_bf16_f32 v244, v240, v241\n\t"
                "v_cvt_pk_bf16_f32 v245, v242, v243\n\t"
                "buffer_store_dwordx2 v[244:245], v221, %[rs], %[oas] offen sc1\n\t"
                PAUSE(PB)
                "8:\n\t"
                LTA(4)
                "s_barrier\n\t"
                PAUSE(1)
                "2:\n\t"
                LD4(A0, "%[l16]", "%[sa]", "0") LD4(A1, "%[l16]", "%[sa]", "1024") LD4(A2, "%[l16]", "%[sa]", "2048") LD4(A3, "%[l16]", "%[sa]", "3072")
                S1_HEAD
                "s_cbranch_vccnz 6f\n\t"
                "5:\n\t"
                LTA(5)
                S1_TAIL("s_waitcnt lgkmcnt(0)\n\ts_barrier\n\t", "")
                "s_setprio 0\n\t"
                "s_branch 9f\n\t"
                "30:\n\t" CN(1)
                LD4(A0, "%[l16]", "%[sc]", "0") LD4(A1, "%[l16]", "%[sc]", "1024")
                "1:\n\t"
                C_TRY(A2, "232", "233", "234", "235", A3, "236", "237", "238", "239")
                C_TRY(A0, "224", "225", "226", "227", A1, "228", "229", "230", "231")
                "s_sub_u32 %[lim], %[lim], 1\n\t"
                "s_cbranch_scc0 1b\n\t"
                "s_mov_b32 %[bad], 2000\n\t"
                "s_mov_b32 %[lim], 0\n\t"
                "s_branch 3b\n\t"
                "6:\n\t" CN(65536)
                "v_mov_b32 v240, %[tg]\n\t"
                "v_mov_b32 v241, %[lf]\n\t"
                "ds_write_b32 v241, v240\n\t"
                "s_nop 0\n\t"
                "s_sub_u32 %[lim], %[lim], 1\n\t"
                "s_cbranch_scc0 2b\n\t"
                "s_mov_b32 %[bad], 1\n\t"
                "s_branch 5b\n\t"
                "9:\n\t"
                : [lim] "+s"(lim), [bad] "+s"(bad), [oas] "+s"(o_as) LTOUT CNOUT
                : U_W, [l16] "v"(l * 16), [rs] "s"(rsS), [sx] "v"(sidx), [rv] "v"(r1v), [rw] "v"(r1w),
                  [sc] "s"(soffc), [sa] "s"(soff), [uu] "s"(u), [pv] "s"(pv), [pa] "s"(pa), [vb] "s"(vb), [lv] "s"(l0 + (s + 2 >= NSEG ? C2_V + 32768 : s & 1 ? C2_K : C2_V) + (u - 1) * 16384), [td] "s"(s >= SL0 ? TD2 : TD), [ts] "s"(c == 0 && (u == 2 || s < NSEG - 3) ? o_tm + grp * 256 + (2 - u) * 2097152 : OOB), [tb] "s"(arena(a.slots) + o_tm + ((half_bits(s, tau) >> grp) & 1u) * 8192),

                  [arm] "s"(o_arm)
,
                  [tg] "s"(s + 1), [lf] "s"(l0 + C2_MB + 16)
                : "memory", "vcc", "scc", "s84", "s85", "s86", "s87", "s88", "s89", "s90", "s91", "s92", "s93", U_CLOB);
            if (__builtin_expect(bad != 0, 0)) TMO_SET((unsigned)bad)
        }
        last_rows(chsm, rsS, wu, grp, c, tau);
        if (u == 3 && widx == 0 && l == 0) __hip_atomic_store((gu64*)(a.boot + 144), *(volatile unsigned long long*)(chsm + C2_MB), __ATOMIC_RELAXED, __HIP_MEMORY_SCOPE_AGENT);
        return;
    }
    const int l = opaque_tid() & 63, i = l & 15, g = l >> 4;
    const int sidx = (i & 1) ? 0xEEEE : 0x4444;
    const int ha = 4 * grp + wu;
    unsigned long long tag;
    v32u wO[2];
    {
        u16* stg = (u16*)(chsm + 122880 + wu * 10240);
        tag = boot_tag(a.boot);
        asm volatile("" ::: "memory");
        v4u rawo[16];
        const u16* src = a.Wo + (size_t)(hcol(ha) + (l >> 3)) * DM + 64 * c + 8 * (l & 7);
#pragma unroll
        for (int e = 0; e < 16; ++e) rawo[e] = *(const v4u*)(src + (size_t)8 * e * DM);
        __builtin_amdgcn_sched_barrier(0);
        if (wu == 0) {
            asm volatile("s_waitcnt vmcnt(16)" ::: "memory");
            unsigned tm[2] = {~0u, ~0u};
            const char* pb = arena(a.slots) + cal_off;
#pragma unroll
            for (int k = 0; k < 2 * NPROBE; ++k) {
                unsigned long long c0, c1; unsigned d;
                asm volatile("s_memtime %0\n\ts_waitcnt lgkmcnt(0)\n\ts_load_dword %2, %3, %4\n\ts_waitcnt lgkmcnt(0)\n\ts_memtime %1\n\ts_waitcnt lgkmcnt(0)"
                             : "=&s"(c0), "=&s"(c1), "=&s"(d) : "s"(pb), "i"((k >> 1) * 256 + (k & 1) * 8192) : "memory");
                tm[k & 1] = min(tm[k & 1], (unsigned)(c1 - c0));
            }
            const unsigned side = (__builtin_amdgcn_s_getreg(20 | (3 << 11)) >> 2) & 1u;
            if (l == 0) __hip_atomic_store((gu64*)(a.boot + widx), (tag << 2) | (((tm[0] > tm[1] ? 1u : 0u) ^ side) << 1) | side, __ATOMIC_RELAXED, __HIP_MEMORY_SCOPE_AGENT);
            PST(7)
        }
#pragma unroll
        for (int m = 0; m < 2; ++m) {
#pragma unroll
            for (int e = 0; e < 8; ++e) *(v4u*)(stg + (8 * e + (l >> 3)) * 72 + 8 * (l & 7)) = rawo[8 * m + e];
            __builtin_amdgcn_s_waitcnt(0xC07F);
#pragma unroll
            for (int ct = 0; ct < 4; ++ct) {
                const u16* p = stg + (8 * g + (i >> 2)) * 72 + 16 * ct + 4 * (i & 3);
                const v2u r0 = tr4(p), r1 = tr4(p + 4 * 72), r2 = tr4(p + 32 * 72), r3 = tr4(p + 36 * 72);
                PUT4(wO[ct >> 1], (ct & 1) * 16 + m * 8, (v4u{r0[0], r0[1], r1[0], r1[1]})) PUT4(wO[ct >> 1], (ct & 1) * 16 + m * 8 + 4, (v4u{r2[0], r2[1], r3[0], r3[1]}))
            }
            __builtin_amdgcn_s_waitcnt(0xC07F);
        }
        if (wu == 0) { PST(5) }
        if (widx == 0 && wu == 0) *(volatile unsigned long long*)(chsm + C2_MB) = tag;
    }
    if (wu >= 2) touch_sc1(arena(a.slots) + (size_t)min(l + (wu - 2) * 64, FX_OFF / 2097152 - 1) * 2097152 + 2097024 - 128, l0 + C2_MB + 256);
    unsigned long long fw = 0;
#pragma nounroll
    for (int spin = 0; spin < 100000; ++spin) {
        asm volatile("" ::: "memory");
        fw = __hip_atomic_load((gu64*)(a.boot + 256 + 16 * grp + (l & 7)), __ATOMIC_RELAXED, __HIP_MEMORY_SCOPE_AGENT);
        if (__all((fw | 255u) == ((tag << 8) | 255u))) break;
        __builtin_amdgcn_s_sleep(4);
    }
    asm volatile("" ::: "memory");
    const unsigned tau = __builtin_amdgcn_readfirstlane((unsigned)fw & 255u);
    if (wu == 3) { *(volatile unsigned*)(chsm + C2_MB + 8) = tau; *(volatile unsigned*)(chsm + C2_MB + 16) = ~0u; __builtin_amdgcn_s_waitcnt(0xC07F); }
    if (wu == 1) { PST(9) }

    const unsigned r3a = l0 + C2_R3 + wu * 2048 + g * 128 + i * 4;
    v32u yb;
    v8u kx;
    v8u sel; v2u sel3;
#pragma unroll
    for (int d = 0; d < 8; ++d) sel[d] = (i >> 1) == d ? (0x3F80u << ((i & 1) * 16)) : 0u;
#pragma unroll
    for (int d = 0; d < 2; ++d) sel3[d] = (i & 3) == 0 && (i >> 3) == d ? (0x3F80u << (((i >> 2) & 1) * 16)) : 0u;
#define MF(d, a, b, c) "v_mfma_f32_16x16x32_bf16 v[" d "], v[" a "], v[" b "], " c "\n\t"
#define PX4(d0, d1, d2, d3, a, w0, w1, w2, w3) MF(d0, a, w0, "v[" d0 "]") MF(d1, a, w1, "v[" d1 "]") MF(d2, a, w2, "v[" d2 "]") MF(d3, a, w3, "v[" d3 "]")
#define PXT(d0, d1, d2, d3, a0, a1, a2, a3) MF(d0, a0, "32:35", "0") MF(d1, a0, "48:51", "0") MF(d2, a0, "64:67", "0") MF(d3, a0, "80:83", "0") \
        PX4(d0, d1, d2, d3, a1, "36:39", "52:55", "68:71", "84:87") PX4(d0, d1, d2, d3, a2, "40:43", "56:59", "72:75", "88:91") PX4(d0, d1, d2, d3, a3, "44:47", "60:63", "76:79", "92:95")
#define CVP(d, a, b) "v_cvt_pk_bf16_f32 v" d ", v" a ", v" b "\n\t"
#define PROJ_X(cnt) \
        "s_waitcnt " cnt "(8)\n\t" PXT("192:195", "196:199", "200:203", "204:207", "160:163", "164:167", "168:171", "172:175") \
        "s_waitcnt " cnt "(4)\n\t" PXT("208:211", "212:215", "216:219", "220:223", "176:179", "180:183", "184:187", "188:191") \
        CVP("128", "192", "193") CVP("129", "194", "195") CVP("136", "196", "197") CVP("137", "198", "199") CVP("144", "200", "201") CVP("145", "202", "203") CVP("152", "204", "205") CVP("153", "206", "207") \
        CVP("132", "208", "209") CVP("133", "210", "211") CVP("140", "212", "213") CVP("141", "214", "215") CVP("148", "216", "217") CVP("149", "218", "219") CVP("156", "220", "221") CVP("157", "222", "223") \
        "s_waitcnt " cnt "(0)\n\t"
#define CLOB_PX CLOB16(160, 161, 162, 163, 164, 165, 166, 167, 168, 169, 170, 171, 172, 173, 174, 175), CLOB16(176, 177, 178, 179, 180, 181, 182, 183, 184, 185, 186, 187, 188, 189, 190, 191), \
        CLOB16(192, 193, 194, 195, 196, 197, 198, 199, 200, 201, 202, 203, 204, 205, 206, 207), CLOB16(208, 209, 210, 211, 212, 213, 214, 215, 216, 217, 218, 219, 220, 221, 222, 223)
#define LDX(d, va, so, o) "buffer_load_dwordx4 v[" d "], " va ", %[rs], " so " offen offset:" o " sc1\n\t"
#define DSV(d, o) "ds_read_b128 v[" d "], %[lv] offset:" o "\n\t"
    const int voffX = ((i >> 1) & 1) * 4096 + g * 256 + (2 * (i >> 2) + (i & 1)) * 16;
    const int pk = (((i >> 1) & 3) << 1) | (i >> 3);
    const unsigned hv = wu >= 2 ? l0 + C2_R1 + 2176 + ((wu & 1) * 128 + ((l >> 4) + 4 * ((l >> 3) & 1)) * 16 + 8 * ((l >> 1) & 1) + 4 * ((l >> 2) & 1) + 2 * (l & 1)) * 4
                                : l0 + C2_R1 + (i & 1) * 544 + (i >> 1) * 64 + g * 16;
    const unsigned hk = l0 + C2_R1 + 1088 + (i & 1) * 544 + pk * 64 + g * 16;
    const int vo1 = wu >= 2 ? l * 4 : wu == 1 ? (g >> 1) * 1024 + i * 16 + (g & 1) * 8 : (i & 1) ? 1 << 30 : (i >> 1) * 16 + 4 * g;
    const int vo2 = (2 * (i & 1) + (g >> 1)) * 128 + pk * 16 + (g & 1) * 8;
    const int voffK = (g >> 1) * 256 + i * 16 + (g & 1) * 8;
#define RD4(w, ad, a, b, c, d) "ds_read_" w " v[" a "], " ad "\n\tds_read_" w " v[" b "], " ad " offset:3200\n\tds_read_" w " v[" c "], " ad " offset:6400\n\tds_read_" w " v[" d "], " ad " offset:9600\n\t"
#define AD4(d0, d1, d2, d3, a0, a1, a2, a3) ADD(d0, d0, a0) ADD(d1, d1, a1) ADD(d2, d2, a2) ADD(d3, d3, a3)
#define QSC(r) "v_mul_f32 v" r ", 0x3e0293ee, v" r "\n\t"
#define FLD "buffer_load_dword v114, %[vf], %[rs], %[fl] offen sc1\n\t"
#define DUM "buffer_store_dword v168, off, %[rs], %[ob] sc1\n\t"
#define QHEAD "s_barrier\n\t" RD4("b128", "%[hv]", "160:163", "164:167", "168:171", "172:175") FLD \
        "s_waitcnt lgkmcnt(2)\n\t" AD4("160", "161", "162", "163", "164", "165", "166", "167") \
        "s_waitcnt lgkmcnt(0)\n\t" AD4("168", "169", "170", "171", "172", "173", "174", "175") \
        "s_barrier\n\t"
#define QTAIL AD4("160", "161", "162", "163", "168", "169", "170", "171") QSC("160") QSC("161") QSC("162") QSC("163")
#define SWA(d, a, b) "v_add_f32_dpp v" d ", v" a ", v" b " quad_perm:[1,0,3,2] row_mask:0xf bank_mask:0xf\n\t"
#define KLD(so) "buffer_load_dwordx2 v[96:97], %[vk], %[rs], " so " offen sc1\n\tbuffer_load_dwordx2 v[102:103], %[vk], %[rs], " so " offen offset:512 sc1\n\t" \
        "s_add_u32 " so ", " so ", 4096\n\t" \
        "buffer_load_dwordx2 v[100:101], %[vk], %[rs], " so " offen sc1\n\tbuffer_load_dwordx2 v[98:99], %[vk], %[rs], " so " offen offset:512 sc1\n\t"
    auto hand_over = [&](const int o_1, const int o_2, const int o_f, const int tg) {
        int dmy = 0;
        asm volatile(
            "s_cmp_eq_u32 %[wu], 0\n\t"
            "s_cbranch_scc1 2f\n\t"
            "s_cmp_eq_u32 %[wu], 1\n\t"
            "s_cbranch_scc1 1f\n\t"
            "s_barrier\n\t"
            RD4("b64", "%[hv]", "160:161", "162:163", "164:165", "166:167") FLD
            "s_waitcnt lgkmcnt(2)\n\t" ADD("160", "160", "162") ADD("161", "161", "163")
            "s_waitcnt lgkmcnt(0)\n\t" ADD("164", "164", "166") ADD("165", "165", "167") ADD("160", "160", "164") ADD("161", "161", "165")
            CVP("168", "160", "161")
            "buffer_store_dword v168, %[v1], %[rs], %[o1] offen sc1\n\t"
            "s_barrier\n\t"
            DUM PAUSE(PV2)
            "s_branch 3f\n\t"
            "5:\n\t"
            "v_mov_b32 v115, %[lf]\n\t"
            "ds_read_b32 v115, v115\n\t"
            "s_waitcnt lgkmcnt(0)\n\t"
            "v_cmp_eq_u32 vcc, %[tg], v115\n\t"
            "s_cbranch_vccnz 4f\n\t" CN(256)
            PAUSE(PL)
            "s_branch 4f\n\t"
            "1:\n\t"
            Z2("176:177") Z2("178:179")
            "s_waitcnt vmcnt(0)\n\t"
            QHEAD QTAIL
            "s_mov_b64 exec, %[em]\n\t"
            CVP("176", "160", "161") CVP("177", "162", "163")
            "s_not_b64 exec, exec\n\t"
            CVP("178", "160", "161") CVP("179", "162", "163")
            "s_mov_b64 exec, -1\n\t"
            "s_nop 0\n\t"
            MF("180:183", "96:99", "176:179", "0") MF("184:187", "100:103", "176:179", "0")
            "s_nop 7\n\t"
            SWA("180", "184", "180") SWA("181", "185", "181") SWA("182", "186", "182") SWA("183", "187", "183")
            CVP("188", "180", "181") CVP("189", "182", "183")
            "buffer_store_dwordx2 v[188:189], %[v1], %[rs], %[o1] offen sc1\n\t"
            DUM PAUSE(PV1)
            "s_branch 3f\n\t"
            "2:\n\t"
            QHEAD
            RD4("b128", "%[hk]", "176:179", "180:183", "184:187", "188:191")
            QTAIL CVP("164", "160", "161") CVP("165", "162", "163")
            "s_waitcnt lgkmcnt(2)\n\t" AD4("176", "177", "178", "179", "180", "181", "182", "183")
            "s_waitcnt lgkmcnt(0)\n\t" AD4("184", "185", "186", "187", "188", "189", "190", "191") AD4("176", "177", "178", "179", "184", "185", "186", "187")
            CVP("166", "176", "177") CVP("167", "178", "179")
            "s_nop 1\n\t"
            "v_mfma_f32_16x16x16_bf16 v[168:171], v[166:167], v[164:165], 0\n\t"
            "buffer_store_dwordx2 v[166:167], %[v2], %[rs], %[o2] offen sc1\n\t"
            "s_nop 6\n\t"
            SWA("172", "169", "168") SWA("173", "171", "170")
            CVP("174", "172", "173")
            "buffer_store_dword v174, %[v1], %[rs], %[o1] offen sc1\n\t"
            "3:\n\t"
            PAUSE(PVA)
            "s_waitcnt vmcnt(2)\n\t"
            "v_cmp_ne_u32 vcc, " STR(SENT) ", v114\n\t"
            "s_cbranch_vccnz 5b\n\t"
            "4:\n\t"
            : [dm] "+s"(dmy) CNOUT : "{v[96:103]}"(kx), [fl] "s"(o_f), [vf] "v"(i * 128), [ob] "s"(1 << 30), [tg] "s"(tg), [lf] "s"(l0 + C2_MB + 16), [wu] "s"(wu), [hv] "v"(hv), [hk] "v"(hk), [v1] "v"(vo1), [v2] "v"(vo2), [rs] "s"(rsS), [o1] "s"(o_1), [o2] "s"(o_2), [em] "s"(0x5555555555555555ull)
            : "memory", "scc", "vcc", "v114", "v115", CLOB16(160, 161, 162, 163, 164, 165, 166, 167, 168, 169, 170, 171, 172, 173, 174, 175), CLOB16(176, 177, 178, 179, 180, 181, 182, 183, 184, 185, 186, 187, 188, 189, 190, 191));
    };
    auto o1_of = [&](const int s) { return wu >= 2 ? v_off(s, hs, tau) + jq * 512 + (wu & 1) * 256 : wu == 1 ? x_off(s, hs, tau) + jq * 256 : m_off(s, hs, tau) + jq * 128; };

    __builtin_amdgcn_s_setprio(1);
    int o_f = f_off(0, grp, tau);
    int o_cs = rec_base(0) + C_OFF + c * 16384 + (int)(half_bits(0, tau) & 1u) * 8192 + wu * 2048 + grp * 256;
    int o_1 = o1_of(0), o_2 = km_off(0, hs, tau) + jq * 512, bq = x_off(0, ha, tau), bm = m_off(0, ha, tau), bb = v_off(0, ha, tau);
    {
            const int b0 = blk_off(0, ha, tau);
            int k0 = KX_OFF + blk_off(0, hs, tau) + jq * 1024;
            asm volatile(
                LDX("160:163", "%[vx]", "%[vb]", "0") LDX("164:167", "%[vx]", "%[vb]", "1024") LDX("168:171", "%[vx]", "%[vb]", "2048") LDX("172:175", "%[vx]", "%[vb]", "3072")
                LDX("176:179", "%[vx]", "%[vb]", "128") LDX("180:183", "%[vx]", "%[vb]", "1152") LDX("184:187", "%[vx]", "%[vb]", "2176") LDX("188:191", "%[vx]", "%[vb]", "3200")
                KLD("%[k0]")
                "v_mov_b32 v131, 0\n\tv_mov_b32 v135, 0\n\tv_mov_b32 v139, 0\n\tv_mov_b32 v143, 0\n\tv_mov_b32 v147, 0\n\tv_mov_b32 v151, 0\n\tv_mov_b32 v155, 0\n\tv_mov_b32 v159, 0\n\t"
                PROJ_X("vmcnt")
                : "=&{v[128:159]}"(yb), "=&{v[96:103]}"(kx), [k0] "+s"(k0)
                : "{v[32:63]}"(wO[0]), "{v[64:95]}"(wO[1]), [vx] "v"(voffX), [vk] "v"(voffK), [rs] "s"(rsS), [vb] "s"(VX_OFF + b0)
                : "memory", "scc", CLOB_PX);
        if (wu == 1) { PST(10) }
    }
    int s_first = 0;
    asm volatile("s_branch 1f\n\t.p2align 8\n1:" : "+s"(s_first));
    for (int s = s_first; s < NSEG - 1; ++s) {
        hand_over(o_1, o_2, o_f, s);
        if (s == 0 && wu == 1) { PST(8) }
        LT(0)
#define LDPA "buffer_load_dwordx2 v[160:161], v254, %[rs], %[bm] offen sc1\n\t" LDX("164:167", "%[l16]", "%[bq]", "0") LDX("168:171", "%[l16]", "%[bq]", "1024")
#define SM(d, a, b, x) "v_smfmac_f32_16x16x64_bf16 v[" d "], v[" a "], v[" b "], " x "\n\t"
#define MX2(a, b) "v_max_u32 v244, v" a ", v" b "\n\t"
#define MX3(a, b) "v_max3_u32 v244, v244, v" a ", v" b "\n\t"
#define ZAM Z2("208:209") Z2("210:211") Z2("212:213") Z2("214:215") Z2("216:217") Z2("218:219") Z2("220:221") Z2("222:223")
#define MUL(d, a, b) "v_mul_f32 v" d ", v" a ", v" b "\n\t"
#define CV0(d, a) "v_cvt_pk_bf16_f32 v" d ", v" a ", 0\n\t"
#define RDC "ds_read_b64 v[224:225], %[rc]\n\tds_read_b64 v[226:227], %[rc] offset:2048\n\tds_read_b64 v[228:229], %[rc] offset:4096\n\tds_read_b64 v[230:231], %[rc] offset:6144\n\t"
#define EXD(d, r) "v_exp_f32 v" d ", v" r "\n\t"
#define SUBM(r) "v_sub_f32 v" r ", v" r ", v250\n\t"
#define PCVM CVP("241", "180", "180") CVP("243", "180", "180")
#define PCVX CVP("242", "178", "179") CVP("240", "176", "177")
#define OUT(t0, t1, a0, a1, a2, a3, ad, o0, o1) ADD(t0, a0, a1) ADD(t1, a2, a3) MUL(t0, t0, "250") MUL(t1, t1, "251") DW2(ad, t0, t1, o0, o1)
#define TS(n)
#define TSOUT
        int lim = dead ? 0 : SPIN_LIMIT, bad = 0;
        asm volatile(
            LDX("192:195", "%[l16]", "%[bb]", "0") LDX("196:199", "%[l16]", "%[bb]", "1024")
            ZAM Z2("224:225") Z2("226:227")
            Z2("16:17") Z2("18:19") Z2("20:21") Z2("22:23") Z2("24:25") Z2("26:27") Z2("28:29") Z2("30:31")
            "v_mov_b32 v200, 0x3f803f80\n\tv_mov_b32 v201, 0x3f803f80\n\tv_mov_b32 v202, 0x3f80\n\tv_mov_b32 v203, 0\n\t"
            "v_mov_b64 v[204:205], v[200:201]\n\tv_mov_b64 v[206:207], v[202:203]\n\t"
            "v_xor_b32 v252, 0xa000, %[sx]\n\tv_add_u32 v253, 1024, %[ra]\n\tv_lshrrev_b32 v254, 1, %[l16]\n\t"
            PAUSE(PV)
            TS(8)
            LDPA
            "4:\n\t"
            "s_waitcnt vmcnt(4)\n\t"
            SM("208:211", "192:195", "32:39", "%[sx]") MX2("192", "193") SM("212:215", "192:195", "48:55", "%[sx]") MX3("194", "195")
            SM("216:219", "192:195", "64:71", "%[sx]") SM("220:223", "192:195", "80:87", "%[sx]")
            "s_waitcnt vmcnt(3)\n\t"
            SM("208:211", "196:199", "40:47", "%[sx]") MX3("196", "197") SM("212:215", "196:199", "56:63", "%[sx]") MX3("198", "199")
            SM("216:219", "196:199", "72:79", "%[sx]") "v_cmp_eq_u32 vcc, " STR(SENT) ", v244\n\t" SM("220:223", "196:199", "88:95", "%[sx]")
            "s_cbranch_vccnz 5f\n\t"
            "1:\n\t" TS(11)
            "s_waitcnt vmcnt(2)\n\t"
            "v_mfma_f32_16x16x16_bf16 v[236:239], v[112:113], v[160:161], 0\n\t"
            MX2("160", "161") ADD("245", "208", "209") ADD("246", "210", "211") CV0("130", "245") CV0("134", "246")
            ADD("245", "212", "213") ADD("246", "214", "215") CV0("138", "245") CV0("142", "246")
            EXD("180", "236")
            "s_waitcnt vmcnt(1)\n\t"
            MF("228:231", "104:107", "164:167", "0")
            MX3("164", "165") MX3("166", "167") ADD("245", "216", "217") ADD("246", "218", "219") CV0("146", "245") CV0("150", "246")
            ADD("245", "220", "221") ADD("246", "222", "223") CV0("154", "245") CV0("158", "246") PCVM
            "s_waitcnt vmcnt(0)\n\t"
            MF("228:231", "108:111", "168:171", "v[228:231]")
            MX3("168", "169") MX3("170", "171")
            "v_cmp_eq_u32 vcc, " STR(SENT) ", v244\n\t"
            "s_cbranch_vccnz 7f\n\t"
            "2:\n\t" TS(9)
            "s_nop 3\n\t"
            EXD("179", "231") EXD("178", "230") EXD("177", "229") EXD("176", "228")
            PCVX
            "v_max3_f32 v250, |v236|, |v231|, |v230|\n\t"
            "v_max3_f32 v250, v250, |v229|, |v228|\n\t"
            SM("224:227", "240:243", "200:207", "v252")
            "v_cmp_nge_f32 vcc, 0x41c00000, v250\n\t"
            "s_nop 0\n\t"
            SM("16:19", "240:243", "128:135", "v252")
            "s_cbranch_vccnz 10f\n\t"
            "13:\n\t"
            "s_nop 0\n\t"
            SM("20:23", "240:243", "136:143", "v252") "s_nop 1\n\t"
            ADD("250", "224", "225") ADD("251", "226", "227")
            SM("24:27", "240:243", "144:151", "v252")
            "v_rcp_f32 v250, v250\n\t"
            SM("28:31", "240:243", "152:159", "v252")
            "v_rcp_f32 v251, v251\n\t"
            OUT("245", "246", "16", "17", "18", "19", "%[ra]", "0", "16")
            OUT("247", "248", "20", "21", "22", "23", "%[ra]", "128", "144")
            OUT("245", "246", "24", "25", "26", "27", "v253", "0", "16")
            OUT("247", "248", "28", "29", "30", "31", "v253", "128", "144")
            TS(10)
            "s_waitcnt lgkmcnt(0)\n\t"
            "s_barrier\n\t"
            RDC
            "v_lshrrev_b32 v247, 2, %[l16]\n\t"
            "s_waitcnt lgkmcnt(2)\n\t"
            ADD("224", "224", "226") ADD("225", "225", "227")
            "s_waitcnt lgkmcnt(0)\n\t"
            ADD("228", "228", "230") ADD("229", "229", "231") ADD("224", "224", "228") ADD("225", "225", "229")
            "v_cvt_pk_bf16_f32 v245, v224, v225\n\t"
            "buffer_store_dword v245, v247, %[rs], %[cs] offen sc1\n\t"
            "s_branch 9f\n\t"
            "10:\n\t"
            "v_max3_f32 v250, v236, v231, v230\n\t"
            "v_max3_f32 v250, v250, v229, v228\n\t"
            "s_nop 1\n\t"
            "v_max_f32_dpp v251, v250, v250 quad_perm:[1,0,3,2] row_mask:0xf bank_mask:0xf\n\t"
            "v_max_f32_dpp v244, v250, v250 quad_perm:[1,0,3,2] row_mask:0xf bank_mask:0xf\n\t"
            "s_nop 1\n\t"
            "v_permlane16_swap_b32 v251, v244\n\t"
            "v_max_f32 v250, v251, v244\n\t"
            "v_max_f32 v251, v251, v244\n\t"
            Z2("224:225") Z2("226:227")
            "v_permlane32_swap_b32 v250, v251\n\t"
            "v_max_f32 v250, v250, v251\n\t"
            Z2("16:17") Z2("18:19")
            SUBM("228") SUBM("229") SUBM("230") SUBM("231") SUBM("236")
            EXD("180", "236") EXD("179", "231") EXD("178", "230") EXD("177", "229") EXD("176", "228")
            PCVM PCVX
            "s_nop 1\n\t"
            SM("224:227", "240:243", "200:207", "v252") "s_nop 1\n\t"
            SM("16:19", "240:243", "128:135", "v252")
            "s_branch 13b\n\t"
            "5:\n\t" CN(1)
            LDX("192:195", "%[l16]", "%[bb]", "0") LDX("196:199", "%[l16]", "%[bb]", "1024")
            LDPA
            "s_sub_u32 %[lim], %[lim], 1\n\t"
            "s_nop 1\n\t"
            ZAM
            "s_cbranch_scc0 4b\n\t"
            "s_mov_b32 %[bad], 1500\n\t"
            "s_mov_b32 %[lim], 0\n\t"
            "s_branch 1b\n\t"
            "7:\n\t" CN(65536)
            LDPA
            "s_sub_u32 %[lim], %[lim], 1\n\t"
            "s_cbranch_scc0 1b\n\t"
            "s_mov_b32 %[bad], 1000\n\t"
            "s_branch 2b\n\t"
            "9:\n\t"
            : [lim] "+s"(lim), [bad] "+s"(bad), "+{v[128:159]}"(yb) TSOUT CNOUT
            : "{v[32:63]}"(wO[0]), "{v[64:95]}"(wO[1]), "{v[104:111]}"(sel), "{v[112:113]}"(sel3), [sx] "v"(sidx), [l16] "v"(l * 16),
              [ra] "v"(r3a), [rs] "s"(rsS), [bb] "s"(bb), [bq] "s"(bq), [bm] "s"(bm), [cs] "s"(o_cs), [rc] "v"(l0 + C2_R3 + wu * 512 + l * 8)
            : "memory", "vcc", "scc", CLOB_PX, CLOB16(16, 17, 18, 19, 20, 21, 22, 23, 24, 25, 26, 27, 28, 29, 30, 31),
              CLOB16(224, 225, 226, 227, 228, 229, 230, 231, 232, 233, 234, 235, 236, 237, 238, 239), CLOB16(240, 241, 242, 243, 244, 245, 246, 247, 248, 249, 250, 251, 252, 253, 254, 254));
        if (__builtin_expect(bad != 0, 0)) TMO_SET((unsigned)bad)
        __builtin_amdgcn_s_setprio(0);
        if (s == 0 && wu == 1) { PST(6) }

        if (__builtin_expect(s <= KVS + 1, 0)) {
            if (s == 0 || __builtin_amdgcn_readfirstlane(*(volatile unsigned*)(chsm + C2_MB + 32 + ((s - 1) & 1) * 16 + 4 + (wu >> 1) * 4)) == 0u) {
                wait_ge<6>(kv_flag(a.sync, s + 1, grp, l), 1u, a.tmo, 3001u);
                const char* kp = arena(a.slots) + VX_OFF + blk_off(s + 1, ha, tau);
#pragma unroll
                for (int e = 0; e < 8; ++e) glds16h((unsigned)l * 16u, kp + e * 1024, l0 + (s & 1 ? C2_V : C2_K) + wu * 8192 + e * 1024);
                asm volatile("s_waitcnt vmcnt(0)" ::: "memory");
            }
        }
        {
            int k0 = KX_OFF + blk_off(s + 1, hs, tau) + jq * 1024;
            asm volatile(
                DSV("160:163", "0") DSV("164:167", "1024") DSV("168:171", "2048") DSV("172:175", "3072") DSV("176:179", "128") DSV("180:183", "1152") DSV("184:187", "2176") DSV("188:191", "3200")
                "s_cmp_lg_u32 %[wu], 1\n\t"
                "s_cbranch_scc1 1f\n\t"
                KLD("%[k0]")
                "1:\n\t"
                "s_waitcnt lgkmcnt(4)\n\t"
                PXT("192:195", "196:199", "200:203", "204:207", "160:163", "164:167", "168:171", "172:175")
                "s_waitcnt lgkmcnt(0)\n\t"
                PXT("208:211", "212:215", "216:219", "220:223", "176:179", "180:183", "184:187", "188:191")
                CVP("128", "192", "193") CVP("129", "194", "195") CVP("136", "196", "197") CVP("137", "198", "199") CVP("144", "200", "201") CVP("145", "202", "203") CVP("152", "204", "205") CVP("153", "206", "207")
                CVP("132", "208", "209") CVP("133", "210", "211") CVP("140", "212", "213") CVP("141", "214", "215") CVP("148", "216", "217") CVP("149", "218", "219") CVP("156", "220", "221") CVP("157", "222", "223")
                : "+{v[128:159]}"(yb), "+{v[96:103]}"(kx), [k0] "+s"(k0)
                : "{v[32:63]}"(wO[0]), "{v[64:95]}"(wO[1]), [lv] "v"(l0 + (s & 1 ? C2_V : C2_K) + wu * 8192 + voffX), [vk] "v"(voffK), [rs] "s"(rsS), [wu] "s"(wu)
                : "memory", "scc", CLOB_PX);
        }
        __builtin_amdgcn_sched_barrier(0);
        o_1 = o1_of(s + 1); o_2 = km_off(s + 1, hs, tau) + jq * 512;
        bq = x_off(s + 1, ha, tau); bm = m_off(s + 1, ha, tau); bb = v_off(s + 1, ha, tau);
        o_f = f_off(s + 1, grp, tau);
        o_cs = rec_base(s + 1) + C_OFF + c * 16384 + (int)(half_bits(s + 1, tau) & 1u) * 8192 + wu * 2048 + grp * 256;
        asm volatile("" : "+s"(o_cs), "+s"(o_f), "+s"(o_1), "+s"(o_2), "+s"(bq), "+s"(bm), "+s"(bb));
        __builtin_amdgcn_s_barrier();
        asm volatile(
            "s_cmp_lg_u32 %[wu], 3\n\t"
            "s_cbranch_scc1 1f\n\t"
            "v_mov_b32 v160, %[lf]\n\t"
            PAUSE(PF)
            "ds_read_b32 v160, v160\n\t"
            "s_waitcnt lgkmcnt(0)\n\t"
            "v_cmp_eq_u32 vcc, %[tg], v160\n\t"
            "s_cbranch_vccz 1f\n\t"
            "s_mov_b32 exec_hi, 0\n\t"
            "buffer_store_dword v160, %[l4], %[rs], %[fo] offen sc1\n\t"
            "s_mov_b64 exec, -1\n\t"
            "1:\n\t"
            :: [wu] "s"(wu), [lf] "s"(l0 + C2_MB + 16), [tg] "s"(s + 1), [l4] "v"(l * 4), [rs] "s"(rsS), [fo] "s"(o_f + c * 128)
            : "memory", "scc", "vcc", "v160");
        __builtin_amdgcn_s_setprio(1);
    }
    {
        constexpr int s = NSEG - 1;
        const int l = lane_now(), i = l & 15, g = l >> 4;
        v4u qf[8], kf[8], pv[2], px[4];
        asm volatile("s_waitcnt vmcnt(0)" ::: "memory");
        __builtin_amdgcn_s_sleep(4);
#pragma unroll
        for (int e = 0; e < 8; ++e) kf[e] = ld_sc1s(rsS, l * 16, KX_OFF + blk_off(s, ha, tau) + e * 1024);
#pragma unroll
        for (int e = 0; e < 8; ++e) qf[e] = *(const v4u*)(chsm + C2_V + 32768 + wu * 8192 + e * 1024 + l * 16);
        f32x4 sx[3] = {ZERO4, ZERO4, ZERO4};
        float mxx[2] = {-3.0e38f, -3.0e38f};
#pragma unroll
        for (int ks = 0; ks < 4; ++ks) {
            sx[0] = MM(kf[ks], qf[ks], sx[0]);
            sx[1] = MM(kf[ks], qf[4 + ks], sx[1]);
            sx[2] = MM(kf[4 + ks], qf[4 + ks], sx[2]);
        }
#pragma unroll
        for (int r = 0; r < 4; ++r) {
            if (4 * g + r > i) { sx[0][r] = -3.0e38f; sx[2][r] = -3.0e38f; }
            mxx[0] = fmaxf(mxx[0], sx[0][r]);
            mxx[1] = fmaxf(mxx[1], fmaxf(sx[1][r], sx[2][r]));
        }
        asm volatile("" : "+v"(sx[0]), "+v"(sx[1]), "+v"(sx[2]), "+v"(mxx[0]), "+v"(mxx[1]));
        hand_over(o_1, o_2, o_f, s);
        const int bk = km_off(s, ha, tau);
        __builtin_amdgcn_s_sleep(DLY_L);
#pragma nounroll
        for (int spin = 0;; ++spin) {
            asm volatile("" ::: "memory");
            pv[0] = ld_sc1s(rsS, l * 16, bb); pv[1] = ld_sc1s(rsS, l * 16, bb + 1024);
#pragma unroll
            for (int ks = 0; ks < 4; ++ks) px[ks] = ld_sc1s(rsS, g * 128 + (i >> 1) * 16, bk + ks * 512);
            if (__all(mx4(mx4(mx4(mx4(mx4(mx4(0u, pv[0]), pv[1]), px[0]), px[1]), px[2]), px[3]) != SENT)) break;
            if (dead) break;
            if (spin > SPIN_LIMIT) { TMO_SET(1700u) break; }
        }
#define SUB8(v, o) v8u{(v)[(o)], (v)[(o) + 1], (v)[(o) + 2], (v)[(o) + 3], (v)[(o) + 4], (v)[(o) + 5], (v)[(o) + 6], (v)[(o) + 7]}
#pragma unroll
        for (int ct = 0; ct < 4; ++ct) {
            f32x4 am = smm(pv[0], SUB8(wO[ct >> 1], (ct & 1) * 16), ZERO4, sidx);
            am = smm(pv[1], SUB8(wO[ct >> 1], (ct & 1) * 16 + 8), am, sidx);
            yb[8 * ct + 2] = cvt_pk_bf16(am[0] + am[1], 0.f); yb[8 * ct + 6] = cvt_pk_bf16(am[2] + am[3], 0.f);
        }
        const v8u one = {0x3F803F80u, 0x3F803F80u, 0x3F80u, 0u, 0x3F803F80u, 0x3F803F80u, 0x3F80u, 0u};
        const float SCL2E = 0.08838834764831845f * 1.4426950408889634f;
        float* xo = (float*)(chsm + C2_K) + (wu * 32 + 4 * g) * XP + i;
#pragma unroll
        for (int qt = 0; qt < 2; ++qt) {
            f32x4 sc[3] = {sx[qt], sx[2], ZERO4};
#pragma unroll
            for (int ks = 0; ks < 4; ++ks) sc[2] = MM(px[ks], qf[4 * qt + ks], sc[2]);
            float mx = fmaxf(mxx[qt], fmaxf(sc[2][1], sc[2][3]));
            mx = xor32_max(xor16_max(mx));
            const float nmx = -mx * SCL2E;
#define PE(x) __builtin_amdgcn_exp2f(fmaf(x, SCL2E, nmx))
            const float m0 = PE(sc[2][1]), m1 = PE(sc[2][3]);
            const v4u ae = {cvt_pk_bf16(PE(sc[0][0]), PE(sc[0][1])), cvt_pk_bf16(m0, m0), cvt_pk_bf16(PE(sc[0][2]), PE(sc[0][3])), 0u};
            const v4u ao = {qt ? cvt_pk_bf16(PE(sc[1][0]), PE(sc[1][1])) : 0u, 0u, qt ? cvt_pk_bf16(PE(sc[1][2]), PE(sc[1][3])) : 0u, cvt_pk_bf16(m1, m1)};
            const f32x4 sum = smm(ao, one, smm(ae, one, ZERO4, 0xE444), 0x4EEE);
#pragma unroll
            for (int ct = 0; ct < 4; ++ct) {
                const f32x4 o = smm(ao, SUB8(yb, 8 * ct), smm(ae, SUB8(yb, 8 * ct), ZERO4, 0xE444), 0x4EEE);
#pragma unroll
                for (int r = 0; r < 4; ++r) xo[(16 * qt + r) * XP + 16 * ct] = o[r] * __builtin_amdgcn_rcpf(sum[r]);
            }
        }
        __builtin_amdgcn_s_waitcnt(0xC07F);
        last_rows(chsm, rsS, wu, grp, c, tau);
        if (wu < 2) {
            v4u sel, p0, p1;
#pragma unroll
            for (int d = 0; d < 4; ++d) sel[d] = (l & 6) == 2 * d ? (0x3F80u << ((l & 1) * 16)) : 0u;
            const int xb = XB_OFF + ((c >> 1) * 8 + grp) * 16384 + (int)(tau & 1u) * 8192 + (c & 1) * 4096 + wu * 256;
            __builtin_amdgcn_s_sleep(DLY_C);
#pragma nounroll
            for (int spin = 0;; ++spin) {
                asm volatile("" ::: "memory");
                p0 = ld_sc1s(rsS, g * 512 + i * 16, xb); p1 = ld_sc1s(rsS, g * 512 + i * 16, xb + 2048);
                if (__all(mx4(mx4(0u, p0), p1) != SENT)) break;
                if (dead) break;
                if (spin > SPIN_LIMIT) { TMO_SET(1800u) break; }
            }
            const f32x4 d = MM(sel, p1, MM(sel, p0, ZERO4));
            if (g < 2) out8(a.out + (size_t)(s * SEG + 4 * grp + 2 * wu + (i >> 3)) * OUTW + 64 * c + 8 * (i & 7) + 4 * g, v2u{cvt_pk_bf16(d[0], d[1]), cvt_pk_bf16(d[2], d[3])});
        }
    }
}

struct cpx { float x, y; };
__device__ __forceinline__ cpx cmul(cpx a, cpx b) { return {a.x * b.x - a.y * b.y, a.x * b.y + a.y * b.x}; }
__device__ __forceinline__ cpx cadd(cpx a, cpx b) { return {a.x + b.x, a.y + b.y}; }
__device__ __forceinline__ cpx csub(cpx a, cpx b) { return {a.x - b.x, a.y - b.y}; }
template <int DIR> __device__ __forceinline__ cpx mul_i(cpx a) {
    return (DIR < 0) ? cpx{a.y, -a.x} : cpx{-a.y, a.x};
}
template <int DIR> __device__ __forceinline__ void r4(cpx& a, cpx& b, cpx& c, cpx& d) {
    cpx t0 = cadd(a, c), t1 = csub(a, c), t2 = cadd(b, d), t3 = mul_i<DIR>(csub(b, d));
    a = cadd(t0, t2); b = cadd(t1, t3); c = csub(t0, t2); d = csub(t1, t3);
}
template <int DIR> __device__ __forceinline__ cpx twc(float c, float s) { return cpx{c, (float)DIR * s}; }
template <int DIR> __device__ __forceinline__ void dft16(cpx (&v)[16]) {
#pragma unroll
    for (int a = 0; a < 4; ++a) r4<DIR>(v[a], v[a + 4], v[a + 8], v[a + 12]);
    const float C1 = 0.92387953251128674f, S1 = 0.38268343236508977f, R2 = 0.70710678118654752f;
    v[1 + 4] = cmul(v[1 + 4], twc<DIR>(C1, S1));
    v[1 + 8] = cmul(v[1 + 8], twc<DIR>(R2, R2));
    v[1 + 12] = cmul(v[1 + 12], twc<DIR>(S1, C1));
    v[2 + 4] = cmul(v[2 + 4], twc<DIR>(R2, R2));
    v[2 + 8] = mul_i<DIR>(v[2 + 8]);
    v[2 + 12] = cmul(v[2 + 12], twc<DIR>(-R2, R2));
    v[3 + 4] = cmul(v[3 + 4], twc<DIR>(S1, C1));
    v[3 + 8] = cmul(v[3 + 8], twc<DIR>(-R2, R2));
    v[3 + 12] = cmul(v[3 + 12], twc<DIR>(-C1, -S1));
#pragma unroll
    for (int d = 0; d < 4; ++d) r4<DIR>(v[4 * d], v[4 * d + 1], v[4 * d + 2], v[4 * d + 3]);
}
__device__ __forceinline__ constexpr int x16(int q) { return 4 * (q & 3) + (q >> 2); }
template <int DIR> __device__ __forceinline__ void dft8(cpx (&v)[8]) {
    r4<DIR>(v[0], v[2], v[4], v[6]);
    r4<DIR>(v[1], v[3], v[5], v[7]);
    const float R2 = 0.70710678118654752f;
    v[3] = cmul(v[3], twc<DIR>(R2, R2));
    v[5] = mul_i<DIR>(v[5]);
    v[7] = cmul(v[7], twc<DIR>(-R2, R2));
#pragma unroll
    for (int d = 0; d < 4; ++d) { cpx p = v[2 * d], q = v[2 * d + 1]; v[2 * d] = cadd(p, q); v[2 * d + 1] = csub(p, q); }
}
__device__ __forceinline__ int pad(int i) { return i + (i >> 4); }
__device__ __forceinline__ cpx expi(float x) { float s, c; sincospif(x, &s, &c); return cpx{c, s}; }
template <int R, int DIR> __device__ __forceinline__ void twiddle(cpx* v, cpx w1) {
    cpx w[R];
    w[1] = w1;
#pragma unroll
    for (int r = 2; r < R; ++r) w[r] = (r & 1) ? cmul(w[r - 1], w1) : cmul(w[r / 2], w[r / 2]);
#pragma unroll
    for (int r = 1; r < R; ++r) v[r] = DIR < 0 ? cmul(v[r], w[r]) : cpx{v[r].x * w[r].x + v[r].y * w[r].y, v[r].y * w[r].x - v[r].x * w[r].y};
}
struct TwBase { cpx b; cpx c[4]; };
template <int N> __device__ __forceinline__ TwBase make_tw(int t) {
    TwBase tb;
    tb.b = expi(-(float)(t & 15) * (1.0f / 128.0f));
    constexpr int NQ = (N == 1024) ? 4 : 2, T = N / 16;
#pragma unroll
    for (int q = 0; q < NQ; ++q) tb.c[q] = expi(-(float)(t + T * q) * (2.0f / (float)N));
    return tb;
}
template <int N> __device__ __forceinline__ void fft_sync() {
    if constexpr (N == 1024) { __builtin_amdgcn_fence(__ATOMIC_ACQ_REL, "wavefront"); __builtin_amdgcn_wave_barrier(); } else __syncthreads();
}
template <int N, int DIR, bool RIN, bool ROUT>
__device__ __forceinline__ void fft_reg(cpx* buf, const TwBase& tb, int t, cpx (&v)[16]) {
    constexpr int T = N / 16;
    if constexpr (!RIN) {
#pragma unroll
        for (int r = 0; r < 16; ++r) v[r] = buf[pad(t + T * r)];
        fft_sync<N>();
    }
    dft16<DIR>(v);
#pragma unroll
    for (int q = 0; q < 16; ++q) buf[pad(16 * t + x16(q))] = v[q];
    fft_sync<N>();
    {
        const int k = t & 15;
#pragma unroll
        for (int r = 0; r < 16; ++r) v[r] = buf[pad(t + T * r)];
        fft_sync<N>();
        twiddle<16, DIR>(v, tb.b);
        dft16<DIR>(v);
        const int o = (t >> 4) * 256 + k;
#pragma unroll
        for (int q = 0; q < 16; ++q) buf[pad(o + 16 * x16(q))] = v[q];
        fft_sync<N>();
    }
    if constexpr (N == 1024) {
#pragma unroll
        for (int q = 0; q < 4; ++q)
#pragma unroll
            for (int r = 0; r < 4; ++r) v[4 * q + r] = buf[pad(t + 64 * q + 256 * r)];
#pragma unroll
        for (int q = 0; q < 4; ++q) {
            twiddle<4, DIR>(&v[4 * q], tb.c[q]);
            r4<DIR>(v[4 * q], v[4 * q + 1], v[4 * q + 2], v[4 * q + 3]);
#pragma unroll
            for (int r = 0; r < 4; ++r) buf[pad(t + 64 * q + 256 * r)] = v[4 * q + r];
        }
        fft_sync<N>();
    } else {
        cpx u[2][8];
#pragma unroll
        for (int q = 0; q < 2; ++q)
#pragma unroll
            for (int r = 0; r < 8; ++r) u[q][r] = buf[pad(t + 128 * q + 256 * r)];
        if constexpr (ROUT) fft_sync<N>();
#pragma unroll
        for (int q = 0; q < 2; ++q) {
            twiddle<8, DIR>(u[q], tb.c[q]);
            dft8<DIR>(u[q]);
#pragma unroll
            for (int d = 0; d < 4; ++d) {
                if constexpr (ROUT) { v[q + 2 * d] = u[q][2 * d]; v[q + 2 * d + 8] = u[q][2 * d + 1]; }
                else { buf[pad(t + 128 * q + 256 * d)] = u[q][2 * d]; buf[pad(t + 128 * q + 256 * (d + 4))] = u[q][2 * d + 1]; }
            }
        }
        if constexpr (!ROUT) fft_sync<N>();
    }
}
__device__ __forceinline__ int f_pos(int rb, int x, unsigned tw) { x = (x + rb * FROT) & 32767; return rb * 65536 + x + (x & ~8191) + (int)((tw ^ (unsigned)(rb >> 5)) & 1u) * 8192; }
__device__ __forceinline__ int w_pos(int k, int n, unsigned tw) { return k * 16384 + ((n * 4 + k * WROT) & 8191) + (int)((tw ^ (unsigned)(k >> 7)) & 1u) * 8192; }
constexpr int PB1 = 1024 + 64, PB2 = 2048 + 128;
typedef float f32x4v __attribute__((ext_vector_type(4)));
typedef unsigned u32x4v __attribute__((ext_vector_type(4)));

__device__ __forceinline__ void fft_rows_fwd(const u16* __restrict__ x, rsrc_t rsF, const unsigned tw, int rb, char* smem, int tid, const char* wr, const char* wi, const unsigned spare) {
    cpx (*buf)[PB1] = (cpx (*)[PB1])smem;
    int f = tid >> 6, t = tid & 63;
    const u16* p0 = x + (size_t)(rb * 8 + 2 * f) * 1024 + 16 * t;
    u32x4v a0 = *(const u32x4v*)p0, a1 = *(const u32x4v*)(p0 + 8), b0 = *(const u32x4v*)(p0 + 1024), b1 = *(const u32x4v*)(p0 + 1032);
    const TwBase tb = make_tw<1024>(t);
    for (int it = 0; it < 4; ++it, rb += 64) {
        asm volatile("" : "+v"(tid));
        f = tid >> 6; t = tid & 63;
        __syncthreads();
        cpx* d = &buf[f][pad(16 * t)];
#pragma unroll
        for (int e = 0; e < 4; ++e) {
            d[2 * e] = cpx{__uint_as_float(a0[e] << 16), __uint_as_float(b0[e] << 16)};
            d[2 * e + 1] = cpx{__uint_as_float(a0[e] & 0xFFFF0000u), __uint_as_float(b0[e] & 0xFFFF0000u)};
            d[8 + 2 * e] = cpx{__uint_as_float(a1[e] << 16), __uint_as_float(b1[e] << 16)};
            d[8 + 2 * e + 1] = cpx{__uint_as_float(a1[e] & 0xFFFF0000u), __uint_as_float(b1[e] & 0xFFFF0000u)};
        }
        if (it < 3) p0 += 512 * 1024;
        if (FTR) touch(((it & 1) ? wi : wr) + (size_t)((rb >> 1) & 31) * 32832 + (it >> 1) * 32 * 32832 + ((rb & 1) * 128 + (tid >> 1)) * 128, spare);
        a0 = *(const u32x4v*)p0; a1 = *(const u32x4v*)(p0 + 8); b0 = *(const u32x4v*)(p0 + 1024); b1 = *(const u32x4v*)(p0 + 1032);
        fft_sync<1024>();
        cpx z[16];
        fft_reg<1024, -1, false, false>(buf[f], tb, t, z);
        __syncthreads();
#pragma unroll
        for (int m = 0; m < 8; ++m) {
            const int idx = tid + 256 * m, k = idx >> 2, ff = idx & 3;
            const cpx zk = buf[ff][pad(k)], zn = buf[ff][pad(k ? 1024 - k : 512)];
            f32x4v o = {0.5f * (zk.x + zn.x), 0.5f * (zk.y - zn.y), 0.5f * (zk.y + zn.y), -0.5f * (zk.x - zn.x)};
            if (m == 0 && k == 0) o = f32x4v{zk.x, zn.x, zk.y, zn.y};
            st_sc1(rsF, f_pos(rb, idx * 16, tw), __builtin_bit_cast(v4u, o));
        }
    }
}
__device__ __forceinline__ void filt_transpose(const u16* __restrict__ wr, const u16* __restrict__ wi, rsrc_t rsW, const unsigned tw, const int tb, char* smem, const int tid) {
    const v4u* s0 = (const v4u*)(wr + (size_t)tb * 32 * 513);
    const v4u* s1 = (const v4u*)(wi + (size_t)tb * 32 * 513);
    v4u r0[5], r1[5];
#pragma unroll
    for (int e = 0; e < 5; ++e) {
        const int idx = tid + 512 * e, ic = idx < 2052 ? idx : 2051;
        r0[e] = s0[ic]; r1[e] = s1[ic];
    }
#pragma unroll
    for (int e = 0; e < 5; ++e) {
        const int idx = tid + 512 * e;
        if (idx < 2052) { *(v4u*)(smem + idx * 16) = r0[e]; *(v4u*)(smem + 32832 + idx * 16) = r1[e]; }
    }
    __syncthreads();
    const u16* a0 = (const u16*)smem;
    const u16* a1 = (const u16*)(smem + 32832);
    for (int idx = tid; idx < 513 * 8; idx += 512) {
        const int k = idx >> 3, n4 = (idx & 7) * 4;
        v4u v;
#pragma unroll
        for (int j = 0; j < 4; ++j) v[j] = (unsigned)a0[(n4 + j) * 513 + k] | ((unsigned)a1[(n4 + j) * 513 + k] << 16);
        st_sc1(rsW, w_pos(k, tb * 32 + n4, tw), v);
    }
}

__device__ __forceinline__ void fft_cols(rsrc_t rsF, rsrc_t rsW, const unsigned tw, int blk, const int n, char* smem, int tid) {
    cpx (*buf)[PB2] = (cpx (*)[PB2])smem;
    int c = tid >> 7, t = tid & 127;
    f32x4v v[8];
    unsigned wv[16], wn[16];
    auto wld = [&](const int k, const int n) { return __builtin_amdgcn_raw_buffer_load_b32(rsW, w_pos(k, n, tw), 0, 0); };
    auto fetch = [&](const int bk) {
        const int k0 = bk * 2, kc = k0 + c;
#pragma unroll
        for (int m = 0; m < 8; ++m) { const int idx = tid + 256 * m; v[m] = __builtin_bit_cast(f32x4v, __builtin_amdgcn_raw_buffer_load_b128(rsF, f_pos(idx >> 3, k0 * 64 + (idx & 7) * 16, tw), 0, 0)); }
#pragma unroll
        for (int m = 0; m < 16; ++m) wn[m] = wld(kc, t + 128 * m);
    };
    fetch(blk);
    const TwBase tb = make_tw<2048>(t);
    for (int it = 0; it < n; ++it, blk += 64) {
        asm volatile("" : "+v"(tid));
        c = tid >> 7; t = tid & 127;
        __syncthreads();
#pragma unroll
        for (int m = 0; m < 8; ++m) {
            const int idx = tid + 256 * m, rb = idx >> 3, pc = idx & 7;
            cpx* d = &buf[pc >> 2][pad(rb * 8 + 2 * (pc & 3))];
            d[0] = cpx{v[m][0], v[m][1]}; d[1] = cpx{v[m][2], v[m][3]};
        }
#pragma unroll
        for (int m = 0; m < 16; ++m) wv[m] = wn[m];
        fetch(it + 1 < n ? blk + 64 : blk);
        __syncthreads();
        cpx z[16];
        fft_reg<2048, -1, false, true>(buf[c], tb, t, z);
        auto wcx = [](const unsigned w) { return cpx{__uint_as_float(w << 16), __uint_as_float(w & 0xFFFF0000u)}; };
        if (blk < 2) {
#pragma unroll
            for (int m = 0; m < 16; ++m) buf[c][pad(t + 128 * m)] = z[m];
            __syncthreads();
            if (blk == 0 && c == 0) {
#pragma unroll
                for (int m = 0; m < 16; ++m) {
                    const int n = t + 128 * m, nn = (2048 - n) & 2047;
                    const cpx zn = buf[0][pad(nn)], a = wcx(wv[m]), b = wcx(wld(0, nn)), e = wcx(wld(512, n)), f = wcx(wld(512, nn));
                    const cpx h0 = cmul(cpx{0.5f * (z[m].x + zn.x), 0.5f * (z[m].y - zn.y)}, cpx{0.5f * (a.x + b.x), 0.5f * (a.y - b.y)});
                    const cpx h5 = cmul(cpx{0.5f * (z[m].y + zn.y), -0.5f * (z[m].x - zn.x)}, cpx{0.5f * (e.x + f.x), 0.5f * (e.y - f.y)});
                    z[m] = cpx{h0.x - h5.y, h0.y + h5.x};
                    wv[m] = 0x3F80u;
                }
            }
            __syncthreads();
        }
#pragma unroll
        for (int m = 0; m < 16; ++m) z[m] = cmul(z[m], wcx(wv[m]));
        fft_reg<2048, 1, true, false>(buf[c], tb, t, z);
        const float sc = 1.0f / 2048.0f;
#pragma unroll
        for (int m = 0; m < 8; ++m) {
            const int idx = tid + 256 * m, rb = idx >> 3, pc = idx & 7;
            const cpx* d = &buf[pc >> 2][pad(rb * 8 + 2 * (pc & 3))];
            st_sc1(rsF, f_pos(rb, blk * 128 + pc * 16, tw), __builtin_bit_cast(v4u, f32x4v{d[0].x * sc, d[0].y * sc, d[1].x * sc, d[1].y * sc}));
        }
    }
}

__device__ __forceinline__ void fft_rows_inv(rsrc_t rsF, const unsigned tw, u16* __restrict__ out, int rb, char* smem, int tid) {
    cpx (*buf)[PB1] = (cpx (*)[PB1])smem;
    int f = tid >> 6, t = tid & 63;
    f32x4v v[8];
    auto fetch = [&](const int r) {
#pragma unroll
        for (int m = 0; m < 8; ++m) v[m] = __builtin_bit_cast(f32x4v, __builtin_amdgcn_raw_buffer_load_b128(rsF, f_pos(r, (tid + 256 * m) * 16, tw), 0, 0));
    };
    fetch(rb);
    const TwBase tb = make_tw<1024>(t);
    for (int it = 0; it < 4; ++it, rb += 64) {
        asm volatile("" : "+v"(tid));
        f = tid >> 6; t = tid & 63;
        __syncthreads();
        cpx* bf = buf[tid & 3];
#pragma unroll
        for (int m = 0; m < 8; ++m) {
            const int k = (tid >> 2) + 64 * m;
            if (m == 0 && k == 0) { bf[0] = cpx{v[0][0], v[0][2]}; bf[pad(512)] = cpx{v[0][1], v[0][3]}; }
            else { bf[pad(k)] = cpx{v[m][0] - v[m][3], v[m][1] + v[m][2]}; bf[pad(1024 - k)] = cpx{v[m][0] + v[m][3], v[m][2] - v[m][1]}; }
        }
        fetch(it < 3 ? rb + 64 : rb);
        __syncthreads();
        cpx z[16];
        fft_reg<1024, 1, false, false>(buf[f], tb, t, z);
        const float sc = 1.0f / 1024.0f;
        const cpx* d = &buf[f][pad(16 * t)];
        u32x4v a[2], b[2];
#pragma unroll
        for (int h = 0; h < 2; ++h)
#pragma unroll
            for (int e = 0; e < 4; ++e) {
                cpx p = d[8 * h + 2 * e], q = d[8 * h + 2 * e + 1];
                a[h][e] = cvt_pk_bf16(p.x * sc, q.x * sc);
                b[h][e] = cvt_pk_bf16(p.y * sc, q.y * sc);
            }
        u16* o0 = out + (size_t)(rb * 8 + 2 * f) * OUTW + 1024 + 16 * t;
        *(u32x4v*)o0 = a[0]; *(u32x4v*)(o0 + 8) = a[1];
        *(u32x4v*)(o0 + OUTW) = b[0]; *(u32x4v*)(o0 + OUTW + 8) = b[1];
    }
}

__device__ __forceinline__ void glds16s(unsigned voff, const void* sbase, unsigned lds_dst) {
    unsigned keep;
    asm volatile("s_mov_b32 %0, m0\n\ts_mov_b32 m0, %3\n\ts_nop 0\n\tglobal_load_lds_dwordx4 %1, %2\n\ts_mov_b32 m0, %0" : "=&s"(keep) : "v"(voff), "s"(sbase), "s"(lds_dst) : "memory");
}
#define K8_BAR do { asm volatile("" ::: "memory"); __builtin_amdgcn_s_barrier(); asm volatile("" ::: "memory"); } while (0)
#define K8_WV(n) asm volatile("s_waitcnt vmcnt(" #n ")" ::: "memory")
#define K8_WL(n) asm volatile("s_waitcnt lgkmcnt(" #n ")" ::: "memory")
#define K8_SCHED __builtin_amdgcn_sched_barrier(0)
template <int BN>
__device__ __forceinline__ int bswz2(int row) {
    if (BN >= 128) return (row & 3) | (((row >> 3) & 1) << 2);
    else return ((row >> 1) & 1) | (((row >> 3) & 1) << 1);
}
template <int BM>
__device__ __forceinline__ void gemm_tile(u16* sm, const char* A, const int s0, const unsigned tau, const u16* B, u16* C) {
    constexpr int K = HD, LDB = DM, LDC = OUTW;
    constexpr int BN = 128, BK = 64, ST = BM == 128 ? 4 : 3, WN = BM == 128 ? 4 : 2;
    constexpr int ATILE = BM * BK, BTILE = BK * BN;
    constexpr int NT = 8 / WN, GA = BM / 64, GB = 2, GS = GA + GB;
    const int tid = opaque_tid(), w = tid >> 6, l = tid & 63, i = l & 15, g = l >> 4;
    const int wr = w / WN, wc = w % WN;
    const unsigned wu = __builtin_amdgcn_readfirstlane(w);
    unsigned vb;
    { const int row = l >> 4, c = l & 15; vb = (unsigned)(row * LDB + 8 * (c ^ ((row | ((wu & 1) << 2)) << 1))) * 2u; }
    const int sg = s0 + (BM == 128 ? wu >> 1 : wu);
    const char* ab = A + (BM == 128 ? (wu & 1) * 4096 : 0);
    const char* bb = (const char*)(B + (size_t)wu * 8 * LDB);
    const unsigned lds0 = (unsigned)(uintptr_t)(__attribute__((address_space(3))) u16*)sm;
    const unsigned va = (unsigned)l * 16u, va2 = va + 2048u, vb2 = vb + 4u * LDB * 2u - 1024u;
    auto req = [&](const int buf, const int kt) {
        const unsigned d = lds0 + buf * (ATILE + BTILE) * 2;
        const int wh = kt >> 1;
        const char* pa = ab + ((blk_off(sg, (wh & 7) * 4 + (wh >> 3), tau) ^ 8192) + (kt & 1) * 2048);
        const char* pb = bb + (size_t)kt * BK * LDB * 2;
        unsigned keep;
        if (BM == 128) asm volatile("s_mov_b32 %0, m0\n\ts_mov_b32 m0, %1\n\ts_nop 0\n\tglobal_load_lds_dwordx4 %3, %5\n\tglobal_load_lds_dwordx4 %3, %5 offset:1024\n\t"
                     "s_mov_b32 m0, %2\n\ts_nop 0\n\tglobal_load_lds_dwordx4 %6, %8\n\tglobal_load_lds_dwordx4 %7, %8 offset:1024\n\ts_mov_b32 m0, %0"
                     : "=&s"(keep) : "s"(d + wu * GA * 1024), "s"(d + ATILE * 2 + wu * GB * 1024), "v"(va), "v"(va2), "s"(pa), "v"(vb), "v"(vb2), "s"(pb) : "memory");
        else asm volatile("s_mov_b32 %0, m0\n\ts_mov_b32 m0, %1\n\ts_nop 0\n\tglobal_load_lds_dwordx4 %3, %5\n\tglobal_load_lds_dwordx4 %3, %5 offset:1024\n\t"
                     "global_load_lds_dwordx4 %4, %5 offset:2048\n\tglobal_load_lds_dwordx4 %4, %5 offset:3072\n\t"
                     "s_mov_b32 m0, %2\n\ts_nop 0\n\tglobal_load_lds_dwordx4 %6, %8\n\tglobal_load_lds_dwordx4 %7, %8 offset:1024\n\ts_mov_b32 m0, %0"
                     : "=&s"(keep) : "s"(d + wu * GA * 1024), "s"(d + ATILE * 2 + wu * GB * 1024), "v"(va), "v"(va2), "s"(pa), "v"(vb), "v"(vb2), "s"(pb) : "memory");
    };
    f32x4 acc[4][NT] = {};
    constexpr int nt = K / BK;
#pragma unroll
    for (int s = 0; s < ST - 1; ++s) req(s, s);
    int boff[NT];
    const int aoff = wr * 4096 + l * 8;
#pragma unroll
    for (int n = 0; n < NT; ++n) { const int row = 8 * g + (i >> 2); boff[n] = row * BN + 16 * ((wc * NT + n) ^ bswz2<BN>(row)) + 4 * (i & 3); }
    asm volatile("s_waitcnt vmcnt(%0)" :: "i"((ST - 2) * GS) : "memory");
    K8_BAR;
    if (wu >= 4) K8_BAR;
    for (int t0 = 0; t0 < nt + ST - 1 - (nt + ST - 1) % ST; t0 += ST) {
#pragma unroll
        for (int rb = 0; rb < ST; ++rb) {
            const int t = t0 + rb;
            if (nt % ST != 0 && t >= nt) break;
            const int kk = min(t + ST - 1, nt - 1);
            const u16* as = sm + rb * (ATILE + BTILE);
            const u16* bs = as + ATILE;
            bf16x8 af[2][4], bf[2][NT];
#pragma unroll
            for (int ks = 0; ks < 2; ++ks) {
#pragma unroll
                for (int n = 0; n < NT; ++n) bf[ks][n] = tr_frag(bs + boff[n] + ks * 32 * BN, bs + boff[n] + ks * 32 * BN + 4 * BN);
#pragma unroll
                for (int m = 0; m < 4; ++m) af[ks][m] = *(const bf16x8*)(as + aoff + m * 1024 + ks * 512);
            }
            K8_SCHED;
            req((rb + ST - 1) % ST, kk);
            K8_WL(0);
            if (wu >= 4) asm volatile("s_waitcnt vmcnt(%0)" :: "i"((ST - 2) * GS) : "memory");
            K8_BAR;
            __builtin_amdgcn_s_setprio(1);
#pragma unroll
            for (int q = 0; q < 8; ++q)
#pragma unroll
                for (int n = 0; n < NT; ++n) acc[q & 3][n] = __builtin_amdgcn_mfma_f32_16x16x32_bf16(bf[q >> 2][n], af[q >> 2][q & 3], acc[q & 3][n], 0, 0, 0);
            __builtin_amdgcn_s_setprio(0);
            K8_SCHED;
            asm volatile("s_waitcnt vmcnt(%0)" :: "i"((ST - 2) * GS) : "memory");
            K8_BAR;
        }
    }
    if (wu < 4) K8_BAR;
    asm volatile("s_waitcnt vmcnt(0)" ::: "memory");
#pragma unroll
    for (int m = 0; m < 4; ++m)
#pragma unroll
        for (int n = 0; n < NT; n += 2) {
            auto r0 = __builtin_amdgcn_permlane16_swap(cvt_pk_bf16(acc[m][n][0], acc[m][n][1]), cvt_pk_bf16(acc[m][n + 1][0], acc[m][n + 1][1]), false, false);
            auto r1 = __builtin_amdgcn_permlane16_swap(cvt_pk_bf16(acc[m][n][2], acc[m][n][3]), cvt_pk_bf16(acc[m][n + 1][2], acc[m][n + 1][3]), false, false);
            *(v4u*)(C + (wr * 64 + m * 16 + i) * LDC + (wc * NT + n + (g & 1)) * 16 + 8 * (g >> 1)) = v4u{r0[0], r1[0], r0[1], r1[1]};
        }
}

__device__ __forceinline__ void xattn_unit(const ChainArgs& a, rsrc_t rsQ, rsrc_t rsK, rsrc_t rsV, rsrc_t rsS, const int s, const int sn, const int h, u16 (*Vs)[VP], unsigned* mbox, const unsigned tau, int& pend, bool& ready) {
    const int l = opaque_tid() & 63, i = l & 15, g = l >> 4;
    const unsigned known = __builtin_amdgcn_readfirstlane(*(volatile unsigned*)mbox);
    const bool early = known > (unsigned)(s + 1);
    const int bq = km_off(s, h, tau), bb = v_off(s, h, tau);
    auto count = [&](const int t) { arrive(a.sync, t < SSEG ? SY_OX + (t >> 2) * 8 : SY_OXS + (t - SSEG) * 8, t < SSEG ? 128u : 32u, 8); };
    const int xk = blk_off(s, h, tau), xq = xk ^ 8192;
    v4u vv[8], vm[2], k0f[4], k1f[4], kmf[4], q0f[4], q1f[4];
#pragma unroll
    for (int e = 0; e < 8; ++e) vv[e] = ld_sc1s(rsV, l * 16 + e * 1024, xk);
#pragma unroll
    for (int ks = 0; ks < 4; ++ks) {
        k0f[ks] = ld_sc1s(rsK, l * 16 + ks * 1024, xk);
        k1f[ks] = ld_sc1s(rsK, l * 16 + ks * 1024, xk + 4096);
        q0f[ks] = ld_sc1s(rsQ, l * 16 + ks * 1024, xq);
        q1f[ks] = ld_sc1s(rsQ, l * 16 + ks * 1024, xq + 4096);
    }
#pragma unroll
    for (int e = 0; e < 2; ++e) vm[e] = ld_sc1s(rsS, l * 16 + e * 1024, early ? bb : 1 << 30);
#pragma unroll
    for (int ks = 0; ks < 4; ++ks) kmf[ks] = ld_sc1s(rsS, ks * 512 + g * 128 + (i & 7) * 16, early ? bq : 1 << 30);
    const unsigned fl = ld_flag(a.sync + (l >= 8 ? SY_PROG * 32 : (((l & 4) ? SY_Q : SY_KV) + (min(sn, NSEG - 1) >> 3) * 8) * 32 + (l & 3)) + (h >> 2) * 32);
    if (pend >= 0) { asm volatile("s_waitcnt vmcnt(31)" ::: "memory"); count(pend); }
#pragma unroll
    for (int e = 0; e < 8; ++e) *(v4u*)&Vs[16 * (e >> 2) + i][32 * (e & 3) + 8 * g] = vv[e];
    const float scale = 0.08838834764831845f;
    const float L2E = 1.4426950408889634f;
    auto round2 = [&](const f32x4 c, float* o) {
#pragma unroll
        for (int p = 0; p < 2; ++p) {
            unsigned u = cvt_pk_bf16(c[2 * p], c[2 * p + 1]);
            float lo = __uint_as_float(u << 16) * scale, hi = __uint_as_float(u & 0xFFFF0000u) * scale;
            unsigned u2 = cvt_pk_bf16(lo, hi);
            o[2 * p] = __uint_as_float(u2 << 16);
            o[2 * p + 1] = __uint_as_float(u2 & 0xFFFF0000u);
        }
    };
    float sv[2][12], mxx[2];
    {
        f32x4 sx[3] = {};
#pragma unroll
        for (int ks = 0; ks < 4; ++ks) {
            sx[0] = __builtin_amdgcn_mfma_f32_16x16x32_bf16(as_frag(k0f[ks]), as_frag(q0f[ks]), sx[0], 0, 0, 0);
            sx[1] = __builtin_amdgcn_mfma_f32_16x16x32_bf16(as_frag(k0f[ks]), as_frag(q1f[ks]), sx[1], 0, 0, 0);
            sx[2] = __builtin_amdgcn_mfma_f32_16x16x32_bf16(as_frag(k1f[ks]), as_frag(q1f[ks]), sx[2], 0, 0, 0);
        }
        round2(sx[0], &sv[0][0]); round2(sx[1], &sv[1][0]); round2(sx[2], &sv[1][4]);
        mxx[0] = mxx[1] = -3.0e38f;
#pragma unroll
        for (int r = 0; r < 4; ++r) {
            if (4 * g + r > i) { sv[0][r] = -3.0e38f; sv[1][4 + r] = -3.0e38f; }
            mxx[0] = fmaxf(mxx[0], sv[0][r]);
            mxx[1] = fmaxf(mxx[1], fmaxf(sv[1][r], sv[1][4 + r]));
        }
    }
    const u16* vb = &Vs[4 * g + (i >> 2)][4 * (i & 3)];
    bf16x8 vfx[8];
#pragma unroll
    for (int t = 0; t < 8; ++t) vfx[t] = tr_frag(vb + 16 * t, vb + 16 * VP + 16 * t);
#pragma unroll
    for (int t = 0; t < 8; ++t) asm volatile("" : "+v"(vfx[t]));
#pragma unroll
    for (int r = 0; r < 4; ++r) asm volatile("" : "+v"(sv[0][r]), "+v"(sv[1][r]), "+v"(sv[1][4 + r]));
    asm volatile("" : "+v"(mxx[0]), "+v"(mxx[1]));
    bool have = false;
    unsigned seen = __builtin_amdgcn_readlane(fl, 8);
    ready = (sn >> 3) == (s >> 3) || (__ballot(fl != 0u) & 255ull) == 255ull;
    if (early) {
        if ((h & 3) == 0 && l == 0) atomicMax(mbox, seen);
        unsigned m = mx4(mx4(0u, vm[0]), vm[1]);
#pragma unroll
        for (int ks = 0; ks < 4; ++ks) m = mx4(m, kmf[ks]);
        have = __all(m != SENT);
    } else if ((h & 3) == 0) {
        const unsigned* pw = a.sync + (SY_PROG + (h >> 2)) * 32;
        unsigned v = seen;
        if (v <= (unsigned)(s + 1)) {
            if (s < NSEG - XA_FAST) { wait_ge<32>(pw, (unsigned)(s + 1), a.tmo, 4000u); __builtin_amdgcn_s_sleep(24); }
            else { wait_ge<6>(pw, (unsigned)(s + 1), a.tmo, 4000u); __builtin_amdgcn_s_sleep(XA_D); }
            v = (unsigned)(s + 1);
        }
        if (l == 0) atomicMax(mbox, v);
    } else {
#pragma nounroll
        for (int spin = 0; spin < 4000000 && *(volatile unsigned*)mbox < (unsigned)(s + 1); ++spin) __builtin_amdgcn_s_sleep(2);
        asm volatile("" ::: "memory");
    }
#pragma nounroll
    for (int spin = 0; !have; ++spin) {
        asm volatile("" ::: "memory");
#pragma unroll
        for (int e = 0; e < 2; ++e) vm[e] = ld_sc1s(rsS, l * 16 + e * 1024, bb);
#pragma unroll
        for (int ks = 0; ks < 4; ++ks) kmf[ks] = ld_sc1s(rsS, ks * 512 + g * 128 + (i & 7) * 16, bq);
        unsigned m = mx4(mx4(0u, vm[0]), vm[1]);
#pragma unroll
        for (int ks = 0; ks < 4; ++ks) m = mx4(m, kmf[ks]);
        if (__all(m != SENT) || spin > 20000) break;
        __builtin_amdgcn_s_sleep(2);
    }
#pragma unroll
    for (int e = 0; e < 8; ++e) *(unsigned*)&Vs[32 + (i >> 2) + 4 * ((i >> 1) & 1)][64 * (e >> 2) + 16 * g + 4 * (e & 3) + 2 * (i & 1)] = vm[e >> 2][e & 3];
    f32x4 sm_[2] = {};
#pragma unroll
    for (int ks = 0; ks < 4; ++ks) {
        sm_[0] = __builtin_amdgcn_mfma_f32_16x16x32_bf16(as_frag(kmf[ks]), as_frag(q0f[ks]), sm_[0], 0, 0, 0);
        sm_[1] = __builtin_amdgcn_mfma_f32_16x16x32_bf16(as_frag(kmf[ks]), as_frag(q1f[ks]), sm_[1], 0, 0, 0);
    }
    v4u pf[2][2];
#pragma unroll
    for (int qt = 0; qt < 2; ++qt) {
        round2(sm_[qt], &sv[qt][8]);
        float mx = fmaxf(fmaxf(mxx[qt], fmaxf(sv[qt][8], sv[qt][9])), fmaxf(sv[qt][10], sv[qt][11]));
        mx = xor16_max(mx);
        mx = xor32_max(mx);
        const float nmx = -mx * L2E;
        float sum = 0.f;
#pragma unroll
        for (int e = 0; e < 12; ++e) {
            if (qt == 0 && e >= 4 && e < 8) { sv[qt][e] = 0.f; continue; }
            float ev = __builtin_amdgcn_exp2f(fmaf(sv[qt][e], L2E, nmx));
            if (e >= 8) ev = g < 2 ? ev : 0.f;
            sv[qt][e] = ev; sum += ev;
        }
        sum = xor16_sum(sum);
        sum = xor32_sum(sum);
        const float inv = 1.0f / sum;
        pf[qt][0][0] = cvt_pk_bf16(sv[qt][0] * inv, sv[qt][1] * inv);
        pf[qt][0][1] = cvt_pk_bf16(sv[qt][2] * inv, sv[qt][3] * inv);
        pf[qt][0][2] = qt ? cvt_pk_bf16(sv[qt][4] * inv, sv[qt][5] * inv) : 0u;
        pf[qt][0][3] = qt ? cvt_pk_bf16(sv[qt][6] * inv, sv[qt][7] * inv) : 0u;
        pf[qt][1][0] = cvt_pk_bf16(sv[qt][8] * inv, sv[qt][9] * inv);
        pf[qt][1][1] = cvt_pk_bf16(sv[qt][10] * inv, sv[qt][11] * inv);
        pf[qt][1][2] = 0u; pf[qt][1][3] = 0u;
    }
    v2u vmf[8];
#pragma unroll
    for (int t = 0; t < 8; ++t) vmf[t] = tr4(&Vs[32 + 4 * (g & 1) + (i >> 2)][16 * t + 4 * (i & 3)]);
#pragma unroll
    for (int t = 0; t < 8; t += 2) {
        unsigned pk[2][2][2];
#pragma unroll
        for (int tt = 0; tt < 2; ++tt) {
            const bf16x8 vf0 = vfx[t + tt];
            const bf16x8 vf1 = as_frag(v4u{vmf[t + tt][0], vmf[t + tt][1], 0u, 0u});
#pragma unroll
            for (int qt = 0; qt < 2; ++qt) {
                f32x4 o = {};
                o = __builtin_amdgcn_mfma_f32_16x16x32_bf16(vf0, as_frag(pf[qt][0]), o, 0, 0, 0);
                o = __builtin_amdgcn_mfma_f32_16x16x32_bf16(vf1, as_frag(pf[qt][1]), o, 0, 0, 0);
                pk[qt][tt][0] = cvt_pk_bf16(o[0], o[1]);
                pk[qt][tt][1] = cvt_pk_bf16(o[2], o[3]);
            }
        }
#pragma unroll
        for (int qt = 0; qt < 2; ++qt) {
            auto r0 = __builtin_amdgcn_permlane16_swap(pk[qt][0][0], pk[qt][1][0], false, false);
            auto r1 = __builtin_amdgcn_permlane16_swap(pk[qt][0][1], pk[qt][1][1], false, false);
            v4u o = {r0[0], r1[0], r0[1], r1[1]};
            st_sc1s(rsV, (t >> 1) * 1024 + ((g & 1) * 2 + (g >> 1)) * 256 + i * 16, xq + qt * 4096, o);
        }
    }
    pend = seen > (unsigned)(sn + 1) && ready ? s : -1;
    if (pend < 0) { asm volatile("s_waitcnt vmcnt(0)" ::: "memory"); count(s); }
}

__device__ __forceinline__ void tile_stream(const ChainArgs& a, const u16* X, unsigned long long todo, const int xc, unsigned& tau, const bool early, const int nw, u16* sm8 WEVP) {
    constexpr int K = DM, LDB = HD;
    const int tid = opaque_tid(), w = tid >> 6, l = tid & 63, i = l & 15, g = l >> 4;
    const int wr = w >> 2, wc = w & 3;
    const unsigned lds0 = (unsigned)(uintptr_t)(__attribute__((address_space(3))) u16*)sm8;
    const unsigned wu = __builtin_amdgcn_readfirstlane(w);
    const unsigned ldsw = lds0 + wu * 2048;
    unsigned voffA, voffB;
    { int row = 16 * w + (l >> 3), c = l & 7; voffA = (unsigned)(row * K + 8 * (c ^ (row & 7))) * 2u; }
    { int row = 8 * w + g, c = i; voffB = (unsigned)(row * LDB + 8 * (c ^ (bswz2<128>(row) << 1))) * 2u; }
    const unsigned voffA2 = voffA + 8u * K * 2u - 1024u, voffB2 = voffB + 4u * LDB * 2u - 1024u;
    auto st2 = [&](const unsigned v0, const unsigned v1, const char* p, const unsigned d) {
        unsigned keep;
        asm volatile("s_mov_b32 %0, m0\n\ts_mov_b32 m0, %1\n\ts_nop 0\n\tglobal_load_lds_dwordx4 %2, %4\n\tglobal_load_lds_dwordx4 %3, %4 offset:1024\n\ts_mov_b32 m0, %0"
                     : "=&s"(keep) : "s"(d), "v"(v0), "v"(v1), "s"(p) : "memory");
    };
    auto stA = [&](int b, int h, const char* p) {
        st2(voffA, voffA2, p + (size_t)(h * 128) * K * 2, ldsw + (2 * b + h) * 16384);
    };
    auto stB = [&](int b, int h, const char* p) {
        st2(voffB, voffB2, p + h * 2048, ldsw + 65536 + (2 * b + h) * 16384);
    };
    constexpr int SA = 128, SB = 64 * LDB * 2;
    auto a_of = [&](const unsigned t) { return (const char*)(X + (size_t)((t >> 4) & 7) * 256 * DM); };
    auto b_of = [&](const unsigned t) { const int kind = t & 7; return (const char*)((kind < 2 ? a.Wk : (kind < 4 ? a.Wv : a.Wq)) + hcol(4 * xc + (kind & 1) * 2)); };
    auto c_of = [&](const unsigned t) { const int kind = t & 7; return ((kind & 6) == 2 ? VX_OFF - KX_OFF : 0) + (int)((t >> 4) & 7) * 4194304 + blk_off(0, 4 * xc + (kind & 1) * 2, kind < 4 ? tau : tau ^ 1u) + (int)((t >> 3) & 1u); };
    auto publish = [&](const unsigned t) {
        const int kind = t & 7, ln = lane_now();
        if (wu == 0 && ln < (kind < 4 ? 1 : 2))
            __hip_atomic_store((gu32*)(a.sync + ((kind < 4 ? SY_KV : SY_Q) + ((t >> 4) & 7) * 8 + xc) * 32 + (kind < 4 ? kind : 2 * kind - 8 + ln)), 16u, __ATOMIC_RELAXED, __HIP_MEMORY_SCOPE_AGENT);
    };
    const char* smc = (const char*)sm8;
    int aoff[2], boff[2];
#pragma unroll
    for (int ks = 0; ks < 2; ++ks) aoff[ks] = (wr * 64 + i) * 128 + (((ks * 4 + g) ^ (i & 7)) << 4);
#pragma unroll
    for (int n = 0; n < 2; ++n) { int row = 8 * g + (i >> 2); boff[n] = 65536 + row * 256 + (((wc * 2 + n) ^ bswz2<128>(row)) << 5) + 8 * (i & 3); }
    f32x4 acc[2][2][4][2] = {};
    bf16x8 At[4][2], B0[2][2], B1[2][2];
#define K8_LDA(b, h) _Pragma("unroll") for (int m = 0; m < 4; ++m) _Pragma("unroll") for (int ks = 0; ks < 2; ++ks) \
        At[m][ks] = *(const bf16x8*)(smc + aoff[ks] + (2 * (b) + (h)) * 16384 + m * 2048)
#define K8_LDB(dst, b, h) _Pragma("unroll") for (int n = 0; n < 2; ++n) _Pragma("unroll") for (int ks = 0; ks < 2; ++ks) { \
        const char* p_ = smc + boff[n] + (2 * (b) + (h)) * 16384 + ks * 8192; \
        v4i16 lo_ = __builtin_amdgcn_ds_read_tr16_b64_v4i16((__attribute__((address_space(3))) v4i16*)p_); \
        v4i16 hi_ = __builtin_amdgcn_ds_read_tr16_b64_v4i16((__attribute__((address_space(3))) v4i16*)(p_ + 1024)); \
        dst[n][ks] = bf16x8{lo_[0], lo_[1], lo_[2], lo_[3], hi_[0], hi_[1], hi_[2], hi_[3]}; }
#define K8_MMA(ai, bj, Bt) do { __builtin_amdgcn_s_setprio(1); \
        _Pragma("unroll") for (int m = 0; m < 4; ++m) _Pragma("unroll") for (int n = 0; n < 2; ++n) _Pragma("unroll") for (int ks = 0; ks < 2; ++ks) \
            acc[ai][bj][m][n] = __builtin_amdgcn_mfma_f32_16x16x32_bf16(Bt[n][ks], At[m][ks], acc[ai][bj][m][n], 0, 0, 0); \
        __builtin_amdgcn_s_setprio(0); } while (0)
    const rsrc_t rsC = __builtin_amdgcn_make_buffer_rsrc((void*)(arena(a.slots) + KX_OFF), 0, FX_OFF - KX_OFF, 0x00020000);
#define K8_EPI(ai, bj, cb) do { const int ln_ = lane_now(); \
        const int vo_ = (int)(wu & 3) * 1024 + (((ln_ >> 4) & 1) * 2 + (ln_ >> 5)) * 256 + (ln_ & 15) * 16; \
        _Pragma("unroll") for (int m = 0; m < 4; ++m) { \
            const f32x4 c0 = acc[ai][bj][m][0], c1 = acc[ai][bj][m][1]; \
            auto r0 = __builtin_amdgcn_permlane16_swap(cvt_pk_bf16(c0[0], c0[1]), cvt_pk_bf16(c1[0], c1[1]), false, false); \
            auto r1 = __builtin_amdgcn_permlane16_swap(cvt_pk_bf16(c0[2], c0[3]), cvt_pk_bf16(c1[2], c1[3]), false, false); \
            const int sg_ = 4 * ai + 2 * (int)(wu >> 2) + (m >> 1); \
            st_sc1s(rsC, vo_, ((ai ? ((cb) & ~1) ^ 8192 : (cb) & 1 ? 1 << 30 : (cb)) ^ ((sg_ & SWZ) << 15)) + sg_ * 524288 + bj * 16384 + (m & 1) * 4096, v4u{r0[0], r1[0], r0[1], r1[1]}); \
            acc[ai][bj][m][0] = ZERO4; acc[ai][bj][m][1] = ZERO4; } } while (0)
    constexpr int nt = K / 64;
    unsigned cur = (unsigned)todo & 255u, prev = 0u;
    const char* pA = a_of(cur); const char* pB = b_of(cur);
    stB(0, 0, pB); stB(0, 1, pB); stA(0, 0, pA); stA(0, 1, pA);
    stB(1, 0, pB + SB); stB(1, 1, pB + SB); stA(1, 0, pA + SA);
    K8_WV(8); K8_BAR;
    if (wu >= 4) K8_BAR;
#define K8_GO(ai) __builtin_amdgcn_s_waitcnt(0xC07F); K8_BAR; K8_MMA(ai, 0, B0); K8_MMA(ai, 1, B1); K8_SCHED; K8_BAR
    int cbp = 0;
    for (;;) {
        todo >>= 8;
        const unsigned nxt = todo != 0ull ? (unsigned)todo & 255u : cur;
        int cb = c_of(cur);
#pragma nounroll
        for (int t = 0; t < nt; t += 2) {
            if (!(TC0 & 1) && __builtin_expect(early && prev == 0u && t == TXT, 0))
                touch((const char*)X + 3 * 524288 + (size_t)(nw * 320 + min(TIDX, 319)) * 128, lds0 + 163840 - 256);
            if (__builtin_expect(early && prev == 0u && t == nt - 4, 0)) {
                tau = __builtin_amdgcn_readfirstlane((unsigned)boot_wait(a.boot) & 255u);
                cb = c_of(cur);
            }
            const bool first = t == 0 && prev != 0u, last = t == nt - 2;
            if (t == 2 && prev != 0u) { publish(prev); WEV(3) }
            K8_LDB(B0, 0, 0); K8_LDA(0, 0); K8_LDB(B1, 0, 1); K8_SCHED; stA(1, 1, pA + SA);
            pA = last ? a_of(nxt) : pA + 2 * SA; pB = last ? b_of(nxt) : pB + 2 * SB;
            if (first) { K8_EPI(1, 0, cbp); K8_EPI(1, 1, cbp); K8_WV(24); } else K8_WV(8);
            K8_GO(0);
            K8_LDA(0, 1); K8_SCHED; stB(0, 0, pB); stB(0, 1, pB); stA(0, 0, pA);
            if (first) K8_WV(24); else K8_WV(8);
            K8_GO(1);
            K8_LDB(B0, 1, 0); K8_LDA(1, 0); K8_LDB(B1, 1, 1); K8_SCHED; stA(0, 1, pA);
            if (first) K8_WV(16); else K8_WV(8);
            K8_GO(0);
            K8_LDA(1, 1); K8_SCHED; stB(1, 0, pB + SB); stB(1, 1, pB + SB); stA(1, 0, pA + SA);
            if (last) { K8_EPI(0, 0, cb); K8_EPI(0, 1, cb); K8_WV(16); } else K8_WV(8);
            K8_GO(1);
        }
        WEV(2)
        prev = cur; cbp = cb; cur = nxt;
        if (todo == 0ull) break;
    }
    K8_EPI(1, 0, cbp); K8_EPI(1, 1, cbp);
    K8_WV(0);
    if (wu < 4) K8_BAR;
    K8_BAR;
    publish(prev);
    WEV(3)
}

__device__ __forceinline__ void oproj_squad(const ChainArgs& a, u16* out, const int ct, const int rh, const int xc, const unsigned tau, char* smem) {
    const int tid = opaque_tid(), w = tid >> 6, l = tid & 63, i = l & 15, g = l >> 4;
    const int wu = __builtin_amdgcn_readfirstlane(w);
    bf16x8 wf[2][16];
    {
        u16* stg = (u16*)(smem + wu * 10240);
#pragma unroll
        for (int ch = 0; ch < 4; ++ch) {
            const u16* src = a.Wo + (size_t)hcol(4 * wu + ch) * DM + 32 * ct;
            v4u raw[8];
#pragma unroll
            for (int e = 0; e < 8; ++e) { int idx = l + 64 * e, r = idx >> 2, q4 = idx & 3; raw[e] = *(const v4u*)(src + (size_t)r * DM + 8 * q4); }
#pragma unroll
            for (int e = 0; e < 8; ++e) { int idx = l + 64 * e, r = idx >> 2, q4 = idx & 3; *(v4u*)(stg + r * 40 + 8 * q4) = raw[e]; }
            __builtin_amdgcn_s_waitcnt(0xC07F);
#pragma unroll
            for (int jj = 0; jj < 2; ++jj)
#pragma unroll
                for (int ks = 0; ks < 4; ++ks) {
                    const u16* p = stg + (32 * ks + 8 * g + (i >> 2)) * 40 + 16 * jj + 4 * (i & 3);
                    wf[jj][4 * ch + ks] = tr_frag(p, p + 4 * 40);
                }
            __builtin_amdgcn_s_waitcnt(0xC07F);
        }
    }
    const rsrc_t rsO = __builtin_amdgcn_make_buffer_rsrc((void*)(arena(a.slots) + VX_OFF), 0, 32 << 20, 0x00020000);
    float* red = (float*)(smem + 81920);
    const int voff = rh * 4096 + l * 16;
    volatile unsigned* rdy = (volatile unsigned*)(smem + 81920 + 2 * 8 * 16 * 36 * 4);
    unsigned ready = 0;
    bool pre = false;
    v4u xf[16];
    for (int s = SSEG; s < NSEG - 1; ++s) {
        if (!(ready & 1u)) {
            if (wu == 0) {
                const unsigned* q = a.sync + (SY_OXS + (s - SSEG) * 8 + xc) * 32;
#pragma nounroll
                for (int spin = 0;; ++spin) {
                    asm volatile("" ::: "memory");
                    if (ld_flag(q) != 0u) break;
                    if (spin > 20000) { __hip_atomic_store((gu32*)a.tmo, 7000u, __ATOMIC_RELAXED, __HIP_MEMORY_SCOPE_AGENT); break; }
                    if (s == SSEG) __builtin_amdgcn_s_sleep(64); else if (s < NSEG - 5) __builtin_amdgcn_s_sleep(16); else __builtin_amdgcn_s_sleep(SQ_SF);
                }
            }
            asm volatile("" ::: "memory");
            __builtin_amdgcn_s_barrier();
            asm volatile("" ::: "memory");
        }
        if (!pre) {
#pragma unroll
            for (int ks = 0; ks < 16; ++ks) xf[ks] = ld_sc1s(rsO, voff + (ks & 3) * 1024, blk_off(s, 4 * wu + (ks >> 2), tau) ^ 8192);
        }
        unsigned nf = 0;
        if (wu == 0) nf = ld_flag(a.sync + (SY_OXS + (s + 1 - SSEG + (l & 1)) * 8 + xc) * 32);
        pre = (ready & 2u) != 0u;
        f32x4 acc[2] = {};
        if (pre) {
#pragma unroll
            for (int ks = 0; ks < 16; ++ks) {
#pragma unroll
                for (int jj = 0; jj < 2; ++jj) acc[jj] = __builtin_amdgcn_mfma_f32_16x16x32_bf16(wf[jj][ks], as_frag(xf[ks]), acc[jj], 0, 0, 0);
                K8_SCHED;
                xf[ks] = ld_sc1s(rsO, voff + (ks & 3) * 1024, blk_off(s + 1, 4 * wu + (ks >> 2), tau) ^ 8192);
                K8_SCHED;
            }
        } else {
#pragma unroll
            for (int ks = 0; ks < 16; ++ks)
#pragma unroll
                for (int jj = 0; jj < 2; ++jj) acc[jj] = __builtin_amdgcn_mfma_f32_16x16x32_bf16(wf[jj][ks], as_frag(xf[ks]), acc[jj], 0, 0, 0);
        }
        float* rp = red + (s & 1) * (8 * 16 * 36);
#pragma unroll
        for (int jj = 0; jj < 2; ++jj) *(f32x4*)&rp[(wu * 16 + i) * 36 + 16 * jj + 4 * g] = acc[jj];
        if (wu == 0) rdy[s & 1] = (unsigned)__ballot(nf != 0u) & 3u;
        K8_WL(0); K8_BAR;
        ready = __builtin_amdgcn_readfirstlane(rdy[s & 1]) | (pre ? 1u : 0u);
        if (tid >= 64 && tid < 192) {
            const int t = tid - 64, row = t >> 3, q4 = t & 7;
            f32x4 sv_[8];
#pragma unroll
            for (int e = 0; e < 8; ++e) sv_[e] = *(const f32x4*)&rp[(e * 16 + row) * 36 + 4 * q4];
            const f32x4 s0 = ((sv_[0] + sv_[1]) + (sv_[2] + sv_[3])) + ((sv_[4] + sv_[5]) + (sv_[6] + sv_[7]));
            v2u o; o[0] = cvt_pk_bf16(s0[0], s0[1]); o[1] = cvt_pk_bf16(s0[2], s0[3]);
            out8(out + (size_t)(s * SEG + 16 * rh + row) * OUTW + 32 * ct + 4 * q4, o);
        }
    }
}

__device__ __forceinline__ void kv_stream(const ChainArgs& a, const u16* __restrict__ X, const u16* __restrict__ B, char* C, const int head, const int xc, const int wk, unsigned& tau, char* smem) {
    const int tid = opaque_tid(), w = tid >> 6, l = tid & 63, i = l & 15, g = l >> 4;
    const int wu = __builtin_amdgcn_readfirstlane(w), kh = wu >> 2, cq = wu & 3;
    const unsigned lds0 = (unsigned)(uintptr_t)(__attribute__((address_space(3))) char*)smem;
    unsigned voA, voB;
    { const int row = (wu & 1) * 8 + (l >> 3), c = l & 7; voA = (unsigned)(row * DM + 8 * (c ^ (row & 7))) * 2u + (wu >> 1) * 512; }
    { const int row = tid >> 4, c = tid & 15; voB = (unsigned)(row * HD + 8 * (c ^ (bswz2<128>(row) << 1))) * 2u; }
    auto stB = [&](int ch, int slot) {
#pragma unroll
        for (int e = 0; e < 4; ++e) glds16s(voB, (const char*)B + (size_t)(4 * ch + e) * 32 * HD * 2, lds0 + 65536 + slot * 32768 + e * 8192 + wu * 1024);
    };
    const unsigned ldA = lds0 + (wu >> 1) * 8192 + (wu & 1) * 1024;
    auto stP = [&](const char* p, const unsigned d) {
        unsigned keep;
        asm volatile("s_mov_b32 %0, m0\n\ts_mov_b32 m0, %1\n\ts_nop 0\n\tglobal_load_lds_dwordx4 %3, %2 offset:-4096\n\tglobal_load_lds_dwordx4 %4, %2 offset:-2048\n\t"
                     "global_load_lds_dwordx4 %5, %2\n\tglobal_load_lds_dwordx4 %6, %2 offset:2048\n\ts_mov_b32 m0, %0"
                     : "=&s"(keep) : "s"(d), "s"(p), "v"(voA + 8192u), "v"(voA + 6272u), "v"(voA + 4352u), "v"(voA + 2432u) : "memory");
    };
    auto srcA = [&](int h) { return (const char*)X + (size_t)min(h, 2 * KVS - 1) * 32768 - 4096; };
    auto stA = [&](int h) { stP(srcA(h), ldA + (h & 3) * 32768 + 4096); };
    stB(0, 0); stA(0); stA(1); stB(1, 1);
    touch((const char*)X + 65536 + min(wk * 512 + tid, (KVS - 1) * 512 - 1) * 128, lds0 + 131072 + wu * 1024);
    if (KVS > 9) touch((const char*)X + 65536 + min(4096 + wk * 512 + tid, (KVS - 1) * 512 - 1) * 128, lds0 + 131072 + wu * 1024);
    stB(2, 2);
    bf16x8 bf[16][2], af[8];
    f32x4 acc[2][2] = {}, keep[2];
    const int rb = 64 * kh + 8 * g + (i >> 2), sw = bswz2<128>(rb);
    const char* bp = smem + 65536 + rb * 256 + 8 * (i & 3);
    const char* sa0 = smem + kh * 2048 + i * 128 + 16 * (g ^ (i & 7));
    const char* sa1 = smem + kh * 2048 + i * 128 + 16 * ((4 + g) ^ (i & 7));
#define KV_AF(slot, q) *(const bf16x8*)((((q) & 1) ? sa1 : sa0) + (slot) * 32768 + ((q) >> 1) * 4096)
#pragma unroll
    for (int ch = 0; ch < 8; ++ch) {
        if (ch == 0) { if (KVS > 9) K8_WV(10); else K8_WV(9); } else if (ch < 6) K8_WV(8); else if (ch == 6) K8_WV(4); else K8_WV(0);
        K8_BAR;
#pragma unroll
        for (int e = 0; e < 4; ++e) { const char* p_ = bp + (ch % 3) * 32768 + (e >> 1) * 8192 + 32 * ((2 * cq + (e & 1)) ^ sw); bf[2 * ch + (e >> 1)][e & 1] = tr_frag(p_, p_ + 1024); }
#pragma unroll
        for (int e = 0; e < 4; ++e) af[e] = KV_AF(e >> 1, 2 * ch + (e & 1));
        K8_WL(0); K8_SCHED; K8_BAR;
        if (ch < 5) stB(ch + 3, ch % 3);
        if (ch == 7) { stA(2); stA(3); }
#pragma unroll
        for (int e = 0; e < 8; ++e) acc[e >> 2][e & 1] = __builtin_amdgcn_mfma_f32_16x16x32_bf16(bf[2 * ch + ((e >> 1) & 1)][e & 1], af[e >> 1], acc[e >> 2][e & 1], 0, 0, 0);
    }
    float* red = (float*)(smem + 131072) + l * 4;
#pragma unroll
    for (int p = 0; p < 2; ++p) { *(f32x4*)(red + (p * 8 + wu) * 256) = kh ? acc[p][0] : acc[p][1]; keep[p] = kh ? acc[p][1] : acc[p][0]; }
    unsigned long long tag = 0;
    if (wu == 4) { tag = boot_wait(a.boot); *(volatile unsigned*)(smem + 163840 - 512) = (unsigned)tag & 255u; }
    const rsrc_t rc = __builtin_amdgcn_make_buffer_rsrc((void*)C, 0, KVS * 524288, 0x00020000);
    const int vo = cq * 1024 + (2 * kh + (g >> 1)) * 256 + i * 16 + (g & 1) * 8;
    auto put = [&](const f32x4 o, const int so) {
        const v2u pk = {cvt_pk_bf16(o[0], o[1]), cvt_pk_bf16(o[2], o[3])};
        asm volatile("buffer_store_dwordx2 %0, %1, %2, %3 offen sc1" :: "v"(pk), "v"(vo), "s"(rc), "s"(so) : "memory");
    };
    K8_WL(0); K8_WV(0); K8_BAR;
    tau = __builtin_amdgcn_readfirstlane(*(volatile unsigned*)(smem + 163840 - 512));
#pragma unroll
    for (int p = 0; p < 2; ++p) put(keep[p] + *(const f32x4*)(red + (p * 8 + (wu ^ 4)) * 256), blk_off(0, head, tau) + p * 4096);
    stA(4);
    K8_WV(4); K8_BAR;
    if (tid == 256) __hip_atomic_store((gu64*)(a.boot + 256 + 16 * xc + wk), tag, __ATOMIC_RELAXED, __HIP_MEMORY_SCOPE_AGENT);
    auto stages = [&](auto kc) {
        constexpr int KH = decltype(kc)::value;
        if (KH) K8_BAR;
        f32x4 ac[2] = {ZERO4, ZERO4}, kp = ZERO4;
        int h_first = 2, so = OOB;
        asm volatile("" : "+s"(h_first));
        const char* pn = srcA(5);
        unsigned dn = ldA + 32768 + 4096;
        int r0 = (int)(sa0 - smem) + 2 * 32768, r1 = (int)(sa1 - smem) + 2 * 32768;
        for (int h = h_first; h < 2 * KVS + 4; ++h) {
            const f32x4 oth = *(const f32x4*)(red + (wu ^ 4) * 256);
            *(f32x4*)(red + wu * 256) = ac[KH ^ 1];
            bf16x8 ax[16];
#pragma unroll
            for (int q = 0; q < 16; ++q) ax[q] = *(const bf16x8*)(smem + ((q & 1) ? r1 : r0) + (q >> 1) * 4096);
            K8_SCHED;
            stP(pn, dn);
            put((KH ? ac[1] : kp) + oth, so);
            kp = ac[0];
            __builtin_amdgcn_s_waitcnt(0xC07F);
            K8_WV(10);
            if (__builtin_expect((STRICT >> h) & 1u, 0)) K8_WV(0);
            K8_BAR;
            if (KH && ((STRICT | (0xAAA80u & ~(STRICT << 2))) >> h) & 1u && tid == 256)
                __hip_atomic_store((gu32*)(a.sync + (SY_KV0 + (h / 2 - ((STRICT >> h) & 1u ? 1 : 2)) * 8 + xc) * 32 + wk), 16u, __ATOMIC_RELAXED, __HIP_MEMORY_SCOPE_AGENT);
            if (__builtin_expect(h < 2 * KVS, 1)) {
                __builtin_amdgcn_s_setprio(1);
#pragma unroll
                for (int q = 0; q < 16; ++q)
#pragma unroll
                    for (int n = 0; n < 2; ++n) ac[n] = __builtin_amdgcn_mfma_f32_16x16x32_bf16(bf[q][n], ax[q], q ? ac[n] : ZERO4, 0, 0, 0);
                __builtin_amdgcn_s_setprio(0);
            }
            {
                const int hs = h - 1 + KH;
                so = blk_off(hs >> 1, head, tau) + (hs & 1) * 4096;
                asm volatile("" : "+s"(so));
                so = hs > 1 && hs < 2 * KVS ? so : OOB;
                pn = srcA(h + 4); dn = ldA + (h & 3) * 32768 + 4096;
                r0 = (r0 + 32768) & 131071; r1 = (r1 + 32768) & 131071;
                asm volatile("" : "+s"(so), "+s"(pn), "+s"(dn), "+v"(r0), "+v"(r1));
            }
            K8_BAR;
        }
        if (!KH) K8_BAR;
    };
    if (kh) stages(std::integral_constant<int, 1>{}); else stages(std::integral_constant<int, 0>{});
    K8_WV(0);
}

struct MegaArgs {
    ChainArgs ch;
    const u16* x; u16* out;
    const u16* bx; const u16* wr; const u16* wi;
};
constexpr int MEGA_LDS = 163840;
#define WSTAMP(n)
__global__ __launch_bounds__(512, 2) void mega_kernel(MegaArgs m) {
    extern __shared__ __attribute__((aligned(256))) char chsm[];
    const int b = BIDX, xc = (b & 3) + 4 * (b >> 7), sl = (b >> 3) & 15;
    if ((b & 4) == CHS) { chain_body(m.ch, xc, sl, chsm); return; }
    const ChainArgs& a = m.ch;
    const int wk = sl, tid = TIDX;
    char* ar = arena(a.slots);
    unsigned tauw = 0;
    WSTAMP(0)
    if (wk < 8) kv_stream(a, m.x, (wk < 4 ? a.Wk : a.Wv) + hcol(4 * xc + (wk & 3)), ar + (wk < 4 ? KX_OFF : VX_OFF), 4 * xc + (wk & 3), xc, wk, tauw, chsm);
    if (TCH && wk < 8) {
        const int t = opaque_tid(), n = xc * 8 + wk;
        const unsigned sp = (unsigned)(uintptr_t)(__attribute__((address_space(3))) char*)chsm + 163840 - 256;
        touch((const char*)m.x + 3 * 524288 + (n * 320 + min(t, 319)) * 128, sp);
        touch((const char*)m.bx + (n * 512 + t) * 128, sp);
        touch((const char*)(t < 256 ? m.wr : m.wi) + (n * 256 + (t & 255)) * 128, sp);
    }
    WSTAMP(8) WEV(1)
    {
        const int p = wk & 1, q = wk & 3;
        const unsigned long long todo = wk < 2 ? 0x84u + p : wk < 4 ? TD23 + 0x01010101u * p : wk < 8 ? 0ull
                                      : wk < 12 ? (wk < 10 ? TD89 : 0x7060503010ull) + (wk < 10 ? TM89 : 0x0101010101ull) * q : (wk < 14 ? 0x54341420u : 0x64442422u) + 0x01010101u * p;
        __syncthreads();
        if (wk >= 12) {
            const int n = (xc * 4 + q) * 512 + opaque_tid();
            const unsigned sp = (unsigned)(uintptr_t)(__attribute__((address_space(3))) char*)chsm + 163840 - 256;
            if (TC0 & 1) { touch((const char*)m.x + 3 * 524288 + n * 128, sp); touch((const char*)m.x + 3 * 524288 + min(16384 + n, 20479) * 128, sp); }
            if (TC0 & 2) { touch((const char*)m.bx + n * 128, sp); touch((const char*)m.bx + (16384 + n) * 128, sp); }
            if (TC0 & 4) { touch((const char*)m.wr + n * 128, sp); touch((const char*)m.wi + n * 128, sp); }
        }
        if (todo != 0ull) tile_stream(a, m.x, todo, xc, tauw, wk >= 8, xc * 8 + (wk & 7), (u16*)chsm WEVA);
    }
    WSTAMP(2)
    if (wk < 2) {
        unsigned* mbox = (unsigned*)(chsm + 131072);
        if (tid < 5) mbox[tid] = tid ? 2u : 0u;
        __syncthreads();
        const int w = __builtin_amdgcn_readfirstlane(tid >> 6), h = 4 * xc + (w & 3);
        const rsrc_t rsQ = __builtin_amdgcn_make_buffer_rsrc((void*)(ar + KX_OFF), 0, S_LEN * HD * 4, 0x00020000);
        const rsrc_t rsK = __builtin_amdgcn_make_buffer_rsrc((void*)(ar + KX_OFF), 0, S_LEN * HD * 4, 0x00020000);
        const rsrc_t rsV = __builtin_amdgcn_make_buffer_rsrc((void*)(ar + VX_OFF), 0, S_LEN * HD * 4, 0x00020000);
        const rsrc_t rsS = slot_rsrc(a.slots);
        u16 (*Vs)[VP] = (u16 (*)[VP])(chsm + w * (40 * VP * 2));
        const unsigned tau = tauw;
        int pend = -1;
        bool ready = false;
        for (int s = 2 * wk + (w >> 2), sn; s < NSEG - 1; s = sn) {
            const int band = s >> 3;
            unsigned n = 0;
            if (lane_now() == 0) n = atomicAdd(mbox + 1 + (w & 3), 1u);
            n = __builtin_amdgcn_readfirstlane(n);
            sn = 2 * wk + (int)(n & 1u) + 4 * (int)(n >> 1);
            if (sn - s > 4) __builtin_amdgcn_s_setprio(2); else __builtin_amdgcn_s_setprio(0);
            if (s < 4) wait_ge<32>(a.sync + (SY_KV0 + (KVS - 1) * 8 + xc) * 32 + (tid & 7), 16u, a.tmo, 4100u);
            if (!ready) {
                if (band > 0) wait_ge<32>(a.sync + (SY_KV + band * 8 + xc) * 32 + (tid & 3), 1u, a.tmo, 4100u);
                wait_ge<32>(a.sync + (SY_Q + band * 8 + xc) * 32 + (tid & 3), 1u, a.tmo, 4200u);
            }
            xattn_unit(a, rsQ, rsK, rsV, rsS, s, sn, h, Vs, mbox, tau, pend, ready);
        }
        return;
    }
    if (wk >= 4 && wk < 8) {
        const int fw = (wk - 4) * 8 + xc;
        const rsrc_t rsF = __builtin_amdgcn_make_buffer_rsrc((void*)(ar + FX_OFF), 0, 16 << 20, 0x00020000);
        const rsrc_t rsW = __builtin_amdgcn_make_buffer_rsrc((void*)(ar + WT_OFF), 0, 10 << 20, 0x00020000);
        const unsigned tw = tauw ^ FNEAR;
        {
            const int tq = opaque_tid();
            for (int z = 0; z < FSR; ++z) if (TIDX >= 256) __builtin_amdgcn_s_barrier();
            fft_rows_fwd(m.bx, rsF, tw, 2 * fw + (tq >> 8), chsm + (tq >> 8) * 36864, tq & 255, (const char*)m.wr, (const char*)m.wi, (unsigned)(uintptr_t)(__attribute__((address_space(3))) char*)chsm + 163840 - 256);
            for (int z = 0; z < FSR; ++z) if (TIDX < 256) __builtin_amdgcn_s_barrier();
        }
        for (int t = fw; t < 64; t += 32) {
            __syncthreads();
            filt_transpose(m.wr, m.wi, rsW, tw, t, chsm, opaque_tid());
        }
        wg_publish(a.sync, SY_DONE, 32u, 8);
        WSTAMP(3) WEV(4)
        wg_wait_acquire<16>(a.sync + (SY_DONE + xc) * 32, a.tmo, 6000u);
        {
            const int tq = opaque_tid();
            for (int z = 0; z < fw; ++z) __builtin_amdgcn_s_sleep(FST);
            for (int z = 0; z < FSG; ++z) if (TIDX >= 256) __builtin_amdgcn_s_barrier();
            fft_cols(rsF, rsW, tw, 2 * fw + (tq >> 8), 4, chsm + (tq >> 8) * 36864, tq & 255);
            for (int z = 0; z < FSG; ++z) if (TIDX < 256) __builtin_amdgcn_s_barrier();
        }
        wg_publish(a.sync, SY_DONE + 8, 32u, 8);
        WSTAMP(4) WEV(5)
        wg_wait_acquire<16>(a.sync + (SY_DONE + 8 + xc) * 32, a.tmo, 6001u);
        {
            const int tq = opaque_tid();
            for (int z = 0; z < FSR; ++z) if (TIDX >= 256) __builtin_amdgcn_s_barrier();
            fft_rows_inv(rsF, tw, m.out, 2 * fw + (tq >> 8), chsm + (tq >> 8) * 36864, tq & 255);
            for (int z = 0; z < FSR; ++z) if (TIDX < 256) __builtin_amdgcn_s_barrier();
        }
        WSTAMP(5) WEV(6)
    }
    if (wk >= 12) {
        const int r = 2 * (wk - 12);
        wg_wait_acquire<16>(a.sync + (SY_OX + r * 8 + xc) * 32, a.tmo, 5000u);
        wg_wait_acquire<16>(a.sync + (SY_OX + (r + 1) * 8 + xc) * 32, a.tmo, 5000u);
        WEV(7)
        gemm_tile<256>((u16*)chsm, ar + VX_OFF, 4 * r, tauw, a.Wo + xc * 128, m.out + (size_t)r * 128 * OUTW + xc * 128);
        WEV(8)
    }
    if (wk < 4) {
        const int r = wk + 6;
        wg_wait_acquire<16>(a.sync + (SY_OX + r * 8 + xc) * 32, a.tmo, 5000u);
        WEV(7)
        gemm_tile<128>((u16*)chsm, ar + VX_OFF, 4 * r, tauw, a.Wo + xc * 128, m.out + (size_t)r * 128 * OUTW + xc * 128);
        WEV(8)
        WSTAMP(9)
    }
    if (wk >= 4 && wk < 12) {
        __syncthreads();
        oproj_squad(a, m.out, (wk - 4) * 4 + (xc & 3), xc >> 2, xc, tauw, chsm);
        WSTAMP(7)
    }
}

#if defined(__HIP_DEVICE_COMPILE__)
#pragma clang attribute pop
#endif
extern "C" void kernel_launch(void* const* d_in, const int* in_sizes, int n_in,
                              void* d_out, int out_size, void* d_ws, size_t ws_size,
                              hipStream_t stream) {
    char* ws = (char*)d_ws;
    size_t off = 0;
    auto alloc = [&](size_t bytes) { void* p = ws + off; off += (bytes + 255) & ~(size_t)255; return p; };
    char* slots = (char*)alloc((size_t)ARENA_BYTES + (2u << 20));
    static_assert((SY_WORDS + 2) * 128 <= 131072, "sync block");
    unsigned* sync = (unsigned*)alloc(131072);
    unsigned long long* boot = (unsigned long long*)alloc(4096);

    MegaArgs m;
    m.ch.mem0 = (const u16*)d_in[1]; m.ch.Wq = (const u16*)d_in[2]; m.ch.Wk = (const u16*)d_in[3]; m.ch.Wv = (const u16*)d_in[4]; m.ch.Wo = (const u16*)d_in[5];
    m.ch.out = (u16*)d_out; m.ch.slots = slots; m.ch.tmo = sync + SY_WORDS * 32;
    m.ch.stamps = (unsigned long long*)(ws + (240ull << 20));
    m.ch.sync = sync; m.ch.boot = boot;
    m.x = (const u16*)d_in[0]; m.out = (u16*)d_out;
    m.bx = (const u16*)d_in[7]; m.wr = (const u16*)d_in[8]; m.wi = (const u16*)d_in[9];
    (void)hipFuncSetAttribute((const void*)mega_kernel, hipFuncAttributeMaxDynamicSharedMemorySize, MEGA_LDS);
    mega_kernel<<<256, 512, MEGA_LDS, stream>>>(m);
}
```

```cpp
#if defined(__HIP_DEVICE_COMPILE__)
#pragma clang attribute push (__attribute__((target("no-packed-fp32-ops"))), apply_to = function)
#endif
#include <hip/hip_runtime.h>
#include <hip/hip_bf16.h>
#include <stdint.h>
#include <type_traits>

typedef unsigned short u16;
#define TIDX ((int)__builtin_amdgcn_workitem_id_x())
#define BIDX ((int)__builtin_amdgcn_workgroup_id_x())
__device__ __forceinline__ int opaque_tid() { int t = TIDX; asm volatile("" : "+v"(t)); return t; }
__device__ __forceinline__ int lane_now() { int t; asm volatile("v_mbcnt_lo_u32_b32 %0, -1, 0\n\tv_mbcnt_hi_u32_b32 %0, -1, %0" : "=v"(t)); return t; }
typedef __attribute__((ext_vector_type(8))) short bf16x8;
typedef __attribute__((ext_vector_type(4))) float f32x4;

constexpr int S_LEN = 2048, DM = 1024, NH = 32, DH = 128, MM = 8, SEG = 32;
constexpr int NSEG = S_LEN / SEG;
constexpr int HD = NH * DH;
constexpr int OUTW = 2048;

typedef unsigned v4u __attribute__((ext_vector_type(4)));
typedef unsigned v2u __attribute__((ext_vector_type(2)));
typedef short v4i16 __attribute__((ext_vector_type(4)));
typedef __amdgpu_buffer_rsrc_t rsrc_t;
#define AUX_SC1 16
__device__ __forceinline__ v4u ld_sc1s(rsrc_t rs, int voff, int soff) { return __builtin_amdgcn_raw_buffer_load_b128(rs, voff, soff, AUX_SC1); }
__device__ __forceinline__ void st_sc1s(rsrc_t rs, int voff, int soff, v4u v) { __builtin_amdgcn_raw_buffer_store_b128(v, rs, voff, soff, AUX_SC1); asm volatile("s_nop 1" :: "v"(v) : "memory"); }
__device__ __forceinline__ void st8_sc1s(rsrc_t rs, int voff, int soff, v2u v) { __builtin_amdgcn_raw_buffer_store_b64(v, rs, voff, soff, AUX_SC1); }
__device__ __forceinline__ void st_sc1(rsrc_t rs, int off, v4u v) { __builtin_amdgcn_raw_buffer_store_b128(v, rs, off, 0, AUX_SC1); }
__device__ __forceinline__ void out8(void* p, v2u v) { asm volatile("global_store_dwordx2 %0, %1, off sc1" :: "v"(p), "v"(v) : "memory"); }
typedef __bf16 bf16x2_t __attribute__((ext_vector_type(2)));
typedef float f32x2_t __attribute__((ext_vector_type(2)));
__device__ __forceinline__ unsigned cvt_pk_bf16(float lo, float hi) {
    f32x2_t f = {lo, hi};
    return __builtin_bit_cast(unsigned, __builtin_convertvector(f, bf16x2_t));
}
#define SENT 0xFFBFFFBF
__device__ __forceinline__ unsigned umax3(unsigned a, unsigned b, unsigned c) { return max(max(a, b), c); }
__device__ __forceinline__ unsigned mx4(unsigned acc, v4u v) { return umax3(umax3(acc, v[0], v[1]), v[2], v[3]); }
__device__ __forceinline__ bf16x8 as_frag(v4u v) { return __builtin_bit_cast(bf16x8, v); }
__device__ __forceinline__ float xor16_sum(float x) {
    auto r = __builtin_amdgcn_permlane16_swap(__float_as_uint(x), __float_as_uint(x), false, false);
    return __uint_as_float(r[0]) + __uint_as_float(r[1]);
}
__device__ __forceinline__ float xor32_sum(float x) {
    auto r = __builtin_amdgcn_permlane32_swap(__float_as_uint(x), __float_as_uint(x), false, false);
    return __uint_as_float(r[0]) + __uint_as_float(r[1]);
}
__device__ __forceinline__ float xor16_max(float x) {
    auto r = __builtin_amdgcn_permlane16_swap(__float_as_uint(x), __float_as_uint(x), false, false);
    return fmaxf(__uint_as_float(r[0]), __uint_as_float(r[1]));
}
__device__ __forceinline__ float xor32_max(float x) {
    auto r = __builtin_amdgcn_permlane32_swap(__float_as_uint(x), __float_as_uint(x), false, false);
    return fmaxf(__uint_as_float(r[0]), __uint_as_float(r[1]));
}
constexpr int SPIN_LIMIT = 400000;
constexpr int OOB = 0x7FFFFF00;
#define PV1 2
#define PV2 11
#define LW 0
#define PVA 8
#define PL 20
#define PF 48
#define CHS 0
#define DLY_U 4
#define SWZ 0
#define FROT 4224
#define WROT 384
#define FNEAR 1u
#define FST 0
#define PG0 2
#define PG1 32
#define PGP 0
#define FTR 1
#define FSG 0
#define FSR 0
#define PB 14
#define TD2 60
#define SL0 44
#define PS 46
#define TE0 10
#define TD 60
#define PV 1
#define PC 31
#define PP 15
#define PK0 34
#define PKP 1
#define PKM 1
#define PKA 28
#define PXD 1
constexpr int NPROBE = 2, DLY_L = 7, DLY_C = 5;
constexpr int M_OFF = 32 * 16384, F_OFF = M_OFF + 2 * 16384, C_OFF = F_OFF + 2 * 16384, REC = C_OFF + 16 * 16384, PERWG = REC / 128, SLOT_BYTES = 34 * 2097152;
static_assert(PERWG == 6656, "arming shares");
__device__ __forceinline__ int rec_base(int s) { return (s >> 1) * 2097152 + (s & 1) * REC; }
__device__ __forceinline__ char* arena(char* slots) { return (char*)(((uintptr_t)slots + 2097151u) & ~(uintptr_t)2097151u); }
constexpr int KX_OFF = SLOT_BYTES, VX_OFF = KX_OFF + (32 << 20), FX_OFF = VX_OFF + (32 << 20), WT_OFF = FX_OFF + (16 << 20), ARENA_BYTES = WT_OFF + (10 << 20);
__device__ __forceinline__ rsrc_t slot_rsrc(char* slots) { return __builtin_amdgcn_make_buffer_rsrc((void*)arena(slots), 0, FX_OFF, 0x00020000); }
__device__ __forceinline__ int blk_off(int seg, int h, unsigned tau) { return seg * 524288 + (((h & 3) >> 1) * 16 + ((h >> 2) ^ (seg & SWZ)) * 2 + (h & 1)) * 16384 + (int)((tau ^ (unsigned)(seg >> 2)) & 1u) * 8192; }
constexpr int TM_OFF = 124 * 16384;
constexpr int CAL_OFF = 32 * 2097152;
__device__ __forceinline__ unsigned half_bits(int s, unsigned tau) { return ((tau ^ (unsigned)(s >> 1)) & 1u) ? 0xFFu : 0u; }
__device__ __forceinline__ int chunk_off(int s, int g, int j, unsigned tau) { return rec_base(s) + (j < 2 ? (g & 3) * 2 + j : 6 + 3 * g + j) * 16384 + (int)(half_bits(s, tau) & 1u) * 8192; }
__device__ __forceinline__ int x_off(int s, int h, unsigned tau) { return chunk_off(s, h >> 2, 2, tau) + (h & 3) * 2048; }
__device__ __forceinline__ int km_off(int s, int h, unsigned tau) { return chunk_off(s, h >> 2, 3, tau) + (h & 3) * 2048; }
__device__ __forceinline__ int m_off(int s, int h, unsigned tau) { return rec_base(s) + M_OFF + (h >> 4) * 16384 + (int)((half_bits(s, tau) >> (h >> 2)) & 1u) * 8192 + (h & 15) * 512; }
__device__ __forceinline__ int v_off(int s, int h, unsigned tau) { return chunk_off(s, h >> 2, 4, tau) + (h & 3) * 2048; }
__device__ __forceinline__ int f_off(int s, int g, unsigned tau) { return rec_base(s) + F_OFF + (g >> 2) * 16384 + (int)((half_bits(s, tau) >> g) & 1u) * 8192 + (g & 3) * 2048; }
__device__ __forceinline__ int hcol(int h) { return ((h & 3) * 8 + (h >> 2)) * 128; }
constexpr int VP = 144;

struct ChainArgs {
    u16* out;
    const u16* mem0; const u16* Wq; const u16* Wk; const u16* Wv; const u16* Wo;
    char* slots;
    unsigned* tmo;
    unsigned long long* stamps;
    unsigned* sync;
    unsigned long long* boot;
};
constexpr int SY_KV = 0;
constexpr int SY_Q = 64;
constexpr int SY_PROG = 128;
constexpr int SY_OX = 136;
constexpr int SY_DONE = 264;
constexpr int SY_OXS = 472;
constexpr int SY_KV0 = 408;
#define TCH 0
#define TXT 10
#define XPR 2
#define F0E 1
#define STRICT 0x20u
#define DLY_T 0
#define TC0 0
#define PLAN 1
#define TD23 0x74704240u
#define TD89 0x60503010ull
#define TM89 0x01010101ull
#define TX0 1
#define KVS 8
constexpr int SY_FLAGS = 680;
constexpr int SY_WORDS = SY_FLAGS + SY_FLAGS / 8;
#define XA_FAST 6
#define XA_D 12
#define SQ_FAST 49
#define SQ_SF 4
#define SSEGV 40
constexpr int SSEG = SSEGV;
typedef __attribute__((address_space(1))) unsigned gu32;
typedef __attribute__((address_space(1))) unsigned long long gu64;
__device__ __forceinline__ unsigned ld_flag(const unsigned* p) { return __hip_atomic_load((gu32*)p, __ATOMIC_RELAXED, __HIP_MEMORY_SCOPE_AGENT); }
__device__ __forceinline__ unsigned long long boot_tag(const unsigned long long* boot) {
    return __hip_atomic_load((gu64*)(boot + 144), __ATOMIC_RELAXED, __HIP_MEMORY_SCOPE_AGENT) + 1ull;
}
__device__ __forceinline__ unsigned long long boot_wait(const unsigned long long* boot) {
    const int l = TIDX & 63;
    unsigned long long tag = 0, x = 0, y = 0;
#pragma nounroll
    for (int spin = 0; spin < 100000; ++spin) {
        asm volatile("" ::: "memory");
        x = __hip_atomic_load((gu64*)(boot + l), __ATOMIC_RELAXED, __HIP_MEMORY_SCOPE_AGENT);
        y = __hip_atomic_load((gu64*)(boot + 64 + l), __ATOMIC_RELAXED, __HIP_MEMORY_SCOPE_AGENT);
        tag = boot_tag(boot);
        if (__all((x | 3u) == ((tag << 2) | 3u) && (y | 3u) == ((tag << 2) | 3u))) break;
        __builtin_amdgcn_s_sleep(16);
    }
    asm volatile("" ::: "memory");
    const unsigned long long side = __ballot(x & 1u);
    const unsigned sd = (unsigned)(side & 1u) | (unsigned)((side >> 15) & 2u) | (unsigned)((side >> 30) & 4u) | (unsigned)((side >> 45) & 8u);
    const unsigned m = (sd ^ (__popcll(__ballot(x & 2u)) + __popcll(__ballot(y & 2u)) > 64 ? 15u : 0u)) & 15u;
    return (tag << 8) | m | ((~m & 15u) << 4);
}
template <int SL>
__device__ __forceinline__ void wait_ge(const unsigned* p, unsigned need, unsigned* tmo, unsigned code) {
#pragma nounroll
    for (int spin = 0;; ++spin) {
        asm volatile("" ::: "memory");
        if (__all(ld_flag(p) >= need)) break;
        if (spin > 400000 / SL) { __hip_atomic_store((gu32*)tmo, code, __ATOMIC_RELAXED, __HIP_MEMORY_SCOPE_AGENT); break; }
        __builtin_amdgcn_s_sleep(SL);
    }
    asm volatile("" ::: "memory");
}
template <int SL>
__device__ __forceinline__ void wg_wait_acquire(const unsigned* p, unsigned* tmo, unsigned code) {
    if (TIDX < 64) {
        wait_ge<SL>(p, 1u, tmo, code);
        __builtin_amdgcn_fence(__ATOMIC_ACQUIRE, "agent");
        asm volatile("s_waitcnt vmcnt(0)" ::: "memory");
    }
    __syncthreads();
}
__device__ __forceinline__ void arrive(unsigned* sync, const int f, const unsigned need, const int n) {
    const int l = lane_now();
    unsigned old = 0;
    if (l == 0) old = __hip_atomic_fetch_add((gu32*)(sync + (SY_FLAGS + (f >> 3)) * 32), 1u, __ATOMIC_RELAXED, __HIP_MEMORY_SCOPE_AGENT);
    if (__builtin_amdgcn_readfirstlane(old) == need - 1u && l < n) __hip_atomic_store((gu32*)(sync + (f + l) * 32), 16u, __ATOMIC_RELAXED, __HIP_MEMORY_SCOPE_AGENT);
}
__device__ __forceinline__ void wg_publish(unsigned* sync, const int f, const unsigned need, const int n) {
    asm volatile("s_waitcnt vmcnt(0)" ::: "memory");
    __syncthreads();
    if (TIDX < 64) arrive(sync, f, need, n);
}
#define TMO_SET(code) { dead = true; if (l == 0) __hip_atomic_store(a.tmo, (code) + s, __ATOMIC_RELAXED, __HIP_MEMORY_SCOPE_AGENT); }

__device__ __forceinline__ void glds16h(unsigned voff, const void* sbase, unsigned lds_dst) {
    unsigned keep;
    asm volatile("s_mov_b32 %0, m0\n\ts_mov_b32 m0, %3\n\ts_nop 0\n\tglobal_load_lds_dwordx4 %1, %2 sc1\n\ts_mov_b32 m0, %0" : "=&s"(keep) : "v"(voff), "s"(sbase), "s"(lds_dst) : "memory");
}
__device__ __forceinline__ void touch(const void* p, unsigned lds_dst) {
    unsigned keep;
    asm volatile("s_mov_b32 %0, m0\n\ts_mov_b32 m0, %2\n\ts_nop 0\n\tglobal_load_lds_dword %1, off\n\ts_mov_b32 m0, %0" : "=&s"(keep) : "v"(p), "s"(lds_dst) : "memory");
}
__device__ __forceinline__ void touch_sc1(const void* p, unsigned lds_dst) {
    unsigned keep;
    asm volatile("s_mov_b32 %0, m0\n\ts_mov_b32 m0, %2\n\ts_nop 0\n\tglobal_load_lds_dword %1, off sc1\n\ts_mov_b32 m0, %0" : "=&s"(keep) : "v"(p), "s"(lds_dst) : "memory");
}
#define WEV(code)
#define WEVP
#define WEVA
constexpr int C2_K = 32768;
constexpr int C2_V = 65536;
constexpr int C2_R1 = 0, R1S = 3200;
constexpr int C2_R3 = C2_R1 + 4 * R1S;
constexpr int C2_MB = 24576;
#define PST(n)
#define LT(n)
#define LTA(n)
#define LTOUT0
#define LTOUT
#define CN(n)
#define CNS
#define CNOUT
#define CLOB16(a, b, c, d, e, f, g, h, i, j, k, l, m, n, o, p) "v" #a, "v" #b, "v" #c, "v" #d, "v" #e, "v" #f, "v" #g, "v" #h, "v" #i, "v" #j, "v" #k, "v" #l, "v" #m, "v" #n, "v" #o, "v" #p
#define LDS3(p) ((__attribute__((address_space(3))) v4i16*)(p))
__device__ __forceinline__ bf16x8 tr_frag(const void* p0, const void* p1) {
    v4i16 lo = __builtin_amdgcn_ds_read_tr16_b64_v4i16(LDS3(p0));
    v4i16 hi = __builtin_amdgcn_ds_read_tr16_b64_v4i16(LDS3(p1));
    return bf16x8{lo[0], lo[1], lo[2], lo[3], hi[0], hi[1], hi[2], hi[3]};
}
typedef __bf16 bf16x8v __attribute__((ext_vector_type(8)));
typedef __bf16 bf16x16v __attribute__((ext_vector_type(16)));
typedef unsigned v8u __attribute__((ext_vector_type(8)));
typedef unsigned v32u __attribute__((ext_vector_type(32)));
#define PUT8(v, o, x) { const v8u x_ = (x); _Pragma("unroll") for (int e_ = 0; e_ < 8; ++e_) (v)[(o) + e_] = x_[e_]; }
#define SUB4(v, o) v4u{(v)[(o)], (v)[(o) + 1], (v)[(o) + 2], (v)[(o) + 3]}
#define PUT4(v, o, x) { const v4u x_ = (x); (v)[(o)] = x_[0]; (v)[(o) + 1] = x_[1]; (v)[(o) + 2] = x_[2]; (v)[(o) + 3] = x_[3]; }
__device__ __forceinline__ v2u tr4(const u16* p) { return __builtin_bit_cast(v2u, __builtin_amdgcn_ds_read_tr16_b64_v4i16(LDS3(p))); }
__device__ __forceinline__ f32x4 smm(v4u a, v8u b, f32x4 c, int idx) {
    return __builtin_amdgcn_smfmac_f32_16x16x64_bf16(__builtin_bit_cast(bf16x8v, a), __builtin_bit_cast(bf16x16v, b), c, idx, 0, 0);
}
__device__ __forceinline__ v8u tr_frag64(const u16* p, const int pitch) {
    const v2u r0 = tr4(p), r1 = tr4(p + 8 * pitch), r2 = tr4(p + 32 * pitch), r3 = tr4(p + 40 * pitch);
    return v8u{r0[0], r1[0], r0[1], r1[1], r2[0], r3[0], r2[1], r3[1]};
}
#define MM(a_, b_, c_) __builtin_amdgcn_mfma_f32_16x16x32_bf16(as_frag(a_), as_frag(b_), c_, 0, 0, 0)
#define STR2(x) #x
#define STR(x) STR2(x)
#define PAUSE(n) ".rept (" STR(n) ") / 4\n\ts_nop 15\n\t.endr\n\t.rept (" STR(n) ") %% 4\n\ts_nop 3\n\t.endr\n\t"
#define ZERO4 f32x4{0.f, 0.f, 0.f, 0.f}
constexpr int XB_OFF = CAL_OFF + 262144, XP = 68;
__device__ __forceinline__ void last_rows(const char* chsm, rsrc_t rsS, const int wu, const int grp, const int c, const unsigned tau) {
    const int l = lane_now();
    __builtin_amdgcn_s_barrier();
    asm volatile("" ::: "memory");
    const float* rp = (const float*)(chsm + C2_K) + (4 * wu + (l >> 4)) * XP + 4 * (l & 15);
    const f32x4 s0 = (*(const f32x4*)rp + *(const f32x4*)(rp + 32 * XP)) + (*(const f32x4*)(rp + 64 * XP) + *(const f32x4*)(rp + 96 * XP));
    v2u o;
    o[0] = cvt_pk_bf16(s0[0], s0[1]); o[1] = cvt_pk_bf16(s0[2], s0[3]);
    st8_sc1s(rsS, l * 8, XB_OFF + ((c >> 1) * 8 + wu) * 16384 + (int)(tau & 1u) * 8192 + (c & 1) * 4096 + grp * 512, o);
}
__device__ __forceinline__ const unsigned* kv_flag(const unsigned* sync, const int sq, const int grp, const int l) { return sync + (sq < KVS ? (SY_KV0 + sq * 8 + grp) * 32 + (l & 7) : (SY_KV + 8 + grp) * 32 + (l & 3)); }
__device__ __forceinline__ void chain_body(const ChainArgs& a, const int grp, const int c, char* chsm) {
    const int wu = __builtin_amdgcn_readfirstlane(TIDX >> 6);
    const int widx = grp * 16 + c;
    const int hs = 4 * grp + (c >> 2), jq = c & 3;
    const rsrc_t rsS = slot_rsrc(a.slots);
    const unsigned l0 = (unsigned)(uintptr_t)(__attribute__((address_space(3))) char*)chsm;
    bool dead = false;

    const int cal_off = CAL_OFF + grp * 32768 + (c >> 3) * 16384 + (c & 7) * 1024;
        if (wu == 0) {
            const int l = lane_now();
            const v4u ff = {SENT, SENT, SENT, SENT};
#pragma unroll
            for (int e = 0; e < 13; ++e) st_sc1s(rsS, l * 16, widx * 2 * PERWG + min(e * 1024, 2 * PERWG - 1024), ff);
            const rsrc_t rsY = __builtin_amdgcn_make_buffer_rsrc((void*)a.sync, 0, 131072, 0x00020000);
            st_sc1s(rsY, l * 16, widx * 1024, v4u{0u, 0u, 0u, 0u});
            __builtin_amdgcn_raw_buffer_store_b32(0u, rsS, (l & 3) * 256 + ((l >> 2) & 1) * 8192, cal_off, AUX_SC1);
            if (c < 2) __builtin_amdgcn_raw_buffer_store_b32(0u, rsS, (l >> 5) * 8192 + (l & 31) * 4, TM_OFF + c * 16384 + grp * 256, AUX_SC1);
        }
    asm volatile("" ::: "memory");
    if (wu >= 4) {
        __builtin_amdgcn_s_sleep(DLY_U);
        const int l = opaque_tid() & 63, i = l & 15, g = l >> 4;
        const int sidx = (i & 1) ? 0xEEEE : 0x4444;
        const int u = wu - 4;
        const int k0 = 256 * u;
        u16* stg = (u16*)(chsm + 40960 + u * 20480);
        v4u af[4];
        v32u wS[6];
        {
            v4u raw[3][16];
            {
                const size_t wo = (size_t)k0 * HD + hcol(hs) + 32 * jq;
                const rsrc_t rq = __builtin_amdgcn_make_buffer_rsrc((void*)(a.Wq + wo), 0, 256 * HD * 2, 0x00020000);
                const rsrc_t rk = __builtin_amdgcn_make_buffer_rsrc((void*)(a.Wk + wo), 0, 256 * HD * 2, 0x00020000);
                const rsrc_t rv = __builtin_amdgcn_make_buffer_rsrc((void*)(a.Wv + wo), 0, 256 * HD * 2, 0x00020000);
                const int vw = (l >> 2) * HD * 2 + (l & 3) * 16;
#pragma unroll
                for (int p = 0; p < 4; ++p) af[p] = *(const v4u*)(a.mem0 + (size_t)(i >> 1) * DM + k0 + 64 * p + 16 * g + 8 * (i & 1));
#pragma unroll
                for (int e = 0; e < 16; ++e) raw[0][e] = __builtin_amdgcn_raw_buffer_load_b128(rq, vw, e * 16 * HD * 2, 0);
#pragma unroll
                for (int e = 0; e < 16; ++e) raw[1][e] = __builtin_amdgcn_raw_buffer_load_b128(rk, vw, e * 16 * HD * 2, 0);
#pragma unroll
                for (int e = 0; e < 12; ++e) raw[2][e] = __builtin_amdgcn_raw_buffer_load_b128(rv, vw, e * 16 * HD * 2, 0);
                __builtin_amdgcn_sched_barrier(0);
#pragma unroll
                for (int e = 0; e < 16; ++e) *(v4u*)(stg + (16 * e + (l >> 2)) * 40 + 8 * (l & 3)) = raw[0][e];
                __builtin_amdgcn_sched_barrier(0);
#pragma unroll
                for (int e = 12; e < 16; ++e) raw[2][e] = __builtin_amdgcn_raw_buffer_load_b128(rv, vw, e * 16 * HD * 2, 0);
            }
            const int fr = 16 * (g >> 1) + 4 * (g & 1) + (i >> 2);
            __builtin_amdgcn_sched_barrier(0);
#pragma unroll
            for (int hx = 0; hx < 3; ++hx) {
                if (hx) {
#pragma unroll
                    for (int e = 0; e < 16; ++e) *(v4u*)(stg + (16 * e + (l >> 2)) * 40 + 8 * (l & 3)) = raw[hx][e];
                }
                __builtin_amdgcn_s_waitcnt(0xC07F);
#pragma unroll
                for (int jj = 0; jj < 2; ++jj)
#pragma unroll
                    for (int p = 0; p < 4; ++p) PUT8(wS[2 * hx + jj], 8 * p, tr_frag64(stg + (64 * p + fr) * 40 + 16 * jj + 4 * (i & 3), 40))
                __builtin_amdgcn_s_waitcnt(0xC07F);
                __builtin_amdgcn_sched_barrier(0);
            }
        }
        PST(u)
        if (u == 0) { PST(4) }
        const unsigned r1w = l0 + C2_R1 + u * R1S + (2 * g * 16 + i) * 4, r1v = l0 + C2_R1 + u * R1S + 2176 + (2 * g * 16 + ((i & 9) | ((i & 2) << 1) | ((i & 4) >> 1))) * 4;
#define LD4(d, va, so, o) "buffer_load_dwordx4 v[" d "], " va ", %[rs], " so " offen offset:" o " sc1\n\t"
#define CHK(a, b, c, d) "v_max3_u32 v244, v244, v" a ", v" b "\n\tv_max3_u32 v244, v244, v" c ", v" d "\n\t"
#define CHK0(a, b, c, d) "v_max_u32 v244, v" a ", v" b "\n\tv_max3_u32 v244, v244, v" c ", v" d "\n\t"
#define SMF(d, a, w) "v_smfmac_f32_16x16x64_bf16 v[" d "], v[" a "], v[" w "], %[sx]\n\t"
#define ADD(d, a, b) "v_add_f32 v" d ", v" a ", v" b "\n\t"
#define Z2(r) "v_mov_b64 v[" r "], 0\n\t"
#define DW2(ad, x, y, o0, o1) "ds_write2_b32 " ad ", v" x ", v" y " offset0:" o0 " offset1:" o1 "\n\t"
#define A0 "224:227"
#define A1 "228:231"
#define A2 "232:235"
#define A3 "236:239"
#define Q0 "200:203"
#define Q1 "204:207"
#define Q2 "208:211"
#define Q3 "212:215"
#define V0 "216:219"
#define V1 "220:223"
#define S1_HEAD \
            Z2("216:217") Z2("218:219") Z2("220:221") Z2("222:223") \
            "s_waitcnt vmcnt(4)\n\t" ZQ \
            "s_waitcnt vmcnt(3)\n\t" SMF(V0, A0, "136:143") CHK0("224", "225", "226", "227") SMF(V1, A0, "168:175") \
            "s_waitcnt vmcnt(2)\n\t" SMF(V0, A1, "144:151") CHK("228", "229", "230", "231") SMF(V1, A1, "176:183") \
            "s_waitcnt vmcnt(1)\n\t" SMF(V0, A2, "152:159") CHK("232", "233", "234", "235") SMF(V1, A2, "184:191") \
            "s_waitcnt vmcnt(0)\n\t" SMF(V0, A3, "160:167") CHK("236", "237", "238", "239") SMF(V1, A3, "192:199") \
            "v_cmp_eq_u32 vcc, " STR(SENT) ", v244\n\t"
#define S1_TAIL(B1, B2) \
            SMF(Q0, A0, "8:15") SMF(Q0, A1, "16:23") SMF(Q0, A2, "24:31") \
            ADD("240", "216", "217") ADD("241", "218", "219") SMF(Q0, A3, "32:39") DW2("%[rv]", "240", "241", "0", "16") \
            ADD("242", "220", "221") ADD("243", "222", "223") SMF(Q1, A0, "40:47") DW2("%[rv]", "242", "243", "128", "144") \
            SMF(Q1, A1, "48:55") SMF(Q1, A2, "56:63") SMF(Q1, A3, "64:71") \
            ADD("240", "200", "201") ADD("241", "202", "203") SMF(Q2, A0, "72:79") DW2("%[rw]", "240", "241", "0", "16") \
            SMF(Q2, A1, "80:87") SMF(Q2, A2, "88:95") "s_nop 1\n\t" \
            ADD("242", "204", "205") ADD("243", "206", "207") SMF(Q2, A3, "96:103") DW2("%[rw]", "242", "243", "136", "152") \
            SMF(Q3, A0, "104:111") SMF(Q3, A1, "112:119") \
            B1        \
            LTA(0) \
            SMF(Q3, A2, "120:127") SMF(Q3, A3, "128:135") "s_nop 0\n\t" \
            ADD("240", "208", "209") ADD("241", "210", "211") DW2("v246", "240", "241", "0", "16") \
            "s_nop 3\n\t" \
            ADD("242", "212", "213") ADD("243", "214", "215") DW2("v246", "242", "243", "136", "152") \
            "s_waitcnt lgkmcnt(0)\n\t" B2 "s_barrier\n\t"
#define ZQ Z2("200:201") Z2("202:203") Z2("204:205") Z2("206:207") Z2("208:209") Z2("210:211") Z2("212:213") Z2("214:215")
#define U_W "{v[8:39]}"(wS[0]), "{v[40:71]}"(wS[1]), "{v[72:103]}"(wS[2]), "{v[104:135]}"(wS[3]), "{v[136:167]}"(wS[4]), "{v[168:199]}"(wS[5])
#define U_CLOB CLOB16(200, 201, 202, 203, 204, 205, 206, 207, 208, 209, 210, 211, 212, 213, 214, 215), CLOB16(216, 217, 218, 219, 220, 221, 222, 223, 224, 225, 226, 227, 228, 229, 230, 231), \
            CLOB16(232, 233, 234, 235, 236, 237, 238, 239, 240, 241, 242, 243, 244, 245, 246, 247)
        __builtin_amdgcn_s_setprio(2);
        {
            asm volatile(
                "v_add_u32 v246, 1088, %[rw]\n\t"
                S1_HEAD "s_nop 0\n\t" S1_TAIL("s_nop 1\n\t", "s_barrier\n\t")
                : LTOUT0
                : U_W, "{v[224:227]}"(af[0]), "{v[228:231]}"(af[1]), "{v[232:235]}"(af[2]), "{v[236:239]}"(af[3]), [sx] "v"(sidx), [rv] "v"(r1v), [rw] "v"(r1w)
                : "memory", "vcc", CLOB16(200, 201, 202, 203, 204, 205, 206, 207, 208, 209, 210, 211, 212, 213, 214, 215), CLOB16(216, 217, 218, 219, 220, 221, 222, 223, 240, 241, 242, 243, 244, 245, 246, 247));
        }
        const unsigned tau = __builtin_amdgcn_readfirstlane(*(volatile unsigned*)(chsm + C2_MB + 8));
        const unsigned long long wmask = u == 3 ? (c == 0 ? ~0ull : 0ull) : u == 1 ? 0x0020202020202000ull : u == 2 ? 1ull << (NSEG - 3) : 0ull;
        const int pvv = u == 3 ? 2 : -1, pvm = u == 3 ? -1 : 0;
        const unsigned* pfl = a.sync + (u == 3 ? SY_PROG + grp : u == 1 ? SY_KV + 16 + grp : SY_Q + 7 * 8 + grp) * 32;
        int s_first = 0;
        asm volatile("s_branch 1f\n\t.p2align 8\n1:" : "+s"(s_first));
        for (int s = s_first; s < NSEG - 1; ++s) {
            __builtin_amdgcn_s_setprio(0);
            LT(1)
            const int pv = (wmask >> s) & 1ull ? pvv + (s & pvm) : 0;
            const unsigned* pa = pfl;
            if (pv < 0) pfl += 256;
            const int cb = grp & 3;
            int soffc = rec_base(s) + C_OFF + c * 16384 + (int)((half_bits(s, tau) >> grp) & 1u) * 8192 + cb * 2048;
            int o_as = rec_base(s + 1) + ((c >> 3) + (grp >> 2) * 4) * 16384 + (int)((half_bits(s + 1, tau) >> grp) & 1u) * 8192 + ((4 * c + cb) & 31) * 256;
            int soff = chunk_off(s + 1, grp, u >> 1, tau) + (u & 1) * 4096;
            const int o_tm = (s >> 1) * 2097152 + TM_OFF + (s & 1) * 16384;
            int lim = dead ? 0 : SPIN_LIMIT, bad = 0;
            const bool rcd = u > 0 || widx < 32;
            const int pc = u > 0 ? 3 * widx + u - 1 : rcd ? 384 + widx : s * 96 + widx - 32;
            const int o_arm = (rcd ? s + 2 < NSEG : pc < 512) ? (rcd ? rec_base(s + 2) : XB_OFF) + (pc >> 3) * 16384 + (int)(half_bits(rcd ? s + 2 : 0, tau) & 1u) * 8192 + (pc & 7) * 1024 : OOB;
            bool there = true;
            if (__builtin_expect(s <= KVS, 0)) {
                there = __all(ld_flag(kv_flag(a.sync, s + 2, grp, l)) != 0u);
                *(volatile unsigned*)(chsm + C2_MB + 32 + (s & 1) * 16 + u * 4) = there ? 1u : 0u;
                __builtin_amdgcn_s_waitcnt(0xC07F);
            }
            const char* vb = !there ? nullptr : arena(a.slots) + (s + 2 >= NSEG ? KX_OFF : VX_OFF) + (blk_off(min(s + 2, NSEG - 1), 4 * grp + 2 * u - 2, tau) ^ (s + 2 >= NSEG ? 8192 : 0));
            asm volatile("" : "+s"(soffc), "+s"(o_as), "+s"(soff), "+s"(lim), "+s"(vb));
#define GL(o) "global_load_lds_dwordx4 %[l16], s[84:85] offset:" o " sc1\n\t" PAUSE(PGP)
#define GL4 GL("0") GL("1024") GL("2048") GL("3072")
#define GLN(d) "s_add_u32 m0, m0, 4096\n\ts_add_u32 s84, s84, " d "\n\ts_addc_u32 s85, s85, 0\n\t"
#define PKE PAUSE(PKA)
#define C_TRY(x, x0, x1, x2, x3, y, y0, y1, y2, y3) \
                "s_waitcnt vmcnt(3)\n\t" \
                "v_mfma_f32_16x16x32_bf16 v[240:243], v[216:219], v[" x "], 0\n\t" CHK0(x0, x1, x2, x3) \
                "s_waitcnt vmcnt(2)\n\t" \
                "v_mfma_f32_16x16x32_bf16 v[240:243], v[216:219], v[" y "], v[240:243]\n\t" CHK(y0, y1, y2, y3) \
                "v_cmp_eq_u32 vcc, " STR(SENT) ", v244\n\t" \
                "s_cbranch_vccz 3b\n\t" CN(1) \
                LD4(x, "%[l16]", "%[sc]", "0") LD4(y, "%[l16]", "%[sc]", "1024")
            asm volatile(
                "v_and_b32 v221, 16, %[l16]\n\t"
                "v_mov_b32 v222, 0x3f80\n\t"
                "v_bfe_u32 v223, %[l16], 5, 2\n\t"
                "v_lshlrev_b32 v221, v221, v222\n\t"
                "v_cmp_eq_u32 vcc, 0, v223\n\tv_and_b32 v220, 0xf0, %[l16]\n\tv_bfe_u32 v222, %[l16], 8, 1\n\tv_cndmask_b32 v216, 0, v221, vcc\n\t"
                "v_cmp_eq_u32 vcc, 1, v223\n\tv_lshl_add_u32 v220, v222, 3, v220\n\tv_lshrrev_b32 v222, 9, %[l16]\n\tv_cndmask_b32 v217, 0, v221, vcc\n\t"
                "v_cmp_eq_u32 vcc, 2, v223\n\tv_lshl_add_u32 v220, v222, 30, v220\n\ts_nop 0\n\tv_cndmask_b32 v218, 0, v221, vcc\n\t"
                "v_cmp_eq_u32 vcc, 3, v223\n\ts_nop 1\n\tv_cndmask_b32 v219, 0, v221, vcc\n\t"
                "v_mul_u32_u24 v222, 0x8000, v222\n\t"
                "v_and_b32 v221, 0xff, v220\n\t"
                "v_add_u32 v221, v221, v222\n\t"
                "v_lshrrev_b32 v247, 2, %[l16]\n\t"
                PKE
                "v_mov_b32 v240, " STR(SENT) "\n\tv_mov_b32 v241, v240\n\tv_mov_b32 v242, v240\n\tv_mov_b32 v243, v240\n\t"
                "buffer_store_dwordx4 v[240:243], %[l16], %[rs], %[arm] offen sc1\n\t"
                LTA(2)
                "s_setprio 2\n\t"
                "s_barrier\n\t"
                LTA(3)
                "v_add_u32 v246, 1088, %[rw]\n\t"
                "s_cmp_lt_u32 %[uu], 1\n\t"
                "s_cbranch_scc1 7f\n\t"
                "s_cmp_eq_u32 %[uu], 3\n\t"
                "s_cbranch_scc1 20f\n\t"
                "s_memrealtime s[84:85]\n\t"
                "v_lshrrev_b32 v241, 9, %[l16]\n\t"
                "v_and_b32 v242, 0x7c, v247\n\t"
                "v_lshl_add_u32 v241, v241, 13, v242\n\t"
                "s_waitcnt lgkmcnt(0)\n\t"
                "v_mov_b32 v240, s84\n\t"
                "buffer_store_dword v240, v241, %[rs], %[ts] offen sc1\n\t"
                "s_cmp_eq_u64 %[vb], 0\n\t"
                "s_cbranch_scc1 11f\n\t"
                "s_mov_b32 s86, m0\n\t"
                "s_mov_b64 s[84:85], %[vb]\n\t"
                "s_mov_b32 m0, %[lv]\n\t"
                PAUSE(PG0)
                "s_cmp_eq_u32 %[uu], 1\n\t"
                "s_cbranch_scc1 12f\n\t"
                PAUSE(PG1)
                "12:\n\t"
                GL4 GLN("4096") GL4 GLN("12288") GL4 GLN("4096") GL4
                "s_mov_b32 m0, s86\n\t"
                "11:\n\t"
                "s_cmp_lt_i32 %[pv], 0\n\t"
                "s_cbranch_scc0 8f\n\t"
                "v_and_b32 v241, 12, v247\n\t"
                "10:\n\t"
                "global_load_dword v240, v241, %[pa] sc1\n\t"
                "s_waitcnt vmcnt(0)\n\t"
                "v_cmp_gt_u32 vcc, 16, v240\n\t"
                "s_cbranch_vccz 8f\n\t"
                "s_sleep 6\n\t"
                "s_sub_u32 %[lim], %[lim], 1\n\t"
                "s_cbranch_scc0 10b\n\t"
                "s_mov_b32 %[bad], 3000\n\t"
                "s_mov_b32 %[lim], 0\n\t"
                "s_branch 8f\n\t"
                "20:\n\t"
                PAUSE(PS)
                "s_memrealtime s[84:85]\n\t"
                "s_load_dword s86, %[tb], 0\n\ts_load_dword s87, %[tb], 0x100\n\ts_load_dword s88, %[tb], 0x200\n\ts_load_dword s89, %[tb], 0x300\n\t"
                "s_load_dword s90, %[tb], 0x400\n\ts_load_dword s91, %[tb], 0x500\n\ts_load_dword s92, %[tb], 0x600\n\ts_load_dword s93, %[tb], 0x700\n\t"
                "s_waitcnt lgkmcnt(0)\n\t"
                "s_sub_u32 s86, s84, s86\n\ts_sub_u32 s87, s84, s87\n\ts_sub_u32 s88, s84, s88\n\ts_sub_u32 s89, s84, s89\n\t"
                "s_sub_u32 s90, s84, s90\n\ts_sub_u32 s91, s84, s91\n\ts_sub_u32 s92, s84, s92\n\ts_sub_u32 s93, s84, s93\n\t"
                "s_or_b32 s85, s86, s87\n\ts_min_u32 s86, s86, s87\n\ts_or_b32 s85, s85, s88\n\ts_min_u32 s86, s86, s88\n\ts_or_b32 s85, s85, s89\n\ts_min_u32 s86, s86, s89\n\t"
                "s_memrealtime s[88:89]\n\t"
                "s_or_b32 s85, s85, s90\n\ts_min_u32 s86, s86, s90\n\ts_or_b32 s85, s85, s91\n\ts_min_u32 s86, s86, s91\n\ts_or_b32 s85, s85, s92\n\ts_min_u32 s86, s86, s92\n\t"
                "s_or_b32 s85, s85, s93\n\ts_min_u32 s86, s86, s93\n\t"
                "s_cmp_gt_u32 s85, 127\n\t"
                CNS
                "s_cselect_b32 s85, " STR(TE0) ", 127\n\t"
                "s_min_u32 s86, s86, s85\n\t"
                "s_sub_u32 s84, s84, s86\n\t"
                "s_add_u32 s84, s84, %[td]\n\t"
                "s_cmp_lt_i32 %[pv], 1\n\t"
                "s_cbranch_scc1 4f\n\t"
                "v_mov_b32 v247, %[pv]\n\t"
                "s_mov_b64 exec, 1\n\t"
                "global_store_dword %[l16], v247, %[pa] sc1\n\t"
                "s_mov_b64 exec, -1\n\t"
                "4:\n\t"
                "s_waitcnt lgkmcnt(0)\n\t"
                "s_sub_i32 s84, s84, s88\n\t"
                "s_max_i32 s84, s84, 0\n\t"
                "s_min_i32 s84, s84, 40\n\t"
                "21:\n\t"
                "s_sub_u32 s84, s84, 1\n\t"
                "s_cbranch_scc0 21b\n\t"
                "s_branch 8f\n\t"
                "7:\n\t"
                PAUSE(PC)
                LD4(A0, "%[l16]", "%[sc]", "0") LD4(A1, "%[l16]", "%[sc]", "1024")
                PAUSE(PP)
                LD4(A2, "%[l16]", "%[sc]", "0") LD4(A3, "%[l16]", "%[sc]", "1024")
                "s_waitcnt vmcnt(3)\n\t"
                "v_mfma_f32_16x16x32_bf16 v[240:243], v[216:219], v[224:227], 0\n\t" CHK0("224", "225", "226", "227")
                "s_waitcnt vmcnt(2)\n\t"
                "v_mfma_f32_16x16x32_bf16 v[240:243], v[216:219], v[228:231], v[240:243]\n\t" CHK("228", "229", "230", "231")
                "v_cmp_eq_u32 vcc, " STR(SENT) ", v244\n\t"
                "s_cbranch_vccnz 30f\n\t"
                "3:\n\t"
                "s_nop 3\n\t"
                "v_cvt_pk_bf16_f32 v244, v240, v241\n\t"
                "v_cvt_pk_bf16_f32 v245, v242, v243\n\t"
                "buffer_store_dwordx2 v[244:245], v221, %[rs], %[oas] offen sc1\n\t"
                PAUSE(PB)
                "8:\n\t"
                LTA(4)
                "s_barrier\n\t"
                PAUSE(1)
                "2:\n\t"
                LD4(A0, "%[l16]", "%[sa]", "0") LD4(A1, "%[l16]", "%[sa]", "1024") LD4(A2, "%[l16]", "%[sa]", "2048") LD4(A3, "%[l16]", "%[sa]", "3072")
                S1_HEAD
                "s_cbranch_vccnz 6f\n\t"
                "5:\n\t"
                LTA(5)
                S1_TAIL("s_waitcnt lgkmcnt(0)\n\ts_barrier\n\t", "")
                "s_setprio 0\n\t"
                "s_branch 9f\n\t"
                "30:\n\t" CN(1)
                LD4(A0, "%[l16]", "%[sc]", "0") LD4(A1, "%[l16]", "%[sc]", "1024")
                "1:\n\t"
                C_TRY(A2, "232", "233", "234", "235", A3, "236", "237", "238", "239")
                C_TRY(A0, "224", "225", "226", "227", A1, "228", "229", "230", "231")
                "s_sub_u32 %[lim], %[lim], 1\n\t"
                "s_cbranch_scc0 1b\n\t"
                "s_mov_b32 %[bad], 2000\n\t"
                "s_mov_b32 %[lim], 0\n\t"
                "s_branch 3b\n\t"
                "6:\n\t" CN(65536)
                "v_mov_b32 v240, %[tg]\n\t"
                "v_mov_b32 v241, %[lf]\n\t"
                "ds_write_b32 v241, v240\n\t"
                "s_nop 0\n\t"
                "s_sub_u32 %[lim], %[lim], 1\n\t"
                "s_cbranch_scc0 2b\n\t"
                "s_mov_b32 %[bad], 1\n\t"
                "s_branch 5b\n\t"
                "9:\n\t"
                : [lim] "+s"(lim), [bad] "+s"(bad), [oas] "+s"(o_as) LTOUT CNOUT
                : U_W, [l16] "v"(l * 16), [rs] "s"(rsS), [sx] "v"(sidx), [rv] "v"(r1v), [rw] "v"(r1w),
                  [sc] "s"(soffc), [sa] "s"(soff), [uu] "s"(u), [pv] "s"(pv), [pa] "s"(pa), [vb] "s"(vb), [lv] "s"(l0 + (s + 2 >= NSEG ? C2_V + 32768 : s & 1 ? C2_K : C2_V) + (u - 1) * 16384), [td] "s"(s >= SL0 ? TD2 : TD), [ts] "s"(c == 0 && (u == 2 || s < NSEG - 3) ? o_tm + grp * 256 + (2 - u) * 2097152 : OOB), [tb] "s"(arena(a.slots) + o_tm + ((half_bits(s, tau) >> grp) & 1u) * 8192),

                  [arm] "s"(o_arm)
,
                  [tg] "s"(s + 1), [lf] "s"(l0 + C2_MB + 16)
                : "memory", "vcc", "scc", "s84", "s85", "s86", "s87", "s88", "s89", "s90", "s91", "s92", "s93", U_CLOB);
            if (__builtin_expect(bad != 0, 0)) TMO_SET((unsigned)bad)
        }
        last_rows(chsm, rsS, wu, grp, c, tau);
        if (u == 3 && widx == 0 && l == 0) __hip_atomic_store((gu64*)(a.boot + 144), *(volatile unsigned long long*)(chsm + C2_MB), __ATOMIC_RELAXED, __HIP_MEMORY_SCOPE_AGENT);
        return;
    }
    const int l = opaque_tid() & 63, i = l & 15, g = l >> 4;
    const int sidx = (i & 1) ? 0xEEEE : 0x4444;
    const int ha = 4 * grp + wu;
    unsigned long long tag;
    v32u wO[2];
    {
        u16* stg = (u16*)(chsm + 122880 + wu * 10240);
        tag = boot_tag(a.boot);
        asm volatile("" ::: "memory");
        v4u rawo[16];
        const u16* src = a.Wo + (size_t)(hcol(ha) + (l >> 3)) * DM + 64 * c + 8 * (l & 7);
#pragma unroll
        for (int e = 0; e < 16; ++e) rawo[e] = *(const v4u*)(src + (size_t)8 * e * DM);
        __builtin_amdgcn_sched_barrier(0);
        if (wu == 0) {
            asm volatile("s_waitcnt vmcnt(16)" ::: "memory");
            unsigned tm[2] = {~0u, ~0u};
            const char* pb = arena(a.slots) + cal_off;
#pragma unroll
            for (int k = 0; k < 2 * NPROBE; ++k) {
                unsigned long long c0, c1; unsigned d;
                asm volatile("s_memtime %0\n\ts_waitcnt lgkmcnt(0)\n\ts_load_dword %2, %3, %4\n\ts_waitcnt lgkmcnt(0)\n\ts_memtime %1\n\ts_waitcnt lgkmcnt(0)"
                             : "=&s"(c0), "=&s"(c1), "=&s"(d) : "s"(pb), "i"((k >> 1) * 256 + (k & 1) * 8192) : "memory");
                tm[k & 1] = min(tm[k & 1], (unsigned)(c1 - c0));
            }
            const unsigned side = (__builtin_amdgcn_s_getreg(20 | (3 << 11)) >> 2) & 1u;
            if (l == 0) __hip_atomic_store((gu64*)(a.boot + widx), (tag << 2) | (((tm[0] > tm[1] ? 1u : 0u) ^ side) << 1) | side, __ATOMIC_RELAXED, __HIP_MEMORY_SCOPE_AGENT);
            PST(7)
        }
#pragma unroll
        for (int m = 0; m < 2; ++m) {
#pragma unroll
            for (int e = 0; e < 8; ++e) *(v4u*)(stg + (8 * e + (l >> 3)) * 72 + 8 * (l & 7)) = rawo[8 * m + e];
            __builtin_amdgcn_s_waitcnt(0xC07F);
#pragma unroll
            for (int ct = 0; ct < 4; ++ct) {
                const u16* p = stg + (8 * g + (i >> 2)) * 72 + 16 * ct + 4 * (i & 3);
                const v2u r0 = tr4(p), r1 = tr4(p + 4 * 72), r2 = tr4(p + 32 * 72), r3 = tr4(p + 36 * 72);
                PUT4(wO[ct >> 1], (ct & 1) * 16 + m * 8, (v4u{r0[0], r0[1], r1[0], r1[1]})) PUT4(wO[ct >> 1], (ct & 1) * 16 + m * 8 + 4, (v4u{r2[0], r2[1], r3[0], r3[1]}))
            }
            __builtin_amdgcn_s_waitcnt(0xC07F);
        }
        if (wu == 0) { PST(5) }
        if (widx == 0 && wu == 0) *(volatile unsigned long long*)(chsm + C2_MB) = tag;
    }
    if (wu >= 2) touch_sc1(arena(a.slots) + (size_t)min(l + (wu - 2) * 64, FX_OFF / 2097152 - 1) * 2097152 + 2097024 - 128, l0 + C2_MB + 256);
    unsigned long long fw = 0;
#pragma nounroll
    for (int spin = 0; spin < 100000; ++spin) {
        asm volatile("" ::: "memory");
        fw = __hip_atomic_load((gu64*)(a.boot + 256 + 16 * grp + (l & 7)), __ATOMIC_RELAXED, __HIP_MEMORY_SCOPE_AGENT);
        if (__all((fw | 255u) == ((tag << 8) | 255u))) break;
        __builtin_amdgcn_s_sleep(4);
    }
    asm volatile("" ::: "memory");
    const unsigned tau = __builtin_amdgcn_readfirstlane((unsigned)fw & 255u);
    if (wu == 3) { *(volatile unsigned*)(chsm + C2_MB + 8) = tau; *(volatile unsigned*)(chsm + C2_MB + 16) = ~0u; __builtin_amdgcn_s_waitcnt(0xC07F); }
    if (wu == 1) { PST(9) }

    const unsigned r3a = l0 + C2_R3 + wu * 2048 + g * 128 + i * 4;
    v32u yb;
    v8u kx;
    v8u sel; v2u sel3;
#pragma unroll
    for (int d = 0; d < 8; ++d) sel[d] = (i >> 1) == d ? (0x3F80u << ((i & 1) * 16)) : 0u;
#pragma unroll
    for (int d = 0; d < 2; ++d) sel3[d] = (i & 3) == 0 && (i >> 3) == d ? (0x3F80u << (((i >> 2) & 1) * 16)) : 0u;
#define MF(d, a, b, c) "v_mfma_f32_16x16x32_bf16 v[" d "], v[" a "], v[" b "], " c "\n\t"
#define PX4(d0, d1, d2, d3, a, w0, w1, w2, w3) MF(d0, a, w0, "v[" d0 "]") MF(d1, a, w1, "v[" d1 "]") MF(d2, a, w2, "v[" d2 "]") MF(d3, a, w3, "v[" d3 "]")
#define PXT(d0, d1, d2, d3, a0, a1, a2, a3) MF(d0, a0, "32:35", "0") MF(d1, a0, "48:51", "0") MF(d2, a0, "64:67", "0") MF(d3, a0, "80:83", "0") \
        PX4(d0, d1, d2, d3, a1, "36:39", "52:55", "68:71", "84:87") PX4(d0, d1, d2, d3, a2, "40:43", "56:59", "72:75", "88:91") PX4(d0, d1, d2, d3, a3, "44:47", "60:63", "76:79", "92:95")
#define CVP(d, a, b) "v_cvt_pk_bf16_f32 v" d ", v" a ", v" b "\n\t"
#define PROJ_X(cnt) \
        "s_waitcnt " cnt "(8)\n\t" PXT("192:195", "196:199", "200:203", "204:207", "160:163", "164:167", "168:171", "172:175") \
        "s_waitcnt " cnt "(4)\n\t" PXT("208:211", "212:215", "216:219", "220:223", "176:179", "180:183", "184:187", "188:191") \
        CVP("128", "192", "193") CVP("129", "194", "195") CVP("136", "196", "197") CVP("137", "198", "199") CVP("144", "200", "201") CVP("145", "202", "203") CVP("152", "204", "205") CVP("153", "206", "207") \
        CVP("132", "208", "209") CVP("133", "210", "211") CVP("140", "212", "213") CVP("141", "214", "215") CVP("148", "216", "217") CVP("149", "218", "219") CVP("156", "220", "221") CVP("157", "222", "223") \
        "s_waitcnt " cnt "(0)\n\t"
#define CLOB_PX CLOB16(160, 161, 162, 163, 164, 165, 166, 167, 168, 169, 170, 171, 172, 173, 174, 175), CLOB16(176, 177, 178, 179, 180, 181, 182, 183, 184, 185, 186, 187, 188, 189, 190, 191), \
        CLOB16(192, 193, 194, 195, 196, 197, 198, 199, 200, 201, 202, 203, 204, 205, 206, 207), CLOB16(208, 209, 210, 211, 212, 213, 214, 215, 216, 217, 218, 219, 220, 221, 222, 223)
#define LDX(d, va, so, o) "buffer_load_dwordx4 v[" d "], " va ", %[rs], " so " offen offset:" o " sc1\n\t"
#define DSV(d, o) "ds_read_b128 v[" d "], %[lv] offset:" o "\n\t"
    const int voffX = ((i >> 1) & 1) * 4096 + g * 256 + (2 * (i >> 2) + (i & 1)) * 16;
    const int pk = (((i >> 1) & 3) << 1) | (i >> 3);
    const unsigned hv = wu >= 2 ? l0 + C2_R1 + 2176 + ((wu & 1) * 128 + ((l >> 4) + 4 * ((l >> 3) & 1)) * 16 + 8 * ((l >> 1) & 1) + 4 * ((l >> 2) & 1) + 2 * (l & 1)) * 4
                                : l0 + C2_R1 + (i & 1) * 544 + (i >> 1) * 64 + g * 16;
    const unsigned hk = l0 + C2_R1 + 1088 + (i & 1) * 544 + pk * 64 + g * 16;
    const int vo1 = wu >= 2 ? l * 4 : wu == 1 ? (g >> 1) * 1024 + i * 16 + (g & 1) * 8 : (i & 1) ? 1 << 30 : (i >> 1) * 16 + 4 * g;
    const int vo2 = (2 * (i & 1) + (g >> 1)) * 128 + pk * 16 + (g & 1) * 8;
    const int voffK = (g >> 1) * 256 + i * 16 + (g & 1) * 8;
#define RD4(w, ad, a, b, c, d) "ds_read_" w " v[" a "], " ad "\n\tds_read_" w " v[" b "], " ad " offset:3200\n\tds_read_" w " v[" c "], " ad " offset:6400\n\tds_read_" w " v[" d "], " ad " offset:9600\n\t"
#define AD4(d0, d1, d2, d3, a0, a1, a2, a3) ADD(d0, d0, a0) ADD(d1, d1, a1) ADD(d2, d2, a2) ADD(d3, d3, a3)
#define QSC(r) "v_mul_f32 v" r ", 0x3e0293ee, v" r "\n\t"
#define FLD "buffer_load_dword v114, %[vf], %[rs], %[fl] offen sc1\n\t"
#define DUM "buffer_store_dword v168, off, %[rs], %[ob] sc1\n\t"
#define QHEAD "s_barrier\n\t" RD4("b128", "%[hv]", "160:163", "164:167", "168:171", "172:175") FLD \
        "s_waitcnt lgkmcnt(2)\n\t" AD4("160", "161", "162", "163", "164", "165", "166", "167") \
        "s_waitcnt lgkmcnt(0)\n\t" AD4("168", "169", "170", "171", "172", "173", "174", "175") \
        "s_barrier\n\t"
#define QTAIL AD4("160", "161", "162", "163", "168", "169", "170", "171") QSC("160") QSC("161") QSC("162") QSC("163")
#define SWA(d, a, b) "v_add_f32_dpp v" d ", v" a ", v" b " quad_perm:[1,0,3,2] row_mask:0xf bank_mask:0xf\n\t"
#define KLD(so) "buffer_load_dwordx2 v[96:97], %[vk], %[rs], " so " offen sc1\n\tbuffer_load_dwordx2 v[102:103], %[vk], %[rs], " so " offen offset:512 sc1\n\t" \
        "s_add_u32 " so ", " so ", 4096\n\t" \
        "buffer_load_dwordx2 v[100:101], %[vk], %[rs], " so " offen sc1\n\tbuffer_load_dwordx2 v[98:99], %[vk], %[rs], " so " offen offset:512 sc1\n\t"
    auto hand_over = [&](const int o_1, const int o_2, const int o_f, const int tg) {
        int dmy = 0;
        asm volatile(
            "s_cmp_eq_u32 %[wu], 0\n\t"
            "s_cbranch_scc1 2f\n\t"
            "s_cmp_eq_u32 %[wu], 1\n\t"
            "s_cbranch_scc1 1f\n\t"
            "s_barrier\n\t"
            RD4("b64", "%[hv]", "160:161", "162:163", "164:165", "166:167") FLD
            "s_waitcnt lgkmcnt(2)\n\t" ADD("160", "160", "162") ADD("161", "161", "163")
            "s_waitcnt lgkmcnt(0)\n\t" ADD("164", "164", "166") ADD("165", "165", "167") ADD("160", "160", "164") ADD("161", "161", "165")
            CVP("168", "160", "161")
            "buffer_store_dword v168, %[v1], %[rs], %[o1] offen sc1\n\t"
            "s_barrier\n\t"
            DUM PAUSE(PV2)
            "s_branch 3f\n\t"
            "5:\n\t"
            "v_mov_b32 v115, %[lf]\n\t"
            "ds_read_b32 v115, v115\n\t"
            "s_waitcnt lgkmcnt(0)\n\t"
            "v_cmp_eq_u32 vcc, %[tg], v115\n\t"
            "s_cbranch_vccnz 4f\n\t" CN(256)
            PAUSE(PL)
            "s_branch 4f\n\t"
            "1:\n\t"
            Z2("176:177") Z2("178:179")
            "s_waitcnt vmcnt(0)\n\t"
            QHEAD QTAIL
            "s_mov_b64 exec, %[em]\n\t"
            CVP("176", "160", "161") CVP("177", "162", "163")
            "s_not_b64 exec, exec\n\t"
            CVP("178", "160", "161") CVP("179", "162", "163")
            "s_mov_b64 exec, -1\n\t"
            "s_nop 0\n\t"
            MF("180:183", "96:99", "176:179", "0") MF("184:187", "100:103", "176:179", "0")
            "s_nop 7\n\t"
            SWA("180", "184", "180") SWA("181", "185", "181") SWA("182", "186", "182") SWA("183", "187", "183")
            CVP("188", "180", "181") CVP("189", "182", "183")
            "buffer_store_dwordx2 v[188:189], %[v1], %[rs], %[o1] offen sc1\n\t"
            DUM PAUSE(PV1)
            "s_branch 3f\n\t"
            "2:\n\t"
            QHEAD
            RD4("b128", "%[hk]", "176:179", "180:183", "184:187", "188:191")
            QTAIL CVP("164", "160", "161") CVP("165", "162", "163")
            "s_waitcnt lgkmcnt(2)\n\t" AD4("176", "177", "178", "179", "180", "181", "182", "183")
            "s_waitcnt lgkmcnt(0)\n\t" AD4("184", "185", "186", "187", "188", "189", "190", "191") AD4("176", "177", "178", "179", "184", "185", "186", "187")
            CVP("166", "176", "177") CVP("167", "178", "179")
            "s_nop 1\n\t"
            "v_mfma_f32_16x16x16_bf16 v[168:171], v[166:167], v[164:165], 0\n\t"
            "buffer_store_dwordx2 v[166:167], %[v2], %[rs], %[o2] offen sc1\n\t"
            "s_nop 6\n\t"
            SWA("172", "169", "168") SWA("173", "171", "170")
            CVP("174", "172", "173")
            "buffer_store_dword v174, %[v1], %[rs], %[o1] offen sc1\n\t"
            "3:\n\t"
            PAUSE(PVA)
            "s_waitcnt vmcnt(2)\n\t"
            "v_cmp_ne_u32 vcc, " STR(SENT) ", v114\n\t"
            "s_cbranch_vccnz 5b\n\t"
            "4:\n\t"
            : [dm] "+s"(dmy) CNOUT : "{v[96:103]}"(kx), [fl] "s"(o_f), [vf] "v"(i * 128), [ob] "s"(1 << 30), [tg] "s"(tg), [lf] "s"(l0 + C2_MB + 16), [wu] "s"(wu), [hv] "v"(hv), [hk] "v"(hk), [v1] "v"(vo1), [v2] "v"(vo2), [rs] "s"(rsS), [o1] "s"(o_1), [o2] "s"(o_2), [em] "s"(0x5555555555555555ull)
            : "memory", "scc", "vcc", "v114", "v115", CLOB16(160, 161, 162, 163, 164, 165, 166, 167, 168, 169, 170, 171, 172, 173, 174, 175), CLOB16(176, 177, 178, 179, 180, 181, 182, 183, 184, 185, 186, 187, 188, 189, 190, 191));
    };
    auto o1_of = [&](const int s) { return wu >= 2 ? v_off(s, hs, tau) + jq * 512 + (wu & 1) * 256 : wu == 1 ? x_off(s, hs, tau) + jq * 256 : m_off(s, hs, tau) + jq * 128; };

    __builtin_amdgcn_s_setprio(1);
    int o_f = f_off(0, grp, tau);
    int o_cs = rec_base(0) + C_OFF + c * 16384 + (int)(half_bits(0, tau) & 1u) * 8192 + wu * 2048 + grp * 256;
    int o_1 = o1_of(0), o_2 = km_off(0, hs, tau) + jq * 512, bq = x_off(0, ha, tau), bm = m_off(0, ha, tau), bb = v_off(0, ha, tau);
    {
            const int b0 = blk_off(0, ha, tau);
            int k0 = KX_OFF + blk_off(0, hs, tau) + jq * 1024;
            asm volatile(
                LDX("160:163", "%[vx]", "%[vb]", "0") LDX("164:167", "%[vx]", "%[vb]", "1024") LDX("168:171", "%[vx]", "%[vb]", "2048") LDX("172:175", "%[vx]", "%[vb]", "3072")
                LDX("176:179", "%[vx]", "%[vb]", "128") LDX("180:183", "%[vx]", "%[vb]", "1152") LDX("184:187", "%[vx]", "%[vb]", "2176") LDX("188:191", "%[vx]", "%[vb]", "3200")
                KLD("%[k0]")
                "v_mov_b32 v131, 0\n\tv_mov_b32 v135, 0\n\tv_mov_b32 v139, 0\n\tv_mov_b32 v143, 0\n\tv_mov_b32 v147, 0\n\tv_mov_b32 v151, 0\n\tv_mov_b32 v155, 0\n\tv_mov_b32 v159, 0\n\t"
                PROJ_X("vmcnt")
                : "=&{v[128:159]}"(yb), "=&{v[96:103]}"(kx), [k0] "+s"(k0)
                : "{v[32:63]}"(wO[0]), "{v[64:95]}"(wO[1]), [vx] "v"(voffX), [vk] "v"(voffK), [rs] "s"(rsS), [vb] "s"(VX_OFF + b0)
                : "memory", "scc", CLOB_PX);
        if (wu == 1) { PST(10) }
    }
    int s_first = 0;
    asm volatile("s_branch 1f\n\t.p2align 8\n1:" : "+s"(s_first));
    for (int s = s_first; s < NSEG - 1; ++s) {
        hand_over(o_1, o_2, o_f, s);
        if (s == 0 && wu == 1) { PST(8) }
        LT(0)
#define LDPA "buffer_load_dwordx2 v[160:161], v254, %[rs], %[bm] offen sc1\n\t" LDX("164:167", "%[l16]", "%[bq]", "0") LDX("168:171", "%[l16]", "%[bq]", "1024")
#define SM(d, a, b, x) "v_smfmac_f32_16x16x64_bf16 v[" d "], v[" a "], v[" b "], " x "\n\t"
#define MX2(a, b) "v_max_u32 v244, v" a ", v" b "\n\t"
#define MX3(a, b) "v_max3_u32 v244, v244, v" a ", v" b "\n\t"
#define ZAM Z2("208:209") Z2("210:211") Z2("212:213") Z2("214:215") Z2("216:217") Z2("218:219") Z2("220:221") Z2("222:223")
#define MUL(d, a, b) "v_mul_f32 v" d ", v" a ", v" b "\n\t"
#define CV0(d, a) "v_cvt_pk_bf16_f32 v" d ", v" a ", 0\n\t"
#define RDC "ds_read_b64 v[224:225], %[rc]\n\tds_read_b64 v[226:227], %[rc] offset:2048\n\tds_read_b64 v[228:229], %[rc] offset:4096\n\tds_read_b64 v[230:231], %[rc] offset:6144\n\t"
#define EXD(d, r) "v_exp_f32 v" d ", v" r "\n\t"
#define SUBM(r) "v_sub_f32 v" r ", v" r ", v250\n\t"
#define PCVM CVP("241", "180", "180") CVP("243", "180", "180")
#define PCVX CVP("242", "178", "179") CVP("240", "176", "177")
#define OUT(t0, t1, a0, a1, a2, a3, ad, o0, o1) ADD(t0, a0, a1) ADD(t1, a2, a3) MUL(t0, t0, "250") MUL(t1, t1, "251") DW2(ad, t0, t1, o0, o1)
#define TS(n)
#define TSOUT
        int lim = dead ? 0 : SPIN_LIMIT, bad = 0;
        asm volatile(
            LDX("192:195", "%[l16]", "%[bb]", "0") LDX("196:199", "%[l16]", "%[bb]", "1024")
            ZAM Z2("224:225") Z2("226:227")
            Z2("16:17") Z2("18:19") Z2("20:21") Z2("22:23") Z2("24:25") Z2("26:27") Z2("28:29") Z2("30:31")
            "v_mov_b32 v200, 0x3f803f80\n\tv_mov_b32 v201, 0x3f803f80\n\tv_mov_b32 v202, 0x3f80\n\tv_mov_b32 v203, 0\n\t"
            "v_mov_b64 v[204:205], v[200:201]\n\tv_mov_b64 v[206:207], v[202:203]\n\t"
            "v_xor_b32 v252, 0xa000, %[sx]\n\tv_add_u32 v253, 1024, %[ra]\n\tv_lshrrev_b32 v254, 1, %[l16]\n\t"
            PAUSE(PV)
            TS(8)
            LDPA
            "4:\n\t"
            "s_waitcnt vmcnt(4)\n\t"
            SM("208:211", "192:195", "32:39", "%[sx]") MX2("192", "193") SM("212:215", "192:195", "48:55", "%[sx]") MX3("194", "195")
            SM("216:219", "192:195", "64:71", "%[sx]") SM("220:223", "192:195", "80:87", "%[sx]")
            "s_waitcnt vmcnt(3)\n\t"
            SM("208:211", "196:199", "40:47", "%[sx]") MX3("196", "197") SM("212:215", "196:199", "56:63", "%[sx]") MX3("198", "199")
            SM("216:219", "196:199", "72:79", "%[sx]") "v_cmp_eq_u32 vcc, " STR(SENT) ", v244\n\t" SM("220:223", "196:199", "88:95", "%[sx]")
            "s_cbranch_vccnz 5f\n\t"
            "1:\n\t" TS(11)
            "s_waitcnt vmcnt(2)\n\t"
            "v_mfma_f32_16x16x16_bf16 v[236:239], v[112:113], v[160:161], 0\n\t"
            MX2("160", "161") ADD("245", "208", "209") ADD("246", "210", "211") CV0("130", "245") CV0("134", "246")
            ADD("245", "212", "213") ADD("246", "214", "215") CV0("138", "245") CV0("142", "246")
            EXD("180", "236")
            "s_waitcnt vmcnt(1)\n\t"
            MF("228:231", "104:107", "164:167", "0")
            MX3("164", "165") MX3("166", "167") ADD("245", "216", "217") ADD("246", "218", "219") CV0("146", "245") CV0("150", "246")
            ADD("245", "220", "221") ADD("246", "222", "223") CV0("154", "245") CV0("158", "246") PCVM
            "s_waitcnt vmcnt(0)\n\t"
            MF("228:231", "108:111", "168:171", "v[228:231]")
            MX3("168", "169") MX3("170", "171")
            "v_cmp_eq_u32 vcc, " STR(SENT) ", v244\n\t"
            "s_cbranch_vccnz 7f\n\t"
            "2:\n\t" TS(9)
            "s_nop 3\n\t"
            EXD("179", "231") EXD("178", "230") EXD("177", "229") EXD("176", "228")
            PCVX
            "v_max3_f32 v250, |v236|, |v231|, |v230|\n\t"
            "v_max3_f32 v250, v250, |v229|, |v228|\n\t"
            SM("224:227", "240:243", "200:207", "v252")
            "v_cmp_nge_f32 vcc, 0x41c00000, v250\n\t"
            "s_nop 0\n\t"
            SM("16:19", "240:243", "128:135", "v252")
            "s_cbranch_vccnz 10f\n\t"
            "13:\n\t"
            "s_nop 0\n\t"
            SM("20:23", "240:243", "136:143", "v252") "s_nop 1\n\t"
            ADD("250", "224", "225") ADD("251", "226", "227")
            SM("24:27", "240:243", "144:151", "v252")
            "v_rcp_f32 v250, v250\n\t"
            SM("28:31", "240:243", "152:159", "v252")
            "v_rcp_f32 v251, v251\n\t"
            OUT("245", "246", "16", "17", "18", "19", "%[ra]", "0", "16")
            OUT("247", "248", "20", "21", "22", "23", "%[ra]", "128", "144")
            OUT("245", "246", "24", "25", "26", "27", "v253", "0", "16")
            OUT("247", "248", "28", "29", "30", "31", "v253", "128", "144")
            TS(10)
            "s_waitcnt lgkmcnt(0)\n\t"
            "s_barrier\n\t"
            RDC
            "v_lshrrev_b32 v247, 2, %[l16]\n\t"
            "s_waitcnt lgkmcnt(2)\n\t"
            ADD("224", "224", "226") ADD("225", "225", "227")
            "s_waitcnt lgkmcnt(0)\n\t"
            ADD("228", "228", "230") ADD("229", "229", "231") ADD("224", "224", "228") ADD("225", "225", "229")
            "v_cvt_pk_bf16_f32 v245, v224, v225\n\t"
            "buffer_store_dword v245, v247, %[rs], %[cs] offen sc1\n\t"
            "s_branch 9f\n\t"
            "10:\n\t"
            "v_max3_f32 v250, v236, v231, v230\n\t"
            "v_max3_f32 v250, v250, v229, v228\n\t"
            "s_nop 1\n\t"
            "v_max_f32_dpp v251, v250, v250 quad_perm:[1,0,3,2] row_mask:0xf bank_mask:0xf\n\t"
            "v_max_f32_dpp v244, v250, v250 quad_perm:[1,0,3,2] row_mask:0xf bank_mask:0xf\n\t"
            "s_nop 1\n\t"
            "v_permlane16_swap_b32 v251, v244\n\t"
            "v_max_f32 v250, v251, v244\n\t"
            "v_max_f32 v251, v251, v244\n\t"
            Z2("224:225") Z2("226:227")
            "v_permlane32_swap_b32 v250, v251\n\t"
            "v_max_f32 v250, v250, v251\n\t"
            Z2("16:17") Z2("18:19")
            SUBM("228") SUBM("229") SUBM("230") SUBM("231") SUBM("236")
            EXD("180", "236") EXD("179", "231") EXD("178", "230") EXD("177", "229") EXD("176", "228")
            PCVM PCVX
            "s_nop 1\n\t"
            SM("224:227", "240:243", "200:207", "v252") "s_nop 1\n\t"
            SM("16:19", "240:243", "128:135", "v252")
            "s_branch 13b\n\t"
            "5:\n\t" CN(1)
            LDX("192:195", "%[l16]", "%[bb]", "0") LDX("196:199", "%[l16]", "%[bb]", "1024")
            LDPA
            "s_sub_u32 %[lim], %[lim], 1\n\t"
            "s_nop 1\n\t"
            ZAM
            "s_cbranch_scc0 4b\n\t"
            "s_mov_b32 %[bad], 1500\n\t"
            "s_mov_b32 %[lim], 0\n\t"
            "s_branch 1b\n\t"
            "7:\n\t" CN(65536)
            LDPA
            "s_sub_u32 %[lim], %[lim], 1\n\t"
            "s_cbranch_scc0 1b\n\t"
            "s_mov_b32 %[bad], 1000\n\t"
            "s_branch 2b\n\t"
            "9:\n\t"
            : [lim] "+s"(lim), [bad] "+s"(bad), "+{v[128:159]}"(yb) TSOUT CNOUT
            : "{v[32:63]}"(wO[0]), "{v[64:95]}"(wO[1]), "{v[104:111]}"(sel), "{v[112:113]}"(sel3), [sx] "v"(sidx), [l16] "v"(l * 16),
              [ra] "v"(r3a), [rs] "s"(rsS), [bb] "s"(bb), [bq] "s"(bq), [bm] "s"(bm), [cs] "s"(o_cs), [rc] "v"(l0 + C2_R3 + wu * 512 + l * 8)
            : "memory", "vcc", "scc", CLOB_PX, CLOB16(16, 17, 18, 19, 20, 21, 22, 23, 24, 25, 26, 27, 28, 29, 30, 31),
              CLOB16(224, 225, 226, 227, 228, 229, 230, 231, 232, 233, 234, 235, 236, 237, 238, 239), CLOB16(240, 241, 242, 243, 244, 245, 246, 247, 248, 249, 250, 251, 252, 253, 254, 254));
        if (__builtin_expect(bad != 0, 0)) TMO_SET((unsigned)bad)
        __builtin_amdgcn_s_setprio(0);
        if (s == 0 && wu == 1) { PST(6) }

        if (__builtin_expect(s <= KVS + 1, 0)) {
            if (s == 0 || __builtin_amdgcn_readfirstlane(*(volatile unsigned*)(chsm + C2_MB + 32 + ((s - 1) & 1) * 16 + 4 + (wu >> 1) * 4)) == 0u) {
                wait_ge<6>(kv_flag(a.sync, s + 1, grp, l), 1u, a.tmo, 3001u);
                const char* kp = arena(a.slots) + VX_OFF + blk_off(s + 1, ha, tau);
#pragma unroll
                for (int e = 0; e < 8; ++e) glds16h((unsigned)l * 16u, kp + e * 1024, l0 + (s & 1 ? C2_V : C2_K) + wu * 8192 + e * 1024);
                asm volatile("s_waitcnt vmcnt(0)" ::: "memory");
            }
        }
        {
            int k0 = KX_OFF + blk_off(s + 1, hs, tau) + jq * 1024;
            asm volatile(
                DSV("160:163", "0") DSV("164:167", "1024") DSV("168:171", "2048") DSV("172:175", "3072") DSV("176:179", "128") DSV("180:183", "1152") DSV("184:187", "2176") DSV("188:191", "3200")
                "s_cmp_lg_u32 %[wu], 1\n\t"
                "s_cbranch_scc1 1f\n\t"
                KLD("%[k0]")
                "1:\n\t"
                "s_waitcnt lgkmcnt(4)\n\t"
                PXT("192:195", "196:199", "200:203", "204:207", "160:163", "164:167", "168:171", "172:175")
                "s_waitcnt lgkmcnt(0)\n\t"
                PXT("208:211", "212:215", "216:219", "220:223", "176:179", "180:183", "184:187", "188:191")
                CVP("128", "192", "193") CVP("129", "194", "195") CVP("136", "196", "197") CVP("137", "198", "199") CVP("144", "200", "201") CVP("145", "202", "203") CVP("152", "204", "205") CVP("153", "206", "207")
                CVP("132", "208", "209") CVP("133", "210", "211") CVP("140", "212", "213") CVP("141", "214", "215") CVP("148", "216", "217") CVP("149", "218", "219") CVP("156", "220", "221") CVP("157", "222", "223")
                : "+{v[128:159]}"(yb), "+{v[96:103]}"(kx), [k0] "+s"(k0)
                : "{v[32:63]}"(wO[0]), "{v[64:95]}"(wO[1]), [lv] "v"(l0 + (s & 1 ? C2_V : C2_K) + wu * 8192 + voffX), [vk] "v"(voffK), [rs] "s"(rsS), [wu] "s"(wu)
                : "memory", "scc", CLOB_PX);
        }
        __builtin_amdgcn_sched_barrier(0);
        o_1 = o1_of(s + 1); o_2 = km_off(s + 1, hs, tau) + jq * 512;
        bq = x_off(s + 1, ha, tau); bm = m_off(s + 1, ha, tau); bb = v_off(s + 1, ha, tau);
        o_f = f_off(s + 1, grp, tau);
        o_cs = rec_base(s + 1) + C_OFF + c * 16384 + (int)(half_bits(s + 1, tau) & 1u) * 8192 + wu * 2048 + grp * 256;
        asm volatile("" : "+s"(o_cs), "+s"(o_f), "+s"(o_1), "+s"(o_2), "+s"(bq), "+s"(bm), "+s"(bb));
        __builtin_amdgcn_s_barrier();
        asm volatile(
            "s_cmp_lg_u32 %[wu], 3\n\t"
            "s_cbranch_scc1 1f\n\t"
            "v_mov_b32 v160, %[lf]\n\t"
            PAUSE(PF)
            "ds_read_b32 v160, v160\n\t"
            "s_waitcnt lgkmcnt(0)\n\t"
            "v_cmp_eq_u32 vcc, %[tg], v160\n\t"
            "s_cbranch_vccz 1f\n\t"
            "s_mov_b32 exec_hi, 0\n\t"
            "buffer_store_dword v160, %[l4], %[rs], %[fo] offen sc1\n\t"
            "s_mov_b64 exec, -1\n\t"
            "1:\n\t"
            :: [wu] "s"(wu), [lf] "s"(l0 + C2_MB + 16), [tg] "s"(s + 1), [l4] "v"(l * 4), [rs] "s"(rsS), [fo] "s"(o_f + c * 128)
            : "memory", "scc", "vcc", "v160");
        __builtin_amdgcn_s_setprio(1);
    }
    {
        constexpr int s = NSEG - 1;
        const int l = lane_now(), i = l & 15, g = l >> 4;
        v4u qf[8], kf[8], pv[2], px[4];
        asm volatile("s_waitcnt vmcnt(0)" ::: "memory");
        __builtin_amdgcn_s_sleep(4);
#pragma unroll
        for (int e = 0; e < 8; ++e) kf[e] = ld_sc1s(rsS, l * 16, KX_OFF + blk_off(s, ha, tau) + e * 1024);
#pragma unroll
        for (int e = 0; e < 8; ++e) qf[e] = *(const v4u*)(chsm + C2_V + 32768 + wu * 8192 + e * 1024 + l * 16);
        f32x4 sx[3] = {ZERO4, ZERO4, ZERO4};
        float mxx[2] = {-3.0e38f, -3.0e38f};
#pragma unroll
        for (int ks = 0; ks < 4; ++ks) {
            sx[0] = MM(kf[ks], qf[ks], sx[0]);
            sx[1] = MM(kf[ks], qf[4 + ks], sx[1]);
            sx[2] = MM(kf[4 + ks], qf[4 + ks], sx[2]);
        }
#pragma unroll
        for (int r = 0; r < 4; ++r) {
            if (4 * g + r > i) { sx[0][r] = -3.0e38f; sx[2][r] = -3.0e38f; }
            mxx[0] = fmaxf(mxx[0], sx[0][r]);
            mxx[1] = fmaxf(mxx[1], fmaxf(sx[1][r], sx[2][r]));
        }
        asm volatile("" : "+v"(sx[0]), "+v"(sx[1]), "+v"(sx[2]), "+v"(mxx[0]), "+v"(mxx[1]));
        hand_over(o_1, o_2, o_f, s);
        const int bk = km_off(s, ha, tau);
        __builtin_amdgcn_s_sleep(DLY_L);
#pragma nounroll
        for (int spin = 0;; ++spin) {
            asm volatile("" ::: "memory");
            pv[0] = ld_sc1s(rsS, l * 16, bb); pv[1] = ld_sc1s(rsS, l * 16, bb + 1024);
#pragma unroll
            for (int ks = 0; ks < 4; ++ks) px[ks] = ld_sc1s(rsS, g * 128 + (i >> 1) * 16, bk + ks * 512);
            if (__all(mx4(mx4(mx4(mx4(mx4(mx4(0u, pv[0]), pv[1]), px[0]), px[1]), px[2]), px[3]) != SENT)) break;
            if (dead) break;
            if (spin > SPIN_LIMIT) { TMO_SET(1700u) break; }
        }
#define SUB8(v, o) v8u{(v)[(o)], (v)[(o) + 1], (v)[(o) + 2], (v)[(o) + 3], (v)[(o) + 4], (v)[(o) + 5], (v)[(o) + 6], (v)[(o) + 7]}
#pragma unroll
        for (int ct = 0; ct < 4; ++ct) {
            f32x4 am = smm(pv[0], SUB8(wO[ct >> 1], (ct & 1) * 16), ZERO4, sidx);
            am = smm(pv[1], SUB8(wO[ct >> 1], (ct & 1) * 16 + 8), am, sidx);
            yb[8 * ct + 2] = cvt_pk_bf16(am[0] + am[1], 0.f); yb[8 * ct + 6] = cvt_pk_bf16(am[2] + am[3], 0.f);
        }
        const v8u one = {0x3F803F80u, 0x3F803F80u, 0x3F80u, 0u, 0x3F803F80u, 0x3F803F80u, 0x3F80u, 0u};
        const float SCL2E = 0.08838834764831845f * 1.4426950408889634f;
        float* xo = (float*)(chsm + C2_K) + (wu * 32 + 4 * g) * XP + i;
#pragma unroll
        for (int qt = 0; qt < 2; ++qt) {
            f32x4 sc[3] = {sx[qt], sx[2], ZERO4};
#pragma unroll
            for (int ks = 0; ks < 4; ++ks) sc[2] = MM(px[ks], qf[4 * qt + ks], sc[2]);
            float mx = fmaxf(mxx[qt], fmaxf(sc[2][1], sc[2][3]));
            mx = xor32_max(xor16_max(mx));
            const float nmx = -mx * SCL2E;
#define PE(x) __builtin_amdgcn_exp2f(fmaf(x, SCL2E, nmx))
            const float m0 = PE(sc[2][1]), m1 = PE(sc[2][3]);
            const v4u ae = {cvt_pk_bf16(PE(sc[0][0]), PE(sc[0][1])), cvt_pk_bf16(m0, m0), cvt_pk_bf16(PE(sc[0][2]), PE(sc[0][3])), 0u};
            const v4u ao = {qt ? cvt_pk_bf16(PE(sc[1][0]), PE(sc[1][1])) : 0u, 0u, qt ? cvt_pk_bf16(PE(sc[1][2]), PE(sc[1][3])) : 0u, cvt_pk_bf16(m1, m1)};
            const f32x4 sum = smm(ao, one, smm(ae, one, ZERO4, 0xE444), 0x4EEE);
#pragma unroll
            for (int ct = 0; ct < 4; ++ct) {
                const f32x4 o = smm(ao, SUB8(yb, 8 * ct), smm(ae, SUB8(yb, 8 * ct), ZERO4, 0xE444), 0x4EEE);
#pragma unroll
                for (int r = 0; r < 4; ++r) xo[(16 * qt + r) * XP + 16 * ct] = o[r] * __builtin_amdgcn_rcpf(sum[r]);
            }
        }
        __builtin_amdgcn_s_waitcnt(0xC07F);
        last_rows(chsm, rsS, wu, grp, c, tau);
        if (wu < 2) {
            v4u sel, p0, p1;
#pragma unroll
            for (int d = 0; d < 4; ++d) sel[d] = (l & 6) == 2 * d ? (0x3F80u << ((l & 1) * 16)) : 0u;
            const int xb = XB_OFF + ((c >> 1) * 8 + grp) * 16384 + (int)(tau & 1u) * 8192 + (c & 1) * 4096 + wu * 256;
            __builtin_amdgcn_s_sleep(DLY_C);
#pragma nounroll
            for (int spin = 0;; ++spin) {
                asm volatile("" ::: "memory");
                p0 = ld_sc1s(rsS, g * 512 + i * 16, xb); p1 = ld_sc1s(rsS, g * 512 + i * 16, xb + 2048);
                if (__all(mx4(mx4(0u, p0), p1) != SENT)) break;
                if (dead) break;
                if (spin > SPIN_LIMIT) { TMO_SET(1800u) break; }
            }
            const f32x4 d = MM(sel, p1, MM(sel, p0, ZERO4));
            if (g < 2) out8(a.out + (size_t)(s * SEG + 4 * grp + 2 * wu + (i >> 3)) * OUTW + 64 * c + 8 * (i & 7) + 4 * g, v2u{cvt_pk_bf16(d[0], d[1]), cvt_pk_bf16(d[2], d[3])});
        }
    }
}

struct cpx { float x, y; };
__device__ __forceinline__ cpx cmul(cpx a, cpx b) { return {a.x * b.x - a.y * b.y, a.x * b.y + a.y * b.x}; }
__device__ __forceinline__ cpx cadd(cpx a, cpx b) { return {a.x + b.x, a.y + b.y}; }
__device__ __forceinline__ cpx csub(cpx a, cpx b) { return {a.x - b.x, a.y - b.y}; }
template <int DIR> __device__ __forceinline__ cpx mul_i(cpx a) {
    return (DIR < 0) ? cpx{a.y, -a.x} : cpx{-a.y, a.x};
}
template <int DIR> __device__ __forceinline__ void r4(cpx& a, cpx& b, cpx& c, cpx& d) {
    cpx t0 = cadd(a, c), t1 = csub(a, c), t2 = cadd(b, d), t3 = mul_i<DIR>(csub(b, d));
    a = cadd(t0, t2); b = cadd(t1, t3); c = csub(t0, t2); d = csub(t1, t3);
}
template <int DIR> __device__ __forceinline__ cpx twc(float c, float s) { return cpx{c, (float)DIR * s}; }
template <int DIR> __device__ __forceinline__ void dft16(cpx (&v)[16]) {
#pragma unroll
    for (int a = 0; a < 4; ++a) r4<DIR>(v[a], v[a + 4], v[a + 8], v[a + 12]);
    const float C1 = 0.92387953251128674f, S1 = 0.38268343236508977f, R2 = 0.70710678118654752f;
    v[1 + 4] = cmul(v[1 + 4], twc<DIR>(C1, S1));
    v[1 + 8] = cmul(v[1 + 8], twc<DIR>(R2, R2));
    v[1 + 12] = cmul(v[1 + 12], twc<DIR>(S1, C1));
    v[2 + 4] = cmul(v[2 + 4], twc<DIR>(R2, R2));
    v[2 + 8] = mul_i<DIR>(v[2 + 8]);
    v[2 + 12] = cmul(v[2 + 12], twc<DIR>(-R2, R2));
    v[3 + 4] = cmul(v[3 + 4], twc<DIR>(S1, C1));
    v[3 + 8] = cmul(v[3 + 8], twc<DIR>(-R2, R2));
    v[3 + 12] = cmul(v[3 + 12], twc<DIR>(-C1, -S1));
#pragma unroll
    for (int d = 0; d < 4; ++d) r4<DIR>(v[4 * d], v[4 * d + 1], v[4 * d + 2], v[4 * d + 3]);
}
__device__ __forceinline__ constexpr int x16(int q) { return 4 * (q & 3) + (q >> 2); }
template <int DIR> __device__ __forceinline__ void dft8(cpx (&v)[8]) {
    r4<DIR>(v[0], v[2], v[4], v[6]);
    r4<DIR>(v[1], v[3], v[5], v[7]);
    const float R2 = 0.70710678118654752f;
    v[3] = cmul(v[3], twc<DIR>(R2, R2));
    v[5] = mul_i<DIR>(v[5]);
    v[7] = cmul(v[7], twc<DIR>(-R2, R2));
#pragma unroll
    for (int d = 0; d < 4; ++d) { cpx p = v[2 * d], q = v[2 * d + 1]; v[2 * d] = cadd(p, q); v[2 * d + 1] = csub(p, q); }
}
__device__ __forceinline__ int pad(int i) { return i + (i >> 4); }
__device__ __forceinline__ cpx expi(float x) { float s, c; sincospif(x, &s, &c); return cpx{c, s}; }
template <int R, int DIR> __device__ __forceinline__ void twiddle(cpx* v, cpx w1) {
    cpx w[R];
    w[1] = w1;
#pragma unroll
    for (int r = 2; r < R; ++r) w[r] = (r & 1) ? cmul(w[r - 1], w1) : cmul(w[r / 2], w[r / 2]);
#pragma unroll
    for (int r = 1; r < R; ++r) v[r] = DIR < 0 ? cmul(v[r], w[r]) : cpx{v[r].x * w[r].x + v[r].y * w[r].y, v[r].y * w[r].x - v[r].x * w[r].y};
}
struct TwBase { cpx b; cpx c[4]; };
template <int N> __device__ __forceinline__ TwBase make_tw(int t) {
    TwBase tb;
    tb.b = expi(-(float)(t & 15) * (1.0f / 128.0f));
    constexpr int NQ = (N == 1024) ? 4 : 2, T = N / 16;
#pragma unroll
    for (int q = 0; q < NQ; ++q) tb.c[q] = expi(-(float)(t + T * q) * (2.0f / (float)N));
    return tb;
}
template <int N> __device__ __forceinline__ void fft_sync() {
    if constexpr (N == 1024) { __builtin_amdgcn_fence(__ATOMIC_ACQ_REL, "wavefront"); __builtin_amdgcn_wave_barrier(); } else __syncthreads();
}
template <int N, int DIR, bool RIN, bool ROUT>
__device__ __forceinline__ void fft_reg(cpx* buf, const TwBase& tb, int t, cpx (&v)[16]) {
    constexpr int T = N / 16;
    if constexpr (!RIN) {
#pragma unroll
        for (int r = 0; r < 16; ++r) v[r] = buf[pad(t + T * r)];
        fft_sync<N>();
    }
    dft16<DIR>(v);
#pragma unroll
    for (int q = 0; q < 16; ++q) buf[pad(16 * t + x16(q))] = v[q];
    fft_sync<N>();
    {
        const int k = t & 15;
#pragma unroll
        for (int r = 0; r < 16; ++r) v[r] = buf[pad(t + T * r)];
        fft_sync<N>();
        twiddle<16, DIR>(v, tb.b);
        dft16<DIR>(v);
        const int o = (t >> 4) * 256 + k;
#pragma unroll
        for (int q = 0; q < 16; ++q) buf[pad(o + 16 * x16(q))] = v[q];
        fft_sync<N>();
    }
    if constexpr (N == 1024) {
#pragma unroll
        for (int q = 0; q < 4; ++q)
#pragma unroll
            for (int r = 0; r < 4; ++r) v[4 * q + r] = buf[pad(t + 64 * q + 256 * r)];
#pragma unroll
        for (int q = 0; q < 4; ++q) {
            twiddle<4, DIR>(&v[4 * q], tb.c[q]);
            r4<DIR>(v[4 * q], v[4 * q + 1], v[4 * q + 2], v[4 * q + 3]);
#pragma unroll
            for (int r = 0; r < 4; ++r) buf[pad(t + 64 * q + 256 * r)] = v[4 * q + r];
        }
        fft_sync<N>();
    } else {
        cpx u[2][8];
#pragma unroll
        for (int q = 0; q < 2; ++q)
#pragma unroll
            for (int r = 0; r < 8; ++r) u[q][r] = buf[pad(t + 128 * q + 256 * r)];
        if constexpr (ROUT) fft_sync<N>();
#pragma unroll
        for (int q = 0; q < 2; ++q) {
            twiddle<8, DIR>(u[q], tb.c[q]);
            dft8<DIR>(u[q]);
#pragma unroll
            for (int d = 0; d < 4; ++d) {
                if constexpr (ROUT) { v[q + 2 * d] = u[q][2 * d]; v[q + 2 * d + 8] = u[q][2 * d + 1]; }
                else { buf[pad(t + 128 * q + 256 * d)] = u[q][2 * d]; buf[pad(t + 128 * q + 256 * (d + 4))] = u[q][2 * d + 1]; }
            }
        }
        if constexpr (!ROUT) fft_sync<N>();
    }
}
__device__ __forceinline__ int f_pos(int rb, int x, unsigned tw) { x = (x + rb * FROT) & 32767; return rb * 65536 + x + (x & ~8191) + (int)((tw ^ (unsigned)(rb >> 5)) & 1u) * 8192; }
__device__ __forceinline__ int w_pos(int k, int n, unsigned tw) { return k * 16384 + ((n * 4 + k * WROT) & 8191) + (int)((tw ^ (unsigned)(k >> 7)) & 1u) * 8192; }
constexpr int PB1 = 1024 + 64, PB2 = 2048 + 128;
typedef float f32x4v __attribute__((ext_vector_type(4)));
typedef unsigned u32x4v __attribute__((ext_vector_type(4)));

__device__ __forceinline__ void fft_rows_fwd(const u16* __restrict__ x, rsrc_t rsF, const unsigned tw, int rb, char* smem, int tid, const char* wr, const char* wi, const unsigned spare) {
    cpx (*buf)[PB1] = (cpx (*)[PB1])smem;
    int f = tid >> 6, t = tid & 63;
    const u16* p0 = x + (size_t)(rb * 8 + 2 * f) * 1024 + 16 * t;
    u32x4v a0 = *(const u32x4v*)p0, a1 = *(const u32x4v*)(p0 + 8), b0 = *(const u32x4v*)(p0 + 1024), b1 = *(const u32x4v*)(p0 + 1032);
    const TwBase tb = make_tw<1024>(t);
    for (int it = 0; it < 4; ++it, rb += 64) {
        asm volatile("" : "+v"(tid));
        f = tid >> 6; t = tid & 63;
        __syncthreads();
        cpx* d = &buf[f][pad(16 * t)];
#pragma unroll
        for (int e = 0; e < 4; ++e) {
            d[2 * e] = cpx{__uint_as_float(a0[e] << 16), __uint_as_float(b0[e] << 16)};
            d[2 * e + 1] = cpx{__uint_as_float(a0[e] & 0xFFFF0000u), __uint_as_float(b0[e] & 0xFFFF0000u)};
            d[8 + 2 * e] = cpx{__uint_as_float(a1[e] << 16), __uint_as_float(b1[e] << 16)};
            d[8 + 2 * e + 1] = cpx{__uint_as_float(a1[e] & 0xFFFF0000u), __uint_as_float(b1[e] & 0xFFFF0000u)};
        }
        if (it < 3) p0 += 512 * 1024;
        if (FTR) touch(((it & 1) ? wi : wr) + (size_t)((rb >> 1) & 31) * 32832 + (it >> 1) * 32 * 32832 + ((rb & 1) * 128 + (tid >> 1)) * 128, spare);
        a0 = *(const u32x4v*)p0; a1 = *(const u32x4v*)(p0 + 8); b0 = *(const u32x4v*)(p0 + 1024); b1 = *(const u32x4v*)(p0 + 1032);
        fft_sync<1024>();
        cpx z[16];
        fft_reg<1024, -1, false, false>(buf[f], tb, t, z);
        __syncthreads();
#pragma unroll
        for (int m = 0; m < 8; ++m) {
            const int idx = tid + 256 * m, k = idx >> 2, ff = idx & 3;
            const cpx zk = buf[ff][pad(k)], zn = buf[ff][pad(k ? 1024 - k : 512)];
            f32x4v o = {0.5f * (zk.x + zn.x), 0.5f * (zk.y - zn.y), 0.5f * (zk.y + zn.y), -0.5f * (zk.x - zn.x)};
            if (m == 0 && k == 0) o = f32x4v{zk.x, zn.x, zk.y, zn.y};
            st_sc1(rsF, f_pos(rb, idx * 16, tw), __builtin_bit_cast(v4u, o));
        }
    }
}
__device__ __forceinline__ void filt_transpose(const u16* __restrict__ wr, const u16* __restrict__ wi, rsrc_t rsW, const unsigned tw, const int tb, char* smem, const int tid) {
    const v4u* s0 = (const v4u*)(wr + (size_t)tb * 32 * 513);
    const v4u* s1 = (const v4u*)(wi + (size_t)tb * 32 * 513);
    v4u r0[5], r1[5];
#pragma unroll
    for (int e = 0; e < 5; ++e) {
        const int idx = tid + 512 * e, ic = idx < 2052 ? idx : 2051;
        r0[e] = s0[ic]; r1[e] = s1[ic];
    }
#pragma unroll
    for (int e = 0; e < 5; ++e) {
        const int idx = tid + 512 * e;
        if (idx < 2052) { *(v4u*)(smem + idx * 16) = r0[e]; *(v4u*)(smem + 32832 + idx * 16) = r1[e]; }
    }
    __syncthreads();
    const u16* a0 = (const u16*)smem;
    const u16* a1 = (const u16*)(smem + 32832);
    for (int idx = tid; idx < 513 * 8; idx += 512) {
        const int k = idx >> 3, n4 = (idx & 7) * 4;
        v4u v;
#pragma unroll
        for (int j = 0; j < 4; ++j) v[j] = (unsigned)a0[(n4 + j) * 513 + k] | ((unsigned)a1[(n4 + j) * 513 + k] << 16);
        st_sc1(rsW, w_pos(k, tb * 32 + n4, tw), v);
    }
}

__device__ __forceinline__ void fft_cols(rsrc_t rsF, rsrc_t rsW, const unsigned tw, int blk, const int n, char* smem, int tid) {
    cpx (*buf)[PB2] = (cpx (*)[PB2])smem;
    int c = tid >> 7, t = tid & 127;
    f32x4v v[8];
    unsigned wv[16], wn[16];
    auto wld = [&](const int k, const int n) { return __builtin_amdgcn_raw_buffer_load_b32(rsW, w_pos(k, n, tw), 0, 0); };
    auto fetch = [&](const int bk) {
        const int k0 = bk * 2, kc = k0 + c;
#pragma unroll
        for (int m = 0; m < 8; ++m) { const int idx = tid + 256 * m; v[m] = __builtin_bit_cast(f32x4v, __builtin_amdgcn_raw_buffer_load_b128(rsF, f_pos(idx >> 3, k0 * 64 + (idx & 7) * 16, tw), 0, 0)); }
#pragma unroll
        for (int m = 0; m < 16; ++m) wn[m] = wld(kc, t + 128 * m);
    };
    fetch(blk);
    const TwBase tb = make_tw<2048>(t);
    for (int it = 0; it < n; ++it, blk += 64) {
        asm volatile("" : "+v"(tid));
        c = tid >> 7; t = tid & 127;
        __syncthreads();
#pragma unroll
        for (int m = 0; m < 8; ++m) {
            const int idx = tid + 256 * m, rb = idx >> 3, pc = idx & 7;
            cpx* d = &buf[pc >> 2][pad(rb * 8 + 2 * (pc & 3))];
            d[0] = cpx{v[m][0], v[m][1]}; d[1] = cpx{v[m][2], v[m][3]};
        }
#pragma unroll
        for (int m = 0; m < 16; ++m) wv[m] = wn[m];
        fetch(it + 1 < n ? blk + 64 : blk);
        __syncthreads();
        cpx z[16];
        fft_reg<2048, -1, false, true>(buf[c], tb, t, z);
        auto wcx = [](const unsigned w) { return cpx{__uint_as_float(w << 16), __uint_as_float(w & 0xFFFF0000u)}; };
        if (blk < 2) {
#pragma unroll
            for (int m = 0; m < 16; ++m) buf[c][pad(t + 128 * m)] = z[m];
            __syncthreads();
            if (blk == 0 && c == 0) {
#pragma unroll
                for (int m = 0; m < 16; ++m) {
                    const int n = t + 128 * m, nn = (2048 - n) & 2047;
                    const cpx zn = buf[0][pad(nn)], a = wcx(wv[m]), b = wcx(wld(0, nn)), e = wcx(wld(512, n)), f = wcx(wld(512, nn));
                    const cpx h0 = cmul(cpx{0.5f * (z[m].x + zn.x), 0.5f * (z[m].y - zn.y)}, cpx{0.5f * (a.x + b.x), 0.5f * (a.y - b.y)});
                    const cpx h5 = cmul(cpx{0.5f * (z[m].y + zn.y), -0.5f * (z[m].x - zn.x)}, cpx{0.5f * (e.x + f.x), 0.5f * (e.y - f.y)});
                    z[m] = cpx{h0.x - h5.y, h0.y + h5.x};
                    wv[m] = 0x3F80u;
                }
            }
            __syncthreads();
        }
#pragma unroll
        for (int m = 0; m < 16; ++m) z[m] = cmul(z[m], wcx(wv[m]));
        fft_reg<2048, 1, true, false>(buf[c], tb, t, z);
        const float sc = 1.0f / 2048.0f;
#pragma unroll
        for (int m = 0; m < 8; ++m) {
            const int idx = tid + 256 * m, rb = idx >> 3, pc = idx & 7;
            const cpx* d = &buf[pc >> 2][pad(rb * 8 + 2 * (pc & 3))];
            st_sc1(rsF, f_pos(rb, blk * 128 + pc * 16, tw), __builtin_bit_cast(v4u, f32x4v{d[0].x * sc, d[0].y * sc, d[1].x * sc, d[1].y * sc}));
        }
    }
}

__device__ __forceinline__ void fft_rows_inv(rsrc_t rsF, const unsigned tw, u16* __restrict__ out, int rb, const int step, const int n, char* smem, int tid) {
    cpx (*buf)[PB1] = (cpx (*)[PB1])smem;
    int f = tid >> 6, t = tid & 63;
    f32x4v v[8];
    auto fetch = [&](const int r) {
#pragma unroll
        for (int m = 0; m < 8; ++m) v[m] = __builtin_bit_cast(f32x4v, __builtin_amdgcn_raw_buffer_load_b128(rsF, f_pos(r, (tid + 256 * m) * 16, tw), 0, 0));
    };
    fetch(rb);
    const TwBase tb = make_tw<1024>(t);
    for (int it = 0; it < n; ++it, rb += step) {
        asm volatile("" : "+v"(tid));
        f = tid >> 6; t = tid & 63;
        __syncthreads();
        cpx* bf = buf[tid & 3];
#pragma unroll
        for (int m = 0; m < 8; ++m) {
            const int k = (tid >> 2) + 64 * m;
            if (m == 0 && k == 0) { bf[0] = cpx{v[0][0], v[0][2]}; bf[pad(512)] = cpx{v[0][1], v[0][3]}; }
            else { bf[pad(k)] = cpx{v[m][0] - v[m][3], v[m][1] + v[m][2]}; bf[pad(1024 - k)] = cpx{v[m][0] + v[m][3], v[m][2] - v[m][1]}; }
        }
        fetch(it + 1 < n ? rb + step : rb);
        __syncthreads();
        cpx z[16];
        fft_reg<1024, 1, false, false>(buf[f], tb, t, z);
        const float sc = 1.0f / 1024.0f;
        const cpx* d = &buf[f][pad(16 * t)];
        u32x4v a[2], b[2];
#pragma unroll
        for (int h = 0; h < 2; ++h)
#pragma unroll
            for (int e = 0; e < 4; ++e) {
                cpx p = d[8 * h + 2 * e], q = d[8 * h + 2 * e + 1];
                a[h][e] = cvt_pk_bf16(p.x * sc, q.x * sc);
                b[h][e] = cvt_pk_bf16(p.y * sc, q.y * sc);
            }
        u16* o0 = out + (size_t)(rb * 8 + 2 * f) * OUTW + 1024 + 16 * t;
        *(u32x4v*)o0 = a[0]; *(u32x4v*)(o0 + 8) = a[1];
        *(u32x4v*)(o0 + OUTW) = b[0]; *(u32x4v*)(o0 + OUTW + 8) = b[1];
    }
}

__device__ __forceinline__ void glds16s(unsigned voff, const void* sbase, unsigned lds_dst) {
    unsigned keep;
    asm volatile("s_mov_b32 %0, m0\n\ts_mov_b32 m0, %3\n\ts_nop 0\n\tglobal_load_lds_dwordx4 %1, %2\n\ts_mov_b32 m0, %0" : "=&s"(keep) : "v"(voff), "s"(sbase), "s"(lds_dst) : "memory");
}
#define K8_BAR do { asm volatile("" ::: "memory"); __builtin_amdgcn_s_barrier(); asm volatile("" ::: "memory"); } while (0)
#define K8_WV(n) asm volatile("s_waitcnt vmcnt(" #n ")" ::: "memory")
#define K8_WL(n) asm volatile("s_waitcnt lgkmcnt(" #n ")" ::: "memory")
#define K8_SCHED __builtin_amdgcn_sched_barrier(0)
template <int BN>
__device__ __forceinline__ int bswz2(int row) {
    if (BN >= 128) return (row & 3) | (((row >> 3) & 1) << 2);
    else return ((row >> 1) & 1) | (((row >> 3) & 1) << 1);
}
template <int BM>
__device__ __forceinline__ void gemm_tile(u16* sm, const char* A, const int s0, const unsigned tau, const u16* B, u16* C) {
    constexpr int K = HD, LDB = DM, LDC = OUTW;
    constexpr int BN = 128, BK = 64, ST = BM == 128 ? 4 : 3, WN = BM == 128 ? 4 : 2;
    constexpr int ATILE = BM * BK, BTILE = BK * BN;
    constexpr int NT = 8 / WN, GA = BM / 64, GB = 2, GS = GA + GB;
    const int tid = opaque_tid(), w = tid >> 6, l = tid & 63, i = l & 15, g = l >> 4;
    const int wr = w / WN, wc = w % WN;
    const unsigned wu = __builtin_amdgcn_readfirstlane(w);
    unsigned vb;
    { const int row = l >> 4, c = l & 15; vb = (unsigned)(row * LDB + 8 * (c ^ ((row | ((wu & 1) << 2)) << 1))) * 2u; }
    const int sg = s0 + (BM == 128 ? wu >> 1 : wu);
    const char* ab = A + (BM == 128 ? (wu & 1) * 4096 : 0);
    const char* bb = (const char*)(B + (size_t)wu * 8 * LDB);
    const unsigned lds0 = (unsigned)(uintptr_t)(__attribute__((address_space(3))) u16*)sm;
    const unsigned va = (unsigned)l * 16u, va2 = va + 2048u, vb2 = vb + 4u * LDB * 2u - 1024u;
    auto req = [&](const int buf, const int kt) {
        const unsigned d = lds0 + buf * (ATILE + BTILE) * 2;
        const int wh = kt >> 1;
        const char* pa = ab + ((blk_off(sg, (wh & 7) * 4 + (wh >> 3), tau) ^ 8192) + (kt & 1) * 2048);
        const char* pb = bb + (size_t)kt * BK * LDB * 2;
        unsigned keep;
        if (BM == 128) asm volatile("s_mov_b32 %0, m0\n\ts_mov_b32 m0, %1\n\ts_nop 0\n\tglobal_load_lds_dwordx4 %3, %5\n\tglobal_load_lds_dwordx4 %3, %5 offset:1024\n\t"
                     "s_mov_b32 m0, %2\n\ts_nop 0\n\tglobal_load_lds_dwordx4 %6, %8\n\tglobal_load_lds_dwordx4 %7, %8 offset:1024\n\ts_mov_b32 m0, %0"
                     : "=&s"(keep) : "s"(d + wu * GA * 1024), "s"(d + ATILE * 2 + wu * GB * 1024), "v"(va), "v"(va2), "s"(pa), "v"(vb), "v"(vb2), "s"(pb) : "memory");
        else asm volatile("s_mov_b32 %0, m0\n\ts_mov_b32 m0, %1\n\ts_nop 0\n\tglobal_load_lds_dwordx4 %3, %5\n\tglobal_load_lds_dwordx4 %3, %5 offset:1024\n\t"
                     "global_load_lds_dwordx4 %4, %5 offset:2048\n\tglobal_load_lds_dwordx4 %4, %5 offset:3072\n\t"
                     "s_mov_b32 m0, %2\n\ts_nop 0\n\tglobal_load_lds_dwordx4 %6, %8\n\tglobal_load_lds_dwordx4 %7, %8 offset:1024\n\ts_mov_b32 m0, %0"
                     : "=&s"(keep) : "s"(d + wu * GA * 1024), "s"(d + ATILE * 2 + wu * GB * 1024), "v"(va), "v"(va2), "s"(pa), "v"(vb), "v"(vb2), "s"(pb) : "memory");
    };
    f32x4 acc[4][NT] = {};
    constexpr int nt = K / BK;
#pragma unroll
    for (int s = 0; s < ST - 1; ++s) req(s, s);
    int boff[NT];
    const int aoff = wr * 4096 + l * 8;
#pragma unroll
    for (int n = 0; n < NT; ++n) { const int row = 8 * g + (i >> 2); boff[n] = row * BN + 16 * ((wc * NT + n) ^ bswz2<BN>(row)) + 4 * (i & 3); }
    asm volatile("s_waitcnt vmcnt(%0)" :: "i"((ST - 2) * GS) : "memory");
    K8_BAR;
    if (wu >= 4) K8_BAR;
    for (int t0 = 0; t0 < nt + ST - 1 - (nt + ST - 1) % ST; t0 += ST) {
#pragma unroll
        for (int rb = 0; rb < ST; ++rb) {
            const int t = t0 + rb;
            if (nt % ST != 0 && t >= nt) break;
            const int kk = min(t + ST - 1, nt - 1);
            const u16* as = sm + rb * (ATILE + BTILE);
            const u16* bs = as + ATILE;
            bf16x8 af[2][4], bf[2][NT];
#pragma unroll
            for (int ks = 0; ks < 2; ++ks) {
#pragma unroll
                for (int n = 0; n < NT; ++n) bf[ks][n] = tr_frag(bs + boff[n] + ks * 32 * BN, bs + boff[n] + ks * 32 * BN + 4 * BN);
#pragma unroll
                for (int m = 0; m < 4; ++m) af[ks][m] = *(const bf16x8*)(as + aoff + m * 1024 + ks * 512);
            }
            K8_SCHED;
            req((rb + ST - 1) % ST, kk);
            K8_WL(0);
            if (wu >= 4) asm volatile("s_waitcnt vmcnt(%0)" :: "i"((ST - 2) * GS) : "memory");
            K8_BAR;
            __builtin_amdgcn_s_setprio(1);
#pragma unroll
            for (int q = 0; q < 8; ++q)
#pragma unroll
                for (int n = 0; n < NT; ++n) acc[q & 3][n] = __builtin_amdgcn_mfma_f32_16x16x32_bf16(bf[q >> 2][n], af[q >> 2][q & 3], acc[q & 3][n], 0, 0, 0);
            __builtin_amdgcn_s_setprio(0);
            K8_SCHED;
            asm volatile("s_waitcnt vmcnt(%0)" :: "i"((ST - 2) * GS) : "memory");
            K8_BAR;
        }
    }
    if (wu < 4) K8_BAR;
    asm volatile("s_waitcnt vmcnt(0)" ::: "memory");
#pragma unroll
    for (int m = 0; m < 4; ++m)
#pragma unroll
        for (int n = 0; n < NT; n += 2) {
            auto r0 = __builtin_amdgcn_permlane16_swap(cvt_pk_bf16(acc[m][n][0], acc[m][n][1]), cvt_pk_bf16(acc[m][n + 1][0], acc[m][n + 1][1]), false, false);
            auto r1 = __builtin_amdgcn_permlane16_swap(cvt_pk_bf16(acc[m][n][2], acc[m][n][3]), cvt_pk_bf16(acc[m][n + 1][2], acc[m][n + 1][3]), false, false);
            *(v4u*)(C + (wr * 64 + m * 16 + i) * LDC + (wc * NT + n + (g & 1)) * 16 + 8 * (g >> 1)) = v4u{r0[0], r1[0], r0[1], r1[1]};
        }
}

__device__ __forceinline__ void xattn_unit(const ChainArgs& a, rsrc_t rsQ, rsrc_t rsK, rsrc_t rsV, rsrc_t rsS, const int s, const int sn, const int h, u16 (*Vs)[VP], unsigned* mbox, const unsigned tau, int& pend, bool& ready) {
    const int l = opaque_tid() & 63, i = l & 15, g = l >> 4;
    const unsigned known = __builtin_amdgcn_readfirstlane(*(volatile unsigned*)mbox);
    const bool early = known > (unsigned)(s + 1);
    const int bq = km_off(s, h, tau), bb = v_off(s, h, tau);
    auto count = [&](const int t) { arrive(a.sync, t < SSEG ? SY_OX + (t >> 2) * 8 : SY_OXS + (t - SSEG) * 8, t < SSEG ? 128u : 32u, 8); };
    const int xk = blk_off(s, h, tau), xq = xk ^ 8192;
    v4u vv[8], vm[2], k0f[4], k1f[4], kmf[4], q0f[4], q1f[4];
#pragma unroll
    for (int e = 0; e < 8; ++e) vv[e] = ld_sc1s(rsV, l * 16 + e * 1024, xk);
#pragma unroll
    for (int ks = 0; ks < 4; ++ks) {
        k0f[ks] = ld_sc1s(rsK, l * 16 + ks * 1024, xk);
        k1f[ks] = ld_sc1s(rsK, l * 16 + ks * 1024, xk + 4096);
        q0f[ks] = ld_sc1s(rsQ, l * 16 + ks * 1024, xq);
        q1f[ks] = ld_sc1s(rsQ, l * 16 + ks * 1024, xq + 4096);
    }
#pragma unroll
    for (int e = 0; e < 2; ++e) vm[e] = ld_sc1s(rsS, l * 16 + e * 1024, early ? bb : 1 << 30);
#pragma unroll
    for (int ks = 0; ks < 4; ++ks) kmf[ks] = ld_sc1s(rsS, ks * 512 + g * 128 + (i & 7) * 16, early ? bq : 1 << 30);
    const unsigned fl = ld_flag(a.sync + (l >= 8 ? SY_PROG * 32 : (((l & 4) ? SY_Q : SY_KV) + (min(sn, NSEG - 1) >> 3) * 8) * 32 + (l & 3)) + (h >> 2) * 32);
    if (pend >= 0) { asm volatile("s_waitcnt vmcnt(31)" ::: "memory"); count(pend); }
#pragma unroll
    for (int e = 0; e < 8; ++e) *(v4u*)&Vs[16 * (e >> 2) + i][32 * (e & 3) + 8 * g] = vv[e];
    const float scale = 0.08838834764831845f;
    const float L2E = 1.4426950408889634f;
    auto round2 = [&](const f32x4 c, float* o) {
#pragma unroll
        for (int p = 0; p < 2; ++p) {
            unsigned u = cvt_pk_bf16(c[2 * p], c[2 * p + 1]);
            float lo = __uint_as_float(u << 16) * scale, hi = __uint_as_float(u & 0xFFFF0000u) * scale;
            unsigned u2 = cvt_pk_bf16(lo, hi);
            o[2 * p] = __uint_as_float(u2 << 16);
            o[2 * p + 1] = __uint_as_float(u2 & 0xFFFF0000u);
        }
    };
    float sv[2][12], mxx[2];
    {
        f32x4 sx[3] = {};
#pragma unroll
        for (int ks = 0; ks < 4; ++ks) {
            sx[0] = __builtin_amdgcn_mfma_f32_16x16x32_bf16(as_frag(k0f[ks]), as_frag(q0f[ks]), sx[0], 0, 0, 0);
            sx[1] = __builtin_amdgcn_mfma_f32_16x16x32_bf16(as_frag(k0f[ks]), as_frag(q1f[ks]), sx[1], 0, 0, 0);
            sx[2] = __builtin_amdgcn_mfma_f32_16x16x32_bf16(as_frag(k1f[ks]), as_frag(q1f[ks]), sx[2], 0, 0, 0);
        }
        round2(sx[0], &sv[0][0]); round2(sx[1], &sv[1][0]); round2(sx[2], &sv[1][4]);
        mxx[0] = mxx[1] = -3.0e38f;
#pragma unroll
        for (int r = 0; r < 4; ++r) {
            if (4 * g + r > i) { sv[0][r] = -3.0e38f; sv[1][4 + r] = -3.0e38f; }
            mxx[0] = fmaxf(mxx[0], sv[0][r]);
            mxx[1] = fmaxf(mxx[1], fmaxf(sv[1][r], sv[1][4 + r]));
        }
    }
    const u16* vb = &Vs[4 * g + (i >> 2)][4 * (i & 3)];
    bf16x8 vfx[8];
#pragma unroll
    for (int t = 0; t < 8; ++t) vfx[t] = tr_frag(vb + 16 * t, vb + 16 * VP + 16 * t);
#pragma unroll
    for (int t = 0; t < 8; ++t) asm volatile("" : "+v"(vfx[t]));
#pragma unroll
    for (int r = 0; r < 4; ++r) asm volatile("" : "+v"(sv[0][r]), "+v"(sv[1][r]), "+v"(sv[1][4 + r]));
    asm volatile("" : "+v"(mxx[0]), "+v"(mxx[1]));
    bool have = false;
    unsigned seen = __builtin_amdgcn_readlane(fl, 8);
    ready = (sn >> 3) == (s >> 3) || (__ballot(fl != 0u) & 255ull) == 255ull;
    if (early) {
        if ((h & 3) == 0 && l == 0) atomicMax(mbox, seen);
        unsigned m = mx4(mx4(0u, vm[0]), vm[1]);
#pragma unroll
        for (int ks = 0; ks < 4; ++ks) m = mx4(m, kmf[ks]);
        have = __all(m != SENT);
    } else if ((h & 3) == 0) {
        const unsigned* pw = a.sync + (SY_PROG + (h >> 2)) * 32;
        unsigned v = seen;
        if (v <= (unsigned)(s + 1)) {
            if (s < NSEG - XA_FAST) { wait_ge<32>(pw, (unsigned)(s + 1), a.tmo, 4000u); __builtin_amdgcn_s_sleep(24); }
            else { wait_ge<6>(pw, (unsigned)(s + 1), a.tmo, 4000u); __builtin_amdgcn_s_sleep(XA_D); }
            v = (unsigned)(s + 1);
        }
        if (l == 0) atomicMax(mbox, v);
    } else {
#pragma nounroll
        for (int spin = 0; spin < 4000000 && *(volatile unsigned*)mbox < (unsigned)(s + 1); ++spin) __builtin_amdgcn_s_sleep(2);
        asm volatile("" ::: "memory");
    }
#pragma nounroll
    for (int spin = 0; !have; ++spin) {
        asm volatile("" ::: "memory");
#pragma unroll
        for (int e = 0; e < 2; ++e) vm[e] = ld_sc1s(rsS, l * 16 + e * 1024, bb);
#pragma unroll
        for (int ks = 0; ks < 4; ++ks) kmf[ks] = ld_sc1s(rsS, ks * 512 + g * 128 + (i & 7) * 16, bq);
        unsigned m = mx4(mx4(0u, vm[0]), vm[1]);
#pragma unroll
        for (int ks = 0; ks < 4; ++ks) m = mx4(m, kmf[ks]);
        if (__all(m != SENT) || spin > 20000) break;
        __builtin_amdgcn_s_sleep(2);
    }
#pragma unroll
    for (int e = 0; e < 8; ++e) *(unsigned*)&Vs[32 + (i >> 2) + 4 * ((i >> 1) & 1)][64 * (e >> 2) + 16 * g + 4 * (e & 3) + 2 * (i & 1)] = vm[e >> 2][e & 3];
    f32x4 sm_[2] = {};
#pragma unroll
    for (int ks = 0; ks < 4; ++ks) {
        sm_[0] = __builtin_amdgcn_mfma_f32_16x16x32_bf16(as_frag(kmf[ks]), as_frag(q0f[ks]), sm_[0], 0, 0, 0);
        sm_[1] = __builtin_amdgcn_mfma_f32_16x16x32_bf16(as_frag(kmf[ks]), as_frag(q1f[ks]), sm_[1], 0, 0, 0);
    }
    v4u pf[2][2];
#pragma unroll
    for (int qt = 0; qt < 2; ++qt) {
        round2(sm_[qt], &sv[qt][8]);
        float mx = fmaxf(fmaxf(mxx[qt], fmaxf(sv[qt][8], sv[qt][9])), fmaxf(sv[qt][10], sv[qt][11]));
        mx = xor16_max(mx);
        mx = xor32_max(mx);
        const float nmx = -mx * L2E;
        float sum = 0.f;
#pragma unroll
        for (int e = 0; e < 12; ++e) {
            if (qt == 0 && e >= 4 && e < 8) { sv[qt][e] = 0.f; continue; }
            float ev = __builtin_amdgcn_exp2f(fmaf(sv[qt][e], L2E, nmx));
            if (e >= 8) ev = g < 2 ? ev : 0.f;
            sv[qt][e] = ev; sum += ev;
        }
        sum = xor16_sum(sum);
        sum = xor32_sum(sum);
        const float inv = 1.0f / sum;
        pf[qt][0][0] = cvt_pk_bf16(sv[qt][0] * inv, sv[qt][1] * inv);
        pf[qt][0][1] = cvt_pk_bf16(sv[qt][2] * inv, sv[qt][3] * inv);
        pf[qt][0][2] = qt ? cvt_pk_bf16(sv[qt][4] * inv, sv[qt][5] * inv) : 0u;
        pf[qt][0][3] = qt ? cvt_pk_bf16(sv[qt][6] * inv, sv[qt][7] * inv) : 0u;
        pf[qt][1][0] = cvt_pk_bf16(sv[qt][8] * inv, sv[qt][9] * inv);
        pf[qt][1][1] = cvt_pk_bf16(sv[qt][10] * inv, sv[qt][11] * inv);
        pf[qt][1][2] = 0u; pf[qt][1][3] = 0u;
    }
    v2u vmf[8];
#pragma unroll
    for (int t = 0; t < 8; ++t) vmf[t] = tr4(&Vs[32 + 4 * (g & 1) + (i >> 2)][16 * t + 4 * (i & 3)]);
#pragma unroll
    for (int t = 0; t < 8; t += 2) {
        unsigned pk[2][2][2];
#pragma unroll
        for (int tt = 0; tt < 2; ++tt) {
            const bf16x8 vf0 = vfx[t + tt];
            const bf16x8 vf1 = as_frag(v4u{vmf[t + tt][0], vmf[t + tt][1], 0u, 0u});
#pragma unroll
            for (int qt = 0; qt < 2; ++qt) {
                f32x4 o = {};
                o = __builtin_amdgcn_mfma_f32_16x16x32_bf16(vf0, as_frag(pf[qt][0]), o, 0, 0, 0);
                o = __builtin_amdgcn_mfma_f32_16x16x32_bf16(vf1, as_frag(pf[qt][1]), o, 0, 0, 0);
                pk[qt][tt][0] = cvt_pk_bf16(o[0], o[1]);
                pk[qt][tt][1] = cvt_pk_bf16(o[2], o[3]);
            }
        }
#pragma unroll
        for (int qt = 0; qt < 2; ++qt) {
            auto r0 = __builtin_amdgcn_permlane16_swap(pk[qt][0][0], pk[qt][1][0], false, false);
            auto r1 = __builtin_amdgcn_permlane16_swap(pk[qt][0][1], pk[qt][1][1], false, false);
            v4u o = {r0[0], r1[0], r0[1], r1[1]};
            st_sc1s(rsV, (t >> 1) * 1024 + ((g & 1) * 2 + (g >> 1)) * 256 + i * 16, xq + qt * 4096, o);
        }
    }
    pend = seen > (unsigned)(sn + 1) && ready ? s : -1;
    if (pend < 0) { asm volatile("s_waitcnt vmcnt(0)" ::: "memory"); count(s); }
}

__device__ __forceinline__ void tile_stream(const ChainArgs& a, const u16* X, unsigned long long todo, const int xc, unsigned& tau, const bool early, const int nw, u16* sm8 WEVP) {
    constexpr int K = DM, LDB = HD;
    const int tid = opaque_tid(), w = tid >> 6, l = tid & 63, i = l & 15, g = l >> 4;
    const int wr = w >> 2, wc = w & 3;
    const unsigned lds0 = (unsigned)(uintptr_t)(__attribute__((address_space(3))) u16*)sm8;
    const unsigned wu = __builtin_amdgcn_readfirstlane(w);
    const unsigned ldsw = lds0 + wu * 2048;
    unsigned voffA, voffB;
    { int row = 16 * w + (l >> 3), c = l & 7; voffA = (unsigned)(row * K + 8 * (c ^ (row & 7))) * 2u; }
    { int row = 8 * w + g, c = i; voffB = (unsigned)(row * LDB + 8 * (c ^ (bswz2<128>(row) << 1))) * 2u; }
    const unsigned voffA2 = voffA + 8u * K * 2u - 1024u, voffB2 = voffB + 4u * LDB * 2u - 1024u;
    auto st2 = [&](const unsigned v0, const unsigned v1, const char* p, const unsigned d) {
        unsigned keep;
        asm volatile("s_mov_b32 %0, m0\n\ts_mov_b32 m0, %1\n\ts_nop 0\n\tglobal_load_lds_dwordx4 %2, %4\n\tglobal_load_lds_dwordx4 %3, %4 offset:1024\n\ts_mov_b32 m0, %0"
                     : "=&s"(keep) : "s"(d), "v"(v0), "v"(v1), "s"(p) : "memory");
    };
    auto stA = [&](int b, int h, const char* p) {
        st2(voffA, voffA2, p + (size_t)(h * 128) * K * 2, ldsw + (2 * b + h) * 16384);
    };
    auto stB = [&](int b, int h, const char* p) {
        st2(voffB, voffB2, p + h * 2048, ldsw + 65536 + (2 * b + h) * 16384);
    };
    constexpr int SA = 128, SB = 64 * LDB * 2;
    auto a_of = [&](const unsigned t) { return (const char*)(X + (size_t)((t >> 4) & 7) * 256 * DM); };
    auto b_of = [&](const unsigned t) { const int kind = t & 7; return (const char*)((kind < 2 ? a.Wk : (kind < 4 ? a.Wv : a.Wq)) + hcol(4 * xc + (kind & 1) * 2)); };
    auto c_of = [&](const unsigned t) { const int kind = t & 7; return ((kind & 6) == 2 ? VX_OFF - KX_OFF : 0) + (int)((t >> 4) & 7) * 4194304 + blk_off(0, 4 * xc + (kind & 1) * 2, kind < 4 ? tau : tau ^ 1u) + (int)((t >> 3) & 1u); };
    auto publish = [&](const unsigned t) {
        const int kind = t & 7, ln = lane_now();
        if (wu == 0 && ln < (kind < 4 ? 1 : 2))
            __hip_atomic_store((gu32*)(a.sync + ((kind < 4 ? SY_KV : SY_Q) + ((t >> 4) & 7) * 8 + xc) * 32 + (kind < 4 ? kind : 2 * kind - 8 + ln)), 16u, __ATOMIC_RELAXED, __HIP_MEMORY_SCOPE_AGENT);
    };
    const char* smc = (const char*)sm8;
    int aoff[2], boff[2];
#pragma unroll
    for (int ks = 0; ks < 2; ++ks) aoff[ks] = (wr * 64 + i) * 128 + (((ks * 4 + g) ^ (i & 7)) << 4);
#pragma unroll
    for (int n = 0; n < 2; ++n) { int row = 8 * g + (i >> 2); boff[n] = 65536 + row * 256 + (((wc * 2 + n) ^ bswz2<128>(row)) << 5) + 8 * (i & 3); }
    f32x4 acc[2][2][4][2] = {};
    bf16x8 At[4][2], B0[2][2], B1[2][2];
#define K8_LDA(b, h) _Pragma("unroll") for (int m = 0; m < 4; ++m) _Pragma("unroll") for (int ks = 0; ks < 2; ++ks) \
        At[m][ks] = *(const bf16x8*)(smc + aoff[ks] + (2 * (b) + (h)) * 16384 + m * 2048)
#define K8_LDB(dst, b, h) _Pragma("unroll") for (int n = 0; n < 2; ++n) _Pragma("unroll") for (int ks = 0; ks < 2; ++ks) { \
        const char* p_ = smc + boff[n] + (2 * (b) + (h)) * 16384 + ks * 8192; \
        v4i16 lo_ = __builtin_amdgcn_ds_read_tr16_b64_v4i16((__attribute__((address_space(3))) v4i16*)p_); \
        v4i16 hi_ = __builtin_amdgcn_ds_read_tr16_b64_v4i16((__attribute__((address_space(3))) v4i16*)(p_ + 1024)); \
        dst[n][ks] = bf16x8{lo_[0], lo_[1], lo_[2], lo_[3], hi_[0], hi_[1], hi_[2], hi_[3]}; }
#define K8_MMA(ai, bj, Bt) do { __builtin_amdgcn_s_setprio(1); \
        _Pragma("unroll") for (int m = 0; m < 4; ++m) _Pragma("unroll") for (int n = 0; n < 2; ++n) _Pragma("unroll") for (int ks = 0; ks < 2; ++ks) \
            acc[ai][bj][m][n] = __builtin_amdgcn_mfma_f32_16x16x32_bf16(Bt[n][ks], At[m][ks], acc[ai][bj][m][n], 0, 0, 0); \
        __builtin_amdgcn_s_setprio(0); } while (0)
    const rsrc_t rsC = __builtin_amdgcn_make_buffer_rsrc((void*)(arena(a.slots) + KX_OFF), 0, FX_OFF - KX_OFF, 0x00020000);
#define K8_EPI(ai, bj, cb) do { const int ln_ = lane_now(); \
        const int vo_ = (int)(wu & 3) * 1024 + (((ln_ >> 4) & 1) * 2 + (ln_ >> 5)) * 256 + (ln_ & 15) * 16; \
        _Pragma("unroll") for (int m = 0; m < 4; ++m) { \
            const f32x4 c0 = acc[ai][bj][m][0], c1 = acc[ai][bj][m][1]; \
            auto r0 = __builtin_amdgcn_permlane16_swap(cvt_pk_bf16(c0[0], c0[1]), cvt_pk_bf16(c1[0], c1[1]), false, false); \
            auto r1 = __builtin_amdgcn_permlane16_swap(cvt_pk_bf16(c0[2], c0[3]), cvt_pk_bf16(c1[2], c1[3]), false, false); \
            const int sg_ = 4 * ai + 2 * (int)(wu >> 2) + (m >> 1); \
            st_sc1s(rsC, vo_, ((ai ? ((cb) & ~1) ^ 8192 : (cb) & 1 ? 1 << 30 : (cb)) ^ ((sg_ & SWZ) << 15)) + sg_ * 524288 + bj * 16384 + (m & 1) * 4096, v4u{r0[0], r1[0], r0[1], r1[1]}); \
            acc[ai][bj][m][0] = ZERO4; acc[ai][bj][m][1] = ZERO4; } } while (0)
    constexpr int nt = K / 64;
    unsigned cur = (unsigned)todo & 255u, prev = 0u;
    const char* pA = a_of(cur); const char* pB = b_of(cur);
    if (TX0 && early) touch((const char*)X + 524288 + (size_t)(nw * 128 + (tid >> 2)) * 128, lds0 + 163840 - 256);
    stB(0, 0, pB); stB(0, 1, pB); stA(0, 0, pA); stA(0, 1, pA);
    stB(1, 0, pB + SB); stB(1, 1, pB + SB); stA(1, 0, pA + SA);
    K8_WV(8); K8_BAR;
    if (wu >= 4) K8_BAR;
#define K8_GO(ai) __builtin_amdgcn_s_waitcnt(0xC07F); K8_BAR; K8_MMA(ai, 0, B0); K8_MMA(ai, 1, B1); K8_SCHED; K8_BAR
    int cbp = 0;
    for (;;) {
        todo >>= 8;
        const unsigned nxt = todo != 0ull ? (unsigned)todo & 255u : cur;
        int cb = c_of(cur);
#pragma nounroll
        for (int t = 0; t < nt; t += 2) {
            if (!(TC0 & 1) && __builtin_expect(early && prev == 0u && t == TXT, 0))
                touch((const char*)X + 3 * 524288 + (size_t)(nw * 320 + min(TIDX, 319)) * 128, lds0 + 163840 - 256);
            if (__builtin_expect(early && prev == 0u && t == nt - 4, 0)) {
                tau = __builtin_amdgcn_readfirstlane((unsigned)boot_wait(a.boot) & 255u);
                cb = c_of(cur);
            }
            const bool first = t == 0 && prev != 0u, last = t == nt - 2;
            if (t == 2 && prev != 0u) { publish(prev); WEV(3) }
            K8_LDB(B0, 0, 0); K8_LDA(0, 0); K8_LDB(B1, 0, 1); K8_SCHED; stA(1, 1, pA + SA);
            pA = last ? a_of(nxt) : pA + 2 * SA; pB = last ? b_of(nxt) : pB + 2 * SB;
            if (first) { K8_EPI(1, 0, cbp); K8_EPI(1, 1, cbp); K8_WV(24); } else K8_WV(8);
            K8_GO(0);
            K8_LDA(0, 1); K8_SCHED; stB(0, 0, pB); stB(0, 1, pB); stA(0, 0, pA);
            if (first) K8_WV(24); else K8_WV(8);
            K8_GO(1);
            K8_LDB(B0, 1, 0); K8_LDA(1, 0); K8_LDB(B1, 1, 1); K8_SCHED; stA(0, 1, pA);
            if (first) K8_WV(16); else K8_WV(8);
            K8_GO(0);
            K8_LDA(1, 1); K8_SCHED; stB(1, 0, pB + SB); stB(1, 1, pB + SB); stA(1, 0, pA + SA);
            if (last) { K8_EPI(0, 0, cb); K8_EPI(0, 1, cb); K8_WV(16); } else K8_WV(8);
            K8_GO(1);
        }
        WEV(2)
        prev = cur; cbp = cb; cur = nxt;
        if (todo == 0ull) break;
    }
    K8_EPI(1, 0, cbp); K8_EPI(1, 1, cbp);
    K8_WV(0);
    if (wu < 4) K8_BAR;
    K8_BAR;
    publish(prev);
    WEV(3)
}

__device__ __forceinline__ void oproj_squad(const ChainArgs& a, u16* out, const int ct, const int rh, const int xc, const unsigned tau, char* smem) {
    const int tid = opaque_tid(), w = tid >> 6, l = tid & 63, i = l & 15, g = l >> 4;
    const int wu = __builtin_amdgcn_readfirstlane(w);
    bf16x8 wf[2][16];
    {
        u16* stg = (u16*)(smem + wu * 10240);
#pragma unroll
        for (int ch = 0; ch < 4; ++ch) {
            const u16* src = a.Wo + (size_t)hcol(4 * wu + ch) * DM + 32 * ct;
            v4u raw[8];
#pragma unroll
            for (int e = 0; e < 8; ++e) { int idx = l + 64 * e, r = idx >> 2, q4 = idx & 3; raw[e] = *(const v4u*)(src + (size_t)r * DM + 8 * q4); }
#pragma unroll
            for (int e = 0; e < 8; ++e) { int idx = l + 64 * e, r = idx >> 2, q4 = idx & 3; *(v4u*)(stg + r * 40 + 8 * q4) = raw[e]; }
            __builtin_amdgcn_s_waitcnt(0xC07F);
#pragma unroll
            for (int jj = 0; jj < 2; ++jj)
#pragma unroll
                for (int ks = 0; ks < 4; ++ks) {
                    const u16* p = stg + (32 * ks + 8 * g + (i >> 2)) * 40 + 16 * jj + 4 * (i & 3);
                    wf[jj][4 * ch + ks] = tr_frag(p, p + 4 * 40);
                }
            __builtin_amdgcn_s_waitcnt(0xC07F);
        }
    }
    const rsrc_t rsO = __builtin_amdgcn_make_buffer_rsrc((void*)(arena(a.slots) + VX_OFF), 0, 32 << 20, 0x00020000);
    float* red = (float*)(smem + 81920);
    const int voff = rh * 4096 + l * 16;
    volatile unsigned* rdy = (volatile unsigned*)(smem + 81920 + 2 * 8 * 16 * 36 * 4);
    unsigned ready = 0;
    bool pre = false;
    v4u xf[16];
    for (int s = SSEG; s < NSEG - 1; ++s) {
        if (!(ready & 1u)) {
            if (wu == 0) {
                const unsigned* q = a.sync + (SY_OXS + (s - SSEG) * 8 + xc) * 32;
#pragma nounroll
                for (int spin = 0;; ++spin) {
                    asm volatile("" ::: "memory");
                    if (ld_flag(q) != 0u) break;
                    if (spin > 20000) { __hip_atomic_store((gu32*)a.tmo, 7000u, __ATOMIC_RELAXED, __HIP_MEMORY_SCOPE_AGENT); break; }
                    if (s == SSEG) __builtin_amdgcn_s_sleep(64); else if (s < NSEG - 5) __builtin_amdgcn_s_sleep(16); else __builtin_amdgcn_s_sleep(SQ_SF);
                }
            }
            asm volatile("" ::: "memory");
            __builtin_amdgcn_s_barrier();
            asm volatile("" ::: "memory");
        }
        if (!pre) {
#pragma unroll
            for (int ks = 0; ks < 16; ++ks) xf[ks] = ld_sc1s(rsO, voff + (ks & 3) * 1024, blk_off(s, 4 * wu + (ks >> 2), tau) ^ 8192);
        }
        unsigned nf = 0;
        if (wu == 0) nf = ld_flag(a.sync + (SY_OXS + (s + 1 - SSEG + (l & 1)) * 8 + xc) * 32);
        pre = (ready & 2u) != 0u;
        f32x4 acc[2] = {};
        if (pre) {
#pragma unroll
            for (int ks = 0; ks < 16; ++ks) {
#pragma unroll
                for (int jj = 0; jj < 2; ++jj) acc[jj] = __builtin_amdgcn_mfma_f32_16x16x32_bf16(wf[jj][ks], as_frag(xf[ks]), acc[jj], 0, 0, 0);
                K8_SCHED;
                xf[ks] = ld_sc1s(rsO, voff + (ks & 3) * 1024, blk_off(s + 1, 4 * wu + (ks >> 2), tau) ^ 8192);
                K8_SCHED;
            }
        } else {
#pragma unroll
            for (int ks = 0; ks < 16; ++ks)
#pragma unroll
                for (int jj = 0; jj < 2; ++jj) acc[jj] = __builtin_amdgcn_mfma_f32_16x16x32_bf16(wf[jj][ks], as_frag(xf[ks]), acc[jj], 0, 0, 0);
        }
        float* rp = red + (s & 1) * (8 * 16 * 36);
#pragma unroll
        for (int jj = 0; jj < 2; ++jj) *(f32x4*)&rp[(wu * 16 + i) * 36 + 16 * jj + 4 * g] = acc[jj];
        if (wu == 0) rdy[s & 1] = (unsigned)__ballot(nf != 0u) & 3u;
        K8_WL(0); K8_BAR;
        ready = __builtin_amdgcn_readfirstlane(rdy[s & 1]) | (pre ? 1u : 0u);
        if (tid >= 64 && tid < 192) {
            const int t = tid - 64, row = t >> 3, q4 = t & 7;
            f32x4 sv_[8];
#pragma unroll
            for (int e = 0; e < 8; ++e) sv_[e] = *(const f32x4*)&rp[(e * 16 + row) * 36 + 4 * q4];
            const f32x4 s0 = ((sv_[0] + sv_[1]) + (sv_[2] + sv_[3])) + ((sv_[4] + sv_[5]) + (sv_[6] + sv_[7]));
            v2u o; o[0] = cvt_pk_bf16(s0[0], s0[1]); o[1] = cvt_pk_bf16(s0[2], s0[3]);
            out8(out + (size_t)(s * SEG + 16 * rh + row) * OUTW + 32 * ct + 4 * q4, o);
        }
    }
}

__device__ __forceinline__ void kv_stream(const ChainArgs& a, const u16* __restrict__ X, const u16* __restrict__ B, char* C, const int head, const int xc, const int wk, unsigned& tau, char* smem) {
    const int tid = opaque_tid(), w = tid >> 6, l = tid & 63, i = l & 15, g = l >> 4;
    const int wu = __builtin_amdgcn_readfirstlane(w), kh = wu >> 2, cq = wu & 3;
    const unsigned lds0 = (unsigned)(uintptr_t)(__attribute__((address_space(3))) char*)smem;
    unsigned voA, voB;
    { const int row = (wu & 1) * 8 + (l >> 3), c = l & 7; voA = (unsigned)(row * DM + 8 * (c ^ (row & 7))) * 2u + (wu >> 1) * 512; }
    { const int row = tid >> 4, c = tid & 15; voB = (unsigned)(row * HD + 8 * (c ^ (bswz2<128>(row) << 1))) * 2u; }
    auto stB = [&](int ch, int slot) {
#pragma unroll
        for (int e = 0; e < 4; ++e) glds16s(voB, (const char*)B + (size_t)(4 * ch + e) * 32 * HD * 2, lds0 + 65536 + slot * 32768 + e * 8192 + wu * 1024);
    };
    const unsigned ldA = lds0 + (wu >> 1) * 8192 + (wu & 1) * 1024;
    auto stP = [&](const char* p, const unsigned d) {
        unsigned keep;
        asm volatile("s_mov_b32 %0, m0\n\ts_mov_b32 m0, %1\n\ts_nop 0\n\tglobal_load_lds_dwordx4 %3, %2 offset:-4096\n\tglobal_load_lds_dwordx4 %4, %2 offset:-2048\n\t"
                     "global_load_lds_dwordx4 %5, %2\n\tglobal_load_lds_dwordx4 %6, %2 offset:2048\n\ts_mov_b32 m0, %0"
                     : "=&s"(keep) : "s"(d), "s"(p), "v"(voA + 8192u), "v"(voA + 6272u), "v"(voA + 4352u), "v"(voA + 2432u) : "memory");
    };
    auto srcA = [&](int h) { return (const char*)X + (size_t)min(h, 2 * KVS - 1) * 32768 - 4096; };
    auto stA = [&](int h) { stP(srcA(h), ldA + (h & 3) * 32768 + 4096); };
    stB(0, 0); stA(0); stA(1); stB(1, 1);
    touch((const char*)X + 65536 + min(wk * 512 + tid, (KVS - 1) * 512 - 1) * 128, lds0 + 131072 + wu * 1024);
    if (KVS > 9) touch((const char*)X + 65536 + min(4096 + wk * 512 + tid, (KVS - 1) * 512 - 1) * 128, lds0 + 131072 + wu * 1024);
    stB(2, 2);
    bf16x8 bf[16][2], af[8];
    f32x4 acc[2][2] = {}, keep[2];
    const int rb = 64 * kh + 8 * g + (i >> 2), sw = bswz2<128>(rb);
    const char* bp = smem + 65536 + rb * 256 + 8 * (i & 3);
    const char* sa0 = smem + kh * 2048 + i * 128 + 16 * (g ^ (i & 7));
    const char* sa1 = smem + kh * 2048 + i * 128 + 16 * ((4 + g) ^ (i & 7));
#define KV_AF(slot, q) *(const bf16x8*)((((q) & 1) ? sa1 : sa0) + (slot) * 32768 + ((q) >> 1) * 4096)
#pragma unroll
    for (int ch = 0; ch < 8; ++ch) {
        if (ch == 0) { if (KVS > 9) K8_WV(10); else K8_WV(9); } else if (ch < 6) K8_WV(8); else if (ch == 6) K8_WV(4); else K8_WV(0);
        K8_BAR;
#pragma unroll
        for (int e = 0; e < 4; ++e) { const char* p_ = bp + (ch % 3) * 32768 + (e >> 1) * 8192 + 32 * ((2 * cq + (e & 1)) ^ sw); bf[2 * ch + (e >> 1)][e & 1] = tr_frag(p_, p_ + 1024); }
#pragma unroll
        for (int e = 0; e < 4; ++e) af[e] = KV_AF(e >> 1, 2 * ch + (e & 1));
        K8_WL(0); K8_SCHED; K8_BAR;
        if (ch < 5) stB(ch + 3, ch % 3);
        if (ch == 7) { stA(2); stA(3); }
#pragma unroll
        for (int e = 0; e < 8; ++e) acc[e >> 2][e & 1] = __builtin_amdgcn_mfma_f32_16x16x32_bf16(bf[2 * ch + ((e >> 1) & 1)][e & 1], af[e >> 1], acc[e >> 2][e & 1], 0, 0, 0);
    }
    float* red = (float*)(smem + 131072) + l * 4;
#pragma unroll
    for (int p = 0; p < 2; ++p) { *(f32x4*)(red + (p * 8 + wu) * 256) = kh ? acc[p][0] : acc[p][1]; keep[p] = kh ? acc[p][1] : acc[p][0]; }
    unsigned long long tag = 0;
    if (wu == 4) { tag = boot_wait(a.boot); *(volatile unsigned*)(smem + 163840 - 512) = (unsigned)tag & 255u; }
    const rsrc_t rc = __builtin_amdgcn_make_buffer_rsrc((void*)C, 0, KVS * 524288, 0x00020000);
    const int vo = cq * 1024 + (2 * kh + (g >> 1)) * 256 + i * 16 + (g & 1) * 8;
    auto put = [&](const f32x4 o, const int so) {
        const v2u pk = {cvt_pk_bf16(o[0], o[1]), cvt_pk_bf16(o[2], o[3])};
        asm volatile("buffer_store_dwordx2 %0, %1, %2, %3 offen sc1" :: "v"(pk), "v"(vo), "s"(rc), "s"(so) : "memory");
    };
    K8_WL(0); K8_WV(0); K8_BAR;
    tau = __builtin_amdgcn_readfirstlane(*(volatile unsigned*)(smem + 163840 - 512));
#pragma unroll
    for (int p = 0; p < 2; ++p) put(keep[p] + *(const f32x4*)(red + (p * 8 + (wu ^ 4)) * 256), blk_off(0, head, tau) + p * 4096);
    stA(4);
    K8_WV(4); K8_BAR;
    if (tid == 256) __hip_atomic_store((gu64*)(a.boot + 256 + 16 * xc + wk), tag, __ATOMIC_RELAXED, __HIP_MEMORY_SCOPE_AGENT);
    auto stages = [&](auto kc) {
        constexpr int KH = decltype(kc)::value;
        if (KH) K8_BAR;
        f32x4 ac[2] = {ZERO4, ZERO4}, kp = ZERO4;
        int h_first = 2, so = OOB;
        asm volatile("" : "+s"(h_first));
        const char* pn = srcA(5);
        unsigned dn = ldA + 32768 + 4096;
        int r0 = (int)(sa0 - smem) + 2 * 32768, r1 = (int)(sa1 - smem) + 2 * 32768;
        for (int h = h_first; h < 2 * KVS + 4; ++h) {
            const f32x4 oth = *(const f32x4*)(red + (wu ^ 4) * 256);
            *(f32x4*)(red + wu * 256) = ac[KH ^ 1];
            bf16x8 ax[16];
#pragma unroll
            for (int q = 0; q < 16; ++q) ax[q] = *(const bf16x8*)(smem + ((q & 1) ? r1 : r0) + (q >> 1) * 4096);
            K8_SCHED;
            stP(pn, dn);
            put((KH ? ac[1] : kp) + oth, so);
            kp = ac[0];
            __builtin_amdgcn_s_waitcnt(0xC07F);
            K8_WV(10);
            if (__builtin_expect((STRICT >> h) & 1u, 0)) K8_WV(0);
            K8_BAR;
            if (KH && ((STRICT | (0xAAA80u & ~(STRICT << 2))) >> h) & 1u && tid == 256)
                __hip_atomic_store((gu32*)(a.sync + (SY_KV0 + (h / 2 - ((STRICT >> h) & 1u ? 1 : 2)) * 8 + xc) * 32 + wk), 16u, __ATOMIC_RELAXED, __HIP_MEMORY_SCOPE_AGENT);
            if (__builtin_expect(h < 2 * KVS, 1)) {
                __builtin_amdgcn_s_setprio(1);
#pragma unroll
                for (int q = 0; q < 16; ++q)
#pragma unroll
                    for (int n = 0; n < 2; ++n) ac[n] = __builtin_amdgcn_mfma_f32_16x16x32_bf16(bf[q][n], ax[q], q ? ac[n] : ZERO4, 0, 0, 0);
                __builtin_amdgcn_s_setprio(0);
            }
            {
                const int hs = h - 1 + KH;
                so = blk_off(hs >> 1, head, tau) + (hs & 1) * 4096;
                asm volatile("" : "+s"(so));
                so = hs > 1 && hs < 2 * KVS ? so : OOB;
                pn = srcA(h + 4); dn = ldA + (h & 3) * 32768 + 4096;
                r0 = (r0 + 32768) & 131071; r1 = (r1 + 32768) & 131071;
                asm volatile("" : "+s"(so), "+s"(pn), "+s"(dn), "+v"(r0), "+v"(r1));
            }
            K8_BAR;
        }
        if (!KH) K8_BAR;
    };
    if (kh) stages(std::integral_constant<int, 1>{}); else stages(std::integral_constant<int, 0>{});
    K8_WV(0);
}

struct MegaArgs {
    ChainArgs ch;
    const u16* x; u16* out;
    const u16* bx; const u16* wr; const u16* wi;
};
constexpr int MEGA_LDS = 163840;
#define WSTAMP(n)
__global__ __launch_bounds__(512, 2) void mega_kernel(MegaArgs m) {
    extern __shared__ __attribute__((aligned(256))) char chsm[];
    const int b = BIDX, xc = (b & 3) + 4 * (b >> 7), sl = (b >> 3) & 15;
    if ((b & 4) == CHS) { chain_body(m.ch, xc, sl, chsm); return; }
    const ChainArgs& a = m.ch;
    const int wk = sl, tid = TIDX;
    char* ar = arena(a.slots);
    unsigned tauw = 0;
    WSTAMP(0)
    if (wk < 8) kv_stream(a, m.x, (wk < 4 ? a.Wk : a.Wv) + hcol(4 * xc + (wk & 3)), ar + (wk < 4 ? KX_OFF : VX_OFF), 4 * xc + (wk & 3), xc, wk, tauw, chsm);
    if (TCH && wk < 8) {
        const int t = opaque_tid(), n = xc * 8 + wk;
        const unsigned sp = (unsigned)(uintptr_t)(__attribute__((address_space(3))) char*)chsm + 163840 - 256;
        touch((const char*)m.x + 3 * 524288 + (n * 320 + min(t, 319)) * 128, sp);
        touch((const char*)m.bx + (n * 512 + t) * 128, sp);
        touch((const char*)(t < 256 ? m.wr : m.wi) + (n * 256 + (t & 255)) * 128, sp);
    }
    WSTAMP(8) WEV(1)
    {
        const int p = wk & 1, q = wk & 3;
        const unsigned long long todo = wk < 2 ? 0x84u + p : wk < 4 ? TD23 + 0x01010101u * p : wk < 8 ? 0ull
                                      : wk < 12 ? (wk < 10 ? TD89 : 0x7060503010ull) + (wk < 10 ? TM89 : 0x0101010101ull) * q : (wk < 14 ? 0x54341420u : 0x64442422u) + 0x01010101u * p;
        __syncthreads();
        if (wk >= 12) {
            const int n = (xc * 4 + q) * 512 + opaque_tid();
            const unsigned sp = (unsigned)(uintptr_t)(__attribute__((address_space(3))) char*)chsm + 163840 - 256;
            if (TC0 & 1) { touch((const char*)m.x + 3 * 524288 + n * 128, sp); touch((const char*)m.x + 3 * 524288 + min(16384 + n, 20479) * 128, sp); }
            if (TC0 & 2) { touch((const char*)m.bx + n * 128, sp); touch((const char*)m.bx + (16384 + n) * 128, sp); }
            if (TC0 & 4) { touch((const char*)m.wr + n * 128, sp); touch((const char*)m.wi + n * 128, sp); }
        }
        if (todo != 0ull) tile_stream(a, m.x, todo, xc, tauw, wk >= 8, xc * 8 + (wk & 7), (u16*)chsm WEVA);
    }
    WSTAMP(2)
    if (wk < 2) {
        unsigned* mbox = (unsigned*)(chsm + 131072);
        if (tid < 5) mbox[tid] = tid ? 2u : 0u;
        __syncthreads();
        const int w = __builtin_amdgcn_readfirstlane(tid >> 6), h = 4 * xc + (w & 3);
        const rsrc_t rsQ = __builtin_amdgcn_make_buffer_rsrc((void*)(ar + KX_OFF), 0, S_LEN * HD * 4, 0x00020000);
        const rsrc_t rsK = __builtin_amdgcn_make_buffer_rsrc((void*)(ar + KX_OFF), 0, S_LEN * HD * 4, 0x00020000);
        const rsrc_t rsV = __builtin_amdgcn_make_buffer_rsrc((void*)(ar + VX_OFF), 0, S_LEN * HD * 4, 0x00020000);
        const rsrc_t rsS = slot_rsrc(a.slots);
        u16 (*Vs)[VP] = (u16 (*)[VP])(chsm + w * (40 * VP * 2));
        const unsigned tau = tauw;
        int pend = -1;
        bool ready = false;
        for (int s = 2 * wk + (w >> 2), sn; s < NSEG - 1; s = sn) {
            const int band = s >> 3;
            unsigned n = 0;
            if (lane_now() == 0) n = atomicAdd(mbox + 1 + (w & 3), 1u);
            n = __builtin_amdgcn_readfirstlane(n);
            sn = 2 * wk + (int)(n & 1u) + 4 * (int)(n >> 1);
            if (sn - s > 4) __builtin_amdgcn_s_setprio(2); else __builtin_amdgcn_s_setprio(0);
            if (s < 4) wait_ge<32>(a.sync + (SY_KV0 + (KVS - 1) * 8 + xc) * 32 + (tid & 7), 16u, a.tmo, 4100u);
            if (!ready) {
                if (band > 0) wait_ge<32>(a.sync + (SY_KV + band * 8 + xc) * 32 + (tid & 3), 1u, a.tmo, 4100u);
                wait_ge<32>(a.sync + (SY_Q + band * 8 + xc) * 32 + (tid & 3), 1u, a.tmo, 4200u);
            }
            xattn_unit(a, rsQ, rsK, rsV, rsS, s, sn, h, Vs, mbox, tau, pend, ready);
        }
        return;
    }
    if (wk >= 4 && wk < 8) {
        const int fw = (wk - 4) * 8 + xc;
        const rsrc_t rsF = __builtin_amdgcn_make_buffer_rsrc((void*)(ar + FX_OFF), 0, 16 << 20, 0x00020000);
        const rsrc_t rsW = __builtin_amdgcn_make_buffer_rsrc((void*)(ar + WT_OFF), 0, 10 << 20, 0x00020000);
        const unsigned tw = tauw ^ FNEAR;
        {
            const int tq = opaque_tid();
            for (int z = 0; z < FSR; ++z) if (TIDX >= 256) __builtin_amdgcn_s_barrier();
            fft_rows_fwd(m.bx, rsF, tw, 2 * fw + (tq >> 8), chsm + (tq >> 8) * 36864, tq & 255, (const char*)m.wr, (const char*)m.wi, (unsigned)(uintptr_t)(__attribute__((address_space(3))) char*)chsm + 163840 - 256);
            for (int z = 0; z < FSR; ++z) if (TIDX < 256) __builtin_amdgcn_s_barrier();
        }
        for (int t = fw; t < 64; t += 32) {
            __syncthreads();
            filt_transpose(m.wr, m.wi, rsW, tw, t, chsm, opaque_tid());
        }
        wg_publish(a.sync, SY_DONE, 32u, 8);
        WSTAMP(3) WEV(4)
        wg_wait_acquire<16>(a.sync + (SY_DONE + xc) * 32, a.tmo, 6000u);
        {
            const int tq = opaque_tid();
            for (int z = 0; z < fw; ++z) __builtin_amdgcn_s_sleep(FST);
            for (int z = 0; z < FSG; ++z) if (TIDX >= 256) __builtin_amdgcn_s_barrier();
            fft_cols(rsF, rsW, tw, 2 * fw + (tq >> 8), 4, chsm + (tq >> 8) * 36864, tq & 255);
            for (int z = 0; z < FSG; ++z) if (TIDX < 256) __builtin_amdgcn_s_barrier();
        }
        wg_publish(a.sync, SY_DONE + 8, 32u, 8);
        WSTAMP(4) WEV(5)
    }
    if (wk >= 4 && wk < 10) {
        const rsrc_t rsF = __builtin_amdgcn_make_buffer_rsrc((void*)(ar + FX_OFF), 0, 16 << 20, 0x00020000);
        wg_wait_acquire<16>(a.sync + (SY_DONE + 8 + xc) * 32, a.tmo, 6001u);
        const int tq = opaque_tid();
        fft_rows_inv(rsF, tauw ^ FNEAR, m.out, 2 * ((wk & 3) * 8 + xc) + (tq >> 8) + (wk < 8 ? 0 : 192), wk < 8 ? 64 : 32, wk < 8 ? 3 : 2, chsm + (tq >> 8) * 36864, tq & 255);
        WSTAMP(5) WEV(6)
    }
    if (wk >= 12) {
        const int r = 2 * (wk - 12);
        wg_wait_acquire<16>(a.sync + (SY_OX + r * 8 + xc) * 32, a.tmo, 5000u);
        wg_wait_acquire<16>(a.sync + (SY_OX + (r + 1) * 8 + xc) * 32, a.tmo, 5000u);
        WEV(7)
        gemm_tile<256>((u16*)chsm, ar + VX_OFF, 4 * r, tauw, a.Wo + xc * 128, m.out + (size_t)r * 128 * OUTW + xc * 128);
        WEV(8)
    }
    if (wk < 4) {
        const int r = wk + 6;
        wg_wait_acquire<16>(a.sync + (SY_OX + r * 8 + xc) * 32, a.tmo, 5000u);
        WEV(7)
        gemm_tile<128>((u16*)chsm, ar + VX_OFF, 4 * r, tauw, a.Wo + xc * 128, m.out + (size_t)r * 128 * OUTW + xc * 128);
        WEV(8)
        WSTAMP(9)
    }
    if (wk >= 4 && wk < 12) {
        __syncthreads();
        oproj_squad(a, m.out, (wk - 4) * 4 + (xc & 3), xc >> 2, xc, tauw, chsm);
        WSTAMP(7)
    }
}

#if defined(__HIP_DEVICE_COMPILE__)
#pragma clang attribute pop
#endif
extern "C" void kernel_launch(void* const* d_in, const int* in_sizes, int n_in,
                              void* d_out, int out_size, void* d_ws, size_t ws_size,
                              hipStream_t stream) {
    char* ws = (char*)d_ws;
    size_t off = 0;
    auto alloc = [&](size_t bytes) { void* p = ws + off; off += (bytes + 255) & ~(size_t)255; return p; };
    char* slots = (char*)alloc((size_t)ARENA_BYTES + (2u << 20));
    static_assert((SY_WORDS + 2) * 128 <= 131072, "sync block");
    unsigned* sync = (unsigned*)alloc(131072);
    unsigned long long* boot = (unsigned long long*)alloc(4096);

    MegaArgs m;
    m.ch.mem0 = (const u16*)d_in[1]; m.ch.Wq = (const u16*)d_in[2]; m.ch.Wk = (const u16*)d_in[3]; m.ch.Wv = (const u16*)d_in[4]; m.ch.Wo = (const u16*)d_in[5];
    m.ch.out = (u16*)d_out; m.ch.slots = slots; m.ch.tmo = sync + SY_WORDS * 32;
    m.ch.stamps = (unsigned long long*)(ws + (240ull << 20));
    m.ch.sync = sync; m.ch.boot = boot;
    m.x = (const u16*)d_in[0]; m.out = (u16*)d_out;
    m.bx = (const u16*)d_in[7]; m.wr = (const u16*)d_in[8]; m.wi = (const u16*)d_in[9];
    (void)hipFuncSetAttribute((const void*)mega_kernel, hipFuncAttributeMaxDynamicSharedMemorySize, MEGA_LDS);
    mega_kernel<<<256, 512, MEGA_LDS, stream>>>(m);
}
```

```cpp
#if defined(__HIP_DEVICE_COMPILE__)
#pragma clang attribute push (__attribute__((target("no-packed-fp32-ops"))), apply_to = function)
#endif
#include <hip/hip_runtime.h>
#include <hip/hip_bf16.h>
#include <stdint.h>
#include <type_traits>

typedef unsigned short u16;
#define TIDX ((int)__builtin_amdgcn_workitem_id_x())
#define BIDX ((int)__builtin_amdgcn_workgroup_id_x())
__device__ __forceinline__ int opaque_tid() { int t = TIDX; asm volatile("" : "+v"(t)); return t; }
__device__ __forceinline__ int lane_now() { int t; asm volatile("v_mbcnt_lo_u32_b32 %0, -1, 0\n\tv_mbcnt_hi_u32_b32 %0, -1, %0" : "=v"(t)); return t; }
typedef __attribute__((ext_vector_type(8))) short bf16x8;
typedef __attribute__((ext_vector_type(4))) float f32x4;

constexpr int S_LEN = 2048, DM = 1024, NH = 32, DH = 128, MM = 8, SEG = 32;
constexpr int NSEG = S_LEN / SEG;
constexpr int HD = NH * DH;
constexpr int OUTW = 2048;

typedef unsigned v4u __attribute__((ext_vector_type(4)));
typedef unsigned v2u __attribute__((ext_vector_type(2)));
typedef short v4i16 __attribute__((ext_vector_type(4)));
typedef __amdgpu_buffer_rsrc_t rsrc_t;
#define AUX_SC1 16
__device__ __forceinline__ v4u ld_sc1s(rsrc_t rs, int voff, int soff) { return __builtin_amdgcn_raw_buffer_load_b128(rs, voff, soff, AUX_SC1); }
__device__ __forceinline__ void st_sc1s(rsrc_t rs, int voff, int soff, v4u v) { __builtin_amdgcn_raw_buffer_store_b128(v, rs, voff, soff, AUX_SC1); asm volatile("s_nop 1" :: "v"(v) : "memory"); }
__device__ __forceinline__ void st8_sc1s(rsrc_t rs, int voff, int soff, v2u v) { __builtin_amdgcn_raw_buffer_store_b64(v, rs, voff, soff, AUX_SC1); }
__device__ __forceinline__ void st_sc1(rsrc_t rs, int off, v4u v) { __builtin_amdgcn_raw_buffer_store_b128(v, rs, off, 0, AUX_SC1); }
__device__ __forceinline__ void out8(void* p, v2u v) { asm volatile("global_store_dwordx2 %0, %1, off sc1" :: "v"(p), "v"(v) : "memory"); }
typedef __bf16 bf16x2_t __attribute__((ext_vector_type(2)));
typedef float f32x2_t __attribute__((ext_vector_type(2)));
__device__ __forceinline__ unsigned cvt_pk_bf16(float lo, float hi) {
    f32x2_t f = {lo, hi};
    return __builtin_bit_cast(unsigned, __builtin_convertvector(f, bf16x2_t));
}
#define SENT 0xFFBFFFBF
__device__ __forceinline__ unsigned umax3(unsigned a, unsigned b, unsigned c) { return max(max(a, b), c); }
__device__ __forceinline__ unsigned mx4(unsigned acc, v4u v) { return umax3(umax3(acc, v[0], v[1]), v[2], v[3]); }
__device__ __forceinline__ bf16x8 as_frag(v4u v) { return __builtin_bit_cast(bf16x8, v); }
__device__ __forceinline__ float xor16_sum(float x) {
    auto r = __builtin_amdgcn_permlane16_swap(__float_as_uint(x), __float_as_uint(x), false, false);
    return __uint_as_float(r[0]) + __uint_as_float(r[1]);
}
__device__ __forceinline__ float xor32_sum(float x) {
    auto r = __builtin_amdgcn_permlane32_swap(__float_as_uint(x), __float_as_uint(x), false, false);
    return __uint_as_float(r[0]) + __uint_as_float(r[1]);
}
__device__ __forceinline__ float xor16_max(float x) {
    auto r = __builtin_amdgcn_permlane16_swap(__float_as_uint(x), __float_as_uint(x), false, false);
    return fmaxf(__uint_as_float(r[0]), __uint_as_float(r[1]));
}
__device__ __forceinline__ float xor32_max(float x) {
    auto r = __builtin_amdgcn_permlane32_swap(__float_as_uint(x), __float_as_uint(x), false, false);
    return fmaxf(__uint_as_float(r[0]), __uint_as_float(r[1]));
}
constexpr int SPIN_LIMIT = 400000;
constexpr int OOB = 0x7FFFFF00;
#define PV1 2
#define PV2 11
#define LW 0
#define PVA 8
#define PL 20
#define PF 48
#define CHS 0
#define DLY_U 4
#define SWZ 0
#define FROT 4224
#define WROT 384
#define FNEAR 1u
#define FST 0
#define PG0 2
#define PG1 32
#define PGP 0
#define FTR 1
#define FSG 0
#define FSR 0
#define PB 14
#define TD2 60
#define SL0 44
#define PS 46
#define TE0 10
#define TD 60
#define PV 1
#define PC 31
#define PP 15
#define PK0 34
#define PKP 1
#define PKM 1
#define PKA 28
#define PXD 1
#define DLY_L 2
#define DLY_C 3
constexpr int NPROBE = 2;
constexpr int M_OFF = 32 * 16384, F_OFF = M_OFF + 2 * 16384, C_OFF = F_OFF + 2 * 16384, REC = C_OFF + 16 * 16384, PERWG = REC / 128, SLOT_BYTES = 34 * 2097152;
static_assert(PERWG == 6656, "arming shares");
__device__ __forceinline__ int rec_base(int s) { return (s >> 1) * 2097152 + (s & 1) * REC; }
__device__ __forceinline__ char* arena(char* slots) { return (char*)(((uintptr_t)slots + 2097151u) & ~(uintptr_t)2097151u); }
constexpr int KX_OFF = SLOT_BYTES, VX_OFF = KX_OFF + (32 << 20), FX_OFF = VX_OFF + (32 << 20), WT_OFF = FX_OFF + (16 << 20), ARENA_BYTES = WT_OFF + (10 << 20);
__device__ __forceinline__ rsrc_t slot_rsrc(char* slots) { return __builtin_amdgcn_make_buffer_rsrc((void*)arena(slots), 0, FX_OFF, 0x00020000); }
__device__ __forceinline__ int blk_off(int seg, int h, unsigned tau) { return seg * 524288 + (((h & 3) >> 1) * 16 + ((h >> 2) ^ (seg & SWZ)) * 2 + (h & 1)) * 16384 + (int)((tau ^ (unsigned)(seg >> 2)) & 1u) * 8192; }
constexpr int TM_OFF = 124 * 16384;
constexpr int CAL_OFF = 32 * 2097152;
__device__ __forceinline__ unsigned half_bits(int s, unsigned tau) { return ((tau ^ (unsigned)(s >> 1)) & 1u) ? 0xFFu : 0u; }
__device__ __forceinline__ int chunk_off(int s, int g, int j, unsigned tau) { return rec_base(s) + (j < 2 ? (g & 3) * 2 + j : 6 + 3 * g + j) * 16384 + (int)(half_bits(s, tau) & 1u) * 8192; }
__device__ __forceinline__ int x_off(int s, int h, unsigned tau) { return chunk_off(s, h >> 2, 2, tau) + (h & 3) * 2048; }
__device__ __forceinline__ int km_off(int s, int h, unsigned tau) { return chunk_off(s, h >> 2, 3, tau) + (h & 3) * 2048; }
__device__ __forceinline__ int m_off(int s, int h, unsigned tau) { return rec_base(s) + M_OFF + (h >> 4) * 16384 + (int)((half_bits(s, tau) >> (h >> 2)) & 1u) * 8192 + (h & 15) * 512; }
__device__ __forceinline__ int v_off(int s, int h, unsigned tau) { return chunk_off(s, h >> 2, 4, tau) + (h & 3) * 2048; }
__device__ __forceinline__ int f_off(int s, int g, unsigned tau) { return rec_base(s) + F_OFF + (g >> 2) * 16384 + (int)((half_bits(s, tau) >> g) & 1u) * 8192 + (g & 3) * 2048; }
__device__ __forceinline__ int hcol(int h) { return ((h & 3) * 8 + (h >> 2)) * 128; }
constexpr int VP = 144;

struct ChainArgs {
    u16* out;
    const u16* mem0; const u16* Wq; const u16* Wk; const u16* Wv; const u16* Wo;
    char* slots;
    unsigned* tmo;
    unsigned long long* stamps;
    unsigned* sync;
    unsigned long long* boot;
};
constexpr int SY_KV = 0;
constexpr int SY_Q = 64;
constexpr int SY_PROG = 128;
constexpr int SY_OX = 136;
constexpr int SY_DONE = 264;
constexpr int SY_OXS = 472;
constexpr int SY_KV0 = 408;
#define TCH 0
#define TXT 10
#define XPR 2
#define F0E 1
#define STRICT 0x20u
#define DLY_T 0
#define TC0 0
#define PLAN 1
#define TD23 0x74704240u
#define TD89 0x60503010ull
#define TM89 0x01010101ull
#define TX0 1
#define KVS 8
constexpr int SY_FLAGS = 680;
constexpr int SY_WORDS = SY_FLAGS + SY_FLAGS / 8;
#define XA_FAST 6
#define XA_D 12
#define SQ_FAST 49
#define SQ_SF 4
#define SSEGV 40
constexpr int SSEG = SSEGV;
typedef __attribute__((address_space(1))) unsigned gu32;
typedef __attribute__((address_space(1))) unsigned long long gu64;
__device__ __forceinline__ unsigned ld_flag(const unsigned* p) { return __hip_atomic_load((gu32*)p, __ATOMIC_RELAXED, __HIP_MEMORY_SCOPE_AGENT); }
__device__ __forceinline__ unsigned long long boot_tag(const unsigned long long* boot) {
    return __hip_atomic_load((gu64*)(boot + 144), __ATOMIC_RELAXED, __HIP_MEMORY_SCOPE_AGENT) + 1ull;
}
__device__ __forceinline__ unsigned long long boot_wait(const unsigned long long* boot) {
    const int l = TIDX & 63;
    unsigned long long tag = 0, x = 0, y = 0;
#pragma nounroll
    for (int spin = 0; spin < 100000; ++spin) {
        asm volatile("" ::: "memory");
        x = __hip_atomic_load((gu64*)(boot + l), __ATOMIC_RELAXED, __HIP_MEMORY_SCOPE_AGENT);
        y = __hip_atomic_load((gu64*)(boot + 64 + l), __ATOMIC_RELAXED, __HIP_MEMORY_SCOPE_AGENT);
        tag = boot_tag(boot);
        if (__all((x | 3u) == ((tag << 2) | 3u) && (y | 3u) == ((tag << 2) | 3u))) break;
        __builtin_amdgcn_s_sleep(16);
    }
    asm volatile("" ::: "memory");
    const unsigned long long side = __ballot(x & 1u);
    const unsigned sd = (unsigned)(side & 1u) | (unsigned)((side >> 15) & 2u) | (unsigned)((side >> 30) & 4u) | (unsigned)((side >> 45) & 8u);
    const unsigned m = (sd ^ (__popcll(__ballot(x & 2u)) + __popcll(__ballot(y & 2u)) > 64 ? 15u : 0u)) & 15u;
    return (tag << 8) | m | ((~m & 15u) << 4);
}
template <int SL>
__device__ __forceinline__ void wait_ge(const unsigned* p, unsigned need, unsigned* tmo, unsigned code) {
#pragma nounroll
    for (int spin = 0;; ++spin) {
        asm volatile("" ::: "memory");
        if (__all(ld_flag(p) >= need)) break;
        if (spin > 400000 / SL) { __hip_atomic_store((gu32*)tmo, code, __ATOMIC_RELAXED, __HIP_MEMORY_SCOPE_AGENT); break; }
        __builtin_amdgcn_s_sleep(SL);
    }
    asm volatile("" ::: "memory");
}
template <int SL>
__device__ __forceinline__ void wg_wait_acquire(const unsigned* p, unsigned* tmo, unsigned code) {
    if (TIDX < 64) {
        wait_ge<SL>(p, 1u, tmo, code);
        __builtin_amdgcn_fence(__ATOMIC_ACQUIRE, "agent");
        asm volatile("s_waitcnt vmcnt(0)" ::: "memory");
    }
    __syncthreads();
}
__device__ __forceinline__ void arrive(unsigned* sync, const int f, const unsigned need, const int n) {
    const int l = lane_now();
    unsigned old = 0;
    if (l == 0) old = __hip_atomic_fetch_add((gu32*)(sync + (SY_FLAGS + (f >> 3)) * 32), 1u, __ATOMIC_RELAXED, __HIP_MEMORY_SCOPE_AGENT);
    if (__builtin_amdgcn_readfirstlane(old) == need - 1u && l < n) __hip_atomic_store((gu32*)(sync + (f + l) * 32), 16u, __ATOMIC_RELAXED, __HIP_MEMORY_SCOPE_AGENT);
}
__device__ __forceinline__ void wg_publish(unsigned* sync, const int f, const unsigned need, const int n) {
    asm volatile("s_waitcnt vmcnt(0)" ::: "memory");
    __syncthreads();
    if (TIDX < 64) arrive(sync, f, need, n);
}
#define TMO_SET(code) { dead = true; if (l == 0) __hip_atomic_store(a.tmo, (code) + s, __ATOMIC_RELAXED, __HIP_MEMORY_SCOPE_AGENT); }

__device__ __forceinline__ void glds16h(unsigned voff, const void* sbase, unsigned lds_dst) {
    unsigned keep;
    asm volatile("s_mov_b32 %0, m0\n\ts_mov_b32 m0, %3\n\ts_nop 0\n\tglobal_load_lds_dwordx4 %1, %2 sc1\n\ts_mov_b32 m0, %0" : "=&s"(keep) : "v"(voff), "s"(sbase), "s"(lds_dst) : "memory");
}
__device__ __forceinline__ void touch(const void* p, unsigned lds_dst) {
    unsigned keep;
    asm volatile("s_mov_b32 %0, m0\n\ts_mov_b32 m0, %2\n\ts_nop 0\n\tglobal_load_lds_dword %1, off\n\ts_mov_b32 m0, %0" : "=&s"(keep) : "v"(p), "s"(lds_dst) : "memory");
}
__device__ __forceinline__ void touch_sc1(const void* p, unsigned lds_dst) {
    unsigned keep;
    asm volatile("s_mov_b32 %0, m0\n\ts_mov_b32 m0, %2\n\ts_nop 0\n\tglobal_load_lds_dword %1, off sc1\n\ts_mov_b32 m0, %0" : "=&s"(keep) : "v"(p), "s"(lds_dst) : "memory");
}
#define WEV(code)
#define WEVP
#define WEVA
constexpr int C2_K = 32768;
constexpr int C2_V = 65536;
constexpr int C2_R1 = 0, R1S = 3200;
constexpr int C2_R3 = C2_R1 + 4 * R1S;
constexpr int C2_MB = 24576;
#define PST(n)
#define LT(n)
#define LTA(n)
#define LTOUT0
#define LTOUT
#define CN(n)
#define CNS
#define CNOUT
#define CLOB16(a, b, c, d, e, f, g, h, i, j, k, l, m, n, o, p) "v" #a, "v" #b, "v" #c, "v" #d, "v" #e, "v" #f, "v" #g, "v" #h, "v" #i, "v" #j, "v" #k, "v" #l, "v" #m, "v" #n, "v" #o, "v" #p
#define LDS3(p) ((__attribute__((address_space(3))) v4i16*)(p))
__device__ __forceinline__ bf16x8 tr_frag(const void* p0, const void* p1) {
    v4i16 lo = __builtin_amdgcn_ds_read_tr16_b64_v4i16(LDS3(p0));
    v4i16 hi = __builtin_amdgcn_ds_read_tr16_b64_v4i16(LDS3(p1));
    return bf16x8{lo[0], lo[1], lo[2], lo[3], hi[0], hi[1], hi[2], hi[3]};
}
typedef __bf16 bf16x8v __attribute__((ext_vector_type(8)));
typedef __bf16 bf16x16v __attribute__((ext_vector_type(16)));
typedef unsigned v8u __attribute__((ext_vector_type(8)));
typedef unsigned v32u __attribute__((ext_vector_type(32)));
#define PUT8(v, o, x) { const v8u x_ = (x); _Pragma("unroll") for (int e_ = 0; e_ < 8; ++e_) (v)[(o) + e_] = x_[e_]; }
#define SUB4(v, o) v4u{(v)[(o)], (v)[(o) + 1], (v)[(o) + 2], (v)[(o) + 3]}
#define PUT4(v, o, x) { const v4u x_ = (x); (v)[(o)] = x_[0]; (v)[(o) + 1] = x_[1]; (v)[(o) + 2] = x_[2]; (v)[(o) + 3] = x_[3]; }
__device__ __forceinline__ v2u tr4(const u16* p) { return __builtin_bit_cast(v2u, __builtin_amdgcn_ds_read_tr16_b64_v4i16(LDS3(p))); }
__device__ __forceinline__ f32x4 smm(v4u a, v8u b, f32x4 c, int idx) {
    return __builtin_amdgcn_smfmac_f32_16x16x64_bf16(__builtin_bit_cast(bf16x8v, a), __builtin_bit_cast(bf16x16v, b), c, idx, 0, 0);
}
__device__ __forceinline__ v8u tr_frag64(const u16* p, const int pitch) {
    const v2u r0 = tr4(p), r1 = tr4(p + 8 * pitch), r2 = tr4(p + 32 * pitch), r3 = tr4(p + 40 * pitch);
    return v8u{r0[0], r1[0], r0[1], r1[1], r2[0], r3[0], r2[1], r3[1]};
}
#define MM(a_, b_, c_) __builtin_amdgcn_mfma_f32_16x16x32_bf16(as_frag(a_), as_frag(b_), c_, 0, 0, 0)
#define STR2(x) #x
#define STR(x) STR2(x)
#define PAUSE(n) ".rept (" STR(n) ") / 4\n\ts_nop 15\n\t.endr\n\t.rept (" STR(n) ") %% 4\n\ts_nop 3\n\t.endr\n\t"
#define ZERO4 f32x4{0.f, 0.f, 0.f, 0.f}
constexpr int XB_OFF = CAL_OFF + 262144, XP = 68;
__device__ __forceinline__ void last_rows(const char* chsm, rsrc_t rsS, const int wu, const int grp, const int c, const unsigned tau) {
    const int l = lane_now();
    __builtin_amdgcn_s_barrier();
    asm volatile("" ::: "memory");
    const float* rp = (const float*)(chsm + C2_K) + (4 * wu + (l >> 4)) * XP + 4 * (l & 15);
    const f32x4 s0 = (*(const f32x4*)rp + *(const f32x4*)(rp + 32 * XP)) + (*(const f32x4*)(rp + 64 * XP) + *(const f32x4*)(rp + 96 * XP));
    v2u o;
    o[0] = cvt_pk_bf16(s0[0], s0[1]); o[1] = cvt_pk_bf16(s0[2], s0[3]);
    st8_sc1s(rsS, l * 8, XB_OFF + ((c >> 1) * 8 + wu) * 16384 + (int)(tau & 1u) * 8192 + (c & 1) * 4096 + grp * 512, o);
}
__device__ __forceinline__ const unsigned* kv_flag(const unsigned* sync, const int sq, const int grp, const int l) { return sync + (sq < KVS ? (SY_KV0 + sq * 8 + grp) * 32 + (l & 7) : (SY_KV + 8 + grp) * 32 + (l & 3)); }
__device__ __forceinline__ void chain_body(const ChainArgs& a, const int grp, const int c, char* chsm) {
    const int wu = __builtin_amdgcn_readfirstlane(TIDX >> 6);
    const int widx = grp * 16 + c;
    const int hs = 4 * grp + (c >> 2), jq = c & 3;
    const rsrc_t rsS = slot_rsrc(a.slots);
    const unsigned l0 = (unsigned)(uintptr_t)(__attribute__((address_space(3))) char*)chsm;
    bool dead = false;

    const int cal_off = CAL_OFF + grp * 32768 + (c >> 3) * 16384 + (c & 7) * 1024;
        if (wu == 0) {
            const int l = lane_now();
            const v4u ff = {SENT, SENT, SENT, SENT};
#pragma unroll
            for (int e = 0; e < 13; ++e) st_sc1s(rsS, l * 16, widx * 2 * PERWG + min(e * 1024, 2 * PERWG - 1024), ff);
            const rsrc_t rsY = __builtin_amdgcn_make_buffer_rsrc((void*)a.sync, 0, 131072, 0x00020000);
            st_sc1s(rsY, l * 16, widx * 1024, v4u{0u, 0u, 0u, 0u});
            __builtin_amdgcn_raw_buffer_store_b32(0u, rsS, (l & 3) * 256 + ((l >> 2) & 1) * 8192, cal_off, AUX_SC1);
            if (c < 2) __builtin_amdgcn_raw_buffer_store_b32(0u, rsS, (l >> 5) * 8192 + (l & 31) * 4, TM_OFF + c * 16384 + grp * 256, AUX_SC1);
        }
    asm volatile("" ::: "memory");
    if (wu >= 4) {
        __builtin_amdgcn_s_sleep(DLY_U);
        const int l = opaque_tid() & 63, i = l & 15, g = l >> 4;
        const int sidx = (i & 1) ? 0xEEEE : 0x4444;
        const int u = wu - 4;
        const int k0 = 256 * u;
        u16* stg = (u16*)(chsm + 40960 + u * 20480);
        v4u af[4];
        v32u wS[6];
        {
            v4u raw[3][16];
            {
                const size_t wo = (size_t)k0 * HD + hcol(hs) + 32 * jq;
                const rsrc_t rq = __builtin_amdgcn_make_buffer_rsrc((void*)(a.Wq + wo), 0, 256 * HD * 2, 0x00020000);
                const rsrc_t rk = __builtin_amdgcn_make_buffer_rsrc((void*)(a.Wk + wo), 0, 256 * HD * 2, 0x00020000);
                const rsrc_t rv = __builtin_amdgcn_make_buffer_rsrc((void*)(a.Wv + wo), 0, 256 * HD * 2, 0x00020000);
                const int vw = (l >> 2) * HD * 2 + (l & 3) * 16;
#pragma unroll
                for (int p = 0; p < 4; ++p) af[p] = *(const v4u*)(a.mem0 + (size_t)(i >> 1) * DM + k0 + 64 * p + 16 * g + 8 * (i & 1));
#pragma unroll
                for (int e = 0; e < 16; ++e) raw[0][e] = __builtin_amdgcn_raw_buffer_load_b128(rq, vw, e * 16 * HD * 2, 0);
#pragma unroll
                for (int e = 0; e < 16; ++e) raw[1][e] = __builtin_amdgcn_raw_buffer_load_b128(rk, vw, e * 16 * HD * 2, 0);
#pragma unroll
                for (int e = 0; e < 12; ++e) raw[2][e] = __builtin_amdgcn_raw_buffer_load_b128(rv, vw, e * 16 * HD * 2, 0);
                __builtin_amdgcn_sched_barrier(0);
#pragma unroll
                for (int e = 0; e < 16; ++e) *(v4u*)(stg + (16 * e + (l >> 2)) * 40 + 8 * (l & 3)) = raw[0][e];
                __builtin_amdgcn_sched_barrier(0);
#pragma unroll
                for (int e = 12; e < 16; ++e) raw[2][e] = __builtin_amdgcn_raw_buffer_load_b128(rv, vw, e * 16 * HD * 2, 0);
            }
            const int fr = 16 * (g >> 1) + 4 * (g & 1) + (i >> 2);
            __builtin_amdgcn_sched_barrier(0);
#pragma unroll
            for (int hx = 0; hx < 3; ++hx) {
                if (hx) {
#pragma unroll
                    for (int e = 0; e < 16; ++e) *(v4u*)(stg + (16 * e + (l >> 2)) * 40 + 8 * (l & 3)) = raw[hx][e];
                }
                __builtin_amdgcn_s_waitcnt(0xC07F);
#pragma unroll
                for (int jj = 0; jj < 2; ++jj)
#pragma unroll
                    for (int p = 0; p < 4; ++p) PUT8(wS[2 * hx + jj], 8 * p, tr_frag64(stg + (64 * p + fr) * 40 + 16 * jj + 4 * (i & 3), 40))
                __builtin_amdgcn_s_waitcnt(0xC07F);
                __builtin_amdgcn_sched_barrier(0);
            }
        }
        PST(u)
        if (u == 0) { PST(4) }
        const unsigned r1w = l0 + C2_R1 + u * R1S + (2 * g * 16 + i) * 4, r1v = l0 + C2_R1 + u * R1S + 2176 + (2 * g * 16 + ((i & 9) | ((i & 2) << 1) | ((i & 4) >> 1))) * 4;
#define LD4(d, va, so, o) "buffer_load_dwordx4 v[" d "], " va ", %[rs], " so " offen offset:" o " sc1\n\t"
#define CHK(a, b, c, d) "v_max3_u32 v244, v244, v" a ", v" b "\n\tv_max3_u32 v244, v244, v" c ", v" d "\n\t"
#define CHK0(a, b, c, d) "v_max_u32 v244, v" a ", v" b "\n\tv_max3_u32 v244, v244, v" c ", v" d "\n\t"
#define SMF(d, a, w) "v_smfmac_f32_16x16x64_bf16 v[" d "], v[" a "], v[" w "], %[sx]\n\t"
#define ADD(d, a, b) "v_add_f32 v" d ", v" a ", v" b "\n\t"
#define Z2(r) "v_mov_b64 v[" r "], 0\n\t"
#define DW2(ad, x, y, o0, o1) "ds_write2_b32 " ad ", v" x ", v" y " offset0:" o0 " offset1:" o1 "\n\t"
#define A0 "224:227"
#define A1 "228:231"
#define A2 "232:235"
#define A3 "236:239"
#define Q0 "200:203"
#define Q1 "204:207"
#define Q2 "208:211"
#define Q3 "212:215"
#define V0 "216:219"
#define V1 "220:223"
#define S1_HEAD \
            Z2("216:217") Z2("218:219") Z2("220:221") Z2("222:223") \
            "s_waitcnt vmcnt(4)\n\t" ZQ \
            "s_waitcnt vmcnt(3)\n\t" SMF(V0, A0, "136:143") CHK0("224", "225", "226", "227") SMF(V1, A0, "168:175") \
            "s_waitcnt vmcnt(2)\n\t" SMF(V0, A1, "144:151") CHK("228", "229", "230", "231") SMF(V1, A1, "176:183") \
            "s_waitcnt vmcnt(1)\n\t" SMF(V0, A2, "152:159") CHK("232", "233", "234", "235") SMF(V1, A2, "184:191") \
            "s_waitcnt vmcnt(0)\n\t" SMF(V0, A3, "160:167") CHK("236", "237", "238", "239") SMF(V1, A3, "192:199") \
            "v_cmp_eq_u32 vcc, " STR(SENT) ", v244\n\t"
#define S1_TAIL(B1, B2) \
            SMF(Q0, A0, "8:15") SMF(Q0, A1, "16:23") SMF(Q0, A2, "24:31") \
            ADD("240", "216", "217") ADD("241", "218", "219") SMF(Q0, A3, "32:39") DW2("%[rv]", "240", "241", "0", "16") \
            ADD("242", "220", "221") ADD("243", "222", "223") SMF(Q1, A0, "40:47") DW2("%[rv]", "242", "243", "128", "144") \
            SMF(Q1, A1, "48:55") SMF(Q1, A2, "56:63") SMF(Q1, A3, "64:71") \
            ADD("240", "200", "201") ADD("241", "202", "203") SMF(Q2, A0, "72:79") DW2("%[rw]", "240", "241", "0", "16") \
            SMF(Q2, A1, "80:87") SMF(Q2, A2, "88:95") "s_nop 1\n\t" \
            ADD("242", "204", "205") ADD("243", "206", "207") SMF(Q2, A3, "96:103") DW2("%[rw]", "242", "243", "136", "152") \
            SMF(Q3, A0, "104:111") SMF(Q3, A1, "112:119") \
            B1        \
            LTA(0) \
            SMF(Q3, A2, "120:127") SMF(Q3, A3, "128:135") "s_nop 0\n\t" \
            ADD("240", "208", "209") ADD("241", "210", "211") DW2("v246", "240", "241", "0", "16") \
            "s_nop 3\n\t" \
            ADD("242", "212", "213") ADD("243", "214", "215") DW2("v246", "242", "243", "136", "152") \
            "s_waitcnt lgkmcnt(0)\n\t" B2 "s_barrier\n\t"
#define ZQ Z2("200:201") Z2("202:203") Z2("204:205") Z2("206:207") Z2("208:209") Z2("210:211") Z2("212:213") Z2("214:215")
#define U_W "{v[8:39]}"(wS[0]), "{v[40:71]}"(wS[1]), "{v[72:103]}"(wS[2]), "{v[104:135]}"(wS[3]), "{v[136:167]}"(wS[4]), "{v[168:199]}"(wS[5])
#define U_CLOB CLOB16(200, 201, 202, 203, 204, 205, 206, 207, 208, 209, 210, 211, 212, 213, 214, 215), CLOB16(216, 217, 218, 219, 220, 221, 222, 223, 224, 225, 226, 227, 228, 229, 230, 231), \
            CLOB16(232, 233, 234, 235, 236, 237, 238, 239, 240, 241, 242, 243, 244, 245, 246, 247)
        __builtin_amdgcn_s_setprio(2);
        {
            asm volatile(
                "v_add_u32 v246, 1088, %[rw]\n\t"
                S1_HEAD "s_nop 0\n\t" S1_TAIL("s_nop 1\n\t", "s_barrier\n\t")
                : LTOUT0
                : U_W, "{v[224:227]}"(af[0]), "{v[228:231]}"(af[1]), "{v[232:235]}"(af[2]), "{v[236:239]}"(af[3]), [sx] "v"(sidx), [rv] "v"(r1v), [rw] "v"(r1w)
                : "memory", "vcc", CLOB16(200, 201, 202, 203, 204, 205, 206, 207, 208, 209, 210, 211, 212, 213, 214, 215), CLOB16(216, 217, 218, 219, 220, 221, 222, 223, 240, 241, 242, 243, 244, 245, 246, 247));
        }
        const unsigned tau = __builtin_amdgcn_readfirstlane(*(volatile unsigned*)(chsm + C2_MB + 8));
        const unsigned long long wmask = u == 3 ? (c == 0 ? ~0ull : 0ull) : u == 1 ? 0x0020202020202000ull : u == 2 ? 1ull << (NSEG - 3) : 0ull;
        const int pvv = u == 3 ? 2 : -1, pvm = u == 3 ? -1 : 0;
        const unsigned* pfl = a.sync + (u == 3 ? SY_PROG + grp : u == 1 ? SY_KV + 16 + grp : SY_Q + 7 * 8 + grp) * 32;
        int s_first = 0;
        asm volatile("s_branch 1f\n\t.p2align 8\n1:" : "+s"(s_first));
        for (int s = s_first; s < NSEG - 1; ++s) {
            __builtin_amdgcn_s_setprio(0);
            LT(1)
            const int pv = (wmask >> s) & 1ull ? pvv + (s & pvm) : 0;
            const unsigned* pa = pfl;
            if (pv < 0) pfl += 256;
            const int cb = grp & 3;
            int soffc = rec_base(s) + C_OFF + c * 16384 + (int)((half_bits(s, tau) >> grp) & 1u) * 8192 + cb * 2048;
            int o_as = rec_base(s + 1) + ((c >> 3) + (grp >> 2) * 4) * 16384 + (int)((half_bits(s + 1, tau) >> grp) & 1u) * 8192 + ((4 * c + cb) & 31) * 256;
            int soff = chunk_off(s + 1, grp, u >> 1, tau) + (u & 1) * 4096;
            const int o_tm = (s >> 1) * 2097152 + TM_OFF + (s & 1) * 16384;
            int lim = dead ? 0 : SPIN_LIMIT, bad = 0;
            const bool rcd = u > 0 || widx < 32;
            const int pc = u > 0 ? 3 * widx + u - 1 : rcd ? 384 + widx : s * 96 + widx - 32;
            const int o_arm = (rcd ? s + 2 < NSEG : pc < 512) ? (rcd ? rec_base(s + 2) : XB_OFF) + (pc >> 3) * 16384 + (int)(half_bits(rcd ? s + 2 : 0, tau) & 1u) * 8192 + (pc & 7) * 1024 : OOB;
            bool there = true;
            if (__builtin_expect(s <= KVS, 0)) {
                there = __all(ld_flag(kv_flag(a.sync, s + 2, grp, l)) != 0u);
                *(volatile unsigned*)(chsm + C2_MB + 32 + (s & 1) * 16 + u * 4) = there ? 1u : 0u;
                __builtin_amdgcn_s_waitcnt(0xC07F);
            }
            const char* vb = !there ? nullptr : arena(a.slots) + (s + 2 >= NSEG ? KX_OFF : VX_OFF) + (blk_off(min(s + 2, NSEG - 1), 4 * grp + 2 * u - 2, tau) ^ (s + 2 >= NSEG ? 8192 : 0));
            asm volatile("" : "+s"(soffc), "+s"(o_as), "+s"(soff), "+s"(lim), "+s"(vb));
#define GL(o) "global_load_lds_dwordx4 %[l16], s[84:85] offset:" o " sc1\n\t" PAUSE(PGP)
#define GL4 GL("0") GL("1024") GL("2048") GL("3072")
#define GLN(d) "s_add_u32 m0, m0, 4096\n\ts_add_u32 s84, s84, " d "\n\ts_addc_u32 s85, s85, 0\n\t"
#define PKE PAUSE(PKA)
#define C_TRY(x, x0, x1, x2, x3, y, y0, y1, y2, y3) \
                "s_waitcnt vmcnt(3)\n\t" \
                "v_mfma_f32_16x16x32_bf16 v[240:243], v[216:219], v[" x "], 0\n\t" CHK0(x0, x1, x2, x3) \
                "s_waitcnt vmcnt(2)\n\t" \
                "v_mfma_f32_16x16x32_bf16 v[240:243], v[216:219], v[" y "], v[240:243]\n\t" CHK(y0, y1, y2, y3) \
                "v_cmp_eq_u32 vcc, " STR(SENT) ", v244\n\t" \
                "s_cbranch_vccz 3b\n\t" CN(1) \
                LD4(x, "%[l16]", "%[sc]", "0") LD4(y, "%[l16]", "%[sc]", "1024")
            asm volatile(
                "v_and_b32 v221, 16, %[l16]\n\t"
                "v_mov_b32 v222, 0x3f80\n\t"
                "v_bfe_u32 v223, %[l16], 5, 2\n\t"
                "v_lshlrev_b32 v221, v221, v222\n\t"
                "v_cmp_eq_u32 vcc, 0, v223\n\tv_and_b32 v220, 0xf0, %[l16]\n\tv_bfe_u32 v222, %[l16], 8, 1\n\tv_cndmask_b32 v216, 0, v221, vcc\n\t"
                "v_cmp_eq_u32 vcc, 1, v223\n\tv_lshl_add_u32 v220, v222, 3, v220\n\tv_lshrrev_b32 v222, 9, %[l16]\n\tv_cndmask_b32 v217, 0, v221, vcc\n\t"
                "v_cmp_eq_u32 vcc, 2, v223\n\tv_lshl_add_u32 v220, v222, 30, v220\n\ts_nop 0\n\tv_cndmask_b32 v218, 0, v221, vcc\n\t"
                "v_cmp_eq_u32 vcc, 3, v223\n\ts_nop 1\n\tv_cndmask_b32 v219, 0, v221, vcc\n\t"
                "v_mul_u32_u24 v222, 0x8000, v222\n\t"
                "v_and_b32 v221, 0xff, v220\n\t"
                "v_add_u32 v221, v221, v222\n\t"
                "v_lshrrev_b32 v247, 2, %[l16]\n\t"
                PKE
                "v_mov_b32 v240, " STR(SENT) "\n\tv_mov_b32 v241, v240\n\tv_mov_b32 v242, v240\n\tv_mov_b32 v243, v240\n\t"
                "buffer_store_dwordx4 v[240:243], %[l16], %[rs], %[arm] offen sc1\n\t"
                LTA(2)
                "s_setprio 2\n\t"
                "s_barrier\n\t"
                LTA(3)
                "v_add_u32 v246, 1088, %[rw]\n\t"
                "s_cmp_lt_u32 %[uu], 1\n\t"
                "s_cbranch_scc1 7f\n\t"
                "s_cmp_eq_u32 %[uu], 3\n\t"
                "s_cbranch_scc1 20f\n\t"
                "s_memrealtime s[84:85]\n\t"
                "v_lshrrev_b32 v241, 9, %[l16]\n\t"
                "v_and_b32 v242, 0x7c, v247\n\t"
                "v_lshl_add_u32 v241, v241, 13, v242\n\t"
                "s_waitcnt lgkmcnt(0)\n\t"
                "v_mov_b32 v240, s84\n\t"
                "buffer_store_dword v240, v241, %[rs], %[ts] offen sc1\n\t"
                "s_cmp_eq_u64 %[vb], 0\n\t"
                "s_cbranch_scc1 11f\n\t"
                "s_mov_b32 s86, m0\n\t"
                "s_mov_b64 s[84:85], %[vb]\n\t"
                "s_mov_b32 m0, %[lv]\n\t"
                PAUSE(PG0)
                "s_cmp_lg_u32 %[ge], 0\n\t"
                "s_cbranch_scc1 12f\n\t"
                PAUSE(PG1)
                "12:\n\t"
                GL4 GLN("4096") GL4 GLN("12288") GL4 GLN("4096") GL4
                "s_mov_b32 m0, s86\n\t"
                "s_cmp_lt_u32 %[ge], 2\n\t"
                "s_cbranch_scc1 11f\n\t"
                "s_waitcnt vmcnt(0)\n\t"
                "11:\n\t"
                "s_cmp_lt_i32 %[pv], 0\n\t"
                "s_cbranch_scc0 8f\n\t"
                "v_and_b32 v241, 12, v247\n\t"
                "10:\n\t"
                "global_load_dword v240, v241, %[pa] sc1\n\t"
                "s_waitcnt vmcnt(0)\n\t"
                "v_cmp_gt_u32 vcc, 16, v240\n\t"
                "s_cbranch_vccz 8f\n\t"
                "s_sleep 6\n\t"
                "s_sub_u32 %[lim], %[lim], 1\n\t"
                "s_cbranch_scc0 10b\n\t"
                "s_mov_b32 %[bad], 3000\n\t"
                "s_mov_b32 %[lim], 0\n\t"
                "s_branch 8f\n\t"
                "20:\n\t"
                PAUSE(PS)
                "s_memrealtime s[84:85]\n\t"
                "s_load_dword s86, %[tb], 0\n\ts_load_dword s87, %[tb], 0x100\n\ts_load_dword s88, %[tb], 0x200\n\ts_load_dword s89, %[tb], 0x300\n\t"
                "s_load_dword s90, %[tb], 0x400\n\ts_load_dword s91, %[tb], 0x500\n\ts_load_dword s92, %[tb], 0x600\n\ts_load_dword s93, %[tb], 0x700\n\t"
                "s_waitcnt lgkmcnt(0)\n\t"
                "s_sub_u32 s86, s84, s86\n\ts_sub_u32 s87, s84, s87\n\ts_sub_u32 s88, s84, s88\n\ts_sub_u32 s89, s84, s89\n\t"
                "s_sub_u32 s90, s84, s90\n\ts_sub_u32 s91, s84, s91\n\ts_sub_u32 s92, s84, s92\n\ts_sub_u32 s93, s84, s93\n\t"
                "s_or_b32 s85, s86, s87\n\ts_min_u32 s86, s86, s87\n\ts_or_b32 s85, s85, s88\n\ts_min_u32 s86, s86, s88\n\ts_or_b32 s85, s85, s89\n\ts_min_u32 s86, s86, s89\n\t"
                "s_memrealtime s[88:89]\n\t"
                "s_or_b32 s85, s85, s90\n\ts_min_u32 s86, s86, s90\n\ts_or_b32 s85, s85, s91\n\ts_min_u32 s86, s86, s91\n\ts_or_b32 s85, s85, s92\n\ts_min_u32 s86, s86, s92\n\t"
                "s_or_b32 s85, s85, s93\n\ts_min_u32 s86, s86, s93\n\t"
                "s_cmp_gt_u32 s85, 127\n\t"
                CNS
                "s_cselect_b32 s85, " STR(TE0) ", 127\n\t"
                "s_min_u32 s86, s86, s85\n\t"
                "s_sub_u32 s84, s84, s86\n\t"
                "s_add_u32 s84, s84, %[td]\n\t"
                "s_cmp_lt_i32 %[pv], 1\n\t"
                "s_cbranch_scc1 4f\n\t"
                "v_mov_b32 v247, %[pv]\n\t"
                "s_mov_b64 exec, 1\n\t"
                "global_store_dword %[l16], v247, %[pa] sc1\n\t"
                "s_mov_b64 exec, -1\n\t"
                "4:\n\t"
                "s_waitcnt lgkmcnt(0)\n\t"
                "s_sub_i32 s84, s84, s88\n\t"
                "s_max_i32 s84, s84, 0\n\t"
                "s_min_i32 s84, s84, 40\n\t"
                "21:\n\t"
                "s_sub_u32 s84, s84, 1\n\t"
                "s_cbranch_scc0 21b\n\t"
                "s_branch 8f\n\t"
                "7:\n\t"
                PAUSE(PC)
                LD4(A0, "%[l16]", "%[sc]", "0") LD4(A1, "%[l16]", "%[sc]", "1024")
                PAUSE(PP)
                LD4(A2, "%[l16]", "%[sc]", "0") LD4(A3, "%[l16]", "%[sc]", "1024")
                "s_waitcnt vmcnt(3)\n\t"
                "v_mfma_f32_16x16x32_bf16 v[240:243], v[216:219], v[224:227], 0\n\t" CHK0("224", "225", "226", "227")
                "s_waitcnt vmcnt(2)\n\t"
                "v_mfma_f32_16x16x32_bf16 v[240:243], v[216:219], v[228:231], v[240:243]\n\t" CHK("228", "229", "230", "231")
                "v_cmp_eq_u32 vcc, " STR(SENT) ", v244\n\t"
                "s_cbranch_vccnz 30f\n\t"
                "3:\n\t"
                "s_nop 3\n\t"
                "v_cvt_pk_bf16_f32 v244, v240, v241\n\t"
                "v_cvt_pk_bf16_f32 v245, v242, v243\n\t"
                "buffer_store_dwordx2 v[244:245], v221, %[rs], %[oas] offen sc1\n\t"
                PAUSE(PB)
                "8:\n\t"
                LTA(4)
                "s_barrier\n\t"
                PAUSE(1)
                "2:\n\t"
                LD4(A0, "%[l16]", "%[sa]", "0") LD4(A1, "%[l16]", "%[sa]", "1024") LD4(A2, "%[l16]", "%[sa]", "2048") LD4(A3, "%[l16]", "%[sa]", "3072")
                S1_HEAD
                "s_cbranch_vccnz 6f\n\t"
                "5:\n\t"
                LTA(5)
                S1_TAIL("s_waitcnt lgkmcnt(0)\n\ts_barrier\n\t", "")
                "s_setprio 0\n\t"
                "s_branch 9f\n\t"
                "30:\n\t" CN(1)
                LD4(A0, "%[l16]", "%[sc]", "0") LD4(A1, "%[l16]", "%[sc]", "1024")
                "1:\n\t"
                C_TRY(A2, "232", "233", "234", "235", A3, "236", "237", "238", "239")
                C_TRY(A0, "224", "225", "226", "227", A1, "228", "229", "230", "231")
                "s_sub_u32 %[lim], %[lim], 1\n\t"
                "s_cbranch_scc0 1b\n\t"
                "s_mov_b32 %[bad], 2000\n\t"
                "s_mov_b32 %[lim], 0\n\t"
                "s_branch 3b\n\t"
                "6:\n\t" CN(65536)
                "v_mov_b32 v240, %[tg]\n\t"
                "v_mov_b32 v241, %[lf]\n\t"
                "ds_write_b32 v241, v240\n\t"
                "s_nop 0\n\t"
                "s_sub_u32 %[lim], %[lim], 1\n\t"
                "s_cbranch_scc0 2b\n\t"
                "s_mov_b32 %[bad], 1\n\t"
                "s_branch 5b\n\t"
                "9:\n\t"
                : [lim] "+s"(lim), [bad] "+s"(bad), [oas] "+s"(o_as) LTOUT CNOUT
                : U_W, [l16] "v"(l * 16), [rs] "s"(rsS), [sx] "v"(sidx), [rv] "v"(r1v), [rw] "v"(r1w),
                  [sc] "s"(soffc), [sa] "s"(soff), [uu] "s"(u), [pv] "s"(pv), [pa] "s"(pa), [vb] "s"(vb), [ge] "s"(s == NSEG - 2 ? 2 : u & 1), [lv] "s"(l0 + (s + 2 >= NSEG ? C2_V + 32768 : s & 1 ? C2_K : C2_V) + (u - 1) * 16384), [td] "s"(s >= SL0 ? TD2 : TD), [ts] "s"(c == 0 && (u == 2 || s < NSEG - 3) ? o_tm + grp * 256 + (2 - u) * 2097152 : OOB), [tb] "s"(arena(a.slots) + o_tm + ((half_bits(s, tau) >> grp) & 1u) * 8192),

                  [arm] "s"(o_arm)
,
                  [tg] "s"(s + 1), [lf] "s"(l0 + C2_MB + 16)
                : "memory", "vcc", "scc", "s84", "s85", "s86", "s87", "s88", "s89", "s90", "s91", "s92", "s93", U_CLOB);
            if (__builtin_expect(bad != 0, 0)) TMO_SET((unsigned)bad)
        }
        last_rows(chsm, rsS, wu, grp, c, tau);
        if (u == 3 && widx == 0 && l == 0) __hip_atomic_store((gu64*)(a.boot + 144), *(volatile unsigned long long*)(chsm + C2_MB), __ATOMIC_RELAXED, __HIP_MEMORY_SCOPE_AGENT);
        return;
    }
    const int l = opaque_tid() & 63, i = l & 15, g = l >> 4;
    const int sidx = (i & 1) ? 0xEEEE : 0x4444;
    const int ha = 4 * grp + wu;
    unsigned long long tag;
    v32u wO[2];
    {
        u16* stg = (u16*)(chsm + 122880 + wu * 10240);
        tag = boot_tag(a.boot);
        asm volatile("" ::: "memory");
        v4u rawo[16];
        const u16* src = a.Wo + (size_t)(hcol(ha) + (l >> 3)) * DM + 64 * c + 8 * (l & 7);
#pragma unroll
        for (int e = 0; e < 16; ++e) rawo[e] = *(const v4u*)(src + (size_t)8 * e * DM);
        __builtin_amdgcn_sched_barrier(0);
        if (wu == 0) {
            asm volatile("s_waitcnt vmcnt(16)" ::: "memory");
            unsigned tm[2] = {~0u, ~0u};
            const char* pb = arena(a.slots) + cal_off;
#pragma unroll
            for (int k = 0; k < 2 * NPROBE; ++k) {
                unsigned long long c0, c1; unsigned d;
                asm volatile("s_memtime %0\n\ts_waitcnt lgkmcnt(0)\n\ts_load_dword %2, %3, %4\n\ts_waitcnt lgkmcnt(0)\n\ts_memtime %1\n\ts_waitcnt lgkmcnt(0)"
                             : "=&s"(c0), "=&s"(c1), "=&s"(d) : "s"(pb), "i"((k >> 1) * 256 + (k & 1) * 8192) : "memory");
                tm[k & 1] = min(tm[k & 1], (unsigned)(c1 - c0));
            }
            const unsigned side = (__builtin_amdgcn_s_getreg(20 | (3 << 11)) >> 2) & 1u;
            if (l == 0) __hip_atomic_store((gu64*)(a.boot + widx), (tag << 2) | (((tm[0] > tm[1] ? 1u : 0u) ^ side) << 1) | side, __ATOMIC_RELAXED, __HIP_MEMORY_SCOPE_AGENT);
            PST(7)
        }
#pragma unroll
        for (int m = 0; m < 2; ++m) {
#pragma unroll
            for (int e = 0; e < 8; ++e) *(v4u*)(stg + (8 * e + (l >> 3)) * 72 + 8 * (l & 7)) = rawo[8 * m + e];
            __builtin_amdgcn_s_waitcnt(0xC07F);
#pragma unroll
            for (int ct = 0; ct < 4; ++ct) {
                const u16* p = stg + (8 * g + (i >> 2)) * 72 + 16 * ct + 4 * (i & 3);
                const v2u r0 = tr4(p), r1 = tr4(p + 4 * 72), r2 = tr4(p + 32 * 72), r3 = tr4(p + 36 * 72);
                PUT4(wO[ct >> 1], (ct & 1) * 16 + m * 8, (v4u{r0[0], r0[1], r1[0], r1[1]})) PUT4(wO[ct >> 1], (ct & 1) * 16 + m * 8 + 4, (v4u{r2[0], r2[1], r3[0], r3[1]}))
            }
            __builtin_amdgcn_s_waitcnt(0xC07F);
        }
        if (wu == 0) { PST(5) }
        if (widx == 0 && wu == 0) *(volatile unsigned long long*)(chsm + C2_MB) = tag;
    }
    if (wu >= 2) touch_sc1(arena(a.slots) + (size_t)min(l + (wu - 2) * 64, FX_OFF / 2097152 - 1) * 2097152 + 2097024 - 128, l0 + C2_MB + 256);
    unsigned long long fw = 0;
#pragma nounroll
    for (int spin = 0; spin < 100000; ++spin) {
        asm volatile("" ::: "memory");
        fw = __hip_atomic_load((gu64*)(a.boot + 256 + 16 * grp + (l & 7)), __ATOMIC_RELAXED, __HIP_MEMORY_SCOPE_AGENT);
        if (__all((fw | 255u) == ((tag << 8) | 255u))) break;
        __builtin_amdgcn_s_sleep(4);
    }
    asm volatile("" ::: "memory");
    const unsigned tau = __builtin_amdgcn_readfirstlane((unsigned)fw & 255u);
    if (wu == 3) { *(volatile unsigned*)(chsm + C2_MB + 8) = tau; *(volatile unsigned*)(chsm + C2_MB + 16) = ~0u; __builtin_amdgcn_s_waitcnt(0xC07F); }
    if (wu == 1) { PST(9) }

    const unsigned r3a = l0 + C2_R3 + wu * 2048 + g * 128 + i * 4;
    v32u yb;
    v8u kx;
    v8u sel; v2u sel3;
#pragma unroll
    for (int d = 0; d < 8; ++d) sel[d] = (i >> 1) == d ? (0x3F80u << ((i & 1) * 16)) : 0u;
#pragma unroll
    for (int d = 0; d < 2; ++d) sel3[d] = (i & 3) == 0 && (i >> 3) == d ? (0x3F80u << (((i >> 2) & 1) * 16)) : 0u;
#define MF(d, a, b, c) "v_mfma_f32_16x16x32_bf16 v[" d "], v[" a "], v[" b "], " c "\n\t"
#define PX4(d0, d1, d2, d3, a, w0, w1, w2, w3) MF(d0, a, w0, "v[" d0 "]") MF(d1, a, w1, "v[" d1 "]") MF(d2, a, w2, "v[" d2 "]") MF(d3, a, w3, "v[" d3 "]")
#define PXT(d0, d1, d2, d3, a0, a1, a2, a3) MF(d0, a0, "32:35", "0") MF(d1, a0, "48:51", "0") MF(d2, a0, "64:67", "0") MF(d3, a0, "80:83", "0") \
        PX4(d0, d1, d2, d3, a1, "36:39", "52:55", "68:71", "84:87") PX4(d0, d1, d2, d3, a2, "40:43", "56:59", "72:75", "88:91") PX4(d0, d1, d2, d3, a3, "44:47", "60:63", "76:79", "92:95")
#define CVP(d, a, b) "v_cvt_pk_bf16_f32 v" d ", v" a ", v" b "\n\t"
#define PROJ_X(cnt) \
        "s_waitcnt " cnt "(8)\n\t" PXT("192:195", "196:199", "200:203", "204:207", "160:163", "164:167", "168:171", "172:175") \
        "s_waitcnt " cnt "(4)\n\t" PXT("208:211", "212:215", "216:219", "220:223", "176:179", "180:183", "184:187", "188:191") \
        CVP("128", "192", "193") CVP("129", "194", "195") CVP("136", "196", "197") CVP("137", "198", "199") CVP("144", "200", "201") CVP("145", "202", "203") CVP("152", "204", "205") CVP("153", "206", "207") \
        CVP("132", "208", "209") CVP("133", "210", "211") CVP("140", "212", "213") CVP("141", "214", "215") CVP("148", "216", "217") CVP("149", "218", "219") CVP("156", "220", "221") CVP("157", "222", "223") \
        "s_waitcnt " cnt "(0)\n\t"
#define CLOB_PX CLOB16(160, 161, 162, 163, 164, 165, 166, 167, 168, 169, 170, 171, 172, 173, 174, 175), CLOB16(176, 177, 178, 179, 180, 181, 182, 183, 184, 185, 186, 187, 188, 189, 190, 191), \
        CLOB16(192, 193, 194, 195, 196, 197, 198, 199, 200, 201, 202, 203, 204, 205, 206, 207), CLOB16(208, 209, 210, 211, 212, 213, 214, 215, 216, 217, 218, 219, 220, 221, 222, 223)
#define LDX(d, va, so, o) "buffer_load_dwordx4 v[" d "], " va ", %[rs], " so " offen offset:" o " sc1\n\t"
#define DSV(d, o) "ds_read_b128 v[" d "], %[lv] offset:" o "\n\t"
    const int voffX = ((i >> 1) & 1) * 4096 + g * 256 + (2 * (i >> 2) + (i & 1)) * 16;
    const int pk = (((i >> 1) & 3) << 1) | (i >> 3);
    const unsigned hv = wu >= 2 ? l0 + C2_R1 + 2176 + ((wu & 1) * 128 + ((l >> 4) + 4 * ((l >> 3) & 1)) * 16 + 8 * ((l >> 1) & 1) + 4 * ((l >> 2) & 1) + 2 * (l & 1)) * 4
                                : l0 + C2_R1 + (i & 1) * 544 + (i >> 1) * 64 + g * 16;
    const unsigned hk = l0 + C2_R1 + 1088 + (i & 1) * 544 + pk * 64 + g * 16;
    const int vo1 = wu >= 2 ? l * 4 : wu == 1 ? (g >> 1) * 1024 + i * 16 + (g & 1) * 8 : (i & 1) ? 1 << 30 : (i >> 1) * 16 + 4 * g;
    const int vo2 = (2 * (i & 1) + (g >> 1)) * 128 + pk * 16 + (g & 1) * 8;
    const int voffK = (g >> 1) * 256 + i * 16 + (g & 1) * 8;
#define RD4(w, ad, a, b, c, d) "ds_read_" w " v[" a "], " ad "\n\tds_read_" w " v[" b "], " ad " offset:3200\n\tds_read_" w " v[" c "], " ad " offset:6400\n\tds_read_" w " v[" d "], " ad " offset:9600\n\t"
#define AD4(d0, d1, d2, d3, a0, a1, a2, a3) ADD(d0, d0, a0) ADD(d1, d1, a1) ADD(d2, d2, a2) ADD(d3, d3, a3)
#define QSC(r) "v_mul_f32 v" r ", 0x3e0293ee, v" r "\n\t"
#define FLD "buffer_load_dword v114, %[vf], %[rs], %[fl] offen sc1\n\t"
#define DUM "buffer_store_dword v168, off, %[rs], %[ob] sc1\n\t"
#define QHEAD "s_barrier\n\t" RD4("b128", "%[hv]", "160:163", "164:167", "168:171", "172:175") FLD \
        "s_waitcnt lgkmcnt(2)\n\t" AD4("160", "161", "162", "163", "164", "165", "166", "167") \
        "s_waitcnt lgkmcnt(0)\n\t" AD4("168", "169", "170", "171", "172", "173", "174", "175") \
        "s_barrier\n\t"
#define QTAIL AD4("160", "161", "162", "163", "168", "169", "170", "171") QSC("160") QSC("161") QSC("162") QSC("163")
#define SWA(d, a, b) "v_add_f32_dpp v" d ", v" a ", v" b " quad_perm:[1,0,3,2] row_mask:0xf bank_mask:0xf\n\t"
#define KLD(so) "buffer_load_dwordx2 v[96:97], %[vk], %[rs], " so " offen sc1\n\tbuffer_load_dwordx2 v[102:103], %[vk], %[rs], " so " offen offset:512 sc1\n\t" \
        "s_add_u32 " so ", " so ", 4096\n\t" \
        "buffer_load_dwordx2 v[100:101], %[vk], %[rs], " so " offen sc1\n\tbuffer_load_dwordx2 v[98:99], %[vk], %[rs], " so " offen offset:512 sc1\n\t"
    auto hand_over = [&](const int o_1, const int o_2, const int o_f, const int tg) {
        int dmy = 0;
        asm volatile(
            "s_cmp_eq_u32 %[wu], 0\n\t"
            "s_cbranch_scc1 2f\n\t"
            "s_cmp_eq_u32 %[wu], 1\n\t"
            "s_cbranch_scc1 1f\n\t"
            "s_barrier\n\t"
            RD4("b64", "%[hv]", "160:161", "162:163", "164:165", "166:167") FLD
            "s_waitcnt lgkmcnt(2)\n\t" ADD("160", "160", "162") ADD("161", "161", "163")
            "s_waitcnt lgkmcnt(0)\n\t" ADD("164", "164", "166") ADD("165", "165", "167") ADD("160", "160", "164") ADD("161", "161", "165")
            CVP("168", "160", "161")
            "buffer_store_dword v168, %[v1], %[rs], %[o1] offen sc1\n\t"
            "s_barrier\n\t"
            DUM PAUSE(PV2)
            "s_branch 3f\n\t"
            "5:\n\t"
            "v_mov_b32 v115, %[lf]\n\t"
            "ds_read_b32 v115, v115\n\t"
            "s_waitcnt lgkmcnt(0)\n\t"
            "v_cmp_eq_u32 vcc, %[tg], v115\n\t"
            "s_cbranch_vccnz 4f\n\t" CN(256)
            PAUSE(PL)
            "s_branch 4f\n\t"
            "1:\n\t"
            Z2("176:177") Z2("178:179")
            "s_waitcnt vmcnt(0)\n\t"
            QHEAD QTAIL
            "s_mov_b64 exec, %[em]\n\t"
            CVP("176", "160", "161") CVP("177", "162", "163")
            "s_not_b64 exec, exec\n\t"
            CVP("178", "160", "161") CVP("179", "162", "163")
            "s_mov_b64 exec, -1\n\t"
            "s_nop 0\n\t"
            MF("180:183", "96:99", "176:179", "0") MF("184:187", "100:103", "176:179", "0")
            "s_nop 7\n\t"
            SWA("180", "184", "180") SWA("181", "185", "181") SWA("182", "186", "182") SWA("183", "187", "183")
            CVP("188", "180", "181") CVP("189", "182", "183")
            "buffer_store_dwordx2 v[188:189], %[v1], %[rs], %[o1] offen sc1\n\t"
            DUM PAUSE(PV1)
            "s_branch 3f\n\t"
            "2:\n\t"
            QHEAD
            RD4("b128", "%[hk]", "176:179", "180:183", "184:187", "188:191")
            QTAIL CVP("164", "160", "161") CVP("165", "162", "163")
            "s_waitcnt lgkmcnt(2)\n\t" AD4("176", "177", "178", "179", "180", "181", "182", "183")
            "s_waitcnt lgkmcnt(0)\n\t" AD4("184", "185", "186", "187", "188", "189", "190", "191") AD4("176", "177", "178", "179", "184", "185", "186", "187")
            CVP("166", "176", "177") CVP("167", "178", "179")
            "s_nop 1\n\t"
            "v_mfma_f32_16x16x16_bf16 v[168:171], v[166:167], v[164:165], 0\n\t"
            "buffer_store_dwordx2 v[166:167], %[v2], %[rs], %[o2] offen sc1\n\t"
            "s_nop 6\n\t"
            SWA("172", "169", "168") SWA("173", "171", "170")
            CVP("174", "172", "173")
            "buffer_store_dword v174, %[v1], %[rs], %[o1] offen sc1\n\t"
            "3:\n\t"
            PAUSE(PVA)
            "s_waitcnt vmcnt(2)\n\t"
            "v_cmp_ne_u32 vcc, " STR(SENT) ", v114\n\t"
            "s_cbranch_vccnz 5b\n\t"
            "4:\n\t"
            : [dm] "+s"(dmy) CNOUT : "{v[96:103]}"(kx), [fl] "s"(o_f), [vf] "v"(i * 128), [ob] "s"(1 << 30), [tg] "s"(tg), [lf] "s"(l0 + C2_MB + 16), [wu] "s"(wu), [hv] "v"(hv), [hk] "v"(hk), [v1] "v"(vo1), [v2] "v"(vo2), [rs] "s"(rsS), [o1] "s"(o_1), [o2] "s"(o_2), [em] "s"(0x5555555555555555ull)
            : "memory", "scc", "vcc", "v114", "v115", CLOB16(160, 161, 162, 163, 164, 165, 166, 167, 168, 169, 170, 171, 172, 173, 174, 175), CLOB16(176, 177, 178, 179, 180, 181, 182, 183, 184, 185, 186, 187, 188, 189, 190, 191));
    };
    auto o1_of = [&](const int s) { return wu >= 2 ? v_off(s, hs, tau) + jq * 512 + (wu & 1) * 256 : wu == 1 ? x_off(s, hs, tau) + jq * 256 : m_off(s, hs, tau) + jq * 128; };

    __builtin_amdgcn_s_setprio(1);
    int o_f = f_off(0, grp, tau);
    int o_cs = rec_base(0) + C_OFF + c * 16384 + (int)(half_bits(0, tau) & 1u) * 8192 + wu * 2048 + grp * 256;
    int o_1 = o1_of(0), o_2 = km_off(0, hs, tau) + jq * 512, bq = x_off(0, ha, tau), bm = m_off(0, ha, tau), bb = v_off(0, ha, tau);
    {
            const int b0 = blk_off(0, ha, tau);
            int k0 = KX_OFF + blk_off(0, hs, tau) + jq * 1024;
            asm volatile(
                LDX("160:163", "%[vx]", "%[vb]", "0") LDX("164:167", "%[vx]", "%[vb]", "1024") LDX("168:171", "%[vx]", "%[vb]", "2048") LDX("172:175", "%[vx]", "%[vb]", "3072")
                LDX("176:179", "%[vx]", "%[vb]", "128") LDX("180:183", "%[vx]", "%[vb]", "1152") LDX("184:187", "%[vx]", "%[vb]", "2176") LDX("188:191", "%[vx]", "%[vb]", "3200")
                KLD("%[k0]")
                "v_mov_b32 v131, 0\n\tv_mov_b32 v135, 0\n\tv_mov_b32 v139, 0\n\tv_mov_b32 v143, 0\n\tv_mov_b32 v147, 0\n\tv_mov_b32 v151, 0\n\tv_mov_b32 v155, 0\n\tv_mov_b32 v159, 0\n\t"
                PROJ_X("vmcnt")
                : "=&{v[128:159]}"(yb), "=&{v[96:103]}"(kx), [k0] "+s"(k0)
                : "{v[32:63]}"(wO[0]), "{v[64:95]}"(wO[1]), [vx] "v"(voffX), [vk] "v"(voffK), [rs] "s"(rsS), [vb] "s"(VX_OFF + b0)
                : "memory", "scc", CLOB_PX);
        if (wu == 1) { PST(10) }
    }
    int s_first = 0;
    asm volatile("s_branch 1f\n\t.p2align 8\n1:" : "+s"(s_first));
    for (int s = s_first; s < NSEG - 1; ++s) {
        hand_over(o_1, o_2, o_f, s);
        if (s == 0 && wu == 1) { PST(8) }
        LT(0)
#define LDPA "buffer_load_dwordx2 v[160:161], v254, %[rs], %[bm] offen sc1\n\t" LDX("164:167", "%[l16]", "%[bq]", "0") LDX("168:171", "%[l16]", "%[bq]", "1024")
#define SM(d, a, b, x) "v_smfmac_f32_16x16x64_bf16 v[" d "], v[" a "], v[" b "], " x "\n\t"
#define MX2(a, b) "v_max_u32 v244, v" a ", v" b "\n\t"
#define MX3(a, b) "v_max3_u32 v244, v244, v" a ", v" b "\n\t"
#define ZAM Z2("208:209") Z2("210:211") Z2("212:213") Z2("214:215") Z2("216:217") Z2("218:219") Z2("220:221") Z2("222:223")
#define MUL(d, a, b) "v_mul_f32 v" d ", v" a ", v" b "\n\t"
#define CV0(d, a) "v_cvt_pk_bf16_f32 v" d ", v" a ", 0\n\t"
#define RDC "ds_read_b64 v[224:225], %[rc]\n\tds_read_b64 v[226:227], %[rc] offset:2048\n\tds_read_b64 v[228:229], %[rc] offset:4096\n\tds_read_b64 v[230:231], %[rc] offset:6144\n\t"
#define EXD(d, r) "v_exp_f32 v" d ", v" r "\n\t"
#define SUBM(r) "v_sub_f32 v" r ", v" r ", v250\n\t"
#define PCVM CVP("241", "180", "180") CVP("243", "180", "180")
#define PCVX CVP("242", "178", "179") CVP("240", "176", "177")
#define OUT(t0, t1, a0, a1, a2, a3, ad, o0, o1) ADD(t0, a0, a1) ADD(t1, a2, a3) MUL(t0, t0, "250") MUL(t1, t1, "251") DW2(ad, t0, t1, o0, o1)
#define TS(n)
#define TSOUT
        int lim = dead ? 0 : SPIN_LIMIT, bad = 0;
        asm volatile(
            LDX("192:195", "%[l16]", "%[bb]", "0") LDX("196:199", "%[l16]", "%[bb]", "1024")
            ZAM Z2("224:225") Z2("226:227")
            Z2("16:17") Z2("18:19") Z2("20:21") Z2("22:23") Z2("24:25") Z2("26:27") Z2("28:29") Z2("30:31")
            "v_mov_b32 v200, 0x3f803f80\n\tv_mov_b32 v201, 0x3f803f80\n\tv_mov_b32 v202, 0x3f80\n\tv_mov_b32 v203, 0\n\t"
            "v_mov_b64 v[204:205], v[200:201]\n\tv_mov_b64 v[206:207], v[202:203]\n\t"
            "v_xor_b32 v252, 0xa000, %[sx]\n\tv_add_u32 v253, 1024, %[ra]\n\tv_lshrrev_b32 v254, 1, %[l16]\n\t"
            PAUSE(PV)
            TS(8)
            LDPA
            "4:\n\t"
            "s_waitcnt vmcnt(4)\n\t"
            SM("208:211", "192:195", "32:39", "%[sx]") MX2("192", "193") SM("212:215", "192:195", "48:55", "%[sx]") MX3("194", "195")
            SM("216:219", "192:195", "64:71", "%[sx]") SM("220:223", "192:195", "80:87", "%[sx]")
            "s_waitcnt vmcnt(3)\n\t"
            SM("208:211", "196:199", "40:47", "%[sx]") MX3("196", "197") SM("212:215", "196:199", "56:63", "%[sx]") MX3("198", "199")
            SM("216:219", "196:199", "72:79", "%[sx]") "v_cmp_eq_u32 vcc, " STR(SENT) ", v244\n\t" SM("220:223", "196:199", "88:95", "%[sx]")
            "s_cbranch_vccnz 5f\n\t"
            "1:\n\t" TS(11)
            "s_waitcnt vmcnt(2)\n\t"
            "v_mfma_f32_16x16x16_bf16 v[236:239], v[112:113], v[160:161], 0\n\t"
            MX2("160", "161") ADD("245", "208", "209") ADD("246", "210", "211") CV0("130", "245") CV0("134", "246")
            ADD("245", "212", "213") ADD("246", "214", "215") CV0("138", "245") CV0("142", "246")
            EXD("180", "236")
            "s_waitcnt vmcnt(1)\n\t"
            MF("228:231", "104:107", "164:167", "0")
            MX3("164", "165") MX3("166", "167") ADD("245", "216", "217") ADD("246", "218", "219") CV0("146", "245") CV0("150", "246")
            ADD("245", "220", "221") ADD("246", "222", "223") CV0("154", "245") CV0("158", "246") PCVM
            "s_waitcnt vmcnt(0)\n\t"
            MF("228:231", "108:111", "168:171", "v[228:231]")
            MX3("168", "169") MX3("170", "171")
            "v_cmp_eq_u32 vcc, " STR(SENT) ", v244\n\t"
            "s_cbranch_vccnz 7f\n\t"
            "2:\n\t" TS(9)
            "s_nop 3\n\t"
            EXD("179", "231") EXD("178", "230") EXD("177", "229") EXD("176", "228")
            PCVX
            "v_max3_f32 v250, |v236|, |v231|, |v230|\n\t"
            "v_max3_f32 v250, v250, |v229|, |v228|\n\t"
            SM("224:227", "240:243", "200:207", "v252")
            "v_cmp_nge_f32 vcc, 0x41c00000, v250\n\t"
            "s_nop 0\n\t"
            SM("16:19", "240:243", "128:135", "v252")
            "s_cbranch_vccnz 10f\n\t"
            "13:\n\t"
            "s_nop 0\n\t"
            SM("20:23", "240:243", "136:143", "v252") "s_nop 1\n\t"
            ADD("250", "224", "225") ADD("251", "226", "227")
            SM("24:27", "240:243", "144:151", "v252")
            "v_rcp_f32 v250, v250\n\t"
            SM("28:31", "240:243", "152:159", "v252")
            "v_rcp_f32 v251, v251\n\t"
            OUT("245", "246", "16", "17", "18", "19", "%[ra]", "0", "16")
            OUT("247", "248", "20", "21", "22", "23", "%[ra]", "128", "144")
            OUT("245", "246", "24", "25", "26", "27", "v253", "0", "16")
            OUT("247", "248", "28", "29", "30", "31", "v253", "128", "144")
            TS(10)
            "s_waitcnt lgkmcnt(0)\n\t"
            "s_barrier\n\t"
            RDC
            "v_lshrrev_b32 v247, 2, %[l16]\n\t"
            "s_waitcnt lgkmcnt(2)\n\t"
            ADD("224", "224", "226") ADD("225", "225", "227")
            "s_waitcnt lgkmcnt(0)\n\t"
            ADD("228", "228", "230") ADD("229", "229", "231") ADD("224", "224", "228") ADD("225", "225", "229")
            "v_cvt_pk_bf16_f32 v245, v224, v225\n\t"
            "buffer_store_dword v245, v247, %[rs], %[cs] offen sc1\n\t"
            "s_branch 9f\n\t"
            "10:\n\t"
            "v_max3_f32 v250, v236, v231, v230\n\t"
            "v_max3_f32 v250, v250, v229, v228\n\t"
            "s_nop 1\n\t"
            "v_max_f32_dpp v251, v250, v250 quad_perm:[1,0,3,2] row_mask:0xf bank_mask:0xf\n\t"
            "v_max_f32_dpp v244, v250, v250 quad_perm:[1,0,3,2] row_mask:0xf bank_mask:0xf\n\t"
            "s_nop 1\n\t"
            "v_permlane16_swap_b32 v251, v244\n\t"
            "v_max_f32 v250, v251, v244\n\t"
            "v_max_f32 v251, v251, v244\n\t"
            Z2("224:225") Z2("226:227")
            "v_permlane32_swap_b32 v250, v251\n\t"
            "v_max_f32 v250, v250, v251\n\t"
            Z2("16:17") Z2("18:19")
            SUBM("228") SUBM("229") SUBM("230") SUBM("231") SUBM("236")
            EXD("180", "236") EXD("179", "231") EXD("178", "230") EXD("177", "229") EXD("176", "228")
            PCVM PCVX
            "s_nop 1\n\t"
            SM("224:227", "240:243", "200:207", "v252") "s_nop 1\n\t"
            SM("16:19", "240:243", "128:135", "v252")
            "s_branch 13b\n\t"
            "5:\n\t" CN(1)
            LDX("192:195", "%[l16]", "%[bb]", "0") LDX("196:199", "%[l16]", "%[bb]", "1024")
            LDPA
            "s_sub_u32 %[lim], %[lim], 1\n\t"
            "s_nop 1\n\t"
            ZAM
            "s_cbranch_scc0 4b\n\t"
            "s_mov_b32 %[bad], 1500\n\t"
            "s_mov_b32 %[lim], 0\n\t"
            "s_branch 1b\n\t"
            "7:\n\t" CN(65536)
            LDPA
            "s_sub_u32 %[lim], %[lim], 1\n\t"
            "s_cbranch_scc0 1b\n\t"
            "s_mov_b32 %[bad], 1000\n\t"
            "s_branch 2b\n\t"
            "9:\n\t"
            : [lim] "+s"(lim), [bad] "+s"(bad), "+{v[128:159]}"(yb) TSOUT CNOUT
            : "{v[32:63]}"(wO[0]), "{v[64:95]}"(wO[1]), "{v[104:111]}"(sel), "{v[112:113]}"(sel3), [sx] "v"(sidx), [l16] "v"(l * 16),
              [ra] "v"(r3a), [rs] "s"(rsS), [bb] "s"(bb), [bq] "s"(bq), [bm] "s"(bm), [cs] "s"(o_cs), [rc] "v"(l0 + C2_R3 + wu * 512 + l * 8)
            : "memory", "vcc", "scc", CLOB_PX, CLOB16(16, 17, 18, 19, 20, 21, 22, 23, 24, 25, 26, 27, 28, 29, 30, 31),
              CLOB16(224, 225, 226, 227, 228, 229, 230, 231, 232, 233, 234, 235, 236, 237, 238, 239), CLOB16(240, 241, 242, 243, 244, 245, 246, 247, 248, 249, 250, 251, 252, 253, 254, 254));
        if (__builtin_expect(bad != 0, 0)) TMO_SET((unsigned)bad)
        __builtin_amdgcn_s_setprio(0);
        if (s == 0 && wu == 1) { PST(6) }

        if (__builtin_expect(s <= KVS + 1, 0)) {
            if (s == 0 || __builtin_amdgcn_readfirstlane(*(volatile unsigned*)(chsm + C2_MB + 32 + ((s - 1) & 1) * 16 + 4 + (wu >> 1) * 4)) == 0u) {
                wait_ge<6>(kv_flag(a.sync, s + 1, grp, l), 1u, a.tmo, 3001u);
                const char* kp = arena(a.slots) + VX_OFF + blk_off(s + 1, ha, tau);
#pragma unroll
                for (int e = 0; e < 8; ++e) glds16h((unsigned)l * 16u, kp + e * 1024, l0 + (s & 1 ? C2_V : C2_K) + wu * 8192 + e * 1024);
                asm volatile("s_waitcnt vmcnt(0)" ::: "memory");
            }
        }
        {
            int k0 = KX_OFF + blk_off(s + 1, hs, tau) + jq * 1024;
            asm volatile(
                DSV("160:163", "0") DSV("164:167", "1024") DSV("168:171", "2048") DSV("172:175", "3072") DSV("176:179", "128") DSV("180:183", "1152") DSV("184:187", "2176") DSV("188:191", "3200")
                "s_cmp_lg_u32 %[wu], 1\n\t"
                "s_cbranch_scc1 1f\n\t"
                KLD("%[k0]")
                "1:\n\t"
                "s_waitcnt lgkmcnt(4)\n\t"
                PXT("192:195", "196:199", "200:203", "204:207", "160:163", "164:167", "168:171", "172:175")
                "s_waitcnt lgkmcnt(0)\n\t"
                PXT("208:211", "212:215", "216:219", "220:223", "176:179", "180:183", "184:187", "188:191")
                CVP("128", "192", "193") CVP("129", "194", "195") CVP("136", "196", "197") CVP("137", "198", "199") CVP("144", "200", "201") CVP("145", "202", "203") CVP("152", "204", "205") CVP("153", "206", "207")
                CVP("132", "208", "209") CVP("133", "210", "211") CVP("140", "212", "213") CVP("141", "214", "215") CVP("148", "216", "217") CVP("149", "218", "219") CVP("156", "220", "221") CVP("157", "222", "223")
                : "+{v[128:159]}"(yb), "+{v[96:103]}"(kx), [k0] "+s"(k0)
                : "{v[32:63]}"(wO[0]), "{v[64:95]}"(wO[1]), [lv] "v"(l0 + (s & 1 ? C2_V : C2_K) + wu * 8192 + voffX), [vk] "v"(voffK), [rs] "s"(rsS), [wu] "s"(wu)
                : "memory", "scc", CLOB_PX);
        }
        __builtin_amdgcn_sched_barrier(0);
        o_1 = o1_of(s + 1); o_2 = km_off(s + 1, hs, tau) + jq * 512;
        bq = x_off(s + 1, ha, tau); bm = m_off(s + 1, ha, tau); bb = v_off(s + 1, ha, tau);
        o_f = f_off(s + 1, grp, tau);
        o_cs = rec_base(s + 1) + C_OFF + c * 16384 + (int)(half_bits(s + 1, tau) & 1u) * 8192 + wu * 2048 + grp * 256;
        asm volatile("" : "+s"(o_cs), "+s"(o_f), "+s"(o_1), "+s"(o_2), "+s"(bq), "+s"(bm), "+s"(bb));
        __builtin_amdgcn_s_barrier();
        asm volatile(
            "s_cmp_lg_u32 %[wu], 3\n\t"
            "s_cbranch_scc1 1f\n\t"
            "v_mov_b32 v160, %[lf]\n\t"
            PAUSE(PF)
            "ds_read_b32 v160, v160\n\t"
            "s_waitcnt lgkmcnt(0)\n\t"
            "v_cmp_eq_u32 vcc, %[tg], v160\n\t"
            "s_cbranch_vccz 1f\n\t"
            "s_mov_b32 exec_hi, 0\n\t"
            "buffer_store_dword v160, %[l4], %[rs], %[fo] offen sc1\n\t"
            "s_mov_b64 exec, -1\n\t"
            "1:\n\t"
            :: [wu] "s"(wu), [lf] "s"(l0 + C2_MB + 16), [tg] "s"(s + 1), [l4] "v"(l * 4), [rs] "s"(rsS), [fo] "s"(o_f + c * 128)
            : "memory", "scc", "vcc", "v160");
        __builtin_amdgcn_s_setprio(1);
    }
    {
        constexpr int s = NSEG - 1;
        const int l = lane_now(), i = l & 15, g = l >> 4;
        v4u qf[8], kf[8], pv[2], px[4];
        asm volatile("s_waitcnt vmcnt(0)" ::: "memory");
        __builtin_amdgcn_s_sleep(4);
#pragma unroll
        for (int e = 0; e < 8; ++e) kf[e] = ld_sc1s(rsS, l * 16, KX_OFF + blk_off(s, ha, tau) + e * 1024);
#pragma unroll
        for (int e = 0; e < 8; ++e) qf[e] = *(const v4u*)(chsm + C2_V + 32768 + wu * 8192 + e * 1024 + l * 16);
        f32x4 sx[3] = {ZERO4, ZERO4, ZERO4};
        float mxx[2] = {-3.0e38f, -3.0e38f};
#pragma unroll
        for (int ks = 0; ks < 4; ++ks) {
            sx[0] = MM(kf[ks], qf[ks], sx[0]);
            sx[1] = MM(kf[ks], qf[4 + ks], sx[1]);
            sx[2] = MM(kf[4 + ks], qf[4 + ks], sx[2]);
        }
#pragma unroll
        for (int r = 0; r < 4; ++r) {
            if (4 * g + r > i) { sx[0][r] = -3.0e38f; sx[2][r] = -3.0e38f; }
            mxx[0] = fmaxf(mxx[0], sx[0][r]);
            mxx[1] = fmaxf(mxx[1], fmaxf(sx[1][r], sx[2][r]));
        }
        asm volatile("" : "+v"(sx[0]), "+v"(sx[1]), "+v"(sx[2]), "+v"(mxx[0]), "+v"(mxx[1]));
        hand_over(o_1, o_2, o_f, s);
        const int bk = km_off(s, ha, tau);
        __builtin_amdgcn_s_sleep(DLY_L);
#pragma nounroll
        for (int spin = 0;; ++spin) {
            asm volatile("" ::: "memory");
            pv[0] = ld_sc1s(rsS, l * 16, bb); pv[1] = ld_sc1s(rsS, l * 16, bb + 1024);
#pragma unroll
            for (int ks = 0; ks < 4; ++ks) px[ks] = ld_sc1s(rsS, g * 128 + (i >> 1) * 16, bk + ks * 512);
            if (__all(mx4(mx4(mx4(mx4(mx4(mx4(0u, pv[0]), pv[1]), px[0]), px[1]), px[2]), px[3]) != SENT)) break;
            if (dead) break;
            if (spin > SPIN_LIMIT) { TMO_SET(1700u) break; }
        }
#define SUB8(v, o) v8u{(v)[(o)], (v)[(o) + 1], (v)[(o) + 2], (v)[(o) + 3], (v)[(o) + 4], (v)[(o) + 5], (v)[(o) + 6], (v)[(o) + 7]}
#pragma unroll
        for (int ct = 0; ct < 4; ++ct) {
            f32x4 am = smm(pv[0], SUB8(wO[ct >> 1], (ct & 1) * 16), ZERO4, sidx);
            am = smm(pv[1], SUB8(wO[ct >> 1], (ct & 1) * 16 + 8), am, sidx);
            yb[8 * ct + 2] = cvt_pk_bf16(am[0] + am[1], 0.f); yb[8 * ct + 6] = cvt_pk_bf16(am[2] + am[3], 0.f);
        }
        const v8u one = {0x3F803F80u, 0x3F803F80u, 0x3F80u, 0u, 0x3F803F80u, 0x3F803F80u, 0x3F80u, 0u};
        const float SCL2E = 0.08838834764831845f * 1.4426950408889634f;
        float* xo = (float*)(chsm + C2_K) + (wu * 32 + 4 * g) * XP + i;
#pragma unroll
        for (int qt = 0; qt < 2; ++qt) {
            f32x4 sc[3] = {sx[qt], sx[2], ZERO4};
#pragma unroll
            for (int ks = 0; ks < 4; ++ks) sc[2] = MM(px[ks], qf[4 * qt + ks], sc[2]);
            float mx = fmaxf(mxx[qt], fmaxf(sc[2][1], sc[2][3]));
            mx = xor32_max(xor16_max(mx));
            const float nmx = -mx * SCL2E;
#define PE(x) __builtin_amdgcn_exp2f(fmaf(x, SCL2E, nmx))
            const float m0 = PE(sc[2][1]), m1 = PE(sc[2][3]);
            const v4u ae = {cvt_pk_bf16(PE(sc[0][0]), PE(sc[0][1])), cvt_pk_bf16(m0, m0), cvt_pk_bf16(PE(sc[0][2]), PE(sc[0][3])), 0u};
            const v4u ao = {qt ? cvt_pk_bf16(PE(sc[1][0]), PE(sc[1][1])) : 0u, 0u, qt ? cvt_pk_bf16(PE(sc[1][2]), PE(sc[1][3])) : 0u, cvt_pk_bf16(m1, m1)};
            const f32x4 sum = smm(ao, one, smm(ae, one, ZERO4, 0xE444), 0x4EEE);
#pragma unroll
            for (int ct = 0; ct < 4; ++ct) {
                const f32x4 o = smm(ao, SUB8(yb, 8 * ct), smm(ae, SUB8(yb, 8 * ct), ZERO4, 0xE444), 0x4EEE);
#pragma unroll
                for (int r = 0; r < 4; ++r) xo[(16 * qt + r) * XP + 16 * ct] = o[r] * __builtin_amdgcn_rcpf(sum[r]);
            }
        }
        __builtin_amdgcn_s_waitcnt(0xC07F);
        last_rows(chsm, rsS, wu, grp, c, tau);
        if (wu < 2) {
            v4u sel, p0, p1;
#pragma unroll
            for (int d = 0; d < 4; ++d) sel[d] = (l & 6) == 2 * d ? (0x3F80u << ((l & 1) * 16)) : 0u;
            const int xb = XB_OFF + ((c >> 1) * 8 + grp) * 16384 + (int)(tau & 1u) * 8192 + (c & 1) * 4096 + wu * 256;
            __builtin_amdgcn_s_sleep(DLY_C);
#pragma nounroll
            for (int spin = 0;; ++spin) {
                asm volatile("" ::: "memory");
                p0 = ld_sc1s(rsS, g * 512 + i * 16, xb); p1 = ld_sc1s(rsS, g * 512 + i * 16, xb + 2048);
                if (__all(mx4(mx4(0u, p0), p1) != SENT)) break;
                if (dead) break;
                if (spin > SPIN_LIMIT) { TMO_SET(1800u) break; }
            }
            const f32x4 d = MM(sel, p1, MM(sel, p0, ZERO4));
            if (g < 2) out8(a.out + (size_t)(s * SEG + 4 * grp + 2 * wu + (i >> 3)) * OUTW + 64 * c + 8 * (i & 7) + 4 * g, v2u{cvt_pk_bf16(d[0], d[1]), cvt_pk_bf16(d[2], d[3])});
        }
    }
}

struct cpx { float x, y; };
__device__ __forceinline__ cpx cmul(cpx a, cpx b) { return {a.x * b.x - a.y * b.y, a.x * b.y + a.y * b.x}; }
__device__ __forceinline__ cpx cadd(cpx a, cpx b) { return {a.x + b.x, a.y + b.y}; }
__device__ __forceinline__ cpx csub(cpx a, cpx b) { return {a.x - b.x, a.y - b.y}; }
template <int DIR> __device__ __forceinline__ cpx mul_i(cpx a) {
    return (DIR < 0) ? cpx{a.y, -a.x} : cpx{-a.y, a.x};
}
template <int DIR> __device__ __forceinline__ void r4(cpx& a, cpx& b, cpx& c, cpx& d) {
    cpx t0 = cadd(a, c), t1 = csub(a, c), t2 = cadd(b, d), t3 = mul_i<DIR>(csub(b, d));
    a = cadd(t0, t2); b = cadd(t1, t3); c = csub(t0, t2); d = csub(t1, t3);
}
template <int DIR> __device__ __forceinline__ cpx twc(float c, float s) { return cpx{c, (float)DIR * s}; }
template <int DIR> __device__ __forceinline__ void dft16(cpx (&v)[16]) {
#pragma unroll
    for (int a = 0; a < 4; ++a) r4<DIR>(v[a], v[a + 4], v[a + 8], v[a + 12]);
    const float C1 = 0.92387953251128674f, S1 = 0.38268343236508977f, R2 = 0.70710678118654752f;
    v[1 + 4] = cmul(v[1 + 4], twc<DIR>(C1, S1));
    v[1 + 8] = cmul(v[1 + 8], twc<DIR>(R2, R2));
    v[1 + 12] = cmul(v[1 + 12], twc<DIR>(S1, C1));
    v[2 + 4] = cmul(v[2 + 4], twc<DIR>(R2, R2));
    v[2 + 8] = mul_i<DIR>(v[2 + 8]);
    v[2 + 12] = cmul(v[2 + 12], twc<DIR>(-R2, R2));
    v[3 + 4] = cmul(v[3 + 4], twc<DIR>(S1, C1));
    v[3 + 8] = cmul(v[3 + 8], twc<DIR>(-R2, R2));
    v[3 + 12] = cmul(v[3 + 12], twc<DIR>(-C1, -S1));
#pragma unroll
    for (int d = 0; d < 4; ++d) r4<DIR>(v[4 * d], v[4 * d + 1], v[4 * d + 2], v[4 * d + 3]);
}
__device__ __forceinline__ constexpr int x16(int q) { return 4 * (q & 3) + (q >> 2); }
template <int DIR> __device__ __forceinline__ void dft8(cpx (&v)[8]) {
    r4<DIR>(v[0], v[2], v[4], v[6]);
    r4<DIR>(v[1], v[3], v[5], v[7]);
    const float R2 = 0.70710678118654752f;
    v[3] = cmul(v[3], twc<DIR>(R2, R2));
    v[5] = mul_i<DIR>(v[5]);
    v[7] = cmul(v[7], twc<DIR>(-R2, R2));
#pragma unroll
    for (int d = 0; d < 4; ++d) { cpx p = v[2 * d], q = v[2 * d + 1]; v[2 * d] = cadd(p, q); v[2 * d + 1] = csub(p, q); }
}
__device__ __forceinline__ int pad(int i) { return i + (i >> 4); }
__device__ __forceinline__ cpx expi(float x) { float s, c; sincospif(x, &s, &c); return cpx{c, s}; }
template <int R, int DIR> __device__ __forceinline__ void twiddle(cpx* v, cpx w1) {
    cpx w[R];
    w[1] = w1;
#pragma unroll
    for (int r = 2; r < R; ++r) w[r] = (r & 1) ? cmul(w[r - 1], w1) : cmul(w[r / 2], w[r / 2]);
#pragma unroll
    for (int r = 1; r < R; ++r) v[r] = DIR < 0 ? cmul(v[r], w[r]) : cpx{v[r].x * w[r].x + v[r].y * w[r].y, v[r].y * w[r].x - v[r].x * w[r].y};
}
struct TwBase { cpx b; cpx c[4]; };
template <int N> __device__ __forceinline__ TwBase make_tw(int t) {
    TwBase tb;
    tb.b = expi(-(float)(t & 15) * (1.0f / 128.0f));
    constexpr int NQ = (N == 1024) ? 4 : 2, T = N / 16;
#pragma unroll
    for (int q = 0; q < NQ; ++q) tb.c[q] = expi(-(float)(t + T * q) * (2.0f / (float)N));
    return tb;
}
template <int N> __device__ __forceinline__ void fft_sync() {
    if constexpr (N == 1024) { __builtin_amdgcn_fence(__ATOMIC_ACQ_REL, "wavefront"); __builtin_amdgcn_wave_barrier(); } else __syncthreads();
}
template <int N, int DIR, bool RIN, bool ROUT>
__device__ __forceinline__ void fft_reg(cpx* buf, const TwBase& tb, int t, cpx (&v)[16]) {
    constexpr int T = N / 16;
    if constexpr (!RIN) {
#pragma unroll
        for (int r = 0; r < 16; ++r) v[r] = buf[pad(t + T * r)];
        fft_sync<N>();
    }
    dft16<DIR>(v);
#pragma unroll
    for (int q = 0; q < 16; ++q) buf[pad(16 * t + x16(q))] = v[q];
    fft_sync<N>();
    {
        const int k = t & 15;
#pragma unroll
        for (int r = 0; r < 16; ++r) v[r] = buf[pad(t + T * r)];
        fft_sync<N>();
        twiddle<16, DIR>(v, tb.b);
        dft16<DIR>(v);
        const int o = (t >> 4) * 256 + k;
#pragma unroll
        for (int q = 0; q < 16; ++q) buf[pad(o + 16 * x16(q))] = v[q];
        fft_sync<N>();
    }
    if constexpr (N == 1024) {
#pragma unroll
        for (int q = 0; q < 4; ++q)
#pragma unroll
            for (int r = 0; r < 4; ++r) v[4 * q + r] = buf[pad(t + 64 * q + 256 * r)];
#pragma unroll
        for (int q = 0; q < 4; ++q) {
            twiddle<4, DIR>(&v[4 * q], tb.c[q]);
            r4<DIR>(v[4 * q], v[4 * q + 1], v[4 * q + 2], v[4 * q + 3]);
#pragma unroll
            for (int r = 0; r < 4; ++r) buf[pad(t + 64 * q + 256 * r)] = v[4 * q + r];
        }
        fft_sync<N>();
    } else {
        cpx u[2][8];
#pragma unroll
        for (int q = 0; q < 2; ++q)
#pragma unroll
            for (int r = 0; r < 8; ++r) u[q][r] = buf[pad(t + 128 * q + 256 * r)];
        if constexpr (ROUT) fft_sync<N>();
#pragma unroll
        for (int q = 0; q < 2; ++q) {
            twiddle<8, DIR>(u[q], tb.c[q]);
            dft8<DIR>(u[q]);
#pragma unroll
            for (int d = 0; d < 4; ++d) {
                if constexpr (ROUT) { v[q + 2 * d] = u[q][2 * d]; v[q + 2 * d + 8] = u[q][2 * d + 1]; }
                else { buf[pad(t + 128 * q + 256 * d)] = u[q][2 * d]; buf[pad(t + 128 * q + 256 * (d + 4))] = u[q][2 * d + 1]; }
            }
        }
        if constexpr (!ROUT) fft_sync<N>();
    }
}
__device__ __forceinline__ int f_pos(int rb, int x, unsigned tw) { x = (x + rb * FROT) & 32767; return rb * 65536 + x + (x & ~8191) + (int)((tw ^ (unsigned)(rb >> 5)) & 1u) * 8192; }
__device__ __forceinline__ int w_pos(int k, int n, unsigned tw) { return k * 16384 + ((n * 4 + k * WROT) & 8191) + (int)((tw ^ (unsigned)(k >> 7)) & 1u) * 8192; }
constexpr int PB1 = 1024 + 64, PB2 = 2048 + 128;
typedef float f32x4v __attribute__((ext_vector_type(4)));
typedef unsigned u32x4v __attribute__((ext_vector_type(4)));

__device__ __forceinline__ void fft_rows_fwd(const u16* __restrict__ x, rsrc_t rsF, const unsigned tw, int rb, char* smem, int tid, const char* wr, const char* wi, const unsigned spare) {
    cpx (*buf)[PB1] = (cpx (*)[PB1])smem;
    int f = tid >> 6, t = tid & 63;
    const u16* p0 = x + (size_t)(rb * 8 + 2 * f) * 1024 + 16 * t;
    u32x4v a0 = *(const u32x4v*)p0, a1 = *(const u32x4v*)(p0 + 8), b0 = *(const u32x4v*)(p0 + 1024), b1 = *(const u32x4v*)(p0 + 1032);
    const TwBase tb = make_tw<1024>(t);
    for (int it = 0; it < 4; ++it, rb += 64) {
        asm volatile("" : "+v"(tid));
        f = tid >> 6; t = tid & 63;
        __syncthreads();
        cpx* d = &buf[f][pad(16 * t)];
#pragma unroll
        for (int e = 0; e < 4; ++e) {
            d[2 * e] = cpx{__uint_as_float(a0[e] << 16), __uint_as_float(b0[e] << 16)};
            d[2 * e + 1] = cpx{__uint_as_float(a0[e] & 0xFFFF0000u), __uint_as_float(b0[e] & 0xFFFF0000u)};
            d[8 + 2 * e] = cpx{__uint_as_float(a1[e] << 16), __uint_as_float(b1[e] << 16)};
            d[8 + 2 * e + 1] = cpx{__uint_as_float(a1[e] & 0xFFFF0000u), __uint_as_float(b1[e] & 0xFFFF0000u)};
        }
        if (it < 3) p0 += 512 * 1024;
        if (FTR) touch(((it & 1) ? wi : wr) + (size_t)((rb >> 1) & 31) * 32832 + (it >> 1) * 32 * 32832 + ((rb & 1) * 128 + (tid >> 1)) * 128, spare);
        a0 = *(const u32x4v*)p0; a1 = *(const u32x4v*)(p0 + 8); b0 = *(const u32x4v*)(p0 + 1024); b1 = *(const u32x4v*)(p0 + 1032);
        fft_sync<1024>();
        cpx z[16];
        fft_reg<1024, -1, false, false>(buf[f], tb, t, z);
        __syncthreads();
#pragma unroll
        for (int m = 0; m < 8; ++m) {
            const int idx = tid + 256 * m, k = idx >> 2, ff = idx & 3;
            const cpx zk = buf[ff][pad(k)], zn = buf[ff][pad(k ? 1024 - k : 512)];
            f32x4v o = {0.5f * (zk.x + zn.x), 0.5f * (zk.y - zn.y), 0.5f * (zk.y + zn.y), -0.5f * (zk.x - zn.x)};
            if (m == 0 && k == 0) o = f32x4v{zk.x, zn.x, zk.y, zn.y};
            st_sc1(rsF, f_pos(rb, idx * 16, tw), __builtin_bit_cast(v4u, o));
        }
    }
}
__device__ __forceinline__ void filt_transpose(const u16* __restrict__ wr, const u16* __restrict__ wi, rsrc_t rsW, const unsigned tw, const int tb, char* smem, const int tid) {
    const v4u* s0 = (const v4u*)(wr + (size_t)tb * 32 * 513);
    const v4u* s1 = (const v4u*)(wi + (size_t)tb * 32 * 513);
    v4u r0[5], r1[5];
#pragma unroll
    for (int e = 0; e < 5; ++e) {
        const int idx = tid + 512 * e, ic = idx < 2052 ? idx : 2051;
        r0[e] = s0[ic]; r1[e] = s1[ic];
    }
#pragma unroll
    for (int e = 0; e < 5; ++e) {
        const int idx = tid + 512 * e;
        if (idx < 2052) { *(v4u*)(smem + idx * 16) = r0[e]; *(v4u*)(smem + 32832 + idx * 16) = r1[e]; }
    }
    __syncthreads();
    const u16* a0 = (const u16*)smem;
    const u16* a1 = (const u16*)(smem + 32832);
    for (int idx = tid; idx < 513 * 8; idx += 512) {
        const int k = idx >> 3, n4 = (idx & 7) * 4;
        v4u v;
#pragma unroll
        for (int j = 0; j < 4; ++j) v[j] = (unsigned)a0[(n4 + j) * 513 + k] | ((unsigned)a1[(n4 + j) * 513 + k] << 16);
        st_sc1(rsW, w_pos(k, tb * 32 + n4, tw), v);
    }
}

__device__ __forceinline__ void fft_cols(rsrc_t rsF, rsrc_t rsW, const unsigned tw, int blk, const int n, char* smem, int tid) {
    cpx (*buf)[PB2] = (cpx (*)[PB2])smem;
    int c = tid >> 7, t = tid & 127;
    f32x4v v[8];
    unsigned wv[16], wn[16];
    auto wld = [&](const int k, const int n) { return __builtin_amdgcn_raw_buffer_load_b32(rsW, w_pos(k, n, tw), 0, 0); };
    auto fetch = [&](const int bk) {
        const int k0 = bk * 2, kc = k0 + c;
#pragma unroll
        for (int m = 0; m < 8; ++m) { const int idx = tid + 256 * m; v[m] = __builtin_bit_cast(f32x4v, __builtin_amdgcn_raw_buffer_load_b128(rsF, f_pos(idx >> 3, k0 * 64 + (idx & 7) * 16, tw), 0, 0)); }
#pragma unroll
        for (int m = 0; m < 16; ++m) wn[m] = wld(kc, t + 128 * m);
    };
    fetch(blk);
    const TwBase tb = make_tw<2048>(t);
    for (int it = 0; it < n; ++it, blk += 64) {
        asm volatile("" : "+v"(tid));
        c = tid >> 7; t = tid & 127;
        __syncthreads();
#pragma unroll
        for (int m = 0; m < 8; ++m) {
            const int idx = tid + 256 * m, rb = idx >> 3, pc = idx & 7;
            cpx* d = &buf[pc >> 2][pad(rb * 8 + 2 * (pc & 3))];
            d[0] = cpx{v[m][0], v[m][1]}; d[1] = cpx{v[m][2], v[m][3]};
        }
#pragma unroll
        for (int m = 0; m < 16; ++m) wv[m] = wn[m];
        fetch(it + 1 < n ? blk + 64 : blk);
        __syncthreads();
        cpx z[16];
        fft_reg<2048, -1, false, true>(buf[c], tb, t, z);
        auto wcx = [](const unsigned w) { return cpx{__uint_as_float(w << 16), __uint_as_float(w & 0xFFFF0000u)}; };
        if (blk < 2) {
#pragma unroll
            for (int m = 0; m < 16; ++m) buf[c][pad(t + 128 * m)] = z[m];
            __syncthreads();
            if (blk == 0 && c == 0) {
#pragma unroll
                for (int m = 0; m < 16; ++m) {
                    const int n = t + 128 * m, nn = (2048 - n) & 2047;
                    const cpx zn = buf[0][pad(nn)], a = wcx(wv[m]), b = wcx(wld(0, nn)), e = wcx(wld(512, n)), f = wcx(wld(512, nn));
                    const cpx h0 = cmul(cpx{0.5f * (z[m].x + zn.x), 0.5f * (z[m].y - zn.y)}, cpx{0.5f * (a.x + b.x), 0.5f * (a.y - b.y)});
                    const cpx h5 = cmul(cpx{0.5f * (z[m].y + zn.y), -0.5f * (z[m].x - zn.x)}, cpx{0.5f * (e.x + f.x), 0.5f * (e.y - f.y)});
                    z[m] = cpx{h0.x - h5.y, h0.y + h5.x};
                    wv[m] = 0x3F80u;
                }
            }
            __syncthreads();
        }
#pragma unroll
        for (int m = 0; m < 16; ++m) z[m] = cmul(z[m], wcx(wv[m]));
        fft_reg<2048, 1, true, false>(buf[c], tb, t, z);
        const float sc = 1.0f / 2048.0f;
#pragma unroll
        for (int m = 0; m < 8; ++m) {
            const int idx = tid + 256 * m, rb = idx >> 3, pc = idx & 7;
            const cpx* d = &buf[pc >> 2][pad(rb * 8 + 2 * (pc & 3))];
            st_sc1(rsF, f_pos(rb, blk * 128 + pc * 16, tw), __builtin_bit_cast(v4u, f32x4v{d[0].x * sc, d[0].y * sc, d[1].x * sc, d[1].y * sc}));
        }
    }
}

__device__ __forceinline__ void fft_rows_inv(rsrc_t rsF, const unsigned tw, u16* __restrict__ out, int rb, const int step, const int n, char* smem, int tid) {
    cpx (*buf)[PB1] = (cpx (*)[PB1])smem;
    int f = tid >> 6, t = tid & 63;
    f32x4v v[8];
    auto fetch = [&](const int r) {
#pragma unroll
        for (int m = 0; m < 8; ++m) v[m] = __builtin_bit_cast(f32x4v, __builtin_amdgcn_raw_buffer_load_b128(rsF, f_pos(r, (tid + 256 * m) * 16, tw), 0, 0));
    };
    fetch(rb);
    const TwBase tb = make_tw<1024>(t);
    for (int it = 0; it < n; ++it, rb += step) {
        asm volatile("" : "+v"(tid));
        f = tid >> 6; t = tid & 63;
        __syncthreads();
        cpx* bf = buf[tid & 3];
#pragma unroll
        for (int m = 0; m < 8; ++m) {
            const int k = (tid >> 2) + 64 * m;
            if (m == 0 && k == 0) { bf[0] = cpx{v[0][0], v[0][2]}; bf[pad(512)] = cpx{v[0][1], v[0][3]}; }
            else { bf[pad(k)] = cpx{v[m][0] - v[m][3], v[m][1] + v[m][2]}; bf[pad(1024 - k)] = cpx{v[m][0] + v[m][3], v[m][2] - v[m][1]}; }
        }
        fetch(it + 1 < n ? rb + step : rb);
        __syncthreads();
        cpx z[16];
        fft_reg<1024, 1, false, false>(buf[f], tb, t, z);
        const float sc = 1.0f / 1024.0f;
        const cpx* d = &buf[f][pad(16 * t)];
        u32x4v a[2], b[2];
#pragma unroll
        for (int h = 0; h < 2; ++h)
#pragma unroll
            for (int e = 0; e < 4; ++e) {
                cpx p = d[8 * h + 2 * e], q = d[8 * h + 2 * e + 1];
                a[h][e] = cvt_pk_bf16(p.x * sc, q.x * sc);
                b[h][e] = cvt_pk_bf16(p.y * sc, q.y * sc);
            }
        u16* o0 = out + (size_t)(rb * 8 + 2 * f) * OUTW + 1024 + 16 * t;
        *(u32x4v*)o0 = a[0]; *(u32x4v*)(o0 + 8) = a[1];
        *(u32x4v*)(o0 + OUTW) = b[0]; *(u32x4v*)(o0 + OUTW + 8) = b[1];
    }
}

__device__ __forceinline__ void glds16s(unsigned voff, const void* sbase, unsigned lds_dst) {
    unsigned keep;
    asm volatile("s_mov_b32 %0, m0\n\ts_mov_b32 m0, %3\n\ts_nop 0\n\tglobal_load_lds_dwordx4 %1, %2\n\ts_mov_b32 m0, %0" : "=&s"(keep) : "v"(voff), "s"(sbase), "s"(lds_dst) : "memory");
}
#define K8_BAR do { asm volatile("" ::: "memory"); __builtin_amdgcn_s_barrier(); asm volatile("" ::: "memory"); } while (0)
#define K8_WV(n) asm volatile("s_waitcnt vmcnt(" #n ")" ::: "memory")
#define K8_WL(n) asm volatile("s_waitcnt lgkmcnt(" #n ")" ::: "memory")
#define K8_SCHED __builtin_amdgcn_sched_barrier(0)
template <int BN>
__device__ __forceinline__ int bswz2(int row) {
    if (BN >= 128) return (row & 3) | (((row >> 3) & 1) << 2);
    else return ((row >> 1) & 1) | (((row >> 3) & 1) << 1);
}
template <int BM>
__device__ __forceinline__ void gemm_tile(u16* sm, const char* A, const int s0, const unsigned tau, const u16* B, u16* C) {
    constexpr int K = HD, LDB = DM, LDC = OUTW;
    constexpr int BN = 128, BK = 64, ST = BM == 128 ? 4 : 3, WN = BM == 128 ? 4 : 2;
    constexpr int ATILE = BM * BK, BTILE = BK * BN;
    constexpr int NT = 8 / WN, GA = BM / 64, GB = 2, GS = GA + GB;
    const int tid = opaque_tid(), w = tid >> 6, l = tid & 63, i = l & 15, g = l >> 4;
    const int wr = w / WN, wc = w % WN;
    const unsigned wu = __builtin_amdgcn_readfirstlane(w);
    unsigned vb;
    { const int row = l >> 4, c = l & 15; vb = (unsigned)(row * LDB + 8 * (c ^ ((row | ((wu & 1) << 2)) << 1))) * 2u; }
    const int sg = s0 + (BM == 128 ? wu >> 1 : wu);
    const char* ab = A + (BM == 128 ? (wu & 1) * 4096 : 0);
    const char* bb = (const char*)(B + (size_t)wu * 8 * LDB);
    const unsigned lds0 = (unsigned)(uintptr_t)(__attribute__((address_space(3))) u16*)sm;
    const unsigned va = (unsigned)l * 16u, va2 = va + 2048u, vb2 = vb + 4u * LDB * 2u - 1024u;
    auto req = [&](const int buf, const int kt) {
        const unsigned d = lds0 + buf * (ATILE + BTILE) * 2;
        const int wh = kt >> 1;
        const char* pa = ab + ((blk_off(sg, (wh & 7) * 4 + (wh >> 3), tau) ^ 8192) + (kt & 1) * 2048);
        const char* pb = bb + (size_t)kt * BK * LDB * 2;
        unsigned keep;
        if (BM == 128) asm volatile("s_mov_b32 %0, m0\n\ts_mov_b32 m0, %1\n\ts_nop 0\n\tglobal_load_lds_dwordx4 %3, %5\n\tglobal_load_lds_dwordx4 %3, %5 offset:1024\n\t"
                     "s_mov_b32 m0, %2\n\ts_nop 0\n\tglobal_load_lds_dwordx4 %6, %8\n\tglobal_load_lds_dwordx4 %7, %8 offset:1024\n\ts_mov_b32 m0, %0"
                     : "=&s"(keep) : "s"(d + wu * GA * 1024), "s"(d + ATILE * 2 + wu * GB * 1024), "v"(va), "v"(va2), "s"(pa), "v"(vb), "v"(vb2), "s"(pb) : "memory");
        else asm volatile("s_mov_b32 %0, m0\n\ts_mov_b32 m0, %1\n\ts_nop 0\n\tglobal_load_lds_dwordx4 %3, %5\n\tglobal_load_lds_dwordx4 %3, %5 offset:1024\n\t"
                     "global_load_lds_dwordx4 %4, %5 offset:2048\n\tglobal_load_lds_dwordx4 %4, %5 offset:3072\n\t"
                     "s_mov_b32 m0, %2\n\ts_nop 0\n\tglobal_load_lds_dwordx4 %6, %8\n\tglobal_load_lds_dwordx4 %7, %8 offset:1024\n\ts_mov_b32 m0, %0"
                     : "=&s"(keep) : "s"(d + wu * GA * 1024), "s"(d + ATILE * 2 + wu * GB * 1024), "v"(va), "v"(va2), "s"(pa), "v"(vb), "v"(vb2), "s"(pb) : "memory");
    };
    f32x4 acc[4][NT] = {};
    constexpr int nt = K / BK;
#pragma unroll
    for (int s = 0; s < ST - 1; ++s) req(s, s);
    int boff[NT];
    const int aoff = wr * 4096 + l * 8;
#pragma unroll
    for (int n = 0; n < NT; ++n) { const int row = 8 * g + (i >> 2); boff[n] = row * BN + 16 * ((wc * NT + n) ^ bswz2<BN>(row)) + 4 * (i & 3); }
    asm volatile("s_waitcnt vmcnt(%0)" :: "i"((ST - 2) * GS) : "memory");
    K8_BAR;
    if (wu >= 4) K8_BAR;
    for (int t0 = 0; t0 < nt + ST - 1 - (nt + ST - 1) % ST; t0 += ST) {
#pragma unroll
        for (int rb = 0; rb < ST; ++rb) {
            const int t = t0 + rb;
            if (nt % ST != 0 && t >= nt) break;
            const int kk = min(t + ST - 1, nt - 1);
            const u16* as = sm + rb * (ATILE + BTILE);
            const u16* bs = as + ATILE;
            bf16x8 af[2][4], bf[2][NT];
#pragma unroll
            for (int ks = 0; ks < 2; ++ks) {
#pragma unroll
                for (int n = 0; n < NT; ++n) bf[ks][n] = tr_frag(bs + boff[n] + ks * 32 * BN, bs + boff[n] + ks * 32 * BN + 4 * BN);
#pragma unroll
                for (int m = 0; m < 4; ++m) af[ks][m] = *(const bf16x8*)(as + aoff + m * 1024 + ks * 512);
            }
            K8_SCHED;
            req((rb + ST - 1) % ST, kk);
            K8_WL(0);
            if (wu >= 4) asm volatile("s_waitcnt vmcnt(%0)" :: "i"((ST - 2) * GS) : "memory");
            K8_BAR;
            __builtin_amdgcn_s_setprio(1);
#pragma unroll
            for (int q = 0; q < 8; ++q)
#pragma unroll
                for (int n = 0; n < NT; ++n) acc[q & 3][n] = __builtin_amdgcn_mfma_f32_16x16x32_bf16(bf[q >> 2][n], af[q >> 2][q & 3], acc[q & 3][n], 0, 0, 0);
            __builtin_amdgcn_s_setprio(0);
            K8_SCHED;
            asm volatile("s_waitcnt vmcnt(%0)" :: "i"((ST - 2) * GS) : "memory");
            K8_BAR;
        }
    }
    if (wu < 4) K8_BAR;
    asm volatile("s_waitcnt vmcnt(0)" ::: "memory");
#pragma unroll
    for (int m = 0; m < 4; ++m)
#pragma unroll
        for (int n = 0; n < NT; n += 2) {
            auto r0 = __builtin_amdgcn_permlane16_swap(cvt_pk_bf16(acc[m][n][0], acc[m][n][1]), cvt_pk_bf16(acc[m][n + 1][0], acc[m][n + 1][1]), false, false);
            auto r1 = __builtin_amdgcn_permlane16_swap(cvt_pk_bf16(acc[m][n][2], acc[m][n][3]), cvt_pk_bf16(acc[m][n + 1][2], acc[m][n + 1][3]), false, false);
            *(v4u*)(C + (wr * 64 + m * 16 + i) * LDC + (wc * NT + n + (g & 1)) * 16 + 8 * (g >> 1)) = v4u{r0[0], r1[0], r0[1], r1[1]};
        }
}

__device__ __forceinline__ void xattn_unit(const ChainArgs& a, rsrc_t rsQ, rsrc_t rsK, rsrc_t rsV, rsrc_t rsS, const int s, const int sn, const int h, u16 (*Vs)[VP], unsigned* mbox, const unsigned tau, int& pend, bool& ready) {
    const int l = opaque_tid() & 63, i = l & 15, g = l >> 4;
    const unsigned known = __builtin_amdgcn_readfirstlane(*(volatile unsigned*)mbox);
    const bool early = known > (unsigned)(s + 1);
    const int bq = km_off(s, h, tau), bb = v_off(s, h, tau);
    auto count = [&](const int t) { arrive(a.sync, t < SSEG ? SY_OX + (t >> 2) * 8 : SY_OXS + (t - SSEG) * 8, t < SSEG ? 128u : 32u, 8); };
    const int xk = blk_off(s, h, tau), xq = xk ^ 8192;
    v4u vv[8], vm[2], k0f[4], k1f[4], kmf[4], q0f[4], q1f[4];
#pragma unroll
    for (int e = 0; e < 8; ++e) vv[e] = ld_sc1s(rsV, l * 16 + e * 1024, xk);
#pragma unroll
    for (int ks = 0; ks < 4; ++ks) {
        k0f[ks] = ld_sc1s(rsK, l * 16 + ks * 1024, xk);
        k1f[ks] = ld_sc1s(rsK, l * 16 + ks * 1024, xk + 4096);
        q0f[ks] = ld_sc1s(rsQ, l * 16 + ks * 1024, xq);
        q1f[ks] = ld_sc1s(rsQ, l * 16 + ks * 1024, xq + 4096);
    }
#pragma unroll
    for (int e = 0; e < 2; ++e) vm[e] = ld_sc1s(rsS, l * 16 + e * 1024, early ? bb : 1 << 30);
#pragma unroll
    for (int ks = 0; ks < 4; ++ks) kmf[ks] = ld_sc1s(rsS, ks * 512 + g * 128 + (i & 7) * 16, early ? bq : 1 << 30);
    const unsigned fl = ld_flag(a.sync + (l >= 8 ? SY_PROG * 32 : (((l & 4) ? SY_Q : SY_KV) + (min(sn, NSEG - 1) >> 3) * 8) * 32 + (l & 3)) + (h >> 2) * 32);
    if (pend >= 0) { asm volatile("s_waitcnt vmcnt(31)" ::: "memory"); count(pend); }
#pragma unroll
    for (int e = 0; e < 8; ++e) *(v4u*)&Vs[16 * (e >> 2) + i][32 * (e & 3) + 8 * g] = vv[e];
    const float scale = 0.08838834764831845f;
    const float L2E = 1.4426950408889634f;
    auto round2 = [&](const f32x4 c, float* o) {
#pragma unroll
        for (int p = 0; p < 2; ++p) {
            unsigned u = cvt_pk_bf16(c[2 * p], c[2 * p + 1]);
            float lo = __uint_as_float(u << 16) * scale, hi = __uint_as_float(u & 0xFFFF0000u) * scale;
            unsigned u2 = cvt_pk_bf16(lo, hi);
            o[2 * p] = __uint_as_float(u2 << 16);
            o[2 * p + 1] = __uint_as_float(u2 & 0xFFFF0000u);
        }
    };
    float sv[2][12], mxx[2];
    {
        f32x4 sx[3] = {};
#pragma unroll
        for (int ks = 0; ks < 4; ++ks) {
            sx[0] = __builtin_amdgcn_mfma_f32_16x16x32_bf16(as_frag(k0f[ks]), as_frag(q0f[ks]), sx[0], 0, 0, 0);
            sx[1] = __builtin_amdgcn_mfma_f32_16x16x32_bf16(as_frag(k0f[ks]), as_frag(q1f[ks]), sx[1], 0, 0, 0);
            sx[2] = __builtin_amdgcn_mfma_f32_16x16x32_bf16(as_frag(k1f[ks]), as_frag(q1f[ks]), sx[2], 0, 0, 0);
        }
        round2(sx[0], &sv[0][0]); round2(sx[1], &sv[1][0]); round2(sx[2], &sv[1][4]);
        mxx[0] = mxx[1] = -3.0e38f;
#pragma unroll
        for (int r = 0; r < 4; ++r) {
            if (4 * g + r > i) { sv[0][r] = -3.0e38f; sv[1][4 + r] = -3.0e38f; }
            mxx[0] = fmaxf(mxx[0], sv[0][r]);
            mxx[1] = fmaxf(mxx[1], fmaxf(sv[1][r], sv[1][4 + r]));
        }
    }
    const u16* vb = &Vs[4 * g + (i >> 2)][4 * (i & 3)];
    bf16x8 vfx[8];
#pragma unroll
    for (int t = 0; t < 8; ++t) vfx[t] = tr_frag(vb + 16 * t, vb + 16 * VP + 16 * t);
#pragma unroll
    for (int t = 0; t < 8; ++t) asm volatile("" : "+v"(vfx[t]));
#pragma unroll
    for (int r = 0; r < 4; ++r) asm volatile("" : "+v"(sv[0][r]), "+v"(sv[1][r]), "+v"(sv[1][4 + r]));
    asm volatile("" : "+v"(mxx[0]), "+v"(mxx[1]));
    bool have = false;
    unsigned seen = __builtin_amdgcn_readlane(fl, 8);
    ready = (sn >> 3) == (s >> 3) || (__ballot(fl != 0u) & 255ull) == 255ull;
    if (early) {
        if ((h & 3) == 0 && l == 0) atomicMax(mbox, seen);
        unsigned m = mx4(mx4(0u, vm[0]), vm[1]);
#pragma unroll
        for (int ks = 0; ks < 4; ++ks) m = mx4(m, kmf[ks]);
        have = __all(m != SENT);
    } else if ((h & 3) == 0) {
        const unsigned* pw = a.sync + (SY_PROG + (h >> 2)) * 32;
        unsigned v = seen;
        if (v <= (unsigned)(s + 1)) {
            if (s < NSEG - XA_FAST) { wait_ge<32>(pw, (unsigned)(s + 1), a.tmo, 4000u); __builtin_amdgcn_s_sleep(24); }
            else { wait_ge<6>(pw, (unsigned)(s + 1), a.tmo, 4000u); __builtin_amdgcn_s_sleep(XA_D); }
            v = (unsigned)(s + 1);
        }
        if (l == 0) atomicMax(mbox, v);
    } else {
#pragma nounroll
        for (int spin = 0; spin < 4000000 && *(volatile unsigned*)mbox < (unsigned)(s + 1); ++spin) __builtin_amdgcn_s_sleep(2);
        asm volatile("" ::: "memory");
    }
#pragma nounroll
    for (int spin = 0; !have; ++spin) {
        asm volatile("" ::: "memory");
#pragma unroll
        for (int e = 0; e < 2; ++e) vm[e] = ld_sc1s(rsS, l * 16 + e * 1024, bb);
#pragma unroll
        for (int ks = 0; ks < 4; ++ks) kmf[ks] = ld_sc1s(rsS, ks * 512 + g * 128 + (i & 7) * 16, bq);
        unsigned m = mx4(mx4(0u, vm[0]), vm[1]);
#pragma unroll
        for (int ks = 0; ks < 4; ++ks) m = mx4(m, kmf[ks]);
        if (__all(m != SENT) || spin > 20000) break;
        __builtin_amdgcn_s_sleep(2);
    }
#pragma unroll
    for (int e = 0; e < 8; ++e) *(unsigned*)&Vs[32 + (i >> 2) + 4 * ((i >> 1) & 1)][64 * (e >> 2) + 16 * g + 4 * (e & 3) + 2 * (i & 1)] = vm[e >> 2][e & 3];
    f32x4 sm_[2] = {};
#pragma unroll
    for (int ks = 0; ks < 4; ++ks) {
        sm_[0] = __builtin_amdgcn_mfma_f32_16x16x32_bf16(as_frag(kmf[ks]), as_frag(q0f[ks]), sm_[0], 0, 0, 0);
        sm_[1] = __builtin_amdgcn_mfma_f32_16x16x32_bf16(as_frag(kmf[ks]), as_frag(q1f[ks]), sm_[1], 0, 0, 0);
    }
    v4u pf[2][2];
#pragma unroll
    for (int qt = 0; qt < 2; ++qt) {
        round2(sm_[qt], &sv[qt][8]);
        float mx = fmaxf(fmaxf(mxx[qt], fmaxf(sv[qt][8], sv[qt][9])), fmaxf(sv[qt][10], sv[qt][11]));
        mx = xor16_max(mx);
        mx = xor32_max(mx);
        const float nmx = -mx * L2E;
        float sum = 0.f;
#pragma unroll
        for (int e = 0; e < 12; ++e) {
            if (qt == 0 && e >= 4 && e < 8) { sv[qt][e] = 0.f; continue; }
            float ev = __builtin_amdgcn_exp2f(fmaf(sv[qt][e], L2E, nmx));
            if (e >= 8) ev = g < 2 ? ev : 0.f;
            sv[qt][e] = ev; sum += ev;
        }
        sum = xor16_sum(sum);
        sum = xor32_sum(sum);
        const float inv = 1.0f / sum;
        pf[qt][0][0] = cvt_pk_bf16(sv[qt][0] * inv, sv[qt][1] * inv);
        pf[qt][0][1] = cvt_pk_bf16(sv[qt][2] * inv, sv[qt][3] * inv);
        pf[qt][0][2] = qt ? cvt_pk_bf16(sv[qt][4] * inv, sv[qt][5] * inv) : 0u;
        pf[qt][0][3] = qt ? cvt_pk_bf16(sv[qt][6] * inv, sv[qt][7] * inv) : 0u;
        pf[qt][1][0] = cvt_pk_bf16(sv[qt][8] * inv, sv[qt][9] * inv);
        pf[qt][1][1] = cvt_pk_bf16(sv[qt][10] * inv, sv[qt][11] * inv);
        pf[qt][1][2] = 0u; pf[qt][1][3] = 0u;
    }
    v2u vmf[8];
#pragma unroll
    for (int t = 0; t < 8; ++t) vmf[t] = tr4(&Vs[32 + 4 * (g & 1) + (i >> 2)][16 * t + 4 * (i & 3)]);
#pragma unroll
    for (int t = 0; t < 8; t += 2) {
        unsigned pk[2][2][2];
#pragma unroll
        for (int tt = 0; tt < 2; ++tt) {
            const bf16x8 vf0 = vfx[t + tt];
            const bf16x8 vf1 = as_frag(v4u{vmf[t + tt][0], vmf[t + tt][1], 0u, 0u});
#pragma unroll
            for (int qt = 0; qt < 2; ++qt) {
                f32x4 o = {};
                o = __builtin_amdgcn_mfma_f32_16x16x32_bf16(vf0, as_frag(pf[qt][0]), o, 0, 0, 0);
                o = __builtin_amdgcn_mfma_f32_16x16x32_bf16(vf1, as_frag(pf[qt][1]), o, 0, 0, 0);
                pk[qt][tt][0] = cvt_pk_bf16(o[0], o[1]);
                pk[qt][tt][1] = cvt_pk_bf16(o[2], o[3]);
            }
        }
#pragma unroll
        for (int qt = 0; qt < 2; ++qt) {
            auto r0 = __builtin_amdgcn_permlane16_swap(pk[qt][0][0], pk[qt][1][0], false, false);
            auto r1 = __builtin_amdgcn_permlane16_swap(pk[qt][0][1], pk[qt][1][1], false, false);
            v4u o = {r0[0], r1[0], r0[1], r1[1]};
            st_sc1s(rsV, (t >> 1) * 1024 + ((g & 1) * 2 + (g >> 1)) * 256 + i * 16, xq + qt * 4096, o);
        }
    }
    pend = seen > (unsigned)(sn + 1) && ready ? s : -1;
    if (pend < 0) { asm volatile("s_waitcnt vmcnt(0)" ::: "memory"); count(s); }
}

__device__ __forceinline__ void tile_stream(const ChainArgs& a, const u16* X, unsigned long long todo, const int xc, unsigned& tau, const bool early, const int nw, u16* sm8 WEVP) {
    constexpr int K = DM, LDB = HD;
    const int tid = opaque_tid(), w = tid >> 6, l = tid & 63, i = l & 15, g = l >> 4;
    const int wr = w >> 2, wc = w & 3;
    const unsigned lds0 = (unsigned)(uintptr_t)(__attribute__((address_space(3))) u16*)sm8;
    const unsigned wu = __builtin_amdgcn_readfirstlane(w);
    const unsigned ldsw = lds0 + wu * 2048;
    unsigned voffA, voffB;
    { int row = 16 * w + (l >> 3), c = l & 7; voffA = (unsigned)(row * K + 8 * (c ^ (row & 7))) * 2u; }
    { int row = 8 * w + g, c = i; voffB = (unsigned)(row * LDB + 8 * (c ^ (bswz2<128>(row) << 1))) * 2u; }
    const unsigned voffA2 = voffA + 8u * K * 2u - 1024u, voffB2 = voffB + 4u * LDB * 2u - 1024u;
    auto st2 = [&](const unsigned v0, const unsigned v1, const char* p, const unsigned d) {
        unsigned keep;
        asm volatile("s_mov_b32 %0, m0\n\ts_mov_b32 m0, %1\n\ts_nop 0\n\tglobal_load_lds_dwordx4 %2, %4\n\tglobal_load_lds_dwordx4 %3, %4 offset:1024\n\ts_mov_b32 m0, %0"
                     : "=&s"(keep) : "s"(d), "v"(v0), "v"(v1), "s"(p) : "memory");
    };
    auto stA = [&](int b, int h, const char* p) {
        st2(voffA, voffA2, p + (size_t)(h * 128) * K * 2, ldsw + (2 * b + h) * 16384);
    };
    auto stB = [&](int b, int h, const char* p) {
        st2(voffB, voffB2, p + h * 2048, ldsw + 65536 + (2 * b + h) * 16384);
    };
    constexpr int SA = 128, SB = 64 * LDB * 2;
    auto a_of = [&](const unsigned t) { return (const char*)(X + (size_t)((t >> 4) & 7) * 256 * DM); };
    auto b_of = [&](const unsigned t) { const int kind = t & 7; return (const char*)((kind < 2 ? a.Wk : (kind < 4 ? a.Wv : a.Wq)) + hcol(4 * xc + (kind & 1) * 2)); };
    auto c_of = [&](const unsigned t) { const int kind = t & 7; return ((kind & 6) == 2 ? VX_OFF - KX_OFF : 0) + (int)((t >> 4) & 7) * 4194304 + blk_off(0, 4 * xc + (kind & 1) * 2, kind < 4 ? tau : tau ^ 1u) + (int)((t >> 3) & 1u); };
    auto publish = [&](const unsigned t) {
        const int kind = t & 7, ln = lane_now();
        if (wu == 0 && ln < (kind < 4 ? 1 : 2))
            __hip_atomic_store((gu32*)(a.sync + ((kind < 4 ? SY_KV : SY_Q) + ((t >> 4) & 7) * 8 + xc) * 32 + (kind < 4 ? kind : 2 * kind - 8 + ln)), 16u, __ATOMIC_RELAXED, __HIP_MEMORY_SCOPE_AGENT);
    };
    const char* smc = (const char*)sm8;
    int aoff[2], boff[2];
#pragma unroll
    for (int ks = 0; ks < 2; ++ks) aoff[ks] = (wr * 64 + i) * 128 + (((ks * 4 + g) ^ (i & 7)) << 4);
#pragma unroll
    for (int n = 0; n < 2; ++n) { int row = 8 * g + (i >> 2); boff[n] = 65536 + row * 256 + (((wc * 2 + n) ^ bswz2<128>(row)) << 5) + 8 * (i & 3); }
    f32x4 acc[2][2][4][2] = {};
    bf16x8 At[4][2], B0[2][2], B1[2][2];
#define K8_LDA(b, h) _Pragma("unroll") for (int m = 0; m < 4; ++m) _Pragma("unroll") for (int ks = 0; ks < 2; ++ks) \
        At[m][ks] = *(const bf16x8*)(smc + aoff[ks] + (2 * (b) + (h)) * 16384 + m * 2048)
#define K8_LDB(dst, b, h) _Pragma("unroll") for (int n = 0; n < 2; ++n) _Pragma("unroll") for (int ks = 0; ks < 2; ++ks) { \
        const char* p_ = smc + boff[n] + (2 * (b) + (h)) * 16384 + ks * 8192; \
        v4i16 lo_ = __builtin_amdgcn_ds_read_tr16_b64_v4i16((__attribute__((address_space(3))) v4i16*)p_); \
        v4i16 hi_ = __builtin_amdgcn_ds_read_tr16_b64_v4i16((__attribute__((address_space(3))) v4i16*)(p_ + 1024)); \
        dst[n][ks] = bf16x8{lo_[0], lo_[1], lo_[2], lo_[3], hi_[0], hi_[1], hi_[2], hi_[3]}; }
#define K8_MMA(ai, bj, Bt) do { __builtin_amdgcn_s_setprio(1); \
        _Pragma("unroll") for (int m = 0; m < 4; ++m) _Pragma("unroll") for (int n = 0; n < 2; ++n) _Pragma("unroll") for (int ks = 0; ks < 2; ++ks) \
            acc[ai][bj][m][n] = __builtin_amdgcn_mfma_f32_16x16x32_bf16(Bt[n][ks], At[m][ks], acc[ai][bj][m][n], 0, 0, 0); \
        __builtin_amdgcn_s_setprio(0); } while (0)
    const rsrc_t rsC = __builtin_amdgcn_make_buffer_rsrc((void*)(arena(a.slots) + KX_OFF), 0, FX_OFF - KX_OFF, 0x00020000);
#define K8_EPI(ai, bj, cb) do { const int ln_ = lane_now(); \
        const int vo_ = (int)(wu & 3) * 1024 + (((ln_ >> 4) & 1) * 2 + (ln_ >> 5)) * 256 + (ln_ & 15) * 16; \
        _Pragma("unroll") for (int m = 0; m < 4; ++m) { \
            const f32x4 c0 = acc[ai][bj][m][0], c1 = acc[ai][bj][m][1]; \
            auto r0 = __builtin_amdgcn_permlane16_swap(cvt_pk_bf16(c0[0], c0[1]), cvt_pk_bf16(c1[0], c1[1]), false, false); \
            auto r1 = __builtin_amdgcn_permlane16_swap(cvt_pk_bf16(c0[2], c0[3]), cvt_pk_bf16(c1[2], c1[3]), false, false); \
            const int sg_ = 4 * ai + 2 * (int)(wu >> 2) + (m >> 1); \
            st_sc1s(rsC, vo_, ((ai ? ((cb) & ~1) ^ 8192 : (cb) & 1 ? 1 << 30 : (cb)) ^ ((sg_ & SWZ) << 15)) + sg_ * 524288 + bj * 16384 + (m & 1) * 4096, v4u{r0[0], r1[0], r0[1], r1[1]}); \
            acc[ai][bj][m][0] = ZERO4; acc[ai][bj][m][1] = ZERO4; } } while (0)
    constexpr int nt = K / 64;
    unsigned cur = (unsigned)todo & 255u, prev = 0u;
    const char* pA = a_of(cur); const char* pB = b_of(cur);
    if (TX0 && early) touch((const char*)X + 524288 + (size_t)(nw * 128 + (tid >> 2)) * 128, lds0 + 163840 - 256);
    stB(0, 0, pB); stB(0, 1, pB); stA(0, 0, pA); stA(0, 1, pA);
    stB(1, 0, pB + SB); stB(1, 1, pB + SB); stA(1, 0, pA + SA);
    K8_WV(8); K8_BAR;
    if (wu >= 4) K8_BAR;
#define K8_GO(ai) __builtin_amdgcn_s_waitcnt(0xC07F); K8_BAR; K8_MMA(ai, 0, B0); K8_MMA(ai, 1, B1); K8_SCHED; K8_BAR
    int cbp = 0;
    for (;;) {
        todo >>= 8;
        const unsigned nxt = todo != 0ull ? (unsigned)todo & 255u : cur;
        int cb = c_of(cur);
#pragma nounroll
        for (int t = 0; t < nt; t += 2) {
            if (!(TC0 & 1) && __builtin_expect(early && prev == 0u && t == TXT, 0))
                touch((const char*)X + 3 * 524288 + (size_t)(nw * 320 + min(TIDX, 319)) * 128, lds0 + 163840 - 256);
            if (__builtin_expect(early && prev == 0u && t == nt - 4, 0)) {
                tau = __builtin_amdgcn_readfirstlane((unsigned)boot_wait(a.boot) & 255u);
                cb = c_of(cur);
            }
            const bool first = t == 0 && prev != 0u, last = t == nt - 2;
            if (t == 2 && prev != 0u) { publish(prev); WEV(3) }
            K8_LDB(B0, 0, 0); K8_LDA(0, 0); K8_LDB(B1, 0, 1); K8_SCHED; stA(1, 1, pA + SA);
            pA = last ? a_of(nxt) : pA + 2 * SA; pB = last ? b_of(nxt) : pB + 2 * SB;
            if (first) { K8_EPI(1, 0, cbp); K8_EPI(1, 1, cbp); K8_WV(24); } else K8_WV(8);
            K8_GO(0);
            K8_LDA(0, 1); K8_SCHED; stB(0, 0, pB); stB(0, 1, pB); stA(0, 0, pA);
            if (first) K8_WV(24); else K8_WV(8);
            K8_GO(1);
            K8_LDB(B0, 1, 0); K8_LDA(1, 0); K8_LDB(B1, 1, 1); K8_SCHED; stA(0, 1, pA);
            if (first) K8_WV(16); else K8_WV(8);
            K8_GO(0);
            K8_LDA(1, 1); K8_SCHED; stB(1, 0, pB + SB); stB(1, 1, pB + SB); stA(1, 0, pA + SA);
            if (last) { K8_EPI(0, 0, cb); K8_EPI(0, 1, cb); K8_WV(16); } else K8_WV(8);
            K8_GO(1);
        }
        WEV(2)
        prev = cur; cbp = cb; cur = nxt;
        if (todo == 0ull) break;
    }
    K8_EPI(1, 0, cbp); K8_EPI(1, 1, cbp);
    K8_WV(0);
    if (wu < 4) K8_BAR;
    K8_BAR;
    publish(prev);
    WEV(3)
}

__device__ __forceinline__ void oproj_squad(const ChainArgs& a, u16* out, const int ct, const int rh, const int xc, const unsigned tau, char* smem) {
    const int tid = opaque_tid(), w = tid >> 6, l = tid & 63, i = l & 15, g = l >> 4;
    const int wu = __builtin_amdgcn_readfirstlane(w);
    bf16x8 wf[2][16];
    {
        u16* stg = (u16*)(smem + wu * 10240);
#pragma unroll
        for (int ch = 0; ch < 4; ++ch) {
            const u16* src = a.Wo + (size_t)hcol(4 * wu + ch) * DM + 32 * ct;
            v4u raw[8];
#pragma unroll
            for (int e = 0; e < 8; ++e) { int idx = l + 64 * e, r = idx >> 2, q4 = idx & 3; raw[e] = *(const v4u*)(src + (size_t)r * DM + 8 * q4); }
#pragma unroll
            for (int e = 0; e < 8; ++e) { int idx = l + 64 * e, r = idx >> 2, q4 = idx & 3; *(v4u*)(stg + r * 40 + 8 * q4) = raw[e]; }
            __builtin_amdgcn_s_waitcnt(0xC07F);
#pragma unroll
            for (int jj = 0; jj < 2; ++jj)
#pragma unroll
                for (int ks = 0; ks < 4; ++ks) {
                    const u16* p = stg + (32 * ks + 8 * g + (i >> 2)) * 40 + 16 * jj + 4 * (i & 3);
                    wf[jj][4 * ch + ks] = tr_frag(p, p + 4 * 40);
                }
            __builtin_amdgcn_s_waitcnt(0xC07F);
        }
    }
    const rsrc_t rsO = __builtin_amdgcn_make_buffer_rsrc((void*)(arena(a.slots) + VX_OFF), 0, 32 << 20, 0x00020000);
    float* red = (float*)(smem + 81920);
    const int voff = rh * 4096 + l * 16;
    volatile unsigned* rdy = (volatile unsigned*)(smem + 81920 + 2 * 8 * 16 * 36 * 4);
    unsigned ready = 0;
    bool pre = false;
    v4u xf[16];
    for (int s = SSEG; s < NSEG - 1; ++s) {
        if (!(ready & 1u)) {
            if (wu == 0) {
                const unsigned* q = a.sync + (SY_OXS + (s - SSEG) * 8 + xc) * 32;
#pragma nounroll
                for (int spin = 0;; ++spin) {
                    asm volatile("" ::: "memory");
                    if (ld_flag(q) != 0u) break;
                    if (spin > 20000) { __hip_atomic_store((gu32*)a.tmo, 7000u, __ATOMIC_RELAXED, __HIP_MEMORY_SCOPE_AGENT); break; }
                    if (s == SSEG) __builtin_amdgcn_s_sleep(64); else if (s < NSEG - 5) __builtin_amdgcn_s_sleep(16); else __builtin_amdgcn_s_sleep(SQ_SF);
                }
            }
            asm volatile("" ::: "memory");
            __builtin_amdgcn_s_barrier();
            asm volatile("" ::: "memory");
        }
        if (!pre) {
#pragma unroll
            for (int ks = 0; ks < 16; ++ks) xf[ks] = ld_sc1s(rsO, voff + (ks & 3) * 1024, blk_off(s, 4 * wu + (ks >> 2), tau) ^ 8192);
        }
        unsigned nf = 0;
        if (wu == 0) nf = ld_flag(a.sync + (SY_OXS + (s + 1 - SSEG + (l & 1)) * 8 + xc) * 32);
        pre = (ready & 2u) != 0u;
        f32x4 acc[2] = {};
        if (pre) {
#pragma unroll
            for (int ks = 0; ks < 16; ++ks) {
#pragma unroll
                for (int jj = 0; jj < 2; ++jj) acc[jj] = __builtin_amdgcn_mfma_f32_16x16x32_bf16(wf[jj][ks], as_frag(xf[ks]), acc[jj], 0, 0, 0);
                K8_SCHED;
                xf[ks] = ld_sc1s(rsO, voff + (ks & 3) * 1024, blk_off(s + 1, 4 * wu + (ks >> 2), tau) ^ 8192);
                K8_SCHED;
            }
        } else {
#pragma unroll
            for (int ks = 0; ks < 16; ++ks)
#pragma unroll
                for (int jj = 0; jj < 2; ++jj) acc[jj] = __builtin_amdgcn_mfma_f32_16x16x32_bf16(wf[jj][ks], as_frag(xf[ks]), acc[jj], 0, 0, 0);
        }
        float* rp = red + (s & 1) * (8 * 16 * 36);
#pragma unroll
        for (int jj = 0; jj < 2; ++jj) *(f32x4*)&rp[(wu * 16 + i) * 36 + 16 * jj + 4 * g] = acc[jj];
        if (wu == 0) rdy[s & 1] = (unsigned)__ballot(nf != 0u) & 3u;
        K8_WL(0); K8_BAR;
        ready = __builtin_amdgcn_readfirstlane(rdy[s & 1]) | (pre ? 1u : 0u);
        if (tid >= 64 && tid < 192) {
            const int t = tid - 64, row = t >> 3, q4 = t & 7;
            f32x4 sv_[8];
#pragma unroll
            for (int e = 0; e < 8; ++e) sv_[e] = *(const f32x4*)&rp[(e * 16 + row) * 36 + 4 * q4];
            const f32x4 s0 = ((sv_[0] + sv_[1]) + (sv_[2] + sv_[3])) + ((sv_[4] + sv_[5]) + (sv_[6] + sv_[7]));
            v2u o; o[0] = cvt_pk_bf16(s0[0], s0[1]); o[1] = cvt_pk_bf16(s0[2], s0[3]);
            out8(out + (size_t)(s * SEG + 16 * rh + row) * OUTW + 32 * ct + 4 * q4, o);
        }
    }
}

__device__ __forceinline__ void kv_stream(const ChainArgs& a, const u16* __restrict__ X, const u16* __restrict__ B, char* C, const int head, const int xc, const int wk, unsigned& tau, char* smem) {
    const int tid = opaque_tid(), w = tid >> 6, l = tid & 63, i = l & 15, g = l >> 4;
    const int wu = __builtin_amdgcn_readfirstlane(w), kh = wu >> 2, cq = wu & 3;
    const unsigned lds0 = (unsigned)(uintptr_t)(__attribute__((address_space(3))) char*)smem;
    unsigned voA, voB;
    { const int row = (wu & 1) * 8 + (l >> 3), c = l & 7; voA = (unsigned)(row * DM + 8 * (c ^ (row & 7))) * 2u + (wu >> 1) * 512; }
    { const int row = tid >> 4, c = tid & 15; voB = (unsigned)(row * HD + 8 * (c ^ (bswz2<128>(row) << 1))) * 2u; }
    auto stB = [&](int ch, int slot) {
#pragma unroll
        for (int e = 0; e < 4; ++e) glds16s(voB, (const char*)B + (size_t)(4 * ch + e) * 32 * HD * 2, lds0 + 65536 + slot * 32768 + e * 8192 + wu * 1024);
    };
    const unsigned ldA = lds0 + (wu >> 1) * 8192 + (wu & 1) * 1024;
    auto stP = [&](const char* p, const unsigned d) {
        unsigned keep;
        asm volatile("s_mov_b32 %0, m0\n\ts_mov_b32 m0, %1\n\ts_nop 0\n\tglobal_load_lds_dwordx4 %3, %2 offset:-4096\n\tglobal_load_lds_dwordx4 %4, %2 offset:-2048\n\t"
                     "global_load_lds_dwordx4 %5, %2\n\tglobal_load_lds_dwordx4 %6, %2 offset:2048\n\ts_mov_b32 m0, %0"
                     : "=&s"(keep) : "s"(d), "s"(p), "v"(voA + 8192u), "v"(voA + 6272u), "v"(voA + 4352u), "v"(voA + 2432u) : "memory");
    };
    auto srcA = [&](int h) { return (const char*)X + (size_t)min(h, 2 * KVS - 1) * 32768 - 4096; };
    auto stA = [&](int h) { stP(srcA(h), ldA + (h & 3) * 32768 + 4096); };
    stB(0, 0); stA(0); stA(1); stB(1, 1);
    touch((const char*)X + 65536 + min(wk * 512 + tid, (KVS - 1) * 512 - 1) * 128, lds0 + 131072 + wu * 1024);
    if (KVS > 9) touch((const char*)X + 65536 + min(4096 + wk * 512 + tid, (KVS - 1) * 512 - 1) * 128, lds0 + 131072 + wu * 1024);
    stB(2, 2);
    bf16x8 bf[16][2], af[8];
    f32x4 acc[2][2] = {}, keep[2];
    const int rb = 64 * kh + 8 * g + (i >> 2), sw = bswz2<128>(rb);
    const char* bp = smem + 65536 + rb * 256 + 8 * (i & 3);
    const char* sa0 = smem + kh * 2048 + i * 128 + 16 * (g ^ (i & 7));
    const char* sa1 = smem + kh * 2048 + i * 128 + 16 * ((4 + g) ^ (i & 7));
#define KV_AF(slot, q) *(const bf16x8*)((((q) & 1) ? sa1 : sa0) + (slot) * 32768 + ((q) >> 1) * 4096)
#pragma unroll
    for (int ch = 0; ch < 8; ++ch) {
        if (ch == 0) { if (KVS > 9) K8_WV(10); else K8_WV(9); } else if (ch < 6) K8_WV(8); else if (ch == 6) K8_WV(4); else K8_WV(0);
        K8_BAR;
#pragma unroll
        for (int e = 0; e < 4; ++e) { const char* p_ = bp + (ch % 3) * 32768 + (e >> 1) * 8192 + 32 * ((2 * cq + (e & 1)) ^ sw); bf[2 * ch + (e >> 1)][e & 1] = tr_frag(p_, p_ + 1024); }
#pragma unroll
        for (int e = 0; e < 4; ++e) af[e] = KV_AF(e >> 1, 2 * ch + (e & 1));
        K8_WL(0); K8_SCHED; K8_BAR;
        if (ch < 5) stB(ch + 3, ch % 3);
        if (ch == 7) { stA(2); stA(3); }
#pragma unroll
        for (int e = 0; e < 8; ++e) acc[e >> 2][e & 1] = __builtin_amdgcn_mfma_f32_16x16x32_bf16(bf[2 * ch + ((e >> 1) & 1)][e & 1], af[e >> 1], acc[e >> 2][e & 1], 0, 0, 0);
    }
    float* red = (float*)(smem + 131072) + l * 4;
#pragma unroll
    for (int p = 0; p < 2; ++p) { *(f32x4*)(red + (p * 8 + wu) * 256) = kh ? acc[p][0] : acc[p][1]; keep[p] = kh ? acc[p][1] : acc[p][0]; }
    unsigned long long tag = 0;
    if (wu == 4) { tag = boot_wait(a.boot); *(volatile unsigned*)(smem + 163840 - 512) = (unsigned)tag & 255u; }
    const rsrc_t rc = __builtin_amdgcn_make_buffer_rsrc((void*)C, 0, KVS * 524288, 0x00020000);
    const int vo = cq * 1024 + (2 * kh + (g >> 1)) * 256 + i * 16 + (g & 1) * 8;
    auto put = [&](const f32x4 o, const int so) {
        const v2u pk = {cvt_pk_bf16(o[0], o[1]), cvt_pk_bf16(o[2], o[3])};
        asm volatile("buffer_store_dwordx2 %0, %1, %2, %3 offen sc1" :: "v"(pk), "v"(vo), "s"(rc), "s"(so) : "memory");
    };
    K8_WL(0); K8_WV(0); K8_BAR;
    tau = __builtin_amdgcn_readfirstlane(*(volatile unsigned*)(smem + 163840 - 512));
#pragma unroll
    for (int p = 0; p < 2; ++p) put(keep[p] + *(const f32x4*)(red + (p * 8 + (wu ^ 4)) * 256), blk_off(0, head, tau) + p * 4096);
    stA(4);
    K8_WV(4); K8_BAR;
    if (tid == 256) __hip_atomic_store((gu64*)(a.boot + 256 + 16 * xc + wk), tag, __ATOMIC_RELAXED, __HIP_MEMORY_SCOPE_AGENT);
    auto stages = [&](auto kc) {
        constexpr int KH = decltype(kc)::value;
        if (KH) K8_BAR;
        f32x4 ac[2] = {ZERO4, ZERO4}, kp = ZERO4;
        int h_first = 2, so = OOB;
        asm volatile("" : "+s"(h_first));
        const char* pn = srcA(5);
        unsigned dn = ldA + 32768 + 4096;
        int r0 = (int)(sa0 - smem) + 2 * 32768, r1 = (int)(sa1 - smem) + 2 * 32768;
        for (int h = h_first; h < 2 * KVS + 4; ++h) {
            const f32x4 oth = *(const f32x4*)(red + (wu ^ 4) * 256);
            *(f32x4*)(red + wu * 256) = ac[KH ^ 1];
            bf16x8 ax[16];
#pragma unroll
            for (int q = 0; q < 16; ++q) ax[q] = *(const bf16x8*)(smem + ((q & 1) ? r1 : r0) + (q >> 1) * 4096);
            K8_SCHED;
            stP(pn, dn);
            put((KH ? ac[1] : kp) + oth, so);
            kp = ac[0];
            __builtin_amdgcn_s_waitcnt(0xC07F);
            K8_WV(10);
            if (__builtin_expect((STRICT >> h) & 1u, 0)) K8_WV(0);
            K8_BAR;
            if (KH && ((STRICT | (0xAAA80u & ~(STRICT << 2))) >> h) & 1u && tid == 256)
                __hip_atomic_store((gu32*)(a.sync + (SY_KV0 + (h / 2 - ((STRICT >> h) & 1u ? 1 : 2)) * 8 + xc) * 32 + wk), 16u, __ATOMIC_RELAXED, __HIP_MEMORY_SCOPE_AGENT);
            if (__builtin_expect(h < 2 * KVS, 1)) {
                __builtin_amdgcn_s_setprio(1);
#pragma unroll
                for (int q = 0; q < 16; ++q)
#pragma unroll
                    for (int n = 0; n < 2; ++n) ac[n] = __builtin_amdgcn_mfma_f32_16x16x32_bf16(bf[q][n], ax[q], q ? ac[n] : ZERO4, 0, 0, 0);
                __builtin_amdgcn_s_setprio(0);
            }
            {
                const int hs = h - 1 + KH;
                so = blk_off(hs >> 1, head, tau) + (hs & 1) * 4096;
                asm volatile("" : "+s"(so));
                so = hs > 1 && hs < 2 * KVS ? so : OOB;
                pn = srcA(h + 4); dn = ldA + (h & 3) * 32768 + 4096;
                r0 = (r0 + 32768) & 131071; r1 = (r1 + 32768) & 131071;
                asm volatile("" : "+s"(so), "+s"(pn), "+s"(dn), "+v"(r0), "+v"(r1));
            }
            K8_BAR;
        }
        if (!KH) K8_BAR;
    };
    if (kh) stages(std::integral_constant<int, 1>{}); else stages(std::integral_constant<int, 0>{});
    K8_WV(0);
}

struct MegaArgs {
    ChainArgs ch;
    const u16* x; u16* out;
    const u16* bx; const u16* wr; const u16* wi;
};
constexpr int MEGA_LDS = 163840;
#define WSTAMP(n)
__global__ __launch_bounds__(512, 2) void mega_kernel(MegaArgs m) {
    extern __shared__ __attribute__((aligned(256))) char chsm[];
    const int b = BIDX, xc = (b & 3) + 4 * (b >> 7), sl = (b >> 3) & 15;
    if ((b & 4) == CHS) { chain_body(m.ch, xc, sl, chsm); return; }
    const ChainArgs& a = m.ch;
    const int wk = sl, tid = TIDX;
    char* ar = arena(a.slots);
    unsigned tauw = 0;
    WSTAMP(0)
    if (wk < 8) kv_stream(a, m.x, (wk < 4 ? a.Wk : a.Wv) + hcol(4 * xc + (wk & 3)), ar + (wk < 4 ? KX_OFF : VX_OFF), 4 * xc + (wk & 3), xc, wk, tauw, chsm);
    if (TCH && wk < 8) {
        const int t = opaque_tid(), n = xc * 8 + wk;
        const unsigned sp = (unsigned)(uintptr_t)(__attribute__((address_space(3))) char*)chsm + 163840 - 256;
        touch((const char*)m.x + 3 * 524288 + (n * 320 + min(t, 319)) * 128, sp);
        touch((const char*)m.bx + (n * 512 + t) * 128, sp);
        touch((const char*)(t < 256 ? m.wr : m.wi) + (n * 256 + (t & 255)) * 128, sp);
    }
    WSTAMP(8) WEV(1)
    {
        const int p = wk & 1, q = wk & 3;
        const unsigned long long todo = wk < 2 ? 0x84u + p : wk < 4 ? TD23 + 0x01010101u * p : wk < 8 ? 0ull
                                      : wk < 12 ? (wk < 10 ? TD89 : 0x7060503010ull) + (wk < 10 ? TM89 : 0x0101010101ull) * q : (wk < 14 ? 0x54341420u : 0x64442422u) + 0x01010101u * p;
        __syncthreads();
        if (wk >= 12) {
            const int n = (xc * 4 + q) * 512 + opaque_tid();
            const unsigned sp = (unsigned)(uintptr_t)(__attribute__((address_space(3))) char*)chsm + 163840 - 256;
            if (TC0 & 1) { touch((const char*)m.x + 3 * 524288 + n * 128, sp); touch((const char*)m.x + 3 * 524288 + min(16384 + n, 20479) * 128, sp); }
            if (TC0 & 2) { touch((const char*)m.bx + n * 128, sp); touch((const char*)m.bx + (16384 + n) * 128, sp); }
            if (TC0 & 4) { touch((const char*)m.wr + n * 128, sp); touch((const char*)m.wi + n * 128, sp); }
        }
        if (todo != 0ull) tile_stream(a, m.x, todo, xc, tauw, wk >= 8, xc * 8 + (wk & 7), (u16*)chsm WEVA);
    }
    WSTAMP(2)
    if (wk < 2) {
        unsigned* mbox = (unsigned*)(chsm + 131072);
        if (tid < 5) mbox[tid] = tid ? 2u : 0u;
        __syncthreads();
        const int w = __builtin_amdgcn_readfirstlane(tid >> 6), h = 4 * xc + (w & 3);
        const rsrc_t rsQ = __builtin_amdgcn_make_buffer_rsrc((void*)(ar + KX_OFF), 0, S_LEN * HD * 4, 0x00020000);
        const rsrc_t rsK = __builtin_amdgcn_make_buffer_rsrc((void*)(ar + KX_OFF), 0, S_LEN * HD * 4, 0x00020000);
        const rsrc_t rsV = __builtin_amdgcn_make_buffer_rsrc((void*)(ar + VX_OFF), 0, S_LEN * HD * 4, 0x00020000);
        const rsrc_t rsS = slot_rsrc(a.slots);
        u16 (*Vs)[VP] = (u16 (*)[VP])(chsm + w * (40 * VP * 2));
        const unsigned tau = tauw;
        int pend = -1;
        bool ready = false;
        for (int s = 2 * wk + (w >> 2), sn; s < NSEG - 1; s = sn) {
            const int band = s >> 3;
            unsigned n = 0;
            if (lane_now() == 0) n = atomicAdd(mbox + 1 + (w & 3), 1u);
            n = __builtin_amdgcn_readfirstlane(n);
            sn = 2 * wk + (int)(n & 1u) + 4 * (int)(n >> 1);
            if (sn - s > 4) __builtin_amdgcn_s_setprio(2); else __builtin_amdgcn_s_setprio(0);
            if (s < 4) wait_ge<32>(a.sync + (SY_KV0 + (KVS - 1) * 8 + xc) * 32 + (tid & 7), 16u, a.tmo, 4100u);
            if (!ready) {
                if (band > 0) wait_ge<32>(a.sync + (SY_KV + band * 8 + xc) * 32 + (tid & 3), 1u, a.tmo, 4100u);
                wait_ge<32>(a.sync + (SY_Q + band * 8 + xc) * 32 + (tid & 3), 1u, a.tmo, 4200u);
            }
            xattn_unit(a, rsQ, rsK, rsV, rsS, s, sn, h, Vs, mbox, tau, pend, ready);
        }
        return;
    }
    if (wk >= 4 && wk < 8) {
        const int fw = (wk - 4) * 8 + xc;
        const rsrc_t rsF = __builtin_amdgcn_make_buffer_rsrc((void*)(ar + FX_OFF), 0, 16 << 20, 0x00020000);
        const rsrc_t rsW = __builtin_amdgcn_make_buffer_rsrc((void*)(ar + WT_OFF), 0, 10 << 20, 0x00020000);
        const unsigned tw = tauw ^ FNEAR;
        {
            const int tq = opaque_tid();
            for (int z = 0; z < FSR; ++z) if (TIDX >= 256) __builtin_amdgcn_s_barrier();
            fft_rows_fwd(m.bx, rsF, tw, 2 * fw + (tq >> 8), chsm + (tq >> 8) * 36864, tq & 255, (const char*)m.wr, (const char*)m.wi, (unsigned)(uintptr_t)(__attribute__((address_space(3))) char*)chsm + 163840 - 256);
            for (int z = 0; z < FSR; ++z) if (TIDX < 256) __builtin_amdgcn_s_barrier();
        }
        for (int t = fw; t < 64; t += 32) {
            __syncthreads();
            filt_transpose(m.wr, m.wi, rsW, tw, t, chsm, opaque_tid());
        }
        wg_publish(a.sync, SY_DONE, 32u, 8);
        WSTAMP(3) WEV(4)
        wg_wait_acquire<16>(a.sync + (SY_DONE + xc) * 32, a.tmo, 6000u);
        {
            const int tq = opaque_tid();
            for (int z = 0; z < fw; ++z) __builtin_amdgcn_s_sleep(FST);
            for (int z = 0; z < FSG; ++z) if (TIDX >= 256) __builtin_amdgcn_s_barrier();
            fft_cols(rsF, rsW, tw, 2 * fw + (tq >> 8), 4, chsm + (tq >> 8) * 36864, tq & 255);
            for (int z = 0; z < FSG; ++z) if (TIDX < 256) __builtin_amdgcn_s_barrier();
        }
        wg_publish(a.sync, SY_DONE + 8, 32u, 8);
        WSTAMP(4) WEV(5)
    }
    if (wk >= 4 && wk < 10) {
        const rsrc_t rsF = __builtin_amdgcn_make_buffer_rsrc((void*)(ar + FX_OFF), 0, 16 << 20, 0x00020000);
        wg_wait_acquire<16>(a.sync + (SY_DONE + 8 + xc) * 32, a.tmo, 6001u);
        const int tq = opaque_tid();
        fft_rows_inv(rsF, tauw ^ FNEAR, m.out, 2 * ((wk & 3) * 8 + xc) + (tq >> 8) + (wk < 8 ? 0 : 192), wk < 8 ? 64 : 32, wk < 8 ? 3 : 2, chsm + (tq >> 8) * 36864, tq & 255);
        WSTAMP(5) WEV(6)
    }
    if (wk >= 12) {
        const int r = 2 * (wk - 12);
        wg_wait_acquire<16>(a.sync + (SY_OX + r * 8 + xc) * 32, a.tmo, 5000u);
        wg_wait_acquire<16>(a.sync + (SY_OX + (r + 1) * 8 + xc) * 32, a.tmo, 5000u);
        WEV(7)
        gemm_tile<256>((u16*)chsm, ar + VX_OFF, 4 * r, tauw, a.Wo + xc * 128, m.out + (size_t)r * 128 * OUTW + xc * 128);
        WEV(8)
    }
    if (wk < 4) {
        const int r = wk + 6;
        wg_wait_acquire<16>(a.sync + (SY_OX + r * 8 + xc) * 32, a.tmo, 5000u);
        WEV(7)
        gemm_tile<128>((u16*)chsm, ar + VX_OFF, 4 * r, tauw, a.Wo + xc * 128, m.out + (size_t)r * 128 * OUTW + xc * 128);
        WEV(8)
        WSTAMP(9)
    }
    if (wk >= 4 && wk < 12) {
        __syncthreads();
        oproj_squad(a, m.out, (wk - 4) * 4 + (xc & 3), xc >> 2, xc, tauw, chsm);
        WSTAMP(7)
    }
}

#if defined(__HIP_DEVICE_COMPILE__)
#pragma clang attribute pop
#endif
extern "C" void kernel_launch(void* const* d_in, const int* in_sizes, int n_in,
                              void* d_out, int out_size, void* d_ws, size_t ws_size,
                              hipStream_t stream) {
    char* ws = (char*)d_ws;
    size_t off = 0;
    auto alloc = [&](size_t bytes) { void* p = ws + off; off += (bytes + 255) & ~(size_t)255; return p; };
    char* slots = (char*)alloc((size_t)ARENA_BYTES + (2u << 20));
    static_assert((SY_WORDS + 2) * 128 <= 131072, "sync block");
    unsigned* sync = (unsigned*)alloc(131072);
    unsigned long long* boot = (unsigned long long*)alloc(4096);

    MegaArgs m;
    m.ch.mem0 = (const u16*)d_in[1]; m.ch.Wq = (const u16*)d_in[2]; m.ch.Wk = (const u16*)d_in[3]; m.ch.Wv = (const u16*)d_in[4]; m.ch.Wo = (const u16*)d_in[5];
    m.ch.out = (u16*)d_out; m.ch.slots = slots; m.ch.tmo = sync + SY_WORDS * 32;
    m.ch.stamps = (unsigned long long*)(ws + (240ull << 20));
    m.ch.sync = sync; m.ch.boot = boot;
    m.x = (const u16*)d_in[0]; m.out = (u16*)d_out;
    m.bx = (const u16*)d_in[7]; m.wr = (const u16*)d_in[8]; m.wi = (const u16*)d_in[9];
    (void)hipFuncSetAttribute((const void*)mega_kernel, hipFuncAttributeMaxDynamicSharedMemorySize, MEGA_LDS);
    mega_kernel<<<256, 512, MEGA_LDS, stream>>>(m);
}
```

```cpp
#if defined(__HIP_DEVICE_COMPILE__)
#pragma clang attribute push (__attribute__((target("no-packed-fp32-ops"))), apply_to = function)
#endif
#include <hip/hip_runtime.h>
#include <hip/hip_bf16.h>
#include <stdint.h>
#include <type_traits>

typedef unsigned short u16;
#define TIDX ((int)__builtin_amdgcn_workitem_id_x())
#define BIDX ((int)__builtin_amdgcn_workgroup_id_x())
__device__ __forceinline__ int opaque_tid() { int t = TIDX; asm volatile("" : "+v"(t)); return t; }
__device__ __forceinline__ int lane_now() { int t; asm volatile("v_mbcnt_lo_u32_b32 %0, -1, 0\n\tv_mbcnt_hi_u32_b32 %0, -1, %0" : "=v"(t)); return t; }
typedef __attribute__((ext_vector_type(8))) short bf16x8;
typedef __attribute__((ext_vector_type(4))) float f32x4;

constexpr int S_LEN = 2048, DM = 1024, NH = 32, DH = 128, MM = 8, SEG = 32;
constexpr int NSEG = S_LEN / SEG;
constexpr int HD = NH * DH;
constexpr int OUTW = 2048;

typedef unsigned v4u __attribute__((ext_vector_type(4)));
typedef unsigned v2u __attribute__((ext_vector_type(2)));
typedef short v4i16 __attribute__((ext_vector_type(4)));
typedef __amdgpu_buffer_rsrc_t rsrc_t;
#define AUX_SC1 16
__device__ __forceinline__ v4u ld_sc1s(rsrc_t rs, int voff, int soff) { return __builtin_amdgcn_raw_buffer_load_b128(rs, voff, soff, AUX_SC1); }
__device__ __forceinline__ void st_sc1s(rsrc_t rs, int voff, int soff, v4u v) { __builtin_amdgcn_raw_buffer_store_b128(v, rs, voff, soff, AUX_SC1); asm volatile("s_nop 1" :: "v"(v) : "memory"); }
__device__ __forceinline__ void st8_sc1s(rsrc_t rs, int voff, int soff, v2u v) { __builtin_amdgcn_raw_buffer_store_b64(v, rs, voff, soff, AUX_SC1); }
__device__ __forceinline__ void st_sc1(rsrc_t rs, int off, v4u v) { __builtin_amdgcn_raw_buffer_store_b128(v, rs, off, 0, AUX_SC1); }
__device__ __forceinline__ void out8(void* p, v2u v) { asm volatile("global_store_dwordx2 %0, %1, off sc1" :: "v"(p), "v"(v) : "memory"); }
typedef __bf16 bf16x2_t __attribute__((ext_vector_type(2)));
typedef float f32x2_t __attribute__((ext_vector_type(2)));
__device__ __forceinline__ unsigned cvt_pk_bf16(float lo, float hi) {
    f32x2_t f = {lo, hi};
    return __builtin_bit_cast(unsigned, __builtin_convertvector(f, bf16x2_t));
}
#define SENT 0xFFBFFFBF
__device__ __forceinline__ unsigned umax3(unsigned a, unsigned b, unsigned c) { return max(max(a, b), c); }
__device__ __forceinline__ unsigned mx4(unsigned acc, v4u v) { return umax3(umax3(acc, v[0], v[1]), v[2], v[3]); }
__device__ __forceinline__ bf16x8 as_frag(v4u v) { return __builtin_bit_cast(bf16x8, v); }
__device__ __forceinline__ float xor16_sum(float x) {
    auto r = __builtin_amdgcn_permlane16_swap(__float_as_uint(x), __float_as_uint(x), false, false);
    return __uint_as_float(r[0]) + __uint_as_float(r[1]);
}
__device__ __forceinline__ float xor32_sum(float x) {
    auto r = __builtin_amdgcn_permlane32_swap(__float_as_uint(x), __float_as_uint(x), false, false);
    return __uint_as_float(r[0]) + __uint_as_float(r[1]);
}
__device__ __forceinline__ float xor16_max(float x) {
    auto r = __builtin_amdgcn_permlane16_swap(__float_as_uint(x), __float_as_uint(x), false, false);
    return fmaxf(__uint_as_float(r[0]), __uint_as_float(r[1]));
}
__device__ __forceinline__ float xor32_max(float x) {
    auto r = __builtin_amdgcn_permlane32_swap(__float_as_uint(x), __float_as_uint(x), false, false);
    return fmaxf(__uint_as_float(r[0]), __uint_as_float(r[1]));
}
constexpr int SPIN_LIMIT = 400000;
constexpr int OOB = 0x7FFFFF00;
#define PB 14
#define TD 60
#define PS 46
#define TE0 10
#define PVA 8
#define PV 1
#define PV1 2
#define PV2 11
#define PL 20
#define PF 48
#define PC 31
#define PP 15
#define PKA 28
#define PG0 2
#define PG1 32
#define DLY_U 4
#define DLY_C 4
#define XA_FAST 6
#define XA_D 12
#define SQ_SF 4
#define LW 0
constexpr int NPROBE = 2;
constexpr int M_OFF = 32 * 16384, F_OFF = M_OFF + 2 * 16384, C_OFF = F_OFF + 2 * 16384, REC = C_OFF + 16 * 16384, PERWG = REC / 128, SLOT_BYTES = 34 * 2097152;
static_assert(PERWG == 6656, "arming shares");
__device__ __forceinline__ int rec_base(int s) { return (s >> 1) * 2097152 + (s & 1) * REC; }
__device__ __forceinline__ char* arena(char* slots) { return (char*)(((uintptr_t)slots + 2097151u) & ~(uintptr_t)2097151u); }
constexpr int KX_OFF = SLOT_BYTES, VX_OFF = KX_OFF + (32 << 20), FX_OFF = VX_OFF + (32 << 20), WT_OFF = FX_OFF + (16 << 20), ARENA_BYTES = WT_OFF + (10 << 20);
__device__ __forceinline__ rsrc_t slot_rsrc(char* slots) { return __builtin_amdgcn_make_buffer_rsrc((void*)arena(slots), 0, FX_OFF, 0x00020000); }
__device__ __forceinline__ int blk_off(int seg, int h, unsigned tau) { return seg * 524288 + (((h & 3) >> 1) * 16 + (h >> 2) * 2 + (h & 1)) * 16384 + (int)((tau ^ (unsigned)(seg >> 2)) & 1u) * 8192; }
constexpr int TM_OFF = 124 * 16384;
constexpr int CAL_OFF = 32 * 2097152;
__device__ __forceinline__ int near_half(int s, unsigned tau) { return (int)((tau ^ (unsigned)(s >> 1)) & 1u) * 8192; }
__device__ __forceinline__ int chunk_off(int s, int g, int j, unsigned tau) { return rec_base(s) + (j < 2 ? (g & 3) * 2 + j : 6 + 3 * g + j) * 16384 + near_half(s, tau); }
__device__ __forceinline__ int x_off(int s, int h, unsigned tau) { return chunk_off(s, h >> 2, 2, tau) + (h & 3) * 2048; }
__device__ __forceinline__ int km_off(int s, int h, unsigned tau) { return chunk_off(s, h >> 2, 3, tau) + (h & 3) * 2048; }
__device__ __forceinline__ int m_off(int s, int h, unsigned tau) { return rec_base(s) + M_OFF + (h >> 4) * 16384 + near_half(s, tau) + (h & 15) * 512; }
__device__ __forceinline__ int v_off(int s, int h, unsigned tau) { return chunk_off(s, h >> 2, 4, tau) + (h & 3) * 2048; }
__device__ __forceinline__ int f_off(int s, int g, unsigned tau) { return rec_base(s) + F_OFF + (g >> 2) * 16384 + near_half(s, tau) + (g & 3) * 2048; }
__device__ __forceinline__ int hcol(int h) { return ((h & 3) * 8 + (h >> 2)) * 128; }
constexpr int VP = 144;

struct ChainArgs {
    u16* out;
    const u16* mem0; const u16* Wq; const u16* Wk; const u16* Wv; const u16* Wo;
    char* slots;
    unsigned* tmo;
    unsigned long long* stamps;
    unsigned* sync;
    unsigned long long* boot;
};
constexpr int SY_KV = 0;
constexpr int SY_Q = 64;
constexpr int SY_PROG = 128;
constexpr int SY_OX = 136;
constexpr int SY_DONE = 264;
constexpr int SY_OXS = 472;
constexpr int SY_KV0 = 408;
constexpr int KVS = 8;
constexpr int SY_FLAGS = 680;
constexpr int SY_WORDS = SY_FLAGS + SY_FLAGS / 8;
constexpr int SSEG = 40;
typedef __attribute__((address_space(1))) unsigned gu32;
typedef __attribute__((address_space(1))) unsigned long long gu64;
__device__ __forceinline__ unsigned ld_flag(const unsigned* p) { return __hip_atomic_load((gu32*)p, __ATOMIC_RELAXED, __HIP_MEMORY_SCOPE_AGENT); }
__device__ __forceinline__ unsigned long long boot_tag(const unsigned long long* boot) {
    return __hip_atomic_load((gu64*)(boot + 144), __ATOMIC_RELAXED, __HIP_MEMORY_SCOPE_AGENT) + 1ull;
}
__device__ __forceinline__ unsigned long long boot_wait(const unsigned long long* boot) {
    const int l = TIDX & 63;
    unsigned long long tag = 0, x = 0, y = 0;
#pragma nounroll
    for (int spin = 0; spin < 100000; ++spin) {
        asm volatile("" ::: "memory");
        x = __hip_atomic_load((gu64*)(boot + l), __ATOMIC_RELAXED, __HIP_MEMORY_SCOPE_AGENT);
        y = __hip_atomic_load((gu64*)(boot + 64 + l), __ATOMIC_RELAXED, __HIP_MEMORY_SCOPE_AGENT);
        tag = boot_tag(boot);
        if (__all((x | 3u) == ((tag << 2) | 3u) && (y | 3u) == ((tag << 2) | 3u))) break;
        __builtin_amdgcn_s_sleep(16);
    }
    asm volatile("" ::: "memory");
    const unsigned m = ((unsigned)__ballot(x & 1u) ^ (__popcll(__ballot(x & 2u)) + __popcll(__ballot(y & 2u)) > 64 ? 1u : 0u)) & 1u;
    return (tag << 8) | m;
}
template <int SL>
__device__ __forceinline__ void wait_ge(const unsigned* p, unsigned need, unsigned* tmo, unsigned code) {
#pragma nounroll
    for (int spin = 0;; ++spin) {
        asm volatile("" ::: "memory");
        if (__all(ld_flag(p) >= need)) break;
        if (spin > 400000 / SL) { __hip_atomic_store((gu32*)tmo, code, __ATOMIC_RELAXED, __HIP_MEMORY_SCOPE_AGENT); break; }
        __builtin_amdgcn_s_sleep(SL);
    }
    asm volatile("" ::: "memory");
}
template <int SL>
__device__ __forceinline__ void wg_wait_acquire(const unsigned* p, unsigned* tmo, unsigned code) {
    if (TIDX < 64) {
        wait_ge<SL>(p, 1u, tmo, code);
        __builtin_amdgcn_fence(__ATOMIC_ACQUIRE, "agent");
        asm volatile("s_waitcnt vmcnt(0)" ::: "memory");
    }
    __syncthreads();
}
__device__ __forceinline__ void arrive(unsigned* sync, const int f, const unsigned need, const int n) {
    const int l = lane_now();
    unsigned old = 0;
    if (l == 0) old = __hip_atomic_fetch_add((gu32*)(sync + (SY_FLAGS + (f >> 3)) * 32), 1u, __ATOMIC_RELAXED, __HIP_MEMORY_SCOPE_AGENT);
    if (__builtin_amdgcn_readfirstlane(old) == need - 1u && l < n) __hip_atomic_store((gu32*)(sync + (f + l) * 32), 16u, __ATOMIC_RELAXED, __HIP_MEMORY_SCOPE_AGENT);
}
__device__ __forceinline__ void wg_publish(unsigned* sync, const int f, const unsigned need, const int n) {
    asm volatile("s_waitcnt vmcnt(0)" ::: "memory");
    __syncthreads();
    if (TIDX < 64) arrive(sync, f, need, n);
}
#define TMO_SET(code) { dead = true; if (l == 0) __hip_atomic_store(a.tmo, (code) + s, __ATOMIC_RELAXED, __HIP_MEMORY_SCOPE_AGENT); }

__device__ __forceinline__ void glds16h(unsigned voff, const void* sbase, unsigned lds_dst) {
    unsigned keep;
    asm volatile("s_mov_b32 %0, m0\n\ts_mov_b32 m0, %3\n\ts_nop 0\n\tglobal_load_lds_dwordx4 %1, %2 sc1\n\ts_mov_b32 m0, %0" : "=&s"(keep) : "v"(voff), "s"(sbase), "s"(lds_dst) : "memory");
}
__device__ __forceinline__ void touch(const void* p, unsigned lds_dst) {
    unsigned keep;
    asm volatile("s_mov_b32 %0, m0\n\ts_mov_b32 m0, %2\n\ts_nop 0\n\tglobal_load_lds_dword %1, off\n\ts_mov_b32 m0, %0" : "=&s"(keep) : "v"(p), "s"(lds_dst) : "memory");
}
__device__ __forceinline__ void touch_sc1(const void* p, unsigned lds_dst) {
    unsigned keep;
    asm volatile("s_mov_b32 %0, m0\n\ts_mov_b32 m0, %2\n\ts_nop 0\n\tglobal_load_lds_dword %1, off sc1\n\ts_mov_b32 m0, %0" : "=&s"(keep) : "v"(p), "s"(lds_dst) : "memory");
}
#define WEV(code)
#define WEVP
#define WEVA
constexpr int C2_K = 32768, C2_V = 65536;
constexpr int C2_R1 = 0, R1S = 3200;
constexpr int C2_R3 = C2_R1 + 4 * R1S;
constexpr int C2_MB = 24576;
#define PST(n)
#define LT(n)
#define LTA(n)
#define LTOUT0
#define LTOUT
#define CN(n)
#define CNS
#define CNOUT
#define CLOB16(a, b, c, d, e, f, g, h, i, j, k, l, m, n, o, p) "v" #a, "v" #b, "v" #c, "v" #d, "v" #e, "v" #f, "v" #g, "v" #h, "v" #i, "v" #j, "v" #k, "v" #l, "v" #m, "v" #n, "v" #o, "v" #p
#define LDS3(p) ((__attribute__((address_space(3))) v4i16*)(p))
__device__ __forceinline__ bf16x8 tr_frag(const void* p0, const void* p1) {
    v4i16 lo = __builtin_amdgcn_ds_read_tr16_b64_v4i16(LDS3(p0));
    v4i16 hi = __builtin_amdgcn_ds_read_tr16_b64_v4i16(LDS3(p1));
    return bf16x8{lo[0], lo[1], lo[2], lo[3], hi[0], hi[1], hi[2], hi[3]};
}
typedef __bf16 bf16x8v __attribute__((ext_vector_type(8)));
typedef __bf16 bf16x16v __attribute__((ext_vector_type(16)));
typedef unsigned v8u __attribute__((ext_vector_type(8)));
typedef unsigned v32u __attribute__((ext_vector_type(32)));
#define PUT8(v, o, x) { const v8u x_ = (x); _Pragma("unroll") for (int e_ = 0; e_ < 8; ++e_) (v)[(o) + e_] = x_[e_]; }
#define SUB4(v, o) v4u{(v)[(o)], (v)[(o) + 1], (v)[(o) + 2], (v)[(o) + 3]}
#define PUT4(v, o, x) { const v4u x_ = (x); (v)[(o)] = x_[0]; (v)[(o) + 1] = x_[1]; (v)[(o) + 2] = x_[2]; (v)[(o) + 3] = x_[3]; }
__device__ __forceinline__ v2u tr4(const u16* p) { return __builtin_bit_cast(v2u, __builtin_amdgcn_ds_read_tr16_b64_v4i16(LDS3(p))); }
__device__ __forceinline__ f32x4 smm(v4u a, v8u b, f32x4 c, int idx) {
    return __builtin_amdgcn_smfmac_f32_16x16x64_bf16(__builtin_bit_cast(bf16x8v, a), __builtin_bit_cast(bf16x16v, b), c, idx, 0, 0);
}
__device__ __forceinline__ v8u tr_frag64(const u16* p, const int pitch) {
    const v2u r0 = tr4(p), r1 = tr4(p + 8 * pitch), r2 = tr4(p + 32 * pitch), r3 = tr4(p + 40 * pitch);
    return v8u{r0[0], r1[0], r0[1], r1[1], r2[0], r3[0], r2[1], r3[1]};
}
#define MM(a_, b_, c_) __builtin_amdgcn_mfma_f32_16x16x32_bf16(as_frag(a_), as_frag(b_), c_, 0, 0, 0)
#define STR2(x) #x
#define STR(x) STR2(x)
#define PAUSE(n) ".rept (" STR(n) ") / 4\n\ts_nop 15\n\t.endr\n\t.rept (" STR(n) ") %% 4\n\ts_nop 3\n\t.endr\n\t"
#define ZERO4 f32x4{0.f, 0.f, 0.f, 0.f}
constexpr int XB_OFF = CAL_OFF + 262144, XP = 68;
__device__ __forceinline__ void last_rows(const char* chsm, rsrc_t rsS, const int wu, const int grp, const int c, const unsigned tau) {
    const int l = lane_now();
    __builtin_amdgcn_s_barrier();
    asm volatile("" ::: "memory");
    const float* rp = (const float*)(chsm + C2_K) + (4 * wu + (l >> 4)) * XP + 4 * (l & 15);
    const f32x4 s0 = (*(const f32x4*)rp + *(const f32x4*)(rp + 32 * XP)) + (*(const f32x4*)(rp + 64 * XP) + *(const f32x4*)(rp + 96 * XP));
    v2u o;
    o[0] = cvt_pk_bf16(s0[0], s0[1]); o[1] = cvt_pk_bf16(s0[2], s0[3]);
    st8_sc1s(rsS, l * 8, XB_OFF + ((c >> 1) * 8 + wu) * 16384 + (int)(tau & 1u) * 8192 + (c & 1) * 4096 + grp * 512, o);
}
__device__ __forceinline__ const unsigned* kv_flag(const unsigned* sync, const int sq, const int grp, const int l) { return sync + (sq < KVS ? (SY_KV0 + sq * 8 + grp) * 32 + (l & 7) : (SY_KV + 8 + grp) * 32 + (l & 3)); }
__device__ __forceinline__ void chain_body(const ChainArgs& a, const int grp, const int c, char* chsm) {
    const int wu = __builtin_amdgcn_readfirstlane(TIDX >> 6);
    const int widx = grp * 16 + c;
    const int hs = 4 * grp + (c >> 2), jq = c & 3;
    const rsrc_t rsS = slot_rsrc(a.slots);
    const unsigned l0 = (unsigned)(uintptr_t)(__attribute__((address_space(3))) char*)chsm;
    bool dead = false;

    const int cal_off = CAL_OFF + grp * 32768 + (c >> 3) * 16384 + (c & 7) * 1024;
        if (wu == 0) {
            const int l = lane_now();
            const v4u ff = {SENT, SENT, SENT, SENT};
#pragma unroll
            for (int e = 0; e < 13; ++e) st_sc1s(rsS, l * 16, widx * 2 * PERWG + min(e * 1024, 2 * PERWG - 1024), ff);
            const rsrc_t rsY = __builtin_amdgcn_make_buffer_rsrc((void*)a.sync, 0, 131072, 0x00020000);
            st_sc1s(rsY, l * 16, widx * 1024, v4u{0u, 0u, 0u, 0u});
            __builtin_amdgcn_raw_buffer_store_b32(0u, rsS, (l & 3) * 256 + ((l >> 2) & 1) * 8192, cal_off, AUX_SC1);
            if (c < 2) __builtin_amdgcn_raw_buffer_store_b32(0u, rsS, (l >> 5) * 8192 + (l & 31) * 4, TM_OFF + c * 16384 + grp * 256, AUX_SC1);
        }
    asm volatile("" ::: "memory");
    if (wu >= 4) {
        __builtin_amdgcn_s_sleep(DLY_U);
        const int l = opaque_tid() & 63, i = l & 15, g = l >> 4;
        const int sidx = (i & 1) ? 0xEEEE : 0x4444;
        const int u = wu - 4;
        const int k0 = 256 * u;
        u16* stg = (u16*)(chsm + 40960 + u * 20480);
        v4u af[4];
        v32u wS[6];
        {
            v4u raw[3][16];
            {
                const size_t wo = (size_t)k0 * HD + hcol(hs) + 32 * jq;
                const rsrc_t rq = __builtin_amdgcn_make_buffer_rsrc((void*)(a.Wq + wo), 0, 256 * HD * 2, 0x00020000);
                const rsrc_t rk = __builtin_amdgcn_make_buffer_rsrc((void*)(a.Wk + wo), 0, 256 * HD * 2, 0x00020000);
                const rsrc_t rv = __builtin_amdgcn_make_buffer_rsrc((void*)(a.Wv + wo), 0, 256 * HD * 2, 0x00020000);
                const int vw = (l >> 2) * HD * 2 + (l & 3) * 16;
#pragma unroll
                for (int p = 0; p < 4; ++p) af[p] = *(const v4u*)(a.mem0 + (size_t)(i >> 1) * DM + k0 + 64 * p + 16 * g + 8 * (i & 1));
#pragma unroll
                for (int e = 0; e < 16; ++e) raw[0][e] = __builtin_amdgcn_raw_buffer_load_b128(rq, vw, e * 16 * HD * 2, 0);
#pragma unroll
                for (int e = 0; e < 16; ++e) raw[1][e] = __builtin_amdgcn_raw_buffer_load_b128(rk, vw, e * 16 * HD * 2, 0);
#pragma unroll
                for (int e = 0; e < 12; ++e) raw[2][e] = __builtin_amdgcn_raw_buffer_load_b128(rv, vw, e * 16 * HD * 2, 0);
                __builtin_amdgcn_sched_barrier(0);
#pragma unroll
                for (int e = 0; e < 16; ++e) *(v4u*)(stg + (16 * e + (l >> 2)) * 40 + 8 * (l & 3)) = raw[0][e];
                __builtin_amdgcn_sched_barrier(0);
#pragma unroll
                for (int e = 12; e < 16; ++e) raw[2][e] = __builtin_amdgcn_raw_buffer_load_b128(rv, vw, e * 16 * HD * 2, 0);
            }
            const int fr = 16 * (g >> 1) + 4 * (g & 1) + (i >> 2);
            __builtin_amdgcn_sched_barrier(0);
#pragma unroll
            for (int hx = 0; hx < 3; ++hx) {
                if (hx) {
#pragma unroll
                    for (int e = 0; e < 16; ++e) *(v4u*)(stg + (16 * e + (l >> 2)) * 40 + 8 * (l & 3)) = raw[hx][e];
                }
                __builtin_amdgcn_s_waitcnt(0xC07F);
#pragma unroll
                for (int jj = 0; jj < 2; ++jj)
#pragma unroll
                    for (int p = 0; p < 4; ++p) PUT8(wS[2 * hx + jj], 8 * p, tr_frag64(stg + (64 * p + fr) * 40 + 16 * jj + 4 * (i & 3), 40))
                __builtin_amdgcn_s_waitcnt(0xC07F);
                __builtin_amdgcn_sched_barrier(0);
            }
        }
        PST(u)
        if (u == 0) { PST(4) }
        const unsigned r1w = l0 + C2_R1 + u * R1S + (2 * g * 16 + i) * 4, r1v = l0 + C2_R1 + u * R1S + 2176 + (2 * g * 16 + ((i & 9) | ((i & 2) << 1) | ((i & 4) >> 1))) * 4;
#define LD4(d, va, so, o) "buffer_load_dwordx4 v[" d "], " va ", %[rs], " so " offen offset:" o " sc1\n\t"
#define CHK(a, b, c, d) "v_max3_u32 v244, v244, v" a ", v" b "\n\tv_max3_u32 v244, v244, v" c ", v" d "\n\t"
#define CHK0(a, b, c, d) "v_max_u32 v244, v" a ", v" b "\n\tv_max3_u32 v244, v244, v" c ", v" d "\n\t"
#define SMF(d, a, w) "v_smfmac_f32_16x16x64_bf16 v[" d "], v[" a "], v[" w "], %[sx]\n\t"
#define ADD(d, a, b) "v_add_f32 v" d ", v" a ", v" b "\n\t"
#define Z2(r) "v_mov_b64 v[" r "], 0\n\t"
#define DW2(ad, x, y, o0, o1) "ds_write2_b32 " ad ", v" x ", v" y " offset0:" o0 " offset1:" o1 "\n\t"
#define A0 "224:227"
#define A1 "228:231"
#define A2 "232:235"
#define A3 "236:239"
#define Q0 "200:203"
#define Q1 "204:207"
#define Q2 "208:211"
#define Q3 "212:215"
#define V0 "216:219"
#define V1 "220:223"
#define S1_HEAD \
            Z2("216:217") Z2("218:219") Z2("220:221") Z2("222:223") \
            "s_waitcnt vmcnt(4)\n\t" ZQ \
            "s_waitcnt vmcnt(3)\n\t" SMF(V0, A0, "136:143") CHK0("224", "225", "226", "227") SMF(V1, A0, "168:175") \
            "s_waitcnt vmcnt(2)\n\t" SMF(V0, A1, "144:151") CHK("228", "229", "230", "231") SMF(V1, A1, "176:183") \
            "s_waitcnt vmcnt(1)\n\t" SMF(V0, A2, "152:159") CHK("232", "233", "234", "235") SMF(V1, A2, "184:191") \
            "s_waitcnt vmcnt(0)\n\t" SMF(V0, A3, "160:167") CHK("236", "237", "238", "239") SMF(V1, A3, "192:199") \
            "v_cmp_eq_u32 vcc, " STR(SENT) ", v244\n\t"
#define S1_TAIL(B1, B2) \
            SMF(Q0, A0, "8:15") SMF(Q0, A1, "16:23") SMF(Q0, A2, "24:31") \
            ADD("240", "216", "217") ADD("241", "218", "219") SMF(Q0, A3, "32:39") DW2("%[rv]", "240", "241", "0", "16") \
            ADD("242", "220", "221") ADD("243", "222", "223") SMF(Q1, A0, "40:47") DW2("%[rv]", "242", "243", "128", "144") \
            SMF(Q1, A1, "48:55") SMF(Q1, A2, "56:63") SMF(Q1, A3, "64:71") \
            ADD("240", "200", "201") ADD("241", "202", "203") SMF(Q2, A0, "72:79") DW2("%[rw]", "240", "241", "0", "16") \
            SMF(Q2, A1, "80:87") SMF(Q2, A2, "88:95") "s_nop 1\n\t" \
            ADD("242", "204", "205") ADD("243", "206", "207") SMF(Q2, A3, "96:103") DW2("%[rw]", "242", "243", "136", "152") \
            SMF(Q3, A0, "104:111") SMF(Q3, A1, "112:119") \
            B1        \
            LTA(0) \
            SMF(Q3, A2, "120:127") SMF(Q3, A3, "128:135") "s_nop 0\n\t" \
            ADD("240", "208", "209") ADD("241", "210", "211") DW2("v246", "240", "241", "0", "16") \
            "s_nop 3\n\t" \
            ADD("242", "212", "213") ADD("243", "214", "215") DW2("v246", "242", "243", "136", "152") \
            "s_waitcnt lgkmcnt(0)\n\t" B2 "s_barrier\n\t"
#define ZQ Z2("200:201") Z2("202:203") Z2("204:205") Z2("206:207") Z2("208:209") Z2("210:211") Z2("212:213") Z2("214:215")
#define U_W "{v[8:39]}"(wS[0]), "{v[40:71]}"(wS[1]), "{v[72:103]}"(wS[2]), "{v[104:135]}"(wS[3]), "{v[136:167]}"(wS[4]), "{v[168:199]}"(wS[5])
#define U_CLOB CLOB16(200, 201, 202, 203, 204, 205, 206, 207, 208, 209, 210, 211, 212, 213, 214, 215), CLOB16(216, 217, 218, 219, 220, 221, 222, 223, 224, 225, 226, 227, 228, 229, 230, 231), \
            CLOB16(232, 233, 234, 235, 236, 237, 238, 239, 240, 241, 242, 243, 244, 245, 246, 247)
        __builtin_amdgcn_s_setprio(2);
        {
            asm volatile(
                "v_add_u32 v246, 1088, %[rw]\n\t"
                S1_HEAD "s_nop 0\n\t" S1_TAIL("s_nop 1\n\t", "s_barrier\n\t")
                : LTOUT0
                : U_W, "{v[224:227]}"(af[0]), "{v[228:231]}"(af[1]), "{v[232:235]}"(af[2]), "{v[236:239]}"(af[3]), [sx] "v"(sidx), [rv] "v"(r1v), [rw] "v"(r1w)
                : "memory", "vcc", CLOB16(200, 201, 202, 203, 204, 205, 206, 207, 208, 209, 210, 211, 212, 213, 214, 215), CLOB16(216, 217, 218, 219, 220, 221, 222, 223, 240, 241, 242, 243, 244, 245, 246, 247));
        }
        const unsigned tau = __builtin_amdgcn_readfirstlane(*(volatile unsigned*)(chsm + C2_MB + 8));
        const unsigned long long wmask = u == 3 ? (c == 0 ? ~0ull : 0ull) : u == 1 ? 0x0020202020202000ull : u == 2 ? 1ull << (NSEG - 3) : 0ull;
        const int pvv = u == 3 ? 2 : -1, pvm = u == 3 ? -1 : 0;
        const unsigned* pfl = a.sync + (u == 3 ? SY_PROG + grp : u == 1 ? SY_KV + 16 + grp : SY_Q + 7 * 8 + grp) * 32;
        int s_first = 0;
        asm volatile("s_branch 1f\n\t.p2align 8\n1:" : "+s"(s_first));
        for (int s = s_first; s < NSEG - 1; ++s) {
            __builtin_amdgcn_s_setprio(0);
            LT(1)
            const int pv = (wmask >> s) & 1ull ? pvv + (s & pvm) : 0;
            const unsigned* pa = pfl;
            if (pv < 0) pfl += 256;
            const int cb = grp & 3;
            int soffc = rec_base(s) + C_OFF + c * 16384 + near_half(s, tau) + cb * 2048;
            int o_as = rec_base(s + 1) + ((c >> 3) + (grp >> 2) * 4) * 16384 + near_half(s + 1, tau) + ((4 * c + cb) & 31) * 256;
            int soff = chunk_off(s + 1, grp, u >> 1, tau) + (u & 1) * 4096;
            const int o_tm = (s >> 1) * 2097152 + TM_OFF + (s & 1) * 16384;
            int lim = dead ? 0 : SPIN_LIMIT, bad = 0;
            const bool rcd = u > 0 || widx < 32;
            const int pc = u > 0 ? 3 * widx + u - 1 : rcd ? 384 + widx : s * 96 + widx - 32;
            const int o_arm = (rcd ? s + 2 < NSEG : pc < 512) ? (rcd ? rec_base(s + 2) : XB_OFF) + (pc >> 3) * 16384 + near_half(rcd ? s + 2 : 0, tau) + (pc & 7) * 1024 : OOB;
            bool there = true;
            if (__builtin_expect(s <= KVS, 0)) {
                there = __all(ld_flag(kv_flag(a.sync, s + 2, grp, l)) != 0u);
                *(volatile unsigned*)(chsm + C2_MB + 32 + (s & 1) * 16 + u * 4) = there ? 1u : 0u;
                __builtin_amdgcn_s_waitcnt(0xC07F);
            }
            const char* vb = !there ? nullptr : arena(a.slots) + (s + 2 >= NSEG ? KX_OFF : VX_OFF) + (blk_off(min(s + 2, NSEG - 1), 4 * grp + 2 * u - 2, tau) ^ (s + 2 >= NSEG ? 8192 : 0));
            asm volatile("" : "+s"(soffc), "+s"(o_as), "+s"(soff), "+s"(lim), "+s"(vb));
#define GL(o) "global_load_lds_dwordx4 %[l16], s[84:85] offset:" o " sc1\n\t"
#define GL4 GL("0") GL("1024") GL("2048") GL("3072")
#define GLN(d) "s_add_u32 m0, m0, 4096\n\ts_add_u32 s84, s84, " d "\n\ts_addc_u32 s85, s85, 0\n\t"
#define PKE PAUSE(PKA)
#define C_TRY(x, x0, x1, x2, x3, y, y0, y1, y2, y3) \
                "s_waitcnt vmcnt(3)\n\t" \
                "v_mfma_f32_16x16x32_bf16 v[240:243], v[216:219], v[" x "], 0\n\t" CHK0(x0, x1, x2, x3) \
                "s_waitcnt vmcnt(2)\n\t" \
                "v_mfma_f32_16x16x32_bf16 v[240:243], v[216:219], v[" y "], v[240:243]\n\t" CHK(y0, y1, y2, y3) \
                "v_cmp_eq_u32 vcc, " STR(SENT) ", v244\n\t" \
                "s_cbranch_vccz 3b\n\t" CN(1) \
                LD4(x, "%[l16]", "%[sc]", "0") LD4(y, "%[l16]", "%[sc]", "1024")
            asm volatile(
                "v_and_b32 v221, 16, %[l16]\n\t"
                "v_mov_b32 v222, 0x3f80\n\t"
                "v_bfe_u32 v223, %[l16], 5, 2\n\t"
                "v_lshlrev_b32 v221, v221, v222\n\t"
                "v_cmp_eq_u32 vcc, 0, v223\n\tv_and_b32 v220, 0xf0, %[l16]\n\tv_bfe_u32 v222, %[l16], 8, 1\n\tv_cndmask_b32 v216, 0, v221, vcc\n\t"
                "v_cmp_eq_u32 vcc, 1, v223\n\tv_lshl_add_u32 v220, v222, 3, v220\n\tv_lshrrev_b32 v222, 9, %[l16]\n\tv_cndmask_b32 v217, 0, v221, vcc\n\t"
                "v_cmp_eq_u32 vcc, 2, v223\n\tv_lshl_add_u32 v220, v222, 30, v220\n\ts_nop 0\n\tv_cndmask_b32 v218, 0, v221, vcc\n\t"
                "v_cmp_eq_u32 vcc, 3, v223\n\ts_nop 1\n\tv_cndmask_b32 v219, 0, v221, vcc\n\t"
                "v_mul_u32_u24 v222, 0x8000, v222\n\t"
                "v_and_b32 v221, 0xff, v220\n\t"
                "v_add_u32 v221, v221, v222\n\t"
                "v_lshrrev_b32 v247, 2, %[l16]\n\t"
                PKE
                "v_mov_b32 v240, " STR(SENT) "\n\tv_mov_b32 v241, v240\n\tv_mov_b32 v242, v240\n\tv_mov_b32 v243, v240\n\t"
                "buffer_store_dwordx4 v[240:243], %[l16], %[rs], %[arm] offen sc1\n\t"
                LTA(2)
                "s_setprio 2\n\t"
                "s_barrier\n\t"
                LTA(3)
                "v_add_u32 v246, 1088, %[rw]\n\t"
                "s_cmp_lt_u32 %[uu], 1\n\t"
                "s_cbranch_scc1 7f\n\t"
                "s_cmp_eq_u32 %[uu], 3\n\t"
                "s_cbranch_scc1 20f\n\t"
                "s_memrealtime s[84:85]\n\t"
                "v_lshrrev_b32 v241, 9, %[l16]\n\t"
                "v_and_b32 v242, 0x7c, v247\n\t"
                "v_lshl_add_u32 v241, v241, 13, v242\n\t"
                "s_waitcnt lgkmcnt(0)\n\t"
                "v_mov_b32 v240, s84\n\t"
                "buffer_store_dword v240, v241, %[rs], %[ts] offen sc1\n\t"
                "s_cmp_eq_u64 %[vb], 0\n\t"
                "s_cbranch_scc1 11f\n\t"
                "s_mov_b32 s86, m0\n\t"
                "s_mov_b64 s[84:85], %[vb]\n\t"
                "s_mov_b32 m0, %[lv]\n\t"
                PAUSE(PG0)
                "s_cmp_eq_u32 %[uu], 1\n\t"
                "s_cbranch_scc1 12f\n\t"
                PAUSE(PG1)
                "12:\n\t"
                GL4 GLN("4096") GL4 GLN("12288") GL4 GLN("4096") GL4
                "s_mov_b32 m0, s86\n\t"
                "11:\n\t"
                "s_cmp_lt_i32 %[pv], 0\n\t"
                "s_cbranch_scc0 8f\n\t"
                "v_and_b32 v241, 12, v247\n\t"
                "10:\n\t"
                "global_load_dword v240, v241, %[pa] sc1\n\t"
                "s_waitcnt vmcnt(0)\n\t"
                "v_cmp_gt_u32 vcc, 16, v240\n\t"
                "s_cbranch_vccz 8f\n\t"
                "s_sleep 6\n\t"
                "s_sub_u32 %[lim], %[lim], 1\n\t"
                "s_cbranch_scc0 10b\n\t"
                "s_mov_b32 %[bad], 3000\n\t"
                "s_mov_b32 %[lim], 0\n\t"
                "s_branch 8f\n\t"
                "20:\n\t"
                PAUSE(PS)
                "s_memrealtime s[84:85]\n\t"
                "s_load_dword s86, %[tb], 0\n\ts_load_dword s87, %[tb], 0x100\n\ts_load_dword s88, %[tb], 0x200\n\ts_load_dword s89, %[tb], 0x300\n\t"
                "s_load_dword s90, %[tb], 0x400\n\ts_load_dword s91, %[tb], 0x500\n\ts_load_dword s92, %[tb], 0x600\n\ts_load_dword s93, %[tb], 0x700\n\t"
                "s_waitcnt lgkmcnt(0)\n\t"
                "s_sub_u32 s86, s84, s86\n\ts_sub_u32 s87, s84, s87\n\ts_sub_u32 s88, s84, s88\n\ts_sub_u32 s89, s84, s89\n\t"
                "s_sub_u32 s90, s84, s90\n\ts_sub_u32 s91, s84, s91\n\ts_sub_u32 s92, s84, s92\n\ts_sub_u32 s93, s84, s93\n\t"
                "s_or_b32 s85, s86, s87\n\ts_min_u32 s86, s86, s87\n\ts_or_b32 s85, s85, s88\n\ts_min_u32 s86, s86, s88\n\ts_or_b32 s85, s85, s89\n\ts_min_u32 s86, s86, s89\n\t"
                "s_memrealtime s[88:89]\n\t"
                "s_or_b32 s85, s85, s90\n\ts_min_u32 s86, s86, s90\n\ts_or_b32 s85, s85, s91\n\ts_min_u32 s86, s86, s91\n\ts_or_b32 s85, s85, s92\n\ts_min_u32 s86, s86, s92\n\t"
                "s_or_b32 s85, s85, s93\n\ts_min_u32 s86, s86, s93\n\t"
                "s_cmp_gt_u32 s85, 127\n\t"
                CNS
                "s_cselect_b32 s85, " STR(TE0) ", 127\n\t"
                "s_min_u32 s86, s86, s85\n\t"
                "s_sub_u32 s84, s84, s86\n\t"
                "s_add_u32 s84, s84, %[td]\n\t"
                "s_cmp_lt_i32 %[pv], 1\n\t"
                "s_cbranch_scc1 4f\n\t"
                "v_mov_b32 v247, %[pv]\n\t"
                "s_mov_b64 exec, 1\n\t"
                "global_store_dword %[l16], v247, %[pa] sc1\n\t"
                "s_mov_b64 exec, -1\n\t"
                "4:\n\t"
                "s_waitcnt lgkmcnt(0)\n\t"
                "s_sub_i32 s84, s84, s88\n\t"
                "s_max_i32 s84, s84, 0\n\t"
                "s_min_i32 s84, s84, 40\n\t"
                "21:\n\t"
                "s_sub_u32 s84, s84, 1\n\t"
                "s_cbranch_scc0 21b\n\t"
                "s_branch 8f\n\t"
                "7:\n\t"
                PAUSE(PC)
                LD4(A0, "%[l16]", "%[sc]", "0") LD4(A1, "%[l16]", "%[sc]", "1024")
                PAUSE(PP)
                LD4(A2, "%[l16]", "%[sc]", "0") LD4(A3, "%[l16]", "%[sc]", "1024")
                "s_waitcnt vmcnt(3)\n\t"
                "v_mfma_f32_16x16x32_bf16 v[240:243], v[216:219], v[224:227], 0\n\t" CHK0("224", "225", "226", "227")
                "s_waitcnt vmcnt(2)\n\t"
                "v_mfma_f32_16x16x32_bf16 v[240:243], v[216:219], v[228:231], v[240:243]\n\t" CHK("228", "229", "230", "231")
                "v_cmp_eq_u32 vcc, " STR(SENT) ", v244\n\t"
                "s_cbranch_vccnz 30f\n\t"
                "3:\n\t"
                "s_nop 3\n\t"
                "v_cvt_pk_bf16_f32 v244, v240, v241\n\t"
                "v_cvt_pk_bf16_f32 v245, v242, v243\n\t"
                "buffer_store_dwordx2 v[244:245], v221, %[rs], %[oas] offen sc1\n\t"
                PAUSE(PB)
                "8:\n\t"
                LTA(4)
                "s_barrier\n\t"
                PAUSE(1)
                "2:\n\t"
                LD4(A0, "%[l16]", "%[sa]", "0") LD4(A1, "%[l16]", "%[sa]", "1024") LD4(A2, "%[l16]", "%[sa]", "2048") LD4(A3, "%[l16]", "%[sa]", "3072")
                S1_HEAD
                "s_cbranch_vccnz 6f\n\t"
                "5:\n\t"
                LTA(5)
                S1_TAIL("s_waitcnt lgkmcnt(0)\n\ts_barrier\n\t", "")
                "s_setprio 0\n\t"
                "s_branch 9f\n\t"
                "30:\n\t" CN(1)
                LD4(A0, "%[l16]", "%[sc]", "0") LD4(A1, "%[l16]", "%[sc]", "1024")
                "1:\n\t"
                C_TRY(A2, "232", "233", "234", "235", A3, "236", "237", "238", "239")
                C_TRY(A0, "224", "225", "226", "227", A1, "228", "229", "230", "231")
                "s_sub_u32 %[lim], %[lim], 1\n\t"
                "s_cbranch_scc0 1b\n\t"
                "s_mov_b32 %[bad], 2000\n\t"
                "s_mov_b32 %[lim], 0\n\t"
                "s_branch 3b\n\t"
                "6:\n\t" CN(65536)
                "v_mov_b32 v240, %[tg]\n\t"
                "v_mov_b32 v241, %[lf]\n\t"
                "ds_write_b32 v241, v240\n\t"
                "s_nop 0\n\t"
                "s_sub_u32 %[lim], %[lim], 1\n\t"
                "s_cbranch_scc0 2b\n\t"
                "s_mov_b32 %[bad], 1\n\t"
                "s_branch 5b\n\t"
                "9:\n\t"
                : [lim] "+s"(lim), [bad] "+s"(bad), [oas] "+s"(o_as) LTOUT CNOUT
                : U_W, [l16] "v"(l * 16), [rs] "s"(rsS), [sx] "v"(sidx), [rv] "v"(r1v), [rw] "v"(r1w),
                  [sc] "s"(soffc), [sa] "s"(soff), [uu] "s"(u), [pv] "s"(pv), [pa] "s"(pa), [vb] "s"(vb), [lv] "s"(l0 + (s + 2 >= NSEG ? C2_V + 32768 : s & 1 ? C2_K : C2_V) + (u - 1) * 16384), [td] "s"(TD), [ts] "s"(c == 0 && (u == 2 || s < NSEG - 3) ? o_tm + grp * 256 + (2 - u) * 2097152 : OOB), [tb] "s"(arena(a.slots) + o_tm + near_half(s, tau)),

                  [arm] "s"(o_arm),
                  [tg] "s"(s + 1), [lf] "s"(l0 + C2_MB + 16)
                : "memory", "vcc", "scc", "s84", "s85", "s86", "s87", "s88", "s89", "s90", "s91", "s92", "s93", U_CLOB);
            if (__builtin_expect(bad != 0, 0)) TMO_SET((unsigned)bad)
        }
        last_rows(chsm, rsS, wu, grp, c, tau);
        if (u == 3 && widx == 0 && l == 0) __hip_atomic_store((gu64*)(a.boot + 144), *(volatile unsigned long long*)(chsm + C2_MB), __ATOMIC_RELAXED, __HIP_MEMORY_SCOPE_AGENT);
        return;
    }
    const int l = opaque_tid() & 63, i = l & 15, g = l >> 4;
    const int sidx = (i & 1) ? 0xEEEE : 0x4444;
    const int ha = 4 * grp + wu;
    unsigned long long tag;
    v32u wO[2];
    {
        u16* stg = (u16*)(chsm + 122880 + wu * 10240);
        tag = boot_tag(a.boot);
        asm volatile("" ::: "memory");
        v4u rawo[16];
        const u16* src = a.Wo + (size_t)(hcol(ha) + (l >> 3)) * DM + 64 * c + 8 * (l & 7);
#pragma unroll
        for (int e = 0; e < 16; ++e) rawo[e] = *(const v4u*)(src + (size_t)8 * e * DM);
        __builtin_amdgcn_sched_barrier(0);
        if (wu == 0) {
            asm volatile("s_waitcnt vmcnt(16)" ::: "memory");
            unsigned tm[2] = {~0u, ~0u};
            const char* pb = arena(a.slots) + cal_off;
#pragma unroll
            for (int k = 0; k < 2 * NPROBE; ++k) {
                unsigned long long c0, c1; unsigned d;
                asm volatile("s_memtime %0\n\ts_waitcnt lgkmcnt(0)\n\ts_load_dword %2, %3, %4\n\ts_waitcnt lgkmcnt(0)\n\ts_memtime %1\n\ts_waitcnt lgkmcnt(0)"
                             : "=&s"(c0), "=&s"(c1), "=&s"(d) : "s"(pb), "i"((k >> 1) * 256 + (k & 1) * 8192) : "memory");
                tm[k & 1] = min(tm[k & 1], (unsigned)(c1 - c0));
            }
            const unsigned side = (__builtin_amdgcn_s_getreg(20 | (3 << 11)) >> 2) & 1u;
            if (l == 0) __hip_atomic_store((gu64*)(a.boot + widx), (tag << 2) | (((tm[0] > tm[1] ? 1u : 0u) ^ side) << 1) | side, __ATOMIC_RELAXED, __HIP_MEMORY_SCOPE_AGENT);
            PST(7)
        }
#pragma unroll
        for (int m = 0; m < 2; ++m) {
#pragma unroll
            for (int e = 0; e < 8; ++e) *(v4u*)(stg + (8 * e + (l >> 3)) * 72 + 8 * (l & 7)) = rawo[8 * m + e];
            __builtin_amdgcn_s_waitcnt(0xC07F);
#pragma unroll
            for (int ct = 0; ct < 4; ++ct) {
                const u16* p = stg + (8 * g + (i >> 2)) * 72 + 16 * ct + 4 * (i & 3);
                const v2u r0 = tr4(p), r1 = tr4(p + 4 * 72), r2 = tr4(p + 32 * 72), r3 = tr4(p + 36 * 72);
                PUT4(wO[ct >> 1], (ct & 1) * 16 + m * 8, (v4u{r0[0], r0[1], r1[0], r1[1]})) PUT4(wO[ct >> 1], (ct & 1) * 16 + m * 8 + 4, (v4u{r2[0], r2[1], r3[0], r3[1]}))
            }
            __builtin_amdgcn_s_waitcnt(0xC07F);
        }
        if (wu == 0) { PST(5) }
        if (widx == 0 && wu == 0) *(volatile unsigned long long*)(chsm + C2_MB) = tag;
    }
    if (wu >= 2) touch_sc1(arena(a.slots) + (size_t)min(l + (wu - 2) * 64, FX_OFF / 2097152 - 1) * 2097152 + 2097024 - 128, l0 + C2_MB + 256);
    unsigned long long fw = 0;
#pragma nounroll
    for (int spin = 0; spin < 100000; ++spin) {
        asm volatile("" ::: "memory");
        fw = __hip_atomic_load((gu64*)(a.boot + 256 + 16 * grp + (l & 7)), __ATOMIC_RELAXED, __HIP_MEMORY_SCOPE_AGENT);
        if (__all((fw | 255u) == ((tag << 8) | 255u))) break;
        __builtin_amdgcn_s_sleep(4);
    }
    asm volatile("" ::: "memory");
    const unsigned tau = __builtin_amdgcn_readfirstlane((unsigned)fw & 255u);
    if (wu == 3) { *(volatile unsigned*)(chsm + C2_MB + 8) = tau; *(volatile unsigned*)(chsm + C2_MB + 16) = ~0u; __builtin_amdgcn_s_waitcnt(0xC07F); }
    if (wu == 1) { PST(9) }

    const unsigned r3a = l0 + C2_R3 + wu * 2048 + g * 128 + i * 4;
    v32u yb;
    v8u kx;
    v8u sel; v2u sel3;
#pragma unroll
    for (int d = 0; d < 8; ++d) sel[d] = (i >> 1) == d ? (0x3C00u << ((i & 1) * 16)) : 0u;
#pragma unroll
    for (int d = 0; d < 2; ++d) sel3[d] = (i & 3) == 0 && (i >> 3) == d ? (0x3C00u << (((i >> 2) & 1) * 16)) : 0u;
    asm volatile("s_setreg_imm32_b32 hwreg(HW_REG_MODE, 23, 1), 1\n\ts_nop 3");
#define MF(d, a, b, c) "v_mfma_f32_16x16x32_bf16 v[" d "], v[" a "], v[" b "], " c "\n\t"
#define PX4(d0, d1, d2, d3, a, w0, w1, w2, w3) MF(d0, a, w0, "v[" d0 "]") MF(d1, a, w1, "v[" d1 "]") MF(d2, a, w2, "v[" d2 "]") MF(d3, a, w3, "v[" d3 "]")
#define PXT(d0, d1, d2, d3, a0, a1, a2, a3) MF(d0, a0, "32:35", "0") MF(d1, a0, "48:51", "0") MF(d2, a0, "64:67", "0") MF(d3, a0, "80:83", "0") \
        PX4(d0, d1, d2, d3, a1, "36:39", "52:55", "68:71", "84:87") PX4(d0, d1, d2, d3, a2, "40:43", "56:59", "72:75", "88:91") PX4(d0, d1, d2, d3, a3, "44:47", "60:63", "76:79", "92:95")
#define CVP(d, a, b) "v_cvt_pk_bf16_f32 v" d ", v" a ", v" b "\n\t"
#define PROJ_X(cnt) \
        "s_waitcnt " cnt "(8)\n\t" PXT("192:195", "196:199", "200:203", "204:207", "160:163", "164:167", "168:171", "172:175") \
        "s_waitcnt " cnt "(4)\n\t" PXT("208:211", "212:215", "216:219", "220:223", "176:179", "180:183", "184:187", "188:191") \
        CVP("128", "192", "193") CVP("129", "194", "195") CVP("136", "196", "197") CVP("137", "198", "199") CVP("144", "200", "201") CVP("145", "202", "203") CVP("152", "204", "205") CVP("153", "206", "207") \
        CVP("132", "208", "209") CVP("133", "210", "211") CVP("140", "212", "213") CVP("141", "214", "215") CVP("148", "216", "217") CVP("149", "218", "219") CVP("156", "220", "221") CVP("157", "222", "223") \
        "s_waitcnt " cnt "(0)\n\t"
#define CLOB_PX CLOB16(160, 161, 162, 163, 164, 165, 166, 167, 168, 169, 170, 171, 172, 173, 174, 175), CLOB16(176, 177, 178, 179, 180, 181, 182, 183, 184, 185, 186, 187, 188, 189, 190, 191), \
        CLOB16(192, 193, 194, 195, 196, 197, 198, 199, 200, 201, 202, 203, 204, 205, 206, 207), CLOB16(208, 209, 210, 211, 212, 213, 214, 215, 216, 217, 218, 219, 220, 221, 222, 223)
#define LDX(d, va, so, o) "buffer_load_dwordx4 v[" d "], " va ", %[rs], " so " offen offset:" o " sc1\n\t"
#define DSV(d, o) "ds_read_b128 v[" d "], %[lv] offset:" o "\n\t"
    const int voffX = ((i >> 1) & 1) * 4096 + g * 256 + (2 * (i >> 2) + (i & 1)) * 16;
    const int pk = (((i >> 1) & 3) << 1) | (i >> 3);
    const unsigned hv = wu >= 2 ? l0 + C2_R1 + 2176 + ((wu & 1) * 128 + ((l >> 4) + 4 * ((l >> 3) & 1)) * 16 + 8 * ((l >> 1) & 1) + 4 * ((l >> 2) & 1) + 2 * (l & 1)) * 4
                                : l0 + C2_R1 + (i & 1) * 544 + (i >> 1) * 64 + g * 16;
    const unsigned hk = l0 + C2_R1 + 1088 + (i & 1) * 544 + pk * 64 + g * 16;
    const int vo1 = wu >= 2 ? l * 4 : wu == 1 ? (g >> 1) * 1024 + i * 16 + (g & 1) * 8 : (i & 1) ? 1 << 30 : (i >> 1) * 16 + 4 * g;
    const int vo2 = (2 * (i & 1) + (g >> 1)) * 128 + pk * 16 + (g & 1) * 8;
    const int voffK = (g >> 1) * 256 + i * 16 + (g & 1) * 8;
#define RD4(w, ad, a, b, c, d) "ds_read_" w " v[" a "], " ad "\n\tds_read_" w " v[" b "], " ad " offset:3200\n\tds_read_" w " v[" c "], " ad " offset:6400\n\tds_read_" w " v[" d "], " ad " offset:9600\n\t"
#define AD4(d0, d1, d2, d3, a0, a1, a2, a3) ADD(d0, d0, a0) ADD(d1, d1, a1) ADD(d2, d2, a2) ADD(d3, d3, a3)
#define QSC(r) "v_mul_f32 v" r ", 0x3e0293ee, v" r "\n\t"
#define FLD "buffer_load_dword v114, %[vf], %[rs], %[fl] offen sc1\n\t"
#define DUM "buffer_store_dword v168, off, %[rs], %[ob] sc1\n\t"
#define QHEAD "s_barrier\n\t" RD4("b128", "%[hv]", "160:163", "164:167", "168:171", "172:175") FLD \
        "s_waitcnt lgkmcnt(2)\n\t" AD4("160", "161", "162", "163", "164", "165", "166", "167") \
        "s_waitcnt lgkmcnt(0)\n\t" AD4("168", "169", "170", "171", "172", "173", "174", "175") \
        "s_barrier\n\t"
#define QTAIL AD4("160", "161", "162", "163", "168", "169", "170", "171") QSC("160") QSC("161") QSC("162") QSC("163")
#define CVH(d, a, b) "v_cvt_pk_f16_f32 v" d ", v" a ", v" b "\n\t"
#define SWA(d, a, b) "v_add_f32_dpp v" d ", v" a ", v" b " quad_perm:[1,0,3,2] row_mask:0xf bank_mask:0xf\n\t"
#define KLD(so) "buffer_load_dwordx2 v[96:97], %[vk], %[rs], " so " offen sc1\n\tbuffer_load_dwordx2 v[102:103], %[vk], %[rs], " so " offen offset:512 sc1\n\t" \
        "s_add_u32 " so ", " so ", 4096\n\t" \
        "buffer_load_dwordx2 v[100:101], %[vk], %[rs], " so " offen sc1\n\tbuffer_load_dwordx2 v[98:99], %[vk], %[rs], " so " offen offset:512 sc1\n\t"
    auto hand_over = [&](const int o_1, const int o_2, const int o_f, const int tg) {
        int dmy = 0;
        asm volatile(
            "s_cmp_eq_u32 %[wu], 0\n\t"
            "s_cbranch_scc1 2f\n\t"
            "s_cmp_eq_u32 %[wu], 1\n\t"
            "s_cbranch_scc1 1f\n\t"
            "s_barrier\n\t"
            RD4("b64", "%[hv]", "160:161", "162:163", "164:165", "166:167") FLD
            "s_waitcnt lgkmcnt(2)\n\t" ADD("160", "160", "162") ADD("161", "161", "163")
            "s_waitcnt lgkmcnt(0)\n\t" ADD("164", "164", "166") ADD("165", "165", "167") ADD("160", "160", "164") ADD("161", "161", "165")
            CVP("168", "160", "161")
            "buffer_store_dword v168, %[v1], %[rs], %[o1] offen sc1\n\t"
            "s_barrier\n\t"
            DUM PAUSE(PV2)
            "s_branch 3f\n\t"
            "5:\n\t"
            "v_mov_b32 v115, %[lf]\n\t"
            "ds_read_b32 v115, v115\n\t"
            "s_waitcnt lgkmcnt(0)\n\t"
            "v_cmp_eq_u32 vcc, %[tg], v115\n\t"
            "s_cbranch_vccnz 4f\n\t" CN(256)
            PAUSE(PL)
            "s_branch 4f\n\t"
            "1:\n\t"
            Z2("176:177") Z2("178:179")
            "s_waitcnt vmcnt(0)\n\t"
            QHEAD QTAIL
            "s_mov_b64 exec, %[em]\n\t"
            CVP("176", "160", "161") CVP("177", "162", "163")
            "s_not_b64 exec, exec\n\t"
            CVP("178", "160", "161") CVP("179", "162", "163")
            "s_mov_b64 exec, -1\n\t"
            "s_nop 0\n\t"
            MF("180:183", "96:99", "176:179", "0") MF("184:187", "100:103", "176:179", "0")
            "s_nop 7\n\t"
            SWA("180", "184", "180") SWA("181", "185", "181") SWA("182", "186", "182") SWA("183", "187", "183")
            CVH("188", "180", "181") CVH("189", "182", "183")
            "buffer_store_dwordx2 v[188:189], %[v1], %[rs], %[o1] offen sc1\n\t"
            DUM PAUSE(PV1)
            "s_branch 3f\n\t"
            "2:\n\t"
            QHEAD
            RD4("b128", "%[hk]", "176:179", "180:183", "184:187", "188:191")
            QTAIL CVP("164", "160", "161") CVP("165", "162", "163")
            "s_waitcnt lgkmcnt(2)\n\t" AD4("176", "177", "178", "179", "180", "181", "182", "183")
            "s_waitcnt lgkmcnt(0)\n\t" AD4("184", "185", "186", "187", "188", "189", "190", "191") AD4("176", "177", "178", "179", "184", "185", "186", "187")
            CVP("166", "176", "177") CVP("167", "178", "179")
            "s_nop 1\n\t"
            "v_mfma_f32_16x16x16_bf16 v[168:171], v[166:167], v[164:165], 0\n\t"
            "buffer_store_dwordx2 v[166:167], %[v2], %[rs], %[o2] offen sc1\n\t"
            "s_nop 6\n\t"
            SWA("172", "169", "168") SWA("173", "171", "170")
            CVH("174", "172", "173")
            "buffer_store_dword v174, %[v1], %[rs], %[o1] offen sc1\n\t"
            "3:\n\t"
            PAUSE(PVA)
            "s_waitcnt vmcnt(2)\n\t"
            "v_cmp_ne_u32 vcc, " STR(SENT) ", v114\n\t"
            "s_cbranch_vccnz 5b\n\t"
            "4:\n\t"
            : [dm] "+s"(dmy) CNOUT : "{v[96:103]}"(kx), [fl] "s"(o_f), [vf] "v"(i * 128), [ob] "s"(1 << 30), [tg] "s"(tg), [lf] "s"(l0 + C2_MB + 16), [wu] "s"(wu), [hv] "v"(hv), [hk] "v"(hk), [v1] "v"(vo1), [v2] "v"(vo2), [rs] "s"(rsS), [o1] "s"(o_1), [o2] "s"(o_2), [em] "s"(0x5555555555555555ull)
            : "memory", "scc", "vcc", "v114", "v115", CLOB16(160, 161, 162, 163, 164, 165, 166, 167, 168, 169, 170, 171, 172, 173, 174, 175), CLOB16(176, 177, 178, 179, 180, 181, 182, 183, 184, 185, 186, 187, 188, 189, 190, 191));
    };
    auto o1_of = [&](const int s) { return wu >= 2 ? v_off(s, hs, tau) + jq * 512 + (wu & 1) * 256 : wu == 1 ? x_off(s, hs, tau) + jq * 256 : m_off(s, hs, tau) + jq * 128; };

    __builtin_amdgcn_s_setprio(1);
    int o_f = f_off(0, grp, tau);
    int o_cs = rec_base(0) + C_OFF + c * 16384 + near_half(0, tau) + wu * 2048 + grp * 256;
    int o_1 = o1_of(0), o_2 = km_off(0, hs, tau) + jq * 512, bq = x_off(0, ha, tau), bm = m_off(0, ha, tau), bb = v_off(0, ha, tau);
    {
            const int b0 = blk_off(0, ha, tau);
            int k0 = KX_OFF + blk_off(0, hs, tau) + jq * 1024;
            asm volatile(
                LDX("160:163", "%[vx]", "%[vb]", "0") LDX("164:167", "%[vx]", "%[vb]", "1024") LDX("168:171", "%[vx]", "%[vb]", "2048") LDX("172:175", "%[vx]", "%[vb]", "3072")
                LDX("176:179", "%[vx]", "%[vb]", "128") LDX("180:183", "%[vx]", "%[vb]", "1152") LDX("184:187", "%[vx]", "%[vb]", "2176") LDX("188:191", "%[vx]", "%[vb]", "3200")
                KLD("%[k0]")
                "v_mov_b32 v131, 0\n\tv_mov_b32 v135, 0\n\tv_mov_b32 v139, 0\n\tv_mov_b32 v143, 0\n\tv_mov_b32 v147, 0\n\tv_mov_b32 v151, 0\n\tv_mov_b32 v155, 0\n\tv_mov_b32 v159, 0\n\t"
                PROJ_X("vmcnt")
                : "=&{v[128:159]}"(yb), "=&{v[96:103]}"(kx), [k0] "+s"(k0)
                : "{v[32:63]}"(wO[0]), "{v[64:95]}"(wO[1]), [vx] "v"(voffX), [vk] "v"(voffK), [rs] "s"(rsS), [vb] "s"(VX_OFF + b0)
                : "memory", "scc", CLOB_PX);
        if (wu == 1) { PST(10) }
    }
    int s_first = 0;
    asm volatile("s_branch 1f\n\t.p2align 8\n1:" : "+s"(s_first));
    for (int s = s_first; s < NSEG - 1; ++s) {
        hand_over(o_1, o_2, o_f, s);
        if (s == 0 && wu == 1) { PST(8) }
        LT(0)
#define LDPA "buffer_load_dwordx2 v[160:161], v254, %[rs], %[bm] offen sc1\n\t" LDX("164:167", "%[l16]", "%[bq]", "0") LDX("168:171", "%[l16]", "%[bq]", "1024")
#define SM(d, a, b, x) "v_smfmac_f32_16x16x64_bf16 v[" d "], v[" a "], v[" b "], " x "\n\t"
#define MX2(a, b) "v_max_u32 v244, v" a ", v" b "\n\t"
#define MX3(a, b) "v_max3_u32 v244, v244, v" a ", v" b "\n\t"
#define ZAM Z2("208:209") Z2("210:211") Z2("212:213") Z2("214:215") Z2("216:217") Z2("218:219") Z2("220:221") Z2("222:223")
#define MUL(d, a, b) "v_mul_f32 v" d ", v" a ", v" b "\n\t"
#define CV0(d, a) "v_cvt_pk_bf16_f32 v" d ", v" a ", 0\n\t"
#define RDC "ds_read_b64 v[224:225], %[rc]\n\tds_read_b64 v[226:227], %[rc] offset:2048\n\tds_read_b64 v[228:229], %[rc] offset:4096\n\tds_read_b64 v[230:231], %[rc] offset:6144\n\t"
#define EXD(d, r) "v_exp_f32 v" d ", v" r "\n\t"
#define SUBM(r) "v_sub_f32 v" r ", v" r ", v250\n\t"
#define PCVM CVP("241", "180", "180") CVP("243", "180", "180")
#define PCVX CVP("242", "178", "179") CVP("240", "176", "177")
#define OUT(t0, t1, a0, a1, a2, a3, ad, o0, o1) ADD(t0, a0, a1) ADD(t1, a2, a3) MUL(t0, t0, "250") MUL(t1, t1, "251") DW2(ad, t0, t1, o0, o1)
#define TS(n)
#define TSOUT
        int lim = dead ? 0 : SPIN_LIMIT, bad = 0;
        asm volatile(
            LDX("192:195", "%[l16]", "%[bb]", "0") LDX("196:199", "%[l16]", "%[bb]", "1024")
            ZAM Z2("224:225") Z2("226:227")
            Z2("16:17") Z2("18:19") Z2("20:21") Z2("22:23") Z2("24:25") Z2("26:27") Z2("28:29") Z2("30:31")
            "v_mov_b32 v200, 0x3f803f80\n\tv_mov_b32 v201, 0x3f803f80\n\tv_mov_b32 v202, 0x3f80\n\tv_mov_b32 v203, 0\n\t"
            "v_mov_b64 v[204:205], v[200:201]\n\tv_mov_b64 v[206:207], v[202:203]\n\t"
            "v_xor_b32 v252, 0xa000, %[sx]\n\tv_add_u32 v253, 1024, %[ra]\n\tv_lshrrev_b32 v254, 1, %[l16]\n\t"
            PAUSE(PV)
            TS(8)
            LDPA
            "4:\n\t"
            "s_waitcnt vmcnt(4)\n\t"
            SM("208:211", "192:195", "32:39", "%[sx]") MX2("192", "193") SM("212:215", "192:195", "48:55", "%[sx]") MX3("194", "195")
            SM("216:219", "192:195", "64:71", "%[sx]") SM("220:223", "192:195", "80:87", "%[sx]")
            "s_waitcnt vmcnt(3)\n\t"
            SM("208:211", "196:199", "40:47", "%[sx]") MX3("196", "197") SM("212:215", "196:199", "56:63", "%[sx]") MX3("198", "199")
            SM("216:219", "196:199", "72:79", "%[sx]") "v_cmp_eq_u32 vcc, " STR(SENT) ", v244\n\t" SM("220:223", "196:199", "88:95", "%[sx]")
            "s_cbranch_vccnz 5f\n\t"
            "1:\n\t" TS(11)
            "s_waitcnt vmcnt(2)\n\t"
            "v_mfma_f32_16x16x16_f16 v[236:239], v[112:113], v[160:161], 0\n\t"
            MX2("160", "161") ADD("245", "208", "209") ADD("246", "210", "211") CV0("130", "245") CV0("134", "246")
            ADD("245", "212", "213") ADD("246", "214", "215") CV0("138", "245") CV0("142", "246")
            EXD("180", "236")
            "s_waitcnt vmcnt(1)\n\t"
            "v_mfma_f32_16x16x32_f16 v[228:231], v[104:107], v[164:167], 0\n\t"
            MX3("164", "165") MX3("166", "167") ADD("245", "216", "217") ADD("246", "218", "219") CV0("146", "245") CV0("150", "246")
            ADD("245", "220", "221") ADD("246", "222", "223") CV0("154", "245") CV0("158", "246") PCVM
            "s_waitcnt vmcnt(0)\n\t"
            "v_mfma_f32_16x16x32_f16 v[228:231], v[108:111], v[168:171], v[228:231]\n\t"
            MX3("168", "169") MX3("170", "171")
            "v_cmp_eq_u32 vcc, " STR(SENT) ", v244\n\t"
            "s_cbranch_vccnz 7f\n\t"
            "2:\n\t" TS(9)
            "s_nop 3\n\t"
            EXD("179", "231") EXD("178", "230") EXD("177", "229") EXD("176", "228")
            PCVX
            "v_max3_f32 v250, |v236|, |v231|, |v230|\n\t"
            "v_max3_f32 v250, v250, |v229|, |v228|\n\t"
            SM("224:227", "240:243", "200:207", "v252")
            "v_cmp_nge_f32 vcc, 0x41c00000, v250\n\t"
            "s_nop 0\n\t"
            SM("16:19", "240:243", "128:135", "v252")
            "s_cbranch_vccnz 10f\n\t"
            "13:\n\t"
            "s_nop 0\n\t"
            SM("20:23", "240:243", "136:143", "v252") "s_nop 1\n\t"
            ADD("250", "224", "225") ADD("251", "226", "227")
            SM("24:27", "240:243", "144:151", "v252")
            "v_rcp_f32 v250, v250\n\t"
            SM("28:31", "240:243", "152:159", "v252")
            "v_rcp_f32 v251, v251\n\t"
            OUT("245", "246", "16", "17", "18", "19", "%[ra]", "0", "16")
            OUT("247", "248", "20", "21", "22", "23", "%[ra]", "128", "144")
            OUT("245", "246", "24", "25", "26", "27", "v253", "0", "16")
            OUT("247", "248", "28", "29", "30", "31", "v253", "128", "144")
            TS(10)
            "s_waitcnt lgkmcnt(0)\n\t"
            "s_barrier\n\t"
            RDC
            "v_lshrrev_b32 v247, 2, %[l16]\n\t"
            "s_waitcnt lgkmcnt(2)\n\t"
            ADD("224", "224", "226") ADD("225", "225", "227")
            "s_waitcnt lgkmcnt(0)\n\t"
            ADD("228", "228", "230") ADD("229", "229", "231") ADD("224", "224", "228") ADD("225", "225", "229")
            "v_cvt_pk_bf16_f32 v245, v224, v225\n\t"
            "buffer_store_dword v245, v247, %[rs], %[cs] offen sc1\n\t"
            "s_branch 9f\n\t"
            "10:\n\t"
            "v_max3_f32 v250, v236, v231, v230\n\t"
            "v_max3_f32 v250, v250, v229, v228\n\t"
            "s_nop 1\n\t"
            "v_max_f32_dpp v251, v250, v250 quad_perm:[1,0,3,2] row_mask:0xf bank_mask:0xf\n\t"
            "v_max_f32_dpp v244, v250, v250 quad_perm:[1,0,3,2] row_mask:0xf bank_mask:0xf\n\t"
            "s_nop 1\n\t"
            "v_permlane16_swap_b32 v251, v244\n\t"
            "v_max_f32 v250, v251, v244\n\t"
            "v_max_f32 v251, v251, v244\n\t"
            Z2("224:225") Z2("226:227")
            "v_permlane32_swap_b32 v250, v251\n\t"
            "v_max_f32 v250, v250, v251\n\t"
            Z2("16:17") Z2("18:19")
            SUBM("228") SUBM("229") SUBM("230") SUBM("231") SUBM("236")
            EXD("180", "236") EXD("179", "231") EXD("178", "230") EXD("177", "229") EXD("176", "228")
            PCVM PCVX
            "s_nop 1\n\t"
            SM("224:227", "240:243", "200:207", "v252") "s_nop 1\n\t"
            SM("16:19", "240:243", "128:135", "v252")
            "s_branch 13b\n\t"
            "5:\n\t" CN(1)
            LDX("192:195", "%[l16]", "%[bb]", "0") LDX("196:199", "%[l16]", "%[bb]", "1024")
            LDPA
            "s_sub_u32 %[lim], %[lim], 1\n\t"
            "s_nop 1\n\t"
            ZAM
            "s_cbranch_scc0 4b\n\t"
            "s_mov_b32 %[bad], 1500\n\t"
            "s_mov_b32 %[lim], 0\n\t"
            "s_branch 1b\n\t"
            "7:\n\t" CN(65536)
            LDPA
            "s_sub_u32 %[lim], %[lim], 1\n\t"
            "s_cbranch_scc0 1b\n\t"
            "s_mov_b32 %[bad], 1000\n\t"
            "s_branch 2b\n\t"
            "9:\n\t"
            : [lim] "+s"(lim), [bad] "+s"(bad), "+{v[128:159]}"(yb) TSOUT CNOUT
            : "{v[32:63]}"(wO[0]), "{v[64:95]}"(wO[1]), "{v[104:111]}"(sel), "{v[112:113]}"(sel3), [sx] "v"(sidx), [l16] "v"(l * 16),
              [ra] "v"(r3a), [rs] "s"(rsS), [bb] "s"(bb), [bq] "s"(bq), [bm] "s"(bm), [cs] "s"(o_cs), [rc] "v"(l0 + C2_R3 + wu * 512 + l * 8)
            : "memory", "vcc", "scc", CLOB_PX, CLOB16(16, 17, 18, 19, 20, 21, 22, 23, 24, 25, 26, 27, 28, 29, 30, 31),
              CLOB16(224, 225, 226, 227, 228, 229, 230, 231, 232, 233, 234, 235, 236, 237, 238, 239), CLOB16(240, 241, 242, 243, 244, 245, 246, 247, 248, 249, 250, 251, 252, 253, 254, 254));
        if (__builtin_expect(bad != 0, 0)) TMO_SET((unsigned)bad)
        __builtin_amdgcn_s_setprio(0);
        if (s == 0 && wu == 1) { PST(6) }

        if (__builtin_expect(s <= KVS + 1, 0)) {
            if (s == 0 || __builtin_amdgcn_readfirstlane(*(volatile unsigned*)(chsm + C2_MB + 32 + ((s - 1) & 1) * 16 + 4 + (wu >> 1) * 4)) == 0u) {
                wait_ge<6>(kv_flag(a.sync, s + 1, grp, l), 1u, a.tmo, 3001u);
                const char* kp = arena(a.slots) + VX_OFF + blk_off(s + 1, ha, tau);
#pragma unroll
                for (int e = 0; e < 8; ++e) glds16h((unsigned)l * 16u, kp + e * 1024, l0 + (s & 1 ? C2_V : C2_K) + wu * 8192 + e * 1024);
                asm volatile("s_waitcnt vmcnt(0)" ::: "memory");
            }
        }
        {
            int k0 = KX_OFF + blk_off(s + 1, hs, tau) + jq * 1024;
            asm volatile(
                DSV("160:163", "0") DSV("164:167", "1024") DSV("168:171", "2048") DSV("172:175", "3072") DSV("176:179", "128") DSV("180:183", "1152") DSV("184:187", "2176") DSV("188:191", "3200")
                "s_cmp_lg_u32 %[wu], 1\n\t"
                "s_cbranch_scc1 1f\n\t"
                KLD("%[k0]")
                "1:\n\t"
                "s_waitcnt lgkmcnt(4)\n\t"
                PXT("192:195", "196:199", "200:203", "204:207", "160:163", "164:167", "168:171", "172:175")
                "s_waitcnt lgkmcnt(0)\n\t"
                PXT("208:211", "212:215", "216:219", "220:223", "176:179", "180:183", "184:187", "188:191")
                CVP("128", "192", "193") CVP("129", "194", "195") CVP("136", "196", "197") CVP("137", "198", "199") CVP("144", "200", "201") CVP("145", "202", "203") CVP("152", "204", "205") CVP("153", "206", "207")
                CVP("132", "208", "209") CVP("133", "210", "211") CVP("140", "212", "213") CVP("141", "214", "215") CVP("148", "216", "217") CVP("149", "218", "219") CVP("156", "220", "221") CVP("157", "222", "223")
                : "+{v[128:159]}"(yb), "+{v[96:103]}"(kx), [k0] "+s"(k0)
                : "{v[32:63]}"(wO[0]), "{v[64:95]}"(wO[1]), [lv] "v"(l0 + (s & 1 ? C2_V : C2_K) + wu * 8192 + voffX), [vk] "v"(voffK), [rs] "s"(rsS), [wu] "s"(wu)
                : "memory", "scc", CLOB_PX);
        }
        __builtin_amdgcn_sched_barrier(0);
        o_1 = o1_of(s + 1); o_2 = km_off(s + 1, hs, tau) + jq * 512;
        bq = x_off(s + 1, ha, tau); bm = m_off(s + 1, ha, tau); bb = v_off(s + 1, ha, tau);
        o_f = f_off(s + 1, grp, tau);
        o_cs = rec_base(s + 1) + C_OFF + c * 16384 + near_half(s + 1, tau) + wu * 2048 + grp * 256;
        asm volatile("" : "+s"(o_cs), "+s"(o_f), "+s"(o_1), "+s"(o_2), "+s"(bq), "+s"(bm), "+s"(bb));
        __builtin_amdgcn_s_barrier();
        asm volatile(
            "s_cmp_lg_u32 %[wu], 3\n\t"
            "s_cbranch_scc1 1f\n\t"
            "v_mov_b32 v160, %[lf]\n\t"
            PAUSE(PF)
            "ds_read_b32 v160, v160\n\t"
            "s_waitcnt lgkmcnt(0)\n\t"
            "v_cmp_eq_u32 vcc, %[tg], v160\n\t"
            "s_cbranch_vccz 1f\n\t"
            "s_mov_b32 exec_hi, 0\n\t"
            "buffer_store_dword v160, %[l4], %[rs], %[fo] offen sc1\n\t"
            "s_mov_b64 exec, -1\n\t"
            "1:\n\t"
            :: [wu] "s"(wu), [lf] "s"(l0 + C2_MB + 16), [tg] "s"(s + 1), [l4] "v"(l * 4), [rs] "s"(rsS), [fo] "s"(o_f + c * 128)
            : "memory", "scc", "vcc", "v160");
        __builtin_amdgcn_s_setprio(1);
    }
    {
        constexpr int s = NSEG - 1;
        const int l = lane_now(), i = l & 15, g = l >> 4;
        v4u qf[8], kf[8], pv[2], px[4];
        asm volatile("s_waitcnt vmcnt(0)" ::: "memory");
        __builtin_amdgcn_s_sleep(4);
#pragma unroll
        for (int e = 0; e < 8; ++e) kf[e] = ld_sc1s(rsS, l * 16, KX_OFF + blk_off(s, ha, tau) + e * 1024);
        hand_over(o_1, o_2, o_f, s);
#pragma unroll
        for (int e = 0; e < 8; ++e) qf[e] = *(const v4u*)(chsm + C2_V + 32768 + wu * 8192 + e * 1024 + l * 16);
        f32x4 sx[3] = {ZERO4, ZERO4, ZERO4};
        float mxx[2] = {-3.0e38f, -3.0e38f};
#pragma unroll
        for (int ks = 0; ks < 4; ++ks) {
            sx[0] = MM(kf[ks], qf[ks], sx[0]);
            sx[1] = MM(kf[ks], qf[4 + ks], sx[1]);
            sx[2] = MM(kf[4 + ks], qf[4 + ks], sx[2]);
        }
#pragma unroll
        for (int r = 0; r < 4; ++r) {
            if (4 * g + r > i) { sx[0][r] = -3.0e38f; sx[2][r] = -3.0e38f; }
            mxx[0] = fmaxf(mxx[0], sx[0][r]);
            mxx[1] = fmaxf(mxx[1], fmaxf(sx[1][r], sx[2][r]));
        }
        asm volatile("" : "+v"(sx[0]), "+v"(sx[1]), "+v"(sx[2]), "+v"(mxx[0]), "+v"(mxx[1]));
        const int bk = km_off(s, ha, tau);
#pragma nounroll
        for (int spin = 0;; ++spin) {
            asm volatile("" ::: "memory");
            pv[0] = ld_sc1s(rsS, l * 16, bb); pv[1] = ld_sc1s(rsS, l * 16, bb + 1024);
#pragma unroll
            for (int ks = 0; ks < 4; ++ks) px[ks] = ld_sc1s(rsS, g * 128 + (i >> 1) * 16, bk + ks * 512);
            if (__all(mx4(mx4(mx4(mx4(mx4(mx4(0u, pv[0]), pv[1]), px[0]), px[1]), px[2]), px[3]) != SENT)) break;
            if (dead) break;
            if (spin > SPIN_LIMIT) { TMO_SET(1700u) break; }
        }
#define SUB8(v, o) v8u{(v)[(o)], (v)[(o) + 1], (v)[(o) + 2], (v)[(o) + 3], (v)[(o) + 4], (v)[(o) + 5], (v)[(o) + 6], (v)[(o) + 7]}
#pragma unroll
        for (int ct = 0; ct < 4; ++ct) {
            f32x4 am = smm(pv[0], SUB8(wO[ct >> 1], (ct & 1) * 16), ZERO4, sidx);
            am = smm(pv[1], SUB8(wO[ct >> 1], (ct & 1) * 16 + 8), am, sidx);
            yb[8 * ct + 2] = cvt_pk_bf16(am[0] + am[1], 0.f); yb[8 * ct + 6] = cvt_pk_bf16(am[2] + am[3], 0.f);
        }
        const v8u one = {0x3F803F80u, 0x3F803F80u, 0x3F80u, 0u, 0x3F803F80u, 0x3F803F80u, 0x3F80u, 0u};
        const float SCL2E = 0.08838834764831845f * 1.4426950408889634f;
        float* xo = (float*)(chsm + C2_K) + (wu * 32 + 4 * g) * XP + i;
#pragma unroll
        for (int qt = 0; qt < 2; ++qt) {
            f32x4 sc[3] = {sx[qt], sx[2], ZERO4};
#pragma unroll
            for (int ks = 0; ks < 4; ++ks) sc[2] = MM(px[ks], qf[4 * qt + ks], sc[2]);
            float mx = fmaxf(mxx[qt], fmaxf(sc[2][1], sc[2][3]));
            mx = xor32_max(xor16_max(mx));
            const float nmx = -mx * SCL2E;
#define PE(x) __builtin_amdgcn_exp2f(fmaf(x, SCL2E, nmx))
            const float m0 = PE(sc[2][1]), m1 = PE(sc[2][3]);
            const v4u ae = {cvt_pk_bf16(PE(sc[0][0]), PE(sc[0][1])), cvt_pk_bf16(m0, m0), cvt_pk_bf16(PE(sc[0][2]), PE(sc[0][3])), 0u};
            const v4u ao = {qt ? cvt_pk_bf16(PE(sc[1][0]), PE(sc[1][1])) : 0u, 0u, qt ? cvt_pk_bf16(PE(sc[1][2]), PE(sc[1][3])) : 0u, cvt_pk_bf16(m1, m1)};
            const f32x4 sum = smm(ao, one, smm(ae, one, ZERO4, 0xE444), 0x4EEE);
#pragma unroll
            for (int ct = 0; ct < 4; ++ct) {
                const f32x4 o = smm(ao, SUB8(yb, 8 * ct), smm(ae, SUB8(yb, 8 * ct), ZERO4, 0xE444), 0x4EEE);
#pragma unroll
                for (int r = 0; r < 4; ++r) xo[(16 * qt + r) * XP + 16 * ct] = o[r] * __builtin_amdgcn_rcpf(sum[r]);
            }
        }
        __builtin_amdgcn_s_waitcnt(0xC07F);
        last_rows(chsm, rsS, wu, grp, c, tau);
        if (wu < 2) {
            v4u sel, p0, p1;
#pragma unroll
            for (int d = 0; d < 4; ++d) sel[d] = (l & 6) == 2 * d ? (0x3F80u << ((l & 1) * 16)) : 0u;
            const int xb = XB_OFF + ((c >> 1) * 8 + grp) * 16384 + (int)(tau & 1u) * 8192 + (c & 1) * 4096 + wu * 256;
            __builtin_amdgcn_s_sleep(DLY_C);
#pragma nounroll
            for (int spin = 0;; ++spin) {
                asm volatile("" ::: "memory");
                p0 = ld_sc1s(rsS, g * 512 + i * 16, xb); p1 = ld_sc1s(rsS, g * 512 + i * 16, xb + 2048);
                if (__all(mx4(mx4(0u, p0), p1) != SENT)) break;
                if (dead) break;
                if (spin > SPIN_LIMIT) { TMO_SET(1800u) break; }
            }
            const f32x4 d = MM(sel, p1, MM(sel, p0, ZERO4));
            if (g < 2) out8(a.out + (size_t)(s * SEG + 4 * grp + 2 * wu + (i >> 3)) * OUTW + 64 * c + 8 * (i & 7) + 4 * g, v2u{cvt_pk_bf16(d[0], d[1]), cvt_pk_bf16(d[2], d[3])});
        }
    }
}

struct cpx { float x, y; };
__device__ __forceinline__ cpx cmul(cpx a, cpx b) { return {a.x * b.x - a.y * b.y, a.x * b.y + a.y * b.x}; }
__device__ __forceinline__ cpx cadd(cpx a, cpx b) { return {a.x + b.x, a.y + b.y}; }
__device__ __forceinline__ cpx csub(cpx a, cpx b) { return {a.x - b.x, a.y - b.y}; }
template <int DIR> __device__ __forceinline__ cpx mul_i(cpx a) {
    return (DIR < 0) ? cpx{a.y, -a.x} : cpx{-a.y, a.x};
}
template <int DIR> __device__ __forceinline__ void r4(cpx& a, cpx& b, cpx& c, cpx& d) {
    cpx t0 = cadd(a, c), t1 = csub(a, c), t2 = cadd(b, d), t3 = mul_i<DIR>(csub(b, d));
    a = cadd(t0, t2); b = cadd(t1, t3); c = csub(t0, t2); d = csub(t1, t3);
}
template <int DIR> __device__ __forceinline__ cpx twc(float c, float s) { return cpx{c, (float)DIR * s}; }
template <int DIR> __device__ __forceinline__ void dft16(cpx (&v)[16]) {
#pragma unroll
    for (int a = 0; a < 4; ++a) r4<DIR>(v[a], v[a + 4], v[a + 8], v[a + 12]);
    const float C1 = 0.92387953251128674f, S1 = 0.38268343236508977f, R2 = 0.70710678118654752f;
    v[1 + 4] = cmul(v[1 + 4], twc<DIR>(C1, S1));
    v[1 + 8] = cmul(v[1 + 8], twc<DIR>(R2, R2));
    v[1 + 12] = cmul(v[1 + 12], twc<DIR>(S1, C1));
    v[2 + 4] = cmul(v[2 + 4], twc<DIR>(R2, R2));
    v[2 + 8] = mul_i<DIR>(v[2 + 8]);
    v[2 + 12] = cmul(v[2 + 12], twc<DIR>(-R2, R2));
    v[3 + 4] = cmul(v[3 + 4], twc<DIR>(S1, C1));
    v[3 + 8] = cmul(v[3 + 8], twc<DIR>(-R2, R2));
    v[3 + 12] = cmul(v[3 + 12], twc<DIR>(-C1, -S1));
#pragma unroll
    for (int d = 0; d < 4; ++d) r4<DIR>(v[4 * d], v[4 * d + 1], v[4 * d + 2], v[4 * d + 3]);
}
__device__ __forceinline__ constexpr int x16(int q) { return 4 * (q & 3) + (q >> 2); }
template <int DIR> __device__ __forceinline__ void dft8(cpx (&v)[8]) {
    r4<DIR>(v[0], v[2], v[4], v[6]);
    r4<DIR>(v[1], v[3], v[5], v[7]);
    const float R2 = 0.70710678118654752f;
    v[3] = cmul(v[3], twc<DIR>(R2, R2));
    v[5] = mul_i<DIR>(v[5]);
    v[7] = cmul(v[7], twc<DIR>(-R2, R2));
#pragma unroll
    for (int d = 0; d < 4; ++d) { cpx p = v[2 * d], q = v[2 * d + 1]; v[2 * d] = cadd(p, q); v[2 * d + 1] = csub(p, q); }
}
__device__ __forceinline__ int pad(int i) { return i + (i >> 4); }
__device__ __forceinline__ cpx expi(float x) { float s, c; sincospif(x, &s, &c); return cpx{c, s}; }
template <int R, int DIR> __device__ __forceinline__ void twiddle(cpx* v, cpx w1) {
    cpx w[R];
    w[1] = w1;
#pragma unroll
    for (int r = 2; r < R; ++r) w[r] = (r & 1) ? cmul(w[r - 1], w1) : cmul(w[r / 2], w[r / 2]);
#pragma unroll
    for (int r = 1; r < R; ++r) v[r] = DIR < 0 ? cmul(v[r], w[r]) : cpx{v[r].x * w[r].x + v[r].y * w[r].y, v[r].y * w[r].x - v[r].x * w[r].y};
}
struct TwBase { cpx b; cpx c[4]; };
template <int N> __device__ __forceinline__ TwBase make_tw(int t) {
    TwBase tb;
    tb.b = expi(-(float)(t & 15) * (1.0f / 128.0f));
    constexpr int NQ = (N == 1024) ? 4 : 2, T = N / 16;
#pragma unroll
    for (int q = 0; q < NQ; ++q) tb.c[q] = expi(-(float)(t + T * q) * (2.0f / (float)N));
    return tb;
}
template <int N> __device__ __forceinline__ void fft_sync() {
    if constexpr (N == 1024) { __builtin_amdgcn_fence(__ATOMIC_ACQ_REL, "wavefront"); __builtin_amdgcn_wave_barrier(); } else __syncthreads();
}
template <int N, int DIR, bool RIN, bool ROUT>
__device__ __forceinline__ void fft_reg(cpx* buf, const TwBase& tb, int t, cpx (&v)[16]) {
    constexpr int T = N / 16;
    if constexpr (!RIN) {
#pragma unroll
        for (int r = 0; r < 16; ++r) v[r] = buf[pad(t + T * r)];
        fft_sync<N>();
    }
    dft16<DIR>(v);
#pragma unroll
    for (int q = 0; q < 16; ++q) buf[pad(16 * t + x16(q))] = v[q];
    fft_sync<N>();
    {
        const int k = t & 15;
#pragma unroll
        for (int r = 0; r < 16; ++r) v[r] = buf[pad(t + T * r)];
        fft_sync<N>();
        twiddle<16, DIR>(v, tb.b);
        dft16<DIR>(v);
        const int o = (t >> 4) * 256 + k;
#pragma unroll
        for (int q = 0; q < 16; ++q) buf[pad(o + 16 * x16(q))] = v[q];
        fft_sync<N>();
    }
    if constexpr (N == 1024) {
#pragma unroll
        for (int q = 0; q < 4; ++q)
#pragma unroll
            for (int r = 0; r < 4; ++r) v[4 * q + r] = buf[pad(t + 64 * q + 256 * r)];
#pragma unroll
        for (int q = 0; q < 4; ++q) {
            twiddle<4, DIR>(&v[4 * q], tb.c[q]);
            r4<DIR>(v[4 * q], v[4 * q + 1], v[4 * q + 2], v[4 * q + 3]);
#pragma unroll
            for (int r = 0; r < 4; ++r) buf[pad(t + 64 * q + 256 * r)] = v[4 * q + r];
        }
        fft_sync<N>();
    } else {
        cpx u[2][8];
#pragma unroll
        for (int q = 0; q < 2; ++q)
#pragma unroll
            for (int r = 0; r < 8; ++r) u[q][r] = buf[pad(t + 128 * q + 256 * r)];
        if constexpr (ROUT) fft_sync<N>();
#pragma unroll
        for (int q = 0; q < 2; ++q) {
            twiddle<8, DIR>(u[q], tb.c[q]);
            dft8<DIR>(u[q]);
#pragma unroll
            for (int d = 0; d < 4; ++d) {
                if constexpr (ROUT) { v[q + 2 * d] = u[q][2 * d]; v[q + 2 * d + 8] = u[q][2 * d + 1]; }
                else { buf[pad(t + 128 * q + 256 * d)] = u[q][2 * d]; buf[pad(t + 128 * q + 256 * (d + 4))] = u[q][2 * d + 1]; }
            }
        }
        if constexpr (!ROUT) fft_sync<N>();
    }
}
__device__ __forceinline__ int f_pos(int rb, int x, unsigned tw) { x = (x + rb * 4224) & 32767; return rb * 65536 + x + (x & ~8191) + (int)((tw ^ (unsigned)(rb >> 5)) & 1u) * 8192; }
__device__ __forceinline__ int w_pos(int k, int n, unsigned tw) { return k * 16384 + ((n * 4 + k * 384) & 8191) + (int)((tw ^ (unsigned)(k >> 7)) & 1u) * 8192; }
constexpr int PB1 = 1024 + 64, PB2 = 2048 + 128;
typedef float f32x4v __attribute__((ext_vector_type(4)));
typedef unsigned u32x4v __attribute__((ext_vector_type(4)));

__device__ __forceinline__ void fft_rows_fwd(const u16* __restrict__ x, rsrc_t rsF, const unsigned tw, int rb, char* smem, int tid, const char* wr, const char* wi, const unsigned spare) {
    cpx (*buf)[PB1] = (cpx (*)[PB1])smem;
    int f = tid >> 6, t = tid & 63;
    const u16* p0 = x + (size_t)(rb * 8 + 2 * f) * 1024 + 16 * t;
    u32x4v a0 = *(const u32x4v*)p0, a1 = *(const u32x4v*)(p0 + 8), b0 = *(const u32x4v*)(p0 + 1024), b1 = *(const u32x4v*)(p0 + 1032);
    const TwBase tb = make_tw<1024>(t);
    for (int it = 0; it < 4; ++it, rb += 64) {
        asm volatile("" : "+v"(tid));
        f = tid >> 6; t = tid & 63;
        __syncthreads();
        cpx* d = &buf[f][pad(16 * t)];
#pragma unroll
        for (int e = 0; e < 4; ++e) {
            d[2 * e] = cpx{__uint_as_float(a0[e] << 16), __uint_as_float(b0[e] << 16)};
            d[2 * e + 1] = cpx{__uint_as_float(a0[e] & 0xFFFF0000u), __uint_as_float(b0[e] & 0xFFFF0000u)};
            d[8 + 2 * e] = cpx{__uint_as_float(a1[e] << 16), __uint_as_float(b1[e] << 16)};
            d[8 + 2 * e + 1] = cpx{__uint_as_float(a1[e] & 0xFFFF0000u), __uint_as_float(b1[e] & 0xFFFF0000u)};
        }
        if (it < 3) p0 += 512 * 1024;
        touch(((it & 1) ? wi : wr) + (size_t)((rb >> 1) & 31) * 32832 + (it >> 1) * 32 * 32832 + ((rb & 1) * 128 + (tid >> 1)) * 128, spare);
        a0 = *(const u32x4v*)p0; a1 = *(const u32x4v*)(p0 + 8); b0 = *(const u32x4v*)(p0 + 1024); b1 = *(const u32x4v*)(p0 + 1032);
        fft_sync<1024>();
        cpx z[16];
        fft_reg<1024, -1, false, false>(buf[f], tb, t, z);
        __syncthreads();
#pragma unroll
        for (int m = 0; m < 8; ++m) {
            const int idx = tid + 256 * m, k = idx >> 2, ff = idx & 3;
            const cpx zk = buf[ff][pad(k)], zn = buf[ff][pad(k ? 1024 - k : 512)];
            f32x4v o = {0.5f * (zk.x + zn.x), 0.5f * (zk.y - zn.y), 0.5f * (zk.y + zn.y), -0.5f * (zk.x - zn.x)};
            if (m == 0 && k == 0) o = f32x4v{zk.x, zn.x, zk.y, zn.y};
            st_sc1(rsF, f_pos(rb, idx * 16, tw), __builtin_bit_cast(v4u, o));
        }
    }
}
__device__ __forceinline__ void filt_transpose(const u16* __restrict__ wr, const u16* __restrict__ wi, rsrc_t rsW, const unsigned tw, const int tb, char* smem, const int tid) {
    const v4u* s0 = (const v4u*)(wr + (size_t)tb * 32 * 513);
    const v4u* s1 = (const v4u*)(wi + (size_t)tb * 32 * 513);
    v4u r0[5], r1[5];
#pragma unroll
    for (int e = 0; e < 5; ++e) {
        const int idx = tid + 512 * e, ic = idx < 2052 ? idx : 2051;
        r0[e] = s0[ic]; r1[e] = s1[ic];
    }
#pragma unroll
    for (int e = 0; e < 5; ++e) {
        const int idx = tid + 512 * e;
        if (idx < 2052) { *(v4u*)(smem + idx * 16) = r0[e]; *(v4u*)(smem + 32832 + idx * 16) = r1[e]; }
    }
    __syncthreads();
    const u16* a0 = (const u16*)smem;
    const u16* a1 = (const u16*)(smem + 32832);
    for (int idx = tid; idx < 513 * 8; idx += 512) {
        const int k = idx >> 3, n4 = (idx & 7) * 4;
        v4u v;
#pragma unroll
        for (int j = 0; j < 4; ++j) v[j] = (unsigned)a0[(n4 + j) * 513 + k] | ((unsigned)a1[(n4 + j) * 513 + k] << 16);
        st_sc1(rsW, w_pos(k, tb * 32 + n4, tw), v);
    }
}

__device__ __forceinline__ void fft_cols(rsrc_t rsF, rsrc_t rsW, const unsigned tw, int blk, const int n, char* smem, int tid) {
    cpx (*buf)[PB2] = (cpx (*)[PB2])smem;
    int c = tid >> 7, t = tid & 127;
    f32x4v v[8];
    unsigned wv[16], wn[16];
    auto wld = [&](const int k, const int n) { return __builtin_amdgcn_raw_buffer_load_b32(rsW, w_pos(k, n, tw), 0, 0); };
    auto fetch = [&](const int bk) {
        const int k0 = bk * 2, kc = k0 + c;
#pragma unroll
        for (int m = 0; m < 8; ++m) { const int idx = tid + 256 * m; v[m] = __builtin_bit_cast(f32x4v, __builtin_amdgcn_raw_buffer_load_b128(rsF, f_pos(idx >> 3, k0 * 64 + (idx & 7) * 16, tw), 0, 0)); }
#pragma unroll
        for (int m = 0; m < 16; ++m) wn[m] = wld(kc, t + 128 * m);
    };
    fetch(blk);
    const TwBase tb = make_tw<2048>(t);
    for (int it = 0; it < n; ++it, blk += 64) {
        asm volatile("" : "+v"(tid));
        c = tid >> 7; t = tid & 127;
        __syncthreads();
#pragma unroll
        for (int m = 0; m < 8; ++m) {
            const int idx = tid + 256 * m, rb = idx >> 3, pc = idx & 7;
            cpx* d = &buf[pc >> 2][pad(rb * 8 + 2 * (pc & 3))];
            d[0] = cpx{v[m][0], v[m][1]}; d[1] = cpx{v[m][2], v[m][3]};
        }
#pragma unroll
        for (int m = 0; m < 16; ++m) wv[m] = wn[m];
        fetch(it + 1 < n ? blk + 64 : blk);
        __syncthreads();
        cpx z[16];
        fft_reg<2048, -1, false, true>(buf[c], tb, t, z);
        auto wcx = [](const unsigned w) { return cpx{__uint_as_float(w << 16), __uint_as_float(w & 0xFFFF0000u)}; };
        if (blk < 2) {
#pragma unroll
            for (int m = 0; m < 16; ++m) buf[c][pad(t + 128 * m)] = z[m];
            __syncthreads();
            if (blk == 0 && c == 0) {
#pragma unroll
                for (int m = 0; m < 16; ++m) {
                    const int n = t + 128 * m, nn = (2048 - n) & 2047;
                    const cpx zn = buf[0][pad(nn)], a = wcx(wv[m]), b = wcx(wld(0, nn)), e = wcx(wld(512, n)), f = wcx(wld(512, nn));
                    const cpx h0 = cmul(cpx{0.5f * (z[m].x + zn.x), 0.5f * (z[m].y - zn.y)}, cpx{0.5f * (a.x + b.x), 0.5f * (a.y - b.y)});
                    const cpx h5 = cmul(cpx{0.5f * (z[m].y + zn.y), -0.5f * (z[m].x - zn.x)}, cpx{0.5f * (e.x + f.x), 0.5f * (e.y - f.y)});
                    z[m] = cpx{h0.x - h5.y, h0.y + h5.x};
                    wv[m] = 0x3F80u;
                }
            }
            __syncthreads();
        }
#pragma unroll
        for (int m = 0; m < 16; ++m) z[m] = cmul(z[m], wcx(wv[m]));
        fft_reg<2048, 1, true, false>(buf[c], tb, t, z);
        const float sc = 1.0f / 2048.0f;
#pragma unroll
        for (int m = 0; m < 8; ++m) {
            const int idx = tid + 256 * m, rb = idx >> 3, pc = idx & 7;
            const cpx* d = &buf[pc >> 2][pad(rb * 8 + 2 * (pc & 3))];
            st_sc1(rsF, f_pos(rb, blk * 128 + pc * 16, tw), __builtin_bit_cast(v4u, f32x4v{d[0].x * sc, d[0].y * sc, d[1].x * sc, d[1].y * sc}));
        }
    }
}

__device__ __forceinline__ void fft_rows_inv(rsrc_t rsF, const unsigned tw, u16* __restrict__ out, int rb, const int step, const int n, char* smem, int tid) {
    cpx (*buf)[PB1] = (cpx (*)[PB1])smem;
    int f = tid >> 6, t = tid & 63;
    f32x4v v[8];
    auto fetch = [&](const int r) {
#pragma unroll
        for (int m = 0; m < 8; ++m) v[m] = __builtin_bit_cast(f32x4v, __builtin_amdgcn_raw_buffer_load_b128(rsF, f_pos(r, (tid + 256 * m) * 16, tw), 0, 0));
    };
    fetch(rb);
    const TwBase tb = make_tw<1024>(t);
    for (int it = 0; it < n; ++it, rb += step) {
        asm volatile("" : "+v"(tid));
        f = tid >> 6; t = tid & 63;
        __syncthreads();
        cpx* bf = buf[tid & 3];
#pragma unroll
        for (int m = 0; m < 8; ++m) {
            const int k = (tid >> 2) + 64 * m;
            if (m == 0 && k == 0) { bf[0] = cpx{v[0][0], v[0][2]}; bf[pad(512)] = cpx{v[0][1], v[0][3]}; }
            else { bf[pad(k)] = cpx{v[m][0] - v[m][3], v[m][1] + v[m][2]}; bf[pad(1024 - k)] = cpx{v[m][0] + v[m][3], v[m][2] - v[m][1]}; }
        }
        fetch(it + 1 < n ? rb + step : rb);
        __syncthreads();
        cpx z[16];
        fft_reg<1024, 1, false, false>(buf[f], tb, t, z);
        const float sc = 1.0f / 1024.0f;
        const cpx* d = &buf[f][pad(16 * t)];
        u32x4v a[2], b[2];
#pragma unroll
        for (int h = 0; h < 2; ++h)
#pragma unroll
            for (int e = 0; e < 4; ++e) {
                cpx p = d[8 * h + 2 * e], q = d[8 * h + 2 * e + 1];
                a[h][e] = cvt_pk_bf16(p.x * sc, q.x * sc);
                b[h][e] = cvt_pk_bf16(p.y * sc, q.y * sc);
            }
        u16* o0 = out + (size_t)(rb * 8 + 2 * f) * OUTW + 1024 + 16 * t;
        *(u32x4v*)o0 = a[0]; *(u32x4v*)(o0 + 8) = a[1];
        *(u32x4v*)(o0 + OUTW) = b[0]; *(u32x4v*)(o0 + OUTW + 8) = b[1];
    }
}

__device__ __forceinline__ void glds16s(unsigned voff, const void* sbase, unsigned lds_dst) {
    unsigned keep;
    asm volatile("s_mov_b32 %0, m0\n\ts_mov_b32 m0, %3\n\ts_nop 0\n\tglobal_load_lds_dwordx4 %1, %2\n\ts_mov_b32 m0, %0" : "=&s"(keep) : "v"(voff), "s"(sbase), "s"(lds_dst) : "memory");
}
#define K8_BAR do { asm volatile("" ::: "memory"); __builtin_amdgcn_s_barrier(); asm volatile("" ::: "memory"); } while (0)
#define K8_WV(n) asm volatile("s_waitcnt vmcnt(" #n ")" ::: "memory")
#define K8_WL(n) asm volatile("s_waitcnt lgkmcnt(" #n ")" ::: "memory")
#define K8_SCHED __builtin_amdgcn_sched_barrier(0)
template <int BN>
__device__ __forceinline__ int bswz2(int row) {
    if (BN >= 128) return (row & 3) | (((row >> 3) & 1) << 2);
    else return ((row >> 1) & 1) | (((row >> 3) & 1) << 1);
}
template <int BM>
__device__ __forceinline__ void gemm_tile(u16* sm, const char* A, const int s0, const unsigned tau, const u16* B, u16* C) {
    constexpr int K = HD, LDB = DM, LDC = OUTW;
    constexpr int BN = 128, BK = 64, ST = BM == 128 ? 4 : 3, WN = BM == 128 ? 4 : 2;
    constexpr int ATILE = BM * BK, BTILE = BK * BN;
    constexpr int NT = 8 / WN, GA = BM / 64, GB = 2, GS = GA + GB;
    const int tid = opaque_tid(), w = tid >> 6, l = tid & 63, i = l & 15, g = l >> 4;
    const int wr = w / WN, wc = w % WN;
    const unsigned wu = __builtin_amdgcn_readfirstlane(w);
    unsigned vb;
    { const int row = l >> 4, c = l & 15; vb = (unsigned)(row * LDB + 8 * (c ^ ((row | ((wu & 1) << 2)) << 1))) * 2u; }
    const int sg = s0 + (BM == 128 ? wu >> 1 : wu);
    const char* ab = A + (BM == 128 ? (wu & 1) * 4096 : 0);
    const char* bb = (const char*)(B + (size_t)wu * 8 * LDB);
    const unsigned lds0 = (unsigned)(uintptr_t)(__attribute__((address_space(3))) u16*)sm;
    const unsigned va = (unsigned)l * 16u, va2 = va + 2048u, vb2 = vb + 4u * LDB * 2u - 1024u;
    auto req = [&](const int buf, const int kt) {
        const unsigned d = lds0 + buf * (ATILE + BTILE) * 2;
        const int wh = kt >> 1;
        const char* pa = ab + ((blk_off(sg, (wh & 7) * 4 + (wh >> 3), tau) ^ 8192) + (kt & 1) * 2048);
        const char* pb = bb + (size_t)kt * BK * LDB * 2;
        unsigned keep;
        if (BM == 128) asm volatile("s_mov_b32 %0, m0\n\ts_mov_b32 m0, %1\n\ts_nop 0\n\tglobal_load_lds_dwordx4 %3, %5\n\tglobal_load_lds_dwordx4 %3, %5 offset:1024\n\t"
                     "s_mov_b32 m0, %2\n\ts_nop 0\n\tglobal_load_lds_dwordx4 %6, %8\n\tglobal_load_lds_dwordx4 %7, %8 offset:1024\n\ts_mov_b32 m0, %0"
                     : "=&s"(keep) : "s"(d + wu * GA * 1024), "s"(d + ATILE * 2 + wu * GB * 1024), "v"(va), "v"(va2), "s"(pa), "v"(vb), "v"(vb2), "s"(pb) : "memory");
        else asm volatile("s_mov_b32 %0, m0\n\ts_mov_b32 m0, %1\n\ts_nop 0\n\tglobal_load_lds_dwordx4 %3, %5\n\tglobal_load_lds_dwordx4 %3, %5 offset:1024\n\t"
                     "global_load_lds_dwordx4 %4, %5 offset:2048\n\tglobal_load_lds_dwordx4 %4, %5 offset:3072\n\t"
                     "s_mov_b32 m0, %2\n\ts_nop 0\n\tglobal_load_lds_dwordx4 %6, %8\n\tglobal_load_lds_dwordx4 %7, %8 offset:1024\n\ts_mov_b32 m0, %0"
                     : "=&s"(keep) : "s"(d + wu * GA * 1024), "s"(d + ATILE * 2 + wu * GB * 1024), "v"(va), "v"(va2), "s"(pa), "v"(vb), "v"(vb2), "s"(pb) : "memory");
    };
    f32x4 acc[4][NT] = {};
    constexpr int nt = K / BK;
#pragma unroll
    for (int s = 0; s < ST - 1; ++s) req(s, s);
    int boff[NT];
    const int aoff = wr * 4096 + l * 8;
#pragma unroll
    for (int n = 0; n < NT; ++n) { const int row = 8 * g + (i >> 2); boff[n] = row * BN + 16 * ((wc * NT + n) ^ bswz2<BN>(row)) + 4 * (i & 3); }
    asm volatile("s_waitcnt vmcnt(%0)" :: "i"((ST - 2) * GS) : "memory");
    K8_BAR;
    if (wu >= 4) K8_BAR;
    for (int t0 = 0; t0 < nt + ST - 1 - (nt + ST - 1) % ST; t0 += ST) {
#pragma unroll
        for (int rb = 0; rb < ST; ++rb) {
            const int t = t0 + rb;
            if (nt % ST != 0 && t >= nt) break;
            const int kk = min(t + ST - 1, nt - 1);
            const u16* as = sm + rb * (ATILE + BTILE);
            const u16* bs = as + ATILE;
            bf16x8 af[2][4], bf[2][NT];
#pragma unroll
            for (int ks = 0; ks < 2; ++ks) {
#pragma unroll
                for (int n = 0; n < NT; ++n) bf[ks][n] = tr_frag(bs + boff[n] + ks * 32 * BN, bs + boff[n] + ks * 32 * BN + 4 * BN);
#pragma unroll
                for (int m = 0; m < 4; ++m) af[ks][m] = *(const bf16x8*)(as + aoff + m * 1024 + ks * 512);
            }
            K8_SCHED;
            req((rb + ST - 1) % ST, kk);
            K8_WL(0);
            if (wu >= 4) asm volatile("s_waitcnt vmcnt(%0)" :: "i"((ST - 2) * GS) : "memory");
            K8_BAR;
            __builtin_amdgcn_s_setprio(1);
#pragma unroll
            for (int q = 0; q < 8; ++q)
#pragma unroll
                for (int n = 0; n < NT; ++n) acc[q & 3][n] = __builtin_amdgcn_mfma_f32_16x16x32_bf16(bf[q >> 2][n], af[q >> 2][q & 3], acc[q & 3][n], 0, 0, 0);
            __builtin_amdgcn_s_setprio(0);
            K8_SCHED;
            asm volatile("s_waitcnt vmcnt(%0)" :: "i"((ST - 2) * GS) : "memory");
            K8_BAR;
        }
    }
    if (wu < 4) K8_BAR;
    asm volatile("s_waitcnt vmcnt(0)" ::: "memory");
#pragma unroll
    for (int m = 0; m < 4; ++m)
#pragma unroll
        for (int n = 0; n < NT; n += 2) {
            auto r0 = __builtin_amdgcn_permlane16_swap(cvt_pk_bf16(acc[m][n][0], acc[m][n][1]), cvt_pk_bf16(acc[m][n + 1][0], acc[m][n + 1][1]), false, false);
            auto r1 = __builtin_amdgcn_permlane16_swap(cvt_pk_bf16(acc[m][n][2], acc[m][n][3]), cvt_pk_bf16(acc[m][n + 1][2], acc[m][n + 1][3]), false, false);
            *(v4u*)(C + (wr * 64 + m * 16 + i) * LDC + (wc * NT + n + (g & 1)) * 16 + 8 * (g >> 1)) = v4u{r0[0], r1[0], r0[1], r1[1]};
        }
}

__device__ __forceinline__ void xattn_unit(const ChainArgs& a, rsrc_t rsQ, rsrc_t rsK, rsrc_t rsV, rsrc_t rsS, const int s, const int sn, const int h, u16 (*Vs)[VP], unsigned* mbox, const unsigned tau, int& pend, bool& ready) {
    const int l = opaque_tid() & 63, i = l & 15, g = l >> 4;
    const unsigned known = __builtin_amdgcn_readfirstlane(*(volatile unsigned*)mbox);
    const bool early = known > (unsigned)(s + 1);
    const int bq = km_off(s, h, tau), bb = v_off(s, h, tau);
    auto count = [&](const int t) { arrive(a.sync, t < SSEG ? SY_OX + (t >> 2) * 8 : SY_OXS + (t - SSEG) * 8, t < SSEG ? 128u : 32u, 8); };
    const int xk = blk_off(s, h, tau), xq = xk ^ 8192;
    v4u vv[8], vm[2], k0f[4], k1f[4], kmf[4], q0f[4], q1f[4];
#pragma unroll
    for (int e = 0; e < 8; ++e) vv[e] = ld_sc1s(rsV, l * 16 + e * 1024, xk);
#pragma unroll
    for (int ks = 0; ks < 4; ++ks) {
        k0f[ks] = ld_sc1s(rsK, l * 16 + ks * 1024, xk);
        k1f[ks] = ld_sc1s(rsK, l * 16 + ks * 1024, xk + 4096);
        q0f[ks] = ld_sc1s(rsQ, l * 16 + ks * 1024, xq);
        q1f[ks] = ld_sc1s(rsQ, l * 16 + ks * 1024, xq + 4096);
    }
#pragma unroll
    for (int e = 0; e < 2; ++e) vm[e] = ld_sc1s(rsS, l * 16 + e * 1024, early ? bb : 1 << 30);
#pragma unroll
    for (int ks = 0; ks < 4; ++ks) kmf[ks] = ld_sc1s(rsS, ks * 512 + g * 128 + (i & 7) * 16, early ? bq : 1 << 30);
    const unsigned fl = ld_flag(a.sync + (l >= 8 ? SY_PROG * 32 : (((l & 4) ? SY_Q : SY_KV) + (min(sn, NSEG - 1) >> 3) * 8) * 32 + (l & 3)) + (h >> 2) * 32);
    if (pend >= 0) { asm volatile("s_waitcnt vmcnt(31)" ::: "memory"); count(pend); }
#pragma unroll
    for (int e = 0; e < 8; ++e) *(v4u*)&Vs[16 * (e >> 2) + i][32 * (e & 3) + 8 * g] = vv[e];
    const float scale = 0.08838834764831845f;
    const float L2E = 1.4426950408889634f;
    auto round2 = [&](const f32x4 c, float* o) {
#pragma unroll
        for (int p = 0; p < 2; ++p) {
            unsigned u = cvt_pk_bf16(c[2 * p], c[2 * p + 1]);
            float lo = __uint_as_float(u << 16) * scale, hi = __uint_as_float(u & 0xFFFF0000u) * scale;
            unsigned u2 = cvt_pk_bf16(lo, hi);
            o[2 * p] = __uint_as_float(u2 << 16);
            o[2 * p + 1] = __uint_as_float(u2 & 0xFFFF0000u);
        }
    };
    float sv[2][12], mxx[2];
    {
        f32x4 sx[3] = {};
#pragma unroll
        for (int ks = 0; ks < 4; ++ks) {
            sx[0] = __builtin_amdgcn_mfma_f32_16x16x32_bf16(as_frag(k0f[ks]), as_frag(q0f[ks]), sx[0], 0, 0, 0);
            sx[1] = __builtin_amdgcn_mfma_f32_16x16x32_bf16(as_frag(k0f[ks]), as_frag(q1f[ks]), sx[1], 0, 0, 0);
            sx[2] = __builtin_amdgcn_mfma_f32_16x16x32_bf16(as_frag(k1f[ks]), as_frag(q1f[ks]), sx[2], 0, 0, 0);
        }
        round2(sx[0], &sv[0][0]); round2(sx[1], &sv[1][0]); round2(sx[2], &sv[1][4]);
        mxx[0] = mxx[1] = -3.0e38f;
#pragma unroll
        for (int r = 0; r < 4; ++r) {
            if (4 * g + r > i) { sv[0][r] = -3.0e38f; sv[1][4 + r] = -3.0e38f; }
            mxx[0] = fmaxf(mxx[0], sv[0][r]);
            mxx[1] = fmaxf(mxx[1], fmaxf(sv[1][r], sv[1][4 + r]));
        }
    }
    const u16* vb = &Vs[4 * g + (i >> 2)][4 * (i & 3)];
    bf16x8 vfx[8];
#pragma unroll
    for (int t = 0; t < 8; ++t) vfx[t] = tr_frag(vb + 16 * t, vb + 16 * VP + 16 * t);
#pragma unroll
    for (int t = 0; t < 8; ++t) asm volatile("" : "+v"(vfx[t]));
#pragma unroll
    for (int r = 0; r < 4; ++r) asm volatile("" : "+v"(sv[0][r]), "+v"(sv[1][r]), "+v"(sv[1][4 + r]));
    asm volatile("" : "+v"(mxx[0]), "+v"(mxx[1]));
    bool have = false;
    unsigned seen = __builtin_amdgcn_readlane(fl, 8);
    ready = (sn >> 3) == (s >> 3) || (__ballot(fl != 0u) & 255ull) == 255ull;
    if (early) {
        if ((h & 3) == 0 && l == 0) atomicMax(mbox, seen);
        unsigned m = mx4(mx4(0u, vm[0]), vm[1]);
#pragma unroll
        for (int ks = 0; ks < 4; ++ks) m = mx4(m, kmf[ks]);
        have = __all(m != SENT);
    } else if ((h & 3) == 0) {
        const unsigned* pw = a.sync + (SY_PROG + (h >> 2)) * 32;
        unsigned v = seen;
        if (v <= (unsigned)(s + 1)) {
            if (s < NSEG - XA_FAST) { wait_ge<32>(pw, (unsigned)(s + 1), a.tmo, 4000u); __builtin_amdgcn_s_sleep(24); }
            else { wait_ge<6>(pw, (unsigned)(s + 1), a.tmo, 4000u); __builtin_amdgcn_s_sleep(XA_D); }
            v = (unsigned)(s + 1);
        }
        if (l == 0) atomicMax(mbox, v);
    } else {
#pragma nounroll
        for (int spin = 0; spin < 4000000 && *(volatile unsigned*)mbox < (unsigned)(s + 1); ++spin) __builtin_amdgcn_s_sleep(2);
        asm volatile("" ::: "memory");
    }
#pragma nounroll
    for (int spin = 0; !have; ++spin) {
        asm volatile("" ::: "memory");
#pragma unroll
        for (int e = 0; e < 2; ++e) vm[e] = ld_sc1s(rsS, l * 16 + e * 1024, bb);
#pragma unroll
        for (int ks = 0; ks < 4; ++ks) kmf[ks] = ld_sc1s(rsS, ks * 512 + g * 128 + (i & 7) * 16, bq);
        unsigned m = mx4(mx4(0u, vm[0]), vm[1]);
#pragma unroll
        for (int ks = 0; ks < 4; ++ks) m = mx4(m, kmf[ks]);
        if (__all(m != SENT) || spin > 20000) break;
        __builtin_amdgcn_s_sleep(2);
    }
#pragma unroll
    for (int e = 0; e < 8; ++e) *(unsigned*)&Vs[32 + (i >> 2) + 4 * ((i >> 1) & 1)][64 * (e >> 2) + 16 * g + 4 * (e & 3) + 2 * (i & 1)] = vm[e >> 2][e & 3];
    f32x4 sm_[2] = {};
#pragma unroll
    for (int ks = 0; ks < 4; ++ks) {
        sm_[0] = __builtin_amdgcn_mfma_f32_16x16x32_bf16(as_frag(kmf[ks]), as_frag(q0f[ks]), sm_[0], 0, 0, 0);
        sm_[1] = __builtin_amdgcn_mfma_f32_16x16x32_bf16(as_frag(kmf[ks]), as_frag(q1f[ks]), sm_[1], 0, 0, 0);
    }
    v4u pf[2][2];
#pragma unroll
    for (int qt = 0; qt < 2; ++qt) {
        round2(sm_[qt], &sv[qt][8]);
        float mx = fmaxf(fmaxf(mxx[qt], fmaxf(sv[qt][8], sv[qt][9])), fmaxf(sv[qt][10], sv[qt][11]));
        mx = xor16_max(mx);
        mx = xor32_max(mx);
        const float nmx = -mx * L2E;
        float sum = 0.f;
#pragma unroll
        for (int e = 0; e < 12; ++e) {
            if (qt == 0 && e >= 4 && e < 8) { sv[qt][e] = 0.f; continue; }
            float ev = __builtin_amdgcn_exp2f(fmaf(sv[qt][e], L2E, nmx));
            if (e >= 8) ev = g < 2 ? ev : 0.f;
            sv[qt][e] = ev; sum += ev;
        }
        sum = xor16_sum(sum);
        sum = xor32_sum(sum);
        const float inv = 1.0f / sum;
        pf[qt][0][0] = cvt_pk_bf16(sv[qt][0] * inv, sv[qt][1] * inv);
        pf[qt][0][1] = cvt_pk_bf16(sv[qt][2] * inv, sv[qt][3] * inv);
        pf[qt][0][2] = qt ? cvt_pk_bf16(sv[qt][4] * inv, sv[qt][5] * inv) : 0u;
        pf[qt][0][3] = qt ? cvt_pk_bf16(sv[qt][6] * inv, sv[qt][7] * inv) : 0u;
        pf[qt][1][0] = cvt_pk_bf16(sv[qt][8] * inv, sv[qt][9] * inv);
        pf[qt][1][1] = cvt_pk_bf16(sv[qt][10] * inv, sv[qt][11] * inv);
        pf[qt][1][2] = 0u; pf[qt][1][3] = 0u;
    }
    v2u vmf[8];
#pragma unroll
    for (int t = 0; t < 8; ++t) vmf[t] = tr4(&Vs[32 + 4 * (g & 1) + (i >> 2)][16 * t + 4 * (i & 3)]);
#pragma unroll
    for (int t = 0; t < 8; t += 2) {
        unsigned pk[2][2][2];
#pragma unroll
        for (int tt = 0; tt < 2; ++tt) {
            const bf16x8 vf0 = vfx[t + tt];
            const bf16x8 vf1 = as_frag(v4u{vmf[t + tt][0], vmf[t + tt][1], 0u, 0u});
#pragma unroll
            for (int qt = 0; qt < 2; ++qt) {
                f32x4 o = {};
                o = __builtin_amdgcn_mfma_f32_16x16x32_bf16(vf0, as_frag(pf[qt][0]), o, 0, 0, 0);
                o = __builtin_amdgcn_mfma_f32_16x16x32_bf16(vf1, as_frag(pf[qt][1]), o, 0, 0, 0);
                pk[qt][tt][0] = cvt_pk_bf16(o[0], o[1]);
                pk[qt][tt][1] = cvt_pk_bf16(o[2], o[3]);
            }
        }
#pragma unroll
        for (int qt = 0; qt < 2; ++qt) {
            auto r0 = __builtin_amdgcn_permlane16_swap(pk[qt][0][0], pk[qt][1][0], false, false);
            auto r1 = __builtin_amdgcn_permlane16_swap(pk[qt][0][1], pk[qt][1][1], false, false);
            v4u o = {r0[0], r1[0], r0[1], r1[1]};
            st_sc1s(rsV, (t >> 1) * 1024 + ((g & 1) * 2 + (g >> 1)) * 256 + i * 16, xq + qt * 4096, o);
        }
    }
    pend = seen > (unsigned)(sn + 1) && ready ? s : -1;
    if (pend < 0) { asm volatile("s_waitcnt vmcnt(0)" ::: "memory"); count(s); }
}

__device__ __forceinline__ void tile_stream(const ChainArgs& a, const u16* X, unsigned long long todo, const int xc, unsigned& tau, const bool early, const int nw, u16* sm8 WEVP) {
    constexpr int K = DM, LDB = HD;
    const int tid = opaque_tid(), w = tid >> 6, l = tid & 63, i = l & 15, g = l >> 4;
    const int wr = w >> 2, wc = w & 3;
    const unsigned lds0 = (unsigned)(uintptr_t)(__attribute__((address_space(3))) u16*)sm8;
    const unsigned wu = __builtin_amdgcn_readfirstlane(w);
    const unsigned ldsw = lds0 + wu * 2048;
    unsigned voffA, voffB;
    { int row = 16 * w + (l >> 3), c = l & 7; voffA = (unsigned)(row * K + 8 * (c ^ (row & 7))) * 2u; }
    { int row = 8 * w + g, c = i; voffB = (unsigned)(row * LDB + 8 * (c ^ (bswz2<128>(row) << 1))) * 2u; }
    const unsigned voffA2 = voffA + 8u * K * 2u - 1024u, voffB2 = voffB + 4u * LDB * 2u - 1024u;
    auto st2 = [&](const unsigned v0, const unsigned v1, const char* p, const unsigned d) {
        unsigned keep;
        asm volatile("s_mov_b32 %0, m0\n\ts_mov_b32 m0, %1\n\ts_nop 0\n\tglobal_load_lds_dwordx4 %2, %4\n\tglobal_load_lds_dwordx4 %3, %4 offset:1024\n\ts_mov_b32 m0, %0"
                     : "=&s"(keep) : "s"(d), "v"(v0), "v"(v1), "s"(p) : "memory");
    };
    auto stA = [&](int b, int h, const char* p) {
        st2(voffA, voffA2, p + (size_t)(h * 128) * K * 2, ldsw + (2 * b + h) * 16384);
    };
    auto stB = [&](int b, int h, const char* p) {
        st2(voffB, voffB2, p + h * 2048, ldsw + 65536 + (2 * b + h) * 16384);
    };
    constexpr int SA = 128, SB = 64 * LDB * 2;
    auto a_of = [&](const unsigned t) { return (const char*)(X + (size_t)((t >> 4) & 7) * 256 * DM); };
    auto b_of = [&](const unsigned t) { const int kind = t & 7; return (const char*)((kind < 2 ? a.Wk : (kind < 4 ? a.Wv : a.Wq)) + hcol(4 * xc + (kind & 1) * 2)); };
    auto c_of = [&](const unsigned t) { const int kind = t & 7; return ((kind & 6) == 2 ? VX_OFF - KX_OFF : 0) + (int)((t >> 4) & 7) * 4194304 + blk_off(0, 4 * xc + (kind & 1) * 2, kind < 4 ? tau : tau ^ 1u); };
    auto publish = [&](const unsigned t) {
        const int kind = t & 7, ln = lane_now();
        if (wu == 0 && ln < (kind < 4 ? 1 : 2))
            __hip_atomic_store((gu32*)(a.sync + ((kind < 4 ? SY_KV : SY_Q) + ((t >> 4) & 7) * 8 + xc) * 32 + (kind < 4 ? kind : 2 * kind - 8 + ln)), 16u, __ATOMIC_RELAXED, __HIP_MEMORY_SCOPE_AGENT);
    };
    const char* smc = (const char*)sm8;
    int aoff[2], boff[2];
#pragma unroll
    for (int ks = 0; ks < 2; ++ks) aoff[ks] = (wr * 64 + i) * 128 + (((ks * 4 + g) ^ (i & 7)) << 4);
#pragma unroll
    for (int n = 0; n < 2; ++n) { int row = 8 * g + (i >> 2); boff[n] = 65536 + row * 256 + (((wc * 2 + n) ^ bswz2<128>(row)) << 5) + 8 * (i & 3); }
    f32x4 acc[2][2][4][2] = {};
    bf16x8 At[4][2], B0[2][2], B1[2][2];
#define K8_LDA(b, h) _Pragma("unroll") for (int m = 0; m < 4; ++m) _Pragma("unroll") for (int ks = 0; ks < 2; ++ks) \
        At[m][ks] = *(const bf16x8*)(smc + aoff[ks] + (2 * (b) + (h)) * 16384 + m * 2048)
#define K8_LDB(dst, b, h) _Pragma("unroll") for (int n = 0; n < 2; ++n) _Pragma("unroll") for (int ks = 0; ks < 2; ++ks) { \
        const char* p_ = smc + boff[n] + (2 * (b) + (h)) * 16384 + ks * 8192; \
        v4i16 lo_ = __builtin_amdgcn_ds_read_tr16_b64_v4i16((__attribute__((address_space(3))) v4i16*)p_); \
        v4i16 hi_ = __builtin_amdgcn_ds_read_tr16_b64_v4i16((__attribute__((address_space(3))) v4i16*)(p_ + 1024)); \
        dst[n][ks] = bf16x8{lo_[0], lo_[1], lo_[2], lo_[3], hi_[0], hi_[1], hi_[2], hi_[3]}; }
#define K8_MMA(ai, bj, Bt) do { __builtin_amdgcn_s_setprio(1); \
        _Pragma("unroll") for (int m = 0; m < 4; ++m) _Pragma("unroll") for (int n = 0; n < 2; ++n) _Pragma("unroll") for (int ks = 0; ks < 2; ++ks) \
            acc[ai][bj][m][n] = __builtin_amdgcn_mfma_f32_16x16x32_bf16(Bt[n][ks], At[m][ks], acc[ai][bj][m][n], 0, 0, 0); \
        __builtin_amdgcn_s_setprio(0); } while (0)
    const rsrc_t rsC = __builtin_amdgcn_make_buffer_rsrc((void*)(arena(a.slots) + KX_OFF), 0, FX_OFF - KX_OFF, 0x00020000);
#define K8_EPI(ai, bj, cb) do { const int ln_ = lane_now(); \
        const int vo_ = (int)(wu & 3) * 1024 + (((ln_ >> 4) & 1) * 2 + (ln_ >> 5)) * 256 + (ln_ & 15) * 16; \
        _Pragma("unroll") for (int m = 0; m < 4; ++m) { \
            const f32x4 c0 = acc[ai][bj][m][0], c1 = acc[ai][bj][m][1]; \
            auto r0 = __builtin_amdgcn_permlane16_swap(cvt_pk_bf16(c0[0], c0[1]), cvt_pk_bf16(c1[0], c1[1]), false, false); \
            auto r1 = __builtin_amdgcn_permlane16_swap(cvt_pk_bf16(c0[2], c0[3]), cvt_pk_bf16(c1[2], c1[3]), false, false); \
            const int sg_ = 4 * ai + 2 * (int)(wu >> 2) + (m >> 1); \
            st_sc1s(rsC, vo_, ((cb) ^ (ai * 8192)) + sg_ * 524288 + bj * 16384 + (m & 1) * 4096, v4u{r0[0], r1[0], r0[1], r1[1]}); \
            acc[ai][bj][m][0] = ZERO4; acc[ai][bj][m][1] = ZERO4; } } while (0)
    constexpr int nt = K / 64;
    unsigned cur = (unsigned)todo & 255u, prev = 0u;
    const char* pA = a_of(cur); const char* pB = b_of(cur);
    if (early) touch((const char*)X + 524288 + (size_t)(nw * 128 + (tid >> 2)) * 128, lds0 + 163840 - 256);
    stB(0, 0, pB); stB(0, 1, pB); stA(0, 0, pA); stA(0, 1, pA);
    stB(1, 0, pB + SB); stB(1, 1, pB + SB); stA(1, 0, pA + SA);
    K8_WV(8); K8_BAR;
    if (wu >= 4) K8_BAR;
#define K8_GO(ai) __builtin_amdgcn_s_waitcnt(0xC07F); K8_BAR; K8_MMA(ai, 0, B0); K8_MMA(ai, 1, B1); K8_SCHED; K8_BAR
    int cbp = 0;
    for (;;) {
        todo >>= 8;
        const unsigned nxt = todo != 0ull ? (unsigned)todo & 255u : cur;
        int cb = c_of(cur);
#pragma nounroll
        for (int t = 0; t < nt; t += 2) {
            if (__builtin_expect(early && prev == 0u && t == 10, 0))
                touch((const char*)X + 3 * 524288 + (size_t)(nw * 320 + min(TIDX, 319)) * 128, lds0 + 163840 - 256);
            if (__builtin_expect(early && prev == 0u && t == nt - 4, 0)) {
                tau = __builtin_amdgcn_readfirstlane((unsigned)boot_wait(a.boot) & 255u);
                cb = c_of(cur);
            }
            const bool first = t == 0 && prev != 0u, last = t == nt - 2;
            if (t == 2 && prev != 0u) { publish(prev); WEV(3) }
            K8_LDB(B0, 0, 0); K8_LDA(0, 0); K8_LDB(B1, 0, 1); K8_SCHED; stA(1, 1, pA + SA);
            pA = last ? a_of(nxt) : pA + 2 * SA; pB = last ? b_of(nxt) : pB + 2 * SB;
            if (first) { K8_EPI(1, 0, cbp); K8_EPI(1, 1, cbp); K8_WV(24); } else K8_WV(8);
            K8_GO(0);
            K8_LDA(0, 1); K8_SCHED; stB(0, 0, pB); stB(0, 1, pB); stA(0, 0, pA);
            if (first) K8_WV(24); else K8_WV(8);
            K8_GO(1);
            K8_LDB(B0, 1, 0); K8_LDA(1, 0); K8_LDB(B1, 1, 1); K8_SCHED; stA(0, 1, pA);
            if (first) K8_WV(16); else K8_WV(8);
            K8_GO(0);
            K8_LDA(1, 1); K8_SCHED; stB(1, 0, pB + SB); stB(1, 1, pB + SB); stA(1, 0, pA + SA);
            if (last) { K8_EPI(0, 0, cb); K8_EPI(0, 1, cb); K8_WV(16); } else K8_WV(8);
            K8_GO(1);
        }
        WEV(2)
        prev = cur; cbp = cb; cur = nxt;
        if (todo == 0ull) break;
    }
    K8_EPI(1, 0, cbp); K8_EPI(1, 1, cbp);
    K8_WV(0);
    if (wu < 4) K8_BAR;
    K8_BAR;
    publish(prev);
    WEV(3)
}

__device__ __forceinline__ void oproj_squad(const ChainArgs& a, u16* out, const int ct, const int rh, const int xc, const unsigned tau, char* smem) {
    const int tid = opaque_tid(), w = tid >> 6, l = tid & 63, i = l & 15, g = l >> 4;
    const int wu = __builtin_amdgcn_readfirstlane(w);
    bf16x8 wf[2][16];
    {
        u16* stg = (u16*)(smem + wu * 10240);
#pragma unroll
        for (int ch = 0; ch < 4; ++ch) {
            const u16* src = a.Wo + (size_t)hcol(4 * wu + ch) * DM + 32 * ct;
            v4u raw[8];
#pragma unroll
            for (int e = 0; e < 8; ++e) { int idx = l + 64 * e, r = idx >> 2, q4 = idx & 3; raw[e] = *(const v4u*)(src + (size_t)r * DM + 8 * q4); }
#pragma unroll
            for (int e = 0; e < 8; ++e) { int idx = l + 64 * e, r = idx >> 2, q4 = idx & 3; *(v4u*)(stg + r * 40 + 8 * q4) = raw[e]; }
            __builtin_amdgcn_s_waitcnt(0xC07F);
#pragma unroll
            for (int jj = 0; jj < 2; ++jj)
#pragma unroll
                for (int ks = 0; ks < 4; ++ks) {
                    const u16* p = stg + (32 * ks + 8 * g + (i >> 2)) * 40 + 16 * jj + 4 * (i & 3);
                    wf[jj][4 * ch + ks] = tr_frag(p, p + 4 * 40);
                }
            __builtin_amdgcn_s_waitcnt(0xC07F);
        }
    }
    const rsrc_t rsO = __builtin_amdgcn_make_buffer_rsrc((void*)(arena(a.slots) + VX_OFF), 0, 32 << 20, 0x00020000);
    float* red = (float*)(smem + 81920);
    const int voff = rh * 4096 + l * 16;
    volatile unsigned* rdy = (volatile unsigned*)(smem + 81920 + 2 * 8 * 16 * 36 * 4);
    unsigned ready = 0;
    bool pre = false;
    v4u xf[16];
    for (int s = SSEG; s < NSEG - 1; ++s) {
        if (!(ready & 1u)) {
            if (wu == 0) {
                const unsigned* q = a.sync + (SY_OXS + (s - SSEG) * 8 + xc) * 32;
#pragma nounroll
                for (int spin = 0;; ++spin) {
                    asm volatile("" ::: "memory");
                    if (ld_flag(q) != 0u) break;
                    if (spin > 20000) { __hip_atomic_store((gu32*)a.tmo, 7000u, __ATOMIC_RELAXED, __HIP_MEMORY_SCOPE_AGENT); break; }
                    if (s == SSEG) __builtin_amdgcn_s_sleep(64); else if (s < NSEG - 5) __builtin_amdgcn_s_sleep(16); else __builtin_amdgcn_s_sleep(SQ_SF);
                }
            }
            asm volatile("" ::: "memory");
            __builtin_amdgcn_s_barrier();
            asm volatile("" ::: "memory");
        }
        if (!pre) {
#pragma unroll
            for (int ks = 0; ks < 16; ++ks) xf[ks] = ld_sc1s(rsO, voff + (ks & 3) * 1024, blk_off(s, 4 * wu + (ks >> 2), tau) ^ 8192);
        }
        unsigned nf = 0;
        if (wu == 0) nf = ld_flag(a.sync + (SY_OXS + (s + 1 - SSEG + (l & 1)) * 8 + xc) * 32);
        pre = (ready & 2u) != 0u;
        f32x4 acc[2] = {};
        if (pre) {
#pragma unroll
            for (int ks = 0; ks < 16; ++ks) {
#pragma unroll
                for (int jj = 0; jj < 2; ++jj) acc[jj] = __builtin_amdgcn_mfma_f32_16x16x32_bf16(wf[jj][ks], as_frag(xf[ks]), acc[jj], 0, 0, 0);
                K8_SCHED;
                xf[ks] = ld_sc1s(rsO, voff + (ks & 3) * 1024, blk_off(s + 1, 4 * wu + (ks >> 2), tau) ^ 8192);
                K8_SCHED;
            }
        } else {
#pragma unroll
            for (int ks = 0; ks < 16; ++ks)
#pragma unroll
                for (int jj = 0; jj < 2; ++jj) acc[jj] = __builtin_amdgcn_mfma_f32_16x16x32_bf16(wf[jj][ks], as_frag(xf[ks]), acc[jj], 0, 0, 0);
        }
        float* rp = red + (s & 1) * (8 * 16 * 36);
#pragma unroll
        for (int jj = 0; jj < 2; ++jj) *(f32x4*)&rp[(wu * 16 + i) * 36 + 16 * jj + 4 * g] = acc[jj];
        if (wu == 0) rdy[s & 1] = (unsigned)__ballot(nf != 0u) & 3u;
        K8_WL(0); K8_BAR;
        ready = __builtin_amdgcn_readfirstlane(rdy[s & 1]) | (pre ? 1u : 0u);
        if (tid >= 64 && tid < 192) {
            const int t = tid - 64, row = t >> 3, q4 = t & 7;
            f32x4 sv_[8];
#pragma unroll
            for (int e = 0; e < 8; ++e) sv_[e] = *(const f32x4*)&rp[(e * 16 + row) * 36 + 4 * q4];
            const f32x4 s0 = ((sv_[0] + sv_[1]) + (sv_[2] + sv_[3])) + ((sv_[4] + sv_[5]) + (sv_[6] + sv_[7]));
            v2u o; o[0] = cvt_pk_bf16(s0[0], s0[1]); o[1] = cvt_pk_bf16(s0[2], s0[3]);
            out8(out + (size_t)(s * SEG + 16 * rh + row) * OUTW + 32 * ct + 4 * q4, o);
        }
    }
}

__device__ __forceinline__ void kv_stream(const ChainArgs& a, const u16* __restrict__ X, const u16* __restrict__ B, char* C, const int head, const int xc, const int wk, unsigned& tau, char* smem) {
    const int tid = opaque_tid(), w = tid >> 6, l = tid & 63, i = l & 15, g = l >> 4;
    const int wu = __builtin_amdgcn_readfirstlane(w), kh = wu >> 2, cq = wu & 3;
    const unsigned lds0 = (unsigned)(uintptr_t)(__attribute__((address_space(3))) char*)smem;
    unsigned voA, voB;
    { const int row = (wu & 1) * 8 + (l >> 3), c = l & 7; voA = (unsigned)(row * DM + 8 * (c ^ (row & 7))) * 2u + (wu >> 1) * 512; }
    { const int row = tid >> 4, c = tid & 15; voB = (unsigned)(row * HD + 8 * (c ^ (bswz2<128>(row) << 1))) * 2u; }
    auto stB = [&](int ch, int slot) {
#pragma unroll
        for (int e = 0; e < 4; ++e) glds16s(voB, (const char*)B + (size_t)(4 * ch + e) * 32 * HD * 2, lds0 + 65536 + slot * 32768 + e * 8192 + wu * 1024);
    };
    const unsigned ldA = lds0 + (wu >> 1) * 8192 + (wu & 1) * 1024;
    auto stP = [&](const char* p, const unsigned d) {
        unsigned keep;
        asm volatile("s_mov_b32 %0, m0\n\ts_mov_b32 m0, %1\n\ts_nop 0\n\tglobal_load_lds_dwordx4 %3, %2 offset:-4096\n\tglobal_load_lds_dwordx4 %4, %2 offset:-2048\n\t"
                     "global_load_lds_dwordx4 %5, %2\n\tglobal_load_lds_dwordx4 %6, %2 offset:2048\n\ts_mov_b32 m0, %0"
                     : "=&s"(keep) : "s"(d), "s"(p), "v"(voA + 8192u), "v"(voA + 6272u), "v"(voA + 4352u), "v"(voA + 2432u) : "memory");
    };
    auto srcA = [&](int h) { return (const char*)X + (size_t)min(h, 2 * KVS - 1) * 32768 - 4096; };
    auto stA = [&](int h) { stP(srcA(h), ldA + (h & 3) * 32768 + 4096); };
    stB(0, 0); stA(0); stA(1); stB(1, 1);
    touch((const char*)X + 65536 + min(wk * 512 + tid, (KVS - 1) * 512 - 1) * 128, lds0 + 131072 + wu * 1024);
    stB(2, 2);
    bf16x8 bf[16][2], af[8];
    f32x4 acc[2][2] = {}, keep[2];
    const int rb = 64 * kh + 8 * g + (i >> 2), sw = bswz2<128>(rb);
    const char* bp = smem + 65536 + rb * 256 + 8 * (i & 3);
    const char* sa0 = smem + kh * 2048 + i * 128 + 16 * (g ^ (i & 7));
    const char* sa1 = smem + kh * 2048 + i * 128 + 16 * ((4 + g) ^ (i & 7));
#define KV_AF(slot, q) *(const bf16x8*)((((q) & 1) ? sa1 : sa0) + (slot) * 32768 + ((q) >> 1) * 4096)
#pragma unroll
    for (int ch = 0; ch < 8; ++ch) {
        if (ch == 0) K8_WV(9); else if (ch < 6) K8_WV(8); else if (ch == 6) K8_WV(4); else K8_WV(0);
        K8_BAR;
#pragma unroll
        for (int e = 0; e < 4; ++e) { const char* p_ = bp + (ch % 3) * 32768 + (e >> 1) * 8192 + 32 * ((2 * cq + (e & 1)) ^ sw); bf[2 * ch + (e >> 1)][e & 1] = tr_frag(p_, p_ + 1024); }
#pragma unroll
        for (int e = 0; e < 4; ++e) af[e] = KV_AF(e >> 1, 2 * ch + (e & 1));
        K8_WL(0); K8_SCHED; K8_BAR;
        if (ch < 5) stB(ch + 3, ch % 3);
        if (ch == 7) { stA(2); stA(3); }
#pragma unroll
        for (int e = 0; e < 8; ++e) acc[e >> 2][e & 1] = __builtin_amdgcn_mfma_f32_16x16x32_bf16(bf[2 * ch + ((e >> 1) & 1)][e & 1], af[e >> 1], acc[e >> 2][e & 1], 0, 0, 0);
    }
    float* red = (float*)(smem + 131072) + l * 4;
#pragma unroll
    for (int p = 0; p < 2; ++p) { *(f32x4*)(red + (p * 8 + wu) * 256) = kh ? acc[p][0] : acc[p][1]; keep[p] = kh ? acc[p][1] : acc[p][0]; }
    unsigned long long tag = 0;
    if (wu == 4) { tag = boot_wait(a.boot); *(volatile unsigned*)(smem + 163840 - 512) = (unsigned)tag & 255u; }
    const rsrc_t rc = __builtin_amdgcn_make_buffer_rsrc((void*)C, 0, KVS * 524288, 0x00020000);
    const int vo = cq * 1024 + (2 * kh + (g >> 1)) * 256 + i * 16 + (g & 1) * 8;
    auto put = [&](const f32x4 o, const int so) {
        const v2u pk = {cvt_pk_bf16(o[0], o[1]), cvt_pk_bf16(o[2], o[3])};
        asm volatile("buffer_store_dwordx2 %0, %1, %2, %3 offen sc1" :: "v"(pk), "v"(vo), "s"(rc), "s"(so) : "memory");
    };
    K8_WL(0); K8_WV(0); K8_BAR;
    tau = __builtin_amdgcn_readfirstlane(*(volatile unsigned*)(smem + 163840 - 512));
#pragma unroll
    for (int p = 0; p < 2; ++p) put(keep[p] + *(const f32x4*)(red + (p * 8 + (wu ^ 4)) * 256), blk_off(0, head, tau) + p * 4096);
    stA(4);
    K8_WV(4); K8_BAR;
    if (tid == 256) __hip_atomic_store((gu64*)(a.boot + 256 + 16 * xc + wk), tag, __ATOMIC_RELAXED, __HIP_MEMORY_SCOPE_AGENT);
    auto stages = [&](auto kc) {
        constexpr int KH = decltype(kc)::value;
        if (KH) K8_BAR;
        f32x4 ac[2] = {ZERO4, ZERO4}, kp = ZERO4;
        int h_first = 2, so = OOB;
        asm volatile("" : "+s"(h_first));
        const char* pn = srcA(5);
        unsigned dn = ldA + 32768 + 4096;
        int r0 = (int)(sa0 - smem) + 2 * 32768, r1 = (int)(sa1 - smem) + 2 * 32768;
        for (int h = h_first; h < 2 * KVS + 4; ++h) {
            const f32x4 oth = *(const f32x4*)(red + (wu ^ 4) * 256);
            *(f32x4*)(red + wu * 256) = ac[KH ^ 1];
            bf16x8 ax[16];
#pragma unroll
            for (int q = 0; q < 16; ++q) ax[q] = *(const bf16x8*)(smem + ((q & 1) ? r1 : r0) + (q >> 1) * 4096);
            K8_SCHED;
            stP(pn, dn);
            put((KH ? ac[1] : kp) + oth, so);
            kp = ac[0];
            __builtin_amdgcn_s_waitcnt(0xC07F);
            K8_WV(10);
            if (__builtin_expect(h == 5, 0)) K8_WV(0);
            K8_BAR;
            if (KH && (0xAAA20u >> h) & 1u && tid == 256)
                __hip_atomic_store((gu32*)(a.sync + (SY_KV0 + (h / 2 - (h == 5 ? 1 : 2)) * 8 + xc) * 32 + wk), 16u, __ATOMIC_RELAXED, __HIP_MEMORY_SCOPE_AGENT);
            if (__builtin_expect(h < 2 * KVS, 1)) {
                __builtin_amdgcn_s_setprio(1);
#pragma unroll
                for (int q = 0; q < 16; ++q)
#pragma unroll
                    for (int n = 0; n < 2; ++n) ac[n] = __builtin_amdgcn_mfma_f32_16x16x32_bf16(bf[q][n], ax[q], q ? ac[n] : ZERO4, 0, 0, 0);
                __builtin_amdgcn_s_setprio(0);
            }
            {
                const int hs = h - 1 + KH;
                so = blk_off(hs >> 1, head, tau) + (hs & 1) * 4096;
                asm volatile("" : "+s"(so));
                so = hs > 1 && hs < 2 * KVS ? so : OOB;
                pn = srcA(h + 4); dn = ldA + (h & 3) * 32768 + 4096;
                r0 = (r0 + 32768) & 131071; r1 = (r1 + 32768) & 131071;
                asm volatile("" : "+s"(so), "+s"(pn), "+s"(dn), "+v"(r0), "+v"(r1));
            }
            K8_BAR;
        }
        if (!KH) K8_BAR;
    };
    if (kh) stages(std::integral_constant<int, 1>{}); else stages(std::integral_constant<int, 0>{});
    K8_WV(0);
}

struct MegaArgs {
    ChainArgs ch;
    const u16* x; u16* out;
    const u16* bx; const u16* wr; const u16* wi;
};
constexpr int MEGA_LDS = 163840;
#define WSTAMP(n)
__global__ __launch_bounds__(512, 2) void mega_kernel(MegaArgs m) {
    extern __shared__ __attribute__((aligned(256))) char chsm[];
    const int b = BIDX, xc = (b & 3) + 4 * (b >> 7), sl = (b >> 3) & 15;
    if ((b & 4) == 0) { chain_body(m.ch, xc, sl, chsm); return; }
    const ChainArgs& a = m.ch;
    const int wk = sl, tid = TIDX;
    char* ar = arena(a.slots);
    unsigned tauw = 0;
    WSTAMP(0)
    if (wk < 8) kv_stream(a, m.x, (wk < 4 ? a.Wk : a.Wv) + hcol(4 * xc + (wk & 3)), ar + (wk < 4 ? KX_OFF : VX_OFF), 4 * xc + (wk & 3), xc, wk, tauw, chsm);
    WSTAMP(8) WEV(1)
    {
        const int p = wk & 1, q = wk & 3;
        const unsigned long long todo = wk < 2 ? 0x84u + p : wk < 4 ? 0x74704240u + 0x01010101u * p : wk < 8 ? 0ull : wk < 10 ? 0x60503010ull + 0x01010101ull * q
                                      : wk < 12 ? 0x7060503010ull + 0x0101010101ull * q : (wk < 14 ? 0x54341420u : 0x64442422u) + 0x01010101u * p;
        __syncthreads();
        if (todo != 0ull) tile_stream(a, m.x, todo, xc, tauw, wk >= 8, xc * 8 + (wk & 7), (u16*)chsm WEVA);
    }
    WSTAMP(2)
    if (wk < 2) {
        unsigned* mbox = (unsigned*)(chsm + 131072);
        if (tid < 5) mbox[tid] = tid ? 2u : 0u;
        __syncthreads();
        const int w = __builtin_amdgcn_readfirstlane(tid >> 6), h = 4 * xc + (w & 3);
        const rsrc_t rsQ = __builtin_amdgcn_make_buffer_rsrc((void*)(ar + KX_OFF), 0, S_LEN * HD * 4, 0x00020000);
        const rsrc_t rsK = __builtin_amdgcn_make_buffer_rsrc((void*)(ar + KX_OFF), 0, S_LEN * HD * 4, 0x00020000);
        const rsrc_t rsV = __builtin_amdgcn_make_buffer_rsrc((void*)(ar + VX_OFF), 0, S_LEN * HD * 4, 0x00020000);
        const rsrc_t rsS = slot_rsrc(a.slots);
        u16 (*Vs)[VP] = (u16 (*)[VP])(chsm + w * (40 * VP * 2));
        const unsigned tau = tauw;
        int pend = -1;
        bool ready = false;
        for (int s = 2 * wk + (w >> 2), sn; s < NSEG - 1; s = sn) {
            const int band = s >> 3;
            unsigned n = 0;
            if (lane_now() == 0) n = atomicAdd(mbox + 1 + (w & 3), 1u);
            n = __builtin_amdgcn_readfirstlane(n);
            sn = 2 * wk + (int)(n & 1u) + 4 * (int)(n >> 1);
            if (sn - s > 4) __builtin_amdgcn_s_setprio(2); else __builtin_amdgcn_s_setprio(0);
            if (s < 4) wait_ge<32>(a.sync + (SY_KV0 + (KVS - 1) * 8 + xc) * 32 + (tid & 7), 16u, a.tmo, 4100u);
            if (!ready) {
                if (band > 0) wait_ge<32>(a.sync + (SY_KV + band * 8 + xc) * 32 + (tid & 3), 1u, a.tmo, 4100u);
                wait_ge<32>(a.sync + (SY_Q + band * 8 + xc) * 32 + (tid & 3), 1u, a.tmo, 4200u);
            }
            xattn_unit(a, rsQ, rsK, rsV, rsS, s, sn, h, Vs, mbox, tau, pend, ready);
        }
        return;
    }
    if (wk >= 4 && wk < 8) {
        const int fw = (wk - 4) * 8 + xc;
        const rsrc_t rsF = __builtin_amdgcn_make_buffer_rsrc((void*)(ar + FX_OFF), 0, 16 << 20, 0x00020000);
        const rsrc_t rsW = __builtin_amdgcn_make_buffer_rsrc((void*)(ar + WT_OFF), 0, 10 << 20, 0x00020000);
        const unsigned tw = tauw ^ 1u;
        {
            const int tq = opaque_tid();
            fft_rows_fwd(m.bx, rsF, tw, 2 * fw + (tq >> 8), chsm + (tq >> 8) * 36864, tq & 255, (const char*)m.wr, (const char*)m.wi, (unsigned)(uintptr_t)(__attribute__((address_space(3))) char*)chsm + 163840 - 256);
        }
        for (int t = fw; t < 64; t += 32) {
            __syncthreads();
            filt_transpose(m.wr, m.wi, rsW, tw, t, chsm, opaque_tid());
        }
        wg_publish(a.sync, SY_DONE, 32u, 8);
        WSTAMP(3) WEV(4)
        wg_wait_acquire<16>(a.sync + (SY_DONE + xc) * 32, a.tmo, 6000u);
        {
            const int tq = opaque_tid();
            fft_cols(rsF, rsW, tw, 2 * fw + (tq >> 8), 4, chsm + (tq >> 8) * 36864, tq & 255);
        }
        wg_publish(a.sync, SY_DONE + 8, 32u, 8);
        WSTAMP(4) WEV(5)
    }
    if (wk >= 4 && wk < 10) {
        const rsrc_t rsF = __builtin_amdgcn_make_buffer_rsrc((void*)(ar + FX_OFF), 0, 16 << 20, 0x00020000);
        wg_wait_acquire<16>(a.sync + (SY_DONE + 8 + xc) * 32, a.tmo, 6001u);
        const int tq = opaque_tid();
        fft_rows_inv(rsF, tauw ^ 1u, m.out, 2 * ((wk & 3) * 8 + xc) + (tq >> 8) + (wk < 8 ? 0 : 192), wk < 8 ? 64 : 32, wk < 8 ? 3 : 2, chsm + (tq >> 8) * 36864, tq & 255);
        WSTAMP(5) WEV(6)
    }
    if (wk >= 12) {
        const int r = 2 * (wk - 12);
        wg_wait_acquire<16>(a.sync + (SY_OX + r * 8 + xc) * 32, a.tmo, 5000u);
        wg_wait_acquire<16>(a.sync + (SY_OX + (r + 1) * 8 + xc) * 32, a.tmo, 5000u);
        WEV(7)
        gemm_tile<256>((u16*)chsm, ar + VX_OFF, 4 * r, tauw, a.Wo + xc * 128, m.out + (size_t)r * 128 * OUTW + xc * 128);
        WEV(8)
    }
    if (wk < 4) {
        const int r = wk + 6;
        wg_wait_acquire<16>(a.sync + (SY_OX + r * 8 + xc) * 32, a.tmo, 5000u);
        WEV(7)
        gemm_tile<128>((u16*)chsm, ar + VX_OFF, 4 * r, tauw, a.Wo + xc * 128, m.out + (size_t)r * 128 * OUTW + xc * 128);
        WEV(8)
        WSTAMP(9)
    }
    if (wk >= 4 && wk < 12) {
        __syncthreads();
        oproj_squad(a, m.out, (wk - 4) * 4 + (xc & 3), xc >> 2, xc, tauw, chsm);
        WSTAMP(7)
    }
}

#if defined(__HIP_DEVICE_COMPILE__)
#pragma clang attribute pop
#endif
extern "C" void kernel_launch(void* const* d_in, const int* in_sizes, int n_in,
                              void* d_out, int out_size, void* d_ws, size_t ws_size,
                              hipStream_t stream) {
    char* ws = (char*)d_ws;
    size_t off = 0;
    auto alloc = [&](size_t bytes) { void* p = ws + off; off += (bytes + 255) & ~(size_t)255; return p; };
    char* slots = (char*)alloc((size_t)ARENA_BYTES + (2u << 20));
    static_assert((SY_WORDS + 2) * 128 <= 131072, "sync block");
    unsigned* sync = (unsigned*)alloc(131072);
    unsigned long long* boot = (unsigned long long*)alloc(4096);

    MegaArgs m;
    m.ch.mem0 = (const u16*)d_in[1]; m.ch.Wq = (const u16*)d_in[2]; m.ch.Wk = (const u16*)d_in[3]; m.ch.Wv = (const u16*)d_in[4]; m.ch.Wo = (const u16*)d_in[5];
    m.ch.out = (u16*)d_out; m.ch.slots = slots; m.ch.tmo = sync + SY_WORDS * 32;
    m.ch.stamps = (unsigned long long*)(ws + (240ull << 20));
    m.ch.sync = sync; m.ch.boot = boot;
    m.x = (const u16*)d_in[0]; m.out = (u16*)d_out;
    m.bx = (const u16*)d_in[7]; m.wr = (const u16*)d_in[8]; m.wi = (const u16*)d_in[9];
    (void)hipFuncSetAttribute((const void*)mega_kernel, hipFuncAttributeMaxDynamicSharedMemorySize, MEGA_LDS);
    mega_kernel<<<256, 512, MEGA_LDS, stream>>>(m);
}
```

```cpp
#if defined(__HIP_DEVICE_COMPILE__)
#pragma clang attribute push (__attribute__((target("no-packed-fp32-ops,no-load-store-opt"))), apply_to = function)
#endif
#include <hip/hip_runtime.h>
#include <hip/hip_bf16.h>
#include <stdint.h>
#include <type_traits>

typedef unsigned short u16;
#define TIDX ((int)__builtin_amdgcn_workitem_id_x())
#define BIDX ((int)__builtin_amdgcn_workgroup_id_x())
__device__ __forceinline__ int opaque_tid() { int t = TIDX; asm volatile("" : "+v"(t)); return t; }
__device__ __forceinline__ int lane_now() { int t; asm volatile("v_mbcnt_lo_u32_b32 %0, -1, 0\n\tv_mbcnt_hi_u32_b32 %0, -1, %0" : "=v"(t)); return t; }
typedef __attribute__((ext_vector_type(8))) short bf16x8;
typedef __attribute__((ext_vector_type(4))) float f32x4;

constexpr int S_LEN = 2048, DM = 1024, NH = 32, DH = 128, MM = 8, SEG = 32;
constexpr int NSEG = S_LEN / SEG;
constexpr int HD = NH * DH;
constexpr int OUTW = 2048;

typedef unsigned v4u __attribute__((ext_vector_type(4)));
typedef unsigned v2u __attribute__((ext_vector_type(2)));
typedef short v4i16 __attribute__((ext_vector_type(4)));
typedef __amdgpu_buffer_rsrc_t rsrc_t;
#define AUX_SC1 16
__device__ __forceinline__ v4u ld_sc1s(rsrc_t rs, int voff, int soff) { return __builtin_amdgcn_raw_buffer_load_b128(rs, voff, soff, AUX_SC1); }
__device__ __forceinline__ void st_sc1s(rsrc_t rs, int voff, int soff, v4u v) { __builtin_amdgcn_raw_buffer_store_b128(v, rs, voff, soff, AUX_SC1); asm volatile("s_nop 1" :: "v"(v) : "memory"); }
__device__ __forceinline__ void st8_sc1s(rsrc_t rs, int voff, int soff, v2u v) { __builtin_amdgcn_raw_buffer_store_b64(v, rs, voff, soff, AUX_SC1); }
__device__ __forceinline__ void st_sc1(rsrc_t rs, int off, v4u v) { __builtin_amdgcn_raw_buffer_store_b128(v, rs, off, 0, AUX_SC1); }
__device__ __forceinline__ void out8(void* p, v2u v) { asm volatile("global_store_dwordx2 %0, %1, off sc1" :: "v"(p), "v"(v) : "memory"); }
typedef __bf16 bf16x2_t __attribute__((ext_vector_type(2)));
typedef float f32x2_t __attribute__((ext_vector_type(2)));
__device__ __forceinline__ unsigned cvt_pk_bf16(float lo, float hi) {
    f32x2_t f = {lo, hi};
    return __builtin_bit_cast(unsigned, __builtin_convertvector(f, bf16x2_t));
}
#define SENT 0xFFBFFFBF
__device__ __forceinline__ unsigned umax3(unsigned a, unsigned b, unsigned c) { return max(max(a, b), c); }
__device__ __forceinline__ unsigned mx4(unsigned acc, v4u v) { return umax3(umax3(acc, v[0], v[1]), v[2], v[3]); }
__device__ __forceinline__ bf16x8 as_frag(v4u v) { return __builtin_bit_cast(bf16x8, v); }
__device__ __forceinline__ float xor16_sum(float x) {
    auto r = __builtin_amdgcn_permlane16_swap(__float_as_uint(x), __float_as_uint(x), false, false);
    return __uint_as_float(r[0]) + __uint_as_float(r[1]);
}
__device__ __forceinline__ float xor32_sum(float x) {
    auto r = __builtin_amdgcn_permlane32_swap(__float_as_uint(x), __float_as_uint(x), false, false);
    return __uint_as_float(r[0]) + __uint_as_float(r[1]);
}
__device__ __forceinline__ float xor16_max(float x) {
    auto r = __builtin_amdgcn_permlane16_swap(__float_as_uint(x), __float_as_uint(x), false, false);
    return fmaxf(__uint_as_float(r[0]), __uint_as_float(r[1]));
}
__device__ __forceinline__ float xor32_max(float x) {
    auto r = __builtin_amdgcn_permlane32_swap(__float_as_uint(x), __float_as_uint(x), false, false);
    return fmaxf(__uint_as_float(r[0]), __uint_as_float(r[1]));
}
constexpr int SPIN_LIMIT = 400000;
constexpr int OOB = 0x7FFFFF00;
#define PB 14
#define TD 60
#define PS 46
#define TE0 10
#define PVA 8
#define PV 1
#define PV1 2
#define PV2 11
#define PL 20
#define PF 48
#define PC 31
#define PP 15
#define PKA 28
#define PG0 2
#define PG1 32
#define DLY_U 4
#define DLY_C 4
#define XA_FAST 6
#define XA_D 12
#define SQ_SF 4
#define LW 0
constexpr int NPROBE = 2;
constexpr int M_OFF = 32 * 16384, F_OFF = M_OFF + 2 * 16384, C_OFF = F_OFF + 2 * 16384, REC = C_OFF + 16 * 16384, PERWG = REC / 128, SLOT_BYTES = 34 * 2097152;
static_assert(PERWG == 6656, "arming shares");
__device__ __forceinline__ int rec_base(int s) { return (s >> 1) * 2097152 + (s & 1) * REC; }
__device__ __forceinline__ char* arena(char* slots) { return (char*)(((uintptr_t)slots + 2097151u) & ~(uintptr_t)2097151u); }
constexpr int KX_OFF = SLOT_BYTES, VX_OFF = KX_OFF + (32 << 20), FX_OFF = VX_OFF + (32 << 20), WT_OFF = FX_OFF + (16 << 20), ARENA_BYTES = WT_OFF + (10 << 20);
__device__ __forceinline__ rsrc_t slot_rsrc(char* slots) { return __builtin_amdgcn_make_buffer_rsrc((void*)arena(slots), 0, FX_OFF, 0x00020000); }
__device__ __forceinline__ int blk_off(int seg, int h, unsigned tau) { return seg * 524288 + (((h & 3) >> 1) * 16 + (h >> 2) * 2 + (h & 1)) * 16384 + (int)((tau ^ (unsigned)(seg >> 2)) & 1u) * 8192; }
constexpr int TM_OFF = 124 * 16384;
constexpr int CAL_OFF = 32 * 2097152;
__device__ __forceinline__ int near_half(int s, unsigned tau) { return (int)((tau ^ (unsigned)(s >> 1)) & 1u) * 8192; }
__device__ __forceinline__ int chunk_off(int s, int g, int j, unsigned tau) { return rec_base(s) + (j < 2 ? (g & 3) * 2 + j : 6 + 3 * g + j) * 16384 + near_half(s, tau); }
__device__ __forceinline__ int x_off(int s, int h, unsigned tau) { return chunk_off(s, h >> 2, 2, tau) + (h & 3) * 2048; }
__device__ __forceinline__ int km_off(int s, int h, unsigned tau) { return chunk_off(s, h >> 2, 3, tau) + (h & 3) * 2048; }
__device__ __forceinline__ int m_off(int s, int h, unsigned tau) { return rec_base(s) + M_OFF + (h >> 4) * 16384 + near_half(s, tau) + (h & 15) * 512; }
__device__ __forceinline__ int v_off(int s, int h, unsigned tau) { return chunk_off(s, h >> 2, 4, tau) + (h & 3) * 2048; }
__device__ __forceinline__ int f_off(int s, int g, unsigned tau) { return rec_base(s) + F_OFF + (g >> 2) * 16384 + near_half(s, tau) + (g & 3) * 2048; }
__device__ __forceinline__ int hcol(int h) { return ((h & 3) * 8 + (h >> 2)) * 128; }
constexpr int VP = 144;

struct ChainArgs {
    u16* out;
    const u16* mem0; const u16* Wq; const u16* Wk; const u16* Wv; const u16* Wo;
    char* slots;
    unsigned* tmo;
    unsigned long long* stamps;
    unsigned* sync;
    unsigned long long* boot;
};
constexpr int SY_KV = 0;
constexpr int SY_Q = 64;
constexpr int SY_PROG = 128;
constexpr int SY_OX = 136;
constexpr int SY_DONE = 264;
constexpr int SY_OXS = 472;
constexpr int SY_KV0 = 408;
constexpr int KVS = 8;
constexpr int SY_FLAGS = 680;
constexpr int SY_WORDS = SY_FLAGS + SY_FLAGS / 8;
#define SHLP 4
constexpr int SSEG = 40;
typedef __attribute__((address_space(1))) unsigned gu32;
typedef __attribute__((address_space(1))) unsigned long long gu64;
__device__ __forceinline__ unsigned ld_flag(const unsigned* p) { return __hip_atomic_load((gu32*)p, __ATOMIC_RELAXED, __HIP_MEMORY_SCOPE_AGENT); }
__device__ __forceinline__ unsigned long long boot_tag(const unsigned long long* boot) {
    return __hip_atomic_load((gu64*)(boot + 144), __ATOMIC_RELAXED, __HIP_MEMORY_SCOPE_AGENT) + 1ull;
}
__device__ __forceinline__ unsigned long long boot_wait(const unsigned long long* boot) {
    const int l = TIDX & 63;
    unsigned long long tag = 0, x = 0, y = 0;
#pragma nounroll
    for (int spin = 0; spin < 100000; ++spin) {
        asm volatile("" ::: "memory");
        x = __hip_atomic_load((gu64*)(boot + l), __ATOMIC_RELAXED, __HIP_MEMORY_SCOPE_AGENT);
        y = __hip_atomic_load((gu64*)(boot + 64 + l), __ATOMIC_RELAXED, __HIP_MEMORY_SCOPE_AGENT);
        tag = boot_tag(boot);
        if (__all((x | 3u) == ((tag << 2) | 3u) && (y | 3u) == ((tag << 2) | 3u))) break;
        __builtin_amdgcn_s_sleep(16);
    }
    asm volatile("" ::: "memory");
    const unsigned m = ((unsigned)__ballot(x & 1u) ^ (__popcll(__ballot(x & 2u)) + __popcll(__ballot(y & 2u)) > 64 ? 1u : 0u)) & 1u;
    return (tag << 8) | m;
}
template <int SL>
__device__ __forceinline__ void wait_ge(const unsigned* p, unsigned need, unsigned* tmo, unsigned code) {
#pragma nounroll
    for (int spin = 0;; ++spin) {
        asm volatile("" ::: "memory");
        if (__all(ld_flag(p) >= need)) break;
        if (spin > 400000 / SL) { __hip_atomic_store((gu32*)tmo, code, __ATOMIC_RELAXED, __HIP_MEMORY_SCOPE_AGENT); break; }
        __builtin_amdgcn_s_sleep(SL);
    }
    asm volatile("" ::: "memory");
}
template <int SL>
__device__ __forceinline__ void wg_wait_acquire(const unsigned* p, unsigned* tmo, unsigned code) {
    if (TIDX < 64) {
        wait_ge<SL>(p, 1u, tmo, code);
        __builtin_amdgcn_fence(__ATOMIC_ACQUIRE, "agent");
        asm volatile("s_waitcnt vmcnt(0)" ::: "memory");
    }
    __syncthreads();
}
__device__ __forceinline__ void arrive(unsigned* sync, const int f, const unsigned need, const int n) {
    const int l = lane_now();
    unsigned old = 0;
    if (l == 0) old = __hip_atomic_fetch_add((gu32*)(sync + (SY_FLAGS + (f >> 3)) * 32), 1u, __ATOMIC_RELAXED, __HIP_MEMORY_SCOPE_AGENT);
    if (__builtin_amdgcn_readfirstlane(old) == need - 1u && l < n) __hip_atomic_store((gu32*)(sync + (f + l) * 32), 16u, __ATOMIC_RELAXED, __HIP_MEMORY_SCOPE_AGENT);
}
__device__ __forceinline__ void wg_publish(unsigned* sync, const int f, const unsigned need, const int n) {
    asm volatile("s_waitcnt vmcnt(0)" ::: "memory");
    __syncthreads();
    if (TIDX < 64) arrive(sync, f, need, n);
}
#define TMO_SET(code) { dead = true; if (l == 0) __hip_atomic_store(a.tmo, (code) + s, __ATOMIC_RELAXED, __HIP_MEMORY_SCOPE_AGENT); }

__device__ __forceinline__ void glds16h(unsigned voff, const void* sbase, unsigned lds_dst) {
    unsigned keep;
    asm volatile("s_mov_b32 %0, m0\n\ts_mov_b32 m0, %3\n\ts_nop 0\n\tglobal_load_lds_dwordx4 %1, %2 sc1\n\ts_mov_b32 m0, %0" : "=&s"(keep) : "v"(voff), "s"(sbase), "s"(lds_dst) : "memory");
}
__device__ __forceinline__ void touch(const void* p, unsigned lds_dst) {
    unsigned keep;
    asm volatile("s_mov_b32 %0, m0\n\ts_mov_b32 m0, %2\n\ts_nop 0\n\tglobal_load_lds_dword %1, off\n\ts_mov_b32 m0, %0" : "=&s"(keep) : "v"(p), "s"(lds_dst) : "memory");
}
__device__ __forceinline__ void touch_sc1(const void* p, unsigned lds_dst) {
    unsigned keep;
    asm volatile("s_mov_b32 %0, m0\n\ts_mov_b32 m0, %2\n\ts_nop 0\n\tglobal_load_lds_dword %1, off sc1\n\ts_mov_b32 m0, %0" : "=&s"(keep) : "v"(p), "s"(lds_dst) : "memory");
}
#define WEV(code)
#define WEVP
#define WEVA
constexpr int C2_K = 32768, C2_V = 65536;
constexpr int C2_R1 = 0, R1S = 3200;
constexpr int C2_R3 = C2_R1 + 4 * R1S;
constexpr int C2_MB = 24576;
#define PST(n)
#define LT(n)
#define LTA(n)
#define LTOUT0
#define LTOUT
#define CN(n)
#define CNS
#define CNOUT
#define CLOB16(a, b, c, d, e, f, g, h, i, j, k, l, m, n, o, p) "v" #a, "v" #b, "v" #c, "v" #d, "v" #e, "v" #f, "v" #g, "v" #h, "v" #i, "v" #j, "v" #k, "v" #l, "v" #m, "v" #n, "v" #o, "v" #p
#define LDS3(p) ((__attribute__((address_space(3))) v4i16*)(p))
__device__ __forceinline__ bf16x8 tr_frag(const void* p0, const void* p1) {
    v4i16 lo = __builtin_amdgcn_ds_read_tr16_b64_v4i16(LDS3(p0));
    v4i16 hi = __builtin_amdgcn_ds_read_tr16_b64_v4i16(LDS3(p1));
    return bf16x8{lo[0], lo[1], lo[2], lo[3], hi[0], hi[1], hi[2], hi[3]};
}
typedef __bf16 bf16x8v __attribute__((ext_vector_type(8)));
typedef __bf16 bf16x16v __attribute__((ext_vector_type(16)));
typedef unsigned v8u __attribute__((ext_vector_type(8)));
typedef unsigned v32u __attribute__((ext_vector_type(32)));
#define PUT8(v, o, x) { const v8u x_ = (x); _Pragma("unroll") for (int e_ = 0; e_ < 8; ++e_) (v)[(o) + e_] = x_[e_]; }
#define SUB4(v, o) v4u{(v)[(o)], (v)[(o) + 1], (v)[(o) + 2], (v)[(o) + 3]}
#define PUT4(v, o, x) { const v4u x_ = (x); (v)[(o)] = x_[0]; (v)[(o) + 1] = x_[1]; (v)[(o) + 2] = x_[2]; (v)[(o) + 3] = x_[3]; }
__device__ __forceinline__ v2u tr4(const u16* p) { return __builtin_bit_cast(v2u, __builtin_amdgcn_ds_read_tr16_b64_v4i16(LDS3(p))); }
__device__ __forceinline__ f32x4 smm(v4u a, v8u b, f32x4 c, int idx) {
    return __builtin_amdgcn_smfmac_f32_16x16x64_bf16(__builtin_bit_cast(bf16x8v, a), __builtin_bit_cast(bf16x16v, b), c, idx, 0, 0);
}
__device__ __forceinline__ v8u tr_frag64(const u16* p, const int pitch) {
    const v2u r0 = tr4(p), r1 = tr4(p + 8 * pitch), r2 = tr4(p + 32 * pitch), r3 = tr4(p + 40 * pitch);
    return v8u{r0[0], r1[0], r0[1], r1[1], r2[0], r3[0], r2[1], r3[1]};
}
#define MM(a_, b_, c_) __builtin_amdgcn_mfma_f32_16x16x32_bf16(as_frag(a_), as_frag(b_), c_, 0, 0, 0)
#define STR2(x) #x
#define STR(x) STR2(x)
#define PAUSE(n) ".rept (" STR(n) ") / 4\n\ts_nop 15\n\t.endr\n\t.rept (" STR(n) ") %% 4\n\ts_nop 3\n\t.endr\n\t"
#define ZERO4 f32x4{0.f, 0.f, 0.f, 0.f}
constexpr int XB_OFF = CAL_OFF + 262144, XP = 68;
__device__ __forceinline__ void last_rows(const char* chsm, rsrc_t rsS, const int wu, const int grp, const int c, const unsigned tau) {
    const int l = lane_now();
    __builtin_amdgcn_s_barrier();
    asm volatile("" ::: "memory");
    const float* rp = (const float*)(chsm + C2_K) + (4 * wu + (l >> 4)) * XP + 4 * (l & 15);
    const f32x4 s0 = (*(const f32x4*)rp + *(const f32x4*)(rp + 32 * XP)) + (*(const f32x4*)(rp + 64 * XP) + *(const f32x4*)(rp + 96 * XP));
    v2u o;
    o[0] = cvt_pk_bf16(s0[0], s0[1]); o[1] = cvt_pk_bf16(s0[2], s0[3]);
    st8_sc1s(rsS, l * 8, XB_OFF + ((c >> 1) * 8 + wu) * 16384 + (int)(tau & 1u) * 8192 + (c & 1) * 4096 + grp * 512, o);
}
__device__ __forceinline__ const unsigned* kv_flag(const unsigned* sync, const int sq, const int grp, const int l) { return sync + (sq < KVS ? (SY_KV0 + sq * 8 + grp) * 32 + (l & 7) : (SY_KV + 8 + grp) * 32 + (l & 3)); }
__device__ __forceinline__ void chain_body(const ChainArgs& a, const int grp, const int c, char* chsm) {
    const int wu = __builtin_amdgcn_readfirstlane(TIDX >> 6);
    const int widx = grp * 16 + c;
    const int hs = 4 * grp + (c >> 2), jq = c & 3;
    const rsrc_t rsS = slot_rsrc(a.slots);
    const unsigned l0 = (unsigned)(uintptr_t)(__attribute__((address_space(3))) char*)chsm;
    bool dead = false;

    const int cal_off = CAL_OFF + grp * 32768 + (c >> 3) * 16384 + (c & 7) * 1024;
        if (wu == 0) {
            const int l = lane_now();
            const v4u ff = {SENT, SENT, SENT, SENT};
#pragma unroll
            for (int e = 0; e < 13; ++e) st_sc1s(rsS, l * 16, widx * 2 * PERWG + min(e * 1024, 2 * PERWG - 1024), ff);
            const rsrc_t rsY = __builtin_amdgcn_make_buffer_rsrc((void*)a.sync, 0, 131072, 0x00020000);
            st_sc1s(rsY, l * 16, widx * 1024, v4u{0u, 0u, 0u, 0u});
            __builtin_amdgcn_raw_buffer_store_b32(0u, rsS, (l & 3) * 256 + ((l >> 2) & 1) * 8192, cal_off, AUX_SC1);
            if (c < 2) __builtin_amdgcn_raw_buffer_store_b32(0u, rsS, (l >> 5) * 8192 + (l & 31) * 4, TM_OFF + c * 16384 + grp * 256, AUX_SC1);
        }
    asm volatile("" ::: "memory");
    if (wu >= 4) {
        __builtin_amdgcn_s_sleep(DLY_U);
        const int l = opaque_tid() & 63, i = l & 15, g = l >> 4;
        const int sidx = (i & 1) ? 0xEEEE : 0x4444;
        const int u = wu - 4;
        const int k0 = 256 * u;
        u16* stg = (u16*)(chsm + 40960 + u * 20480);
        v4u af[4];
        v32u wS[6];
        {
            v4u raw[3][16];
            {
                const size_t wo = (size_t)k0 * HD + hcol(hs) + 32 * jq;
                const rsrc_t rq = __builtin_amdgcn_make_buffer_rsrc((void*)(a.Wq + wo), 0, 256 * HD * 2, 0x00020000);
                const rsrc_t rk = __builtin_amdgcn_make_buffer_rsrc((void*)(a.Wk + wo), 0, 256 * HD * 2, 0x00020000);
                const rsrc_t rv = __builtin_amdgcn_make_buffer_rsrc((void*)(a.Wv + wo), 0, 256 * HD * 2, 0x00020000);
                const int vw = (l >> 2) * HD * 2 + (l & 3) * 16;
#pragma unroll
                for (int p = 0; p < 4; ++p) af[p] = *(const v4u*)(a.mem0 + (size_t)(i >> 1) * DM + k0 + 64 * p + 16 * g + 8 * (i & 1));
#pragma unroll
                for (int e = 0; e < 16; ++e) raw[0][e] = __builtin_amdgcn_raw_buffer_load_b128(rq, vw, e * 16 * HD * 2, 0);
#pragma unroll
                for (int e = 0; e < 16; ++e) raw[1][e] = __builtin_amdgcn_raw_buffer_load_b128(rk, vw, e * 16 * HD * 2, 0);
#pragma unroll
                for (int e = 0; e < 12; ++e) raw[2][e] = __builtin_amdgcn_raw_buffer_load_b128(rv, vw, e * 16 * HD * 2, 0);
                __builtin_amdgcn_sched_barrier(0);
#pragma unroll
                for (int e = 0; e < 16; ++e) *(v4u*)(stg + (16 * e + (l >> 2)) * 40 + 8 * (l & 3)) = raw[0][e];
                __builtin_amdgcn_sched_barrier(0);
#pragma unroll
                for (int e = 12; e < 16; ++e) raw[2][e] = __builtin_amdgcn_raw_buffer_load_b128(rv, vw, e * 16 * HD * 2, 0);
            }
            const int fr = 16 * (g >> 1) + 4 * (g & 1) + (i >> 2);
            __builtin_amdgcn_sched_barrier(0);
#pragma unroll
            for (int hx = 0; hx < 3; ++hx) {
                if (hx) {
#pragma unroll
                    for (int e = 0; e < 16; ++e) *(v4u*)(stg + (16 * e + (l >> 2)) * 40 + 8 * (l & 3)) = raw[hx][e];
                }
                __builtin_amdgcn_s_waitcnt(0xC07F);
#pragma unroll
                for (int jj = 0; jj < 2; ++jj)
#pragma unroll
                    for (int p = 0; p < 4; ++p) PUT8(wS[2 * hx + jj], 8 * p, tr_frag64(stg + (64 * p + fr) * 40 + 16 * jj + 4 * (i & 3), 40))
                __builtin_amdgcn_s_waitcnt(0xC07F);
                __builtin_amdgcn_sched_barrier(0);
            }
        }
        PST(u)
        if (u == 0) { PST(4) }
        const unsigned r1w = l0 + C2_R1 + u * R1S + (2 * g * 16 + i) * 4, r1v = l0 + C2_R1 + u * R1S + 2176 + (2 * g * 16 + ((i & 9) | ((i & 2) << 1) | ((i & 4) >> 1))) * 4;
#define LD4(d, va, so, o) "buffer_load_dwordx4 v[" d "], " va ", %[rs], " so " offen offset:" o " sc1\n\t"
#define CHK(a, b, c, d) "v_max3_u32 v244, v244, v" a ", v" b "\n\tv_max3_u32 v244, v244, v" c ", v" d "\n\t"
#define CHK0(a, b, c, d) "v_max_u32 v244, v" a ", v" b "\n\tv_max3_u32 v244, v244, v" c ", v" d "\n\t"
#define SMF(d, a, w) "v_smfmac_f32_16x16x64_bf16 v[" d "], v[" a "], v[" w "], %[sx]\n\t"
#define ADD(d, a, b) "v_add_f32 v" d ", v" a ", v" b "\n\t"
#define Z2(r) "v_mov_b64 v[" r "], 0\n\t"
#define DW2(ad, x, y, o0, o1) "ds_write2_b32 " ad ", v" x ", v" y " offset0:" o0 " offset1:" o1 "\n\t"
#define A0 "224:227"
#define A1 "228:231"
#define A2 "232:235"
#define A3 "236:239"
#define Q0 "200:203"
#define Q1 "204:207"
#define Q2 "208:211"
#define Q3 "212:215"
#define V0 "216:219"
#define V1 "220:223"
#define S1_HEAD \
            Z2("216:217") Z2("218:219") Z2("220:221") Z2("222:223") \
            "s_waitcnt vmcnt(4)\n\t" ZQ \
            "s_waitcnt vmcnt(3)\n\t" SMF(V0, A0, "136:143") CHK0("224", "225", "226", "227") SMF(V1, A0, "168:175") \
            "s_waitcnt vmcnt(2)\n\t" SMF(V0, A1, "144:151") CHK("228", "229", "230", "231") SMF(V1, A1, "176:183") \
            "s_waitcnt vmcnt(1)\n\t" SMF(V0, A2, "152:159") CHK("232", "233", "234", "235") SMF(V1, A2, "184:191") \
            "s_waitcnt vmcnt(0)\n\t" SMF(V0, A3, "160:167") CHK("236", "237", "238", "239") SMF(V1, A3, "192:199") \
            "v_cmp_eq_u32 vcc, " STR(SENT) ", v244\n\t"
#define S1_TAIL(B1, B2) \
            SMF(Q0, A0, "8:15") SMF(Q0, A1, "16:23") SMF(Q0, A2, "24:31") \
            ADD("240", "216", "217") ADD("241", "218", "219") SMF(Q0, A3, "32:39") DW2("%[rv]", "240", "241", "0", "16") \
            ADD("242", "220", "221") ADD("243", "222", "223") SMF(Q1, A0, "40:47") DW2("%[rv]", "242", "243", "128", "144") \
            SMF(Q1, A1, "48:55") SMF(Q1, A2, "56:63") SMF(Q1, A3, "64:71") \
            ADD("240", "200", "201") ADD("241", "202", "203") SMF(Q2, A0, "72:79") DW2("%[rw]", "240", "241", "0", "16") \
            SMF(Q2, A1, "80:87") SMF(Q2, A2, "88:95") "s_nop 1\n\t" \
            ADD("242", "204", "205") ADD("243", "206", "207") SMF(Q2, A3, "96:103") DW2("%[rw]", "242", "243", "136", "152") \
            SMF(Q3, A0, "104:111") SMF(Q3, A1, "112:119") \
            B1        \
            LTA(0) \
            SMF(Q3, A2, "120:127") SMF(Q3, A3, "128:135") "s_nop 0\n\t" \
            ADD("240", "208", "209") ADD("241", "210", "211") DW2("v246", "240", "241", "0", "16") \
            "s_nop 3\n\t" \
            ADD("242", "212", "213") ADD("243", "214", "215") DW2("v246", "242", "243", "136", "152") \
            "s_waitcnt lgkmcnt(0)\n\t" B2 "s_barrier\n\t"
#define ZQ Z2("200:201") Z2("202:203") Z2("204:205") Z2("206:207") Z2("208:209") Z2("210:211") Z2("212:213") Z2("214:215")
#define U_W "{v[8:39]}"(wS[0]), "{v[40:71]}"(wS[1]), "{v[72:103]}"(wS[2]), "{v[104:135]}"(wS[3]), "{v[136:167]}"(wS[4]), "{v[168:199]}"(wS[5])
#define U_CLOB CLOB16(200, 201, 202, 203, 204, 205, 206, 207, 208, 209, 210, 211, 212, 213, 214, 215), CLOB16(216, 217, 218, 219, 220, 221, 222, 223, 224, 225, 226, 227, 228, 229, 230, 231), \
            CLOB16(232, 233, 234, 235, 236, 237, 238, 239, 240, 241, 242, 243, 244, 245, 246, 247)
        __builtin_amdgcn_s_setprio(2);
        {
            asm volatile(
                "v_add_u32 v246, 1088, %[rw]\n\t"
                S1_HEAD "s_nop 0\n\t" S1_TAIL("s_nop 1\n\t", "s_barrier\n\t")
                : LTOUT0
                : U_W, "{v[224:227]}"(af[0]), "{v[228:231]}"(af[1]), "{v[232:235]}"(af[2]), "{v[236:239]}"(af[3]), [sx] "v"(sidx), [rv] "v"(r1v), [rw] "v"(r1w)
                : "memory", "vcc", CLOB16(200, 201, 202, 203, 204, 205, 206, 207, 208, 209, 210, 211, 212, 213, 214, 215), CLOB16(216, 217, 218, 219, 220, 221, 222, 223, 240, 241, 242, 243, 244, 245, 246, 247));
        }
        const unsigned tau = __builtin_amdgcn_readfirstlane(*(volatile unsigned*)(chsm + C2_MB + 8));
        const unsigned long long wmask = u == 3 ? (c == 0 ? ~0ull : 0ull) : u == 1 ? 0x0020202020202000ull : u == 2 ? 1ull << (NSEG - 3) : 0ull;
        const int pvv = u == 3 ? 2 : -1, pvm = u == 3 ? -1 : 0;
        const unsigned* pfl = a.sync + (u == 3 ? SY_PROG + grp : u == 1 ? SY_KV + 16 + grp : SY_Q + 7 * 8 + grp) * 32;
        int s_first = 0;
        asm volatile("s_branch 1f\n\t.p2align 8\n1:" : "+s"(s_first));
        for (int s = s_first; s < NSEG - 1; ++s) {
            __builtin_amdgcn_s_setprio(0);
            LT(1)
            const int pv = (wmask >> s) & 1ull ? pvv + (s & pvm) : 0;
            const unsigned* pa = pfl;
            if (pv < 0) pfl += 256;
            const int cb = grp & 3;
            int soffc = rec_base(s) + C_OFF + c * 16384 + near_half(s, tau) + cb * 2048;
            int o_as = rec_base(s + 1) + ((c >> 3) + (grp >> 2) * 4) * 16384 + near_half(s + 1, tau) + ((4 * c + cb) & 31) * 256;
            int soff = chunk_off(s + 1, grp, u >> 1, tau) + (u & 1) * 4096;
            const int o_tm = (s >> 1) * 2097152 + TM_OFF + (s & 1) * 16384;
            int lim = dead ? 0 : SPIN_LIMIT, bad = 0;
            const bool rcd = u > 0 || widx < 32;
            const int pc = u > 0 ? 3 * widx + u - 1 : rcd ? 384 + widx : s * 96 + widx - 32;
            const int o_arm = (rcd ? s + 2 < NSEG : pc < 512) ? (rcd ? rec_base(s + 2) : XB_OFF) + (pc >> 3) * 16384 + near_half(rcd ? s + 2 : 0, tau) + (pc & 7) * 1024 : OOB;
            bool there = true;
            if (__builtin_expect(s <= KVS, 0)) {
                there = __all(ld_flag(kv_flag(a.sync, s + 2, grp, l)) != 0u);
                *(volatile unsigned*)(chsm + C2_MB + 32 + (s & 1) * 16 + u * 4) = there ? 1u : 0u;
                __builtin_amdgcn_s_waitcnt(0xC07F);
            }
            const char* vb = !there ? nullptr : arena(a.slots) + (s + 2 >= NSEG ? KX_OFF : VX_OFF) + (blk_off(min(s + 2, NSEG - 1), 4 * grp + 2 * u - 2, tau) ^ (s + 2 >= NSEG ? 8192 : 0));
            asm volatile("" : "+s"(soffc), "+s"(o_as), "+s"(soff), "+s"(lim), "+s"(vb));
#define GL(o) "global_load_lds_dwordx4 %[l16], s[84:85] offset:" o " sc1\n\t"
#define GL4 GL("0") GL("1024") GL("2048") GL("3072")
#define GLN(d) "s_add_u32 m0, m0, 4096\n\ts_add_u32 s84, s84, " d "\n\ts_addc_u32 s85, s85, 0\n\t"
#define PKE PAUSE(PKA)
#define C_TRY(x, x0, x1, x2, x3, y, y0, y1, y2, y3) \
                "s_waitcnt vmcnt(3)\n\t" \
                "v_mfma_f32_16x16x32_bf16 v[240:243], v[216:219], v[" x "], 0\n\t" CHK0(x0, x1, x2, x3) \
                "s_waitcnt vmcnt(2)\n\t" \
                "v_mfma_f32_16x16x32_bf16 v[240:243], v[216:219], v[" y "], v[240:243]\n\t" CHK(y0, y1, y2, y3) \
                "v_cmp_eq_u32 vcc, " STR(SENT) ", v244\n\t" \
                "s_cbranch_vccz 3b\n\t" CN(1) \
                LD4(x, "%[l16]", "%[sc]", "0") LD4(y, "%[l16]", "%[sc]", "1024")
            asm volatile(
                "v_and_b32 v221, 16, %[l16]\n\t"
                "v_mov_b32 v222, 0x3f80\n\t"
                "v_bfe_u32 v223, %[l16], 5, 2\n\t"
                "v_lshlrev_b32 v221, v221, v222\n\t"
                "v_cmp_eq_u32 vcc, 0, v223\n\tv_and_b32 v220, 0xf0, %[l16]\n\tv_bfe_u32 v222, %[l16], 8, 1\n\tv_cndmask_b32 v216, 0, v221, vcc\n\t"
                "v_cmp_eq_u32 vcc, 1, v223\n\tv_lshl_add_u32 v220, v222, 3, v220\n\tv_lshrrev_b32 v222, 9, %[l16]\n\tv_cndmask_b32 v217, 0, v221, vcc\n\t"
                "v_cmp_eq_u32 vcc, 2, v223\n\tv_lshl_add_u32 v220, v222, 30, v220\n\ts_nop 0\n\tv_cndmask_b32 v218, 0, v221, vcc\n\t"
                "v_cmp_eq_u32 vcc, 3, v223\n\ts_nop 1\n\tv_cndmask_b32 v219, 0, v221, vcc\n\t"
                "v_mul_u32_u24 v222, 0x8000, v222\n\t"
                "v_and_b32 v221, 0xff, v220\n\t"
                "v_add_u32 v221, v221, v222\n\t"
                "v_lshrrev_b32 v247, 2, %[l16]\n\t"
                PKE
                "v_mov_b32 v240, " STR(SENT) "\n\tv_mov_b32 v241, v240\n\tv_mov_b32 v242, v240\n\tv_mov_b32 v243, v240\n\t"
                "buffer_store_dwordx4 v[240:243], %[l16], %[rs], %[arm] offen sc1\n\t"
                LTA(2)
                "s_setprio 2\n\t"
                "s_barrier\n\t"
                LTA(3)
                "v_add_u32 v246, 1088, %[rw]\n\t"
                "s_cmp_lt_u32 %[uu], 1\n\t"
                "s_cbranch_scc1 7f\n\t"
                "s_cmp_eq_u32 %[uu], 3\n\t"
                "s_cbranch_scc1 20f\n\t"
                "s_memrealtime s[84:85]\n\t"
                "v_lshrrev_b32 v241, 9, %[l16]\n\t"
                "v_and_b32 v242, 0x7c, v247\n\t"
                "v_lshl_add_u32 v241, v241, 13, v242\n\t"
                "s_waitcnt lgkmcnt(0)\n\t"
                "v_mov_b32 v240, s84\n\t"
                "buffer_store_dword v240, v241, %[rs], %[ts] offen sc1\n\t"
                "s_cmp_eq_u64 %[vb], 0\n\t"
                "s_cbranch_scc1 11f\n\t"
                "s_mov_b32 s86, m0\n\t"
                "s_mov_b64 s[84:85], %[vb]\n\t"
                "s_mov_b32 m0, %[lv]\n\t"
                PAUSE(PG0)
                "s_cmp_eq_u32 %[uu], 1\n\t"
                "s_cbranch_scc1 12f\n\t"
                PAUSE(PG1)
                "12:\n\t"
                GL4 GLN("4096") GL4 GLN("12288") GL4 GLN("4096") GL4
                "s_mov_b32 m0, s86\n\t"
                "11:\n\t"
                "s_cmp_lt_i32 %[pv], 0\n\t"
                "s_cbranch_scc0 8f\n\t"
                "v_and_b32 v241, 12, v247\n\t"
                "10:\n\t"
                "global_load_dword v240, v241, %[pa] sc1\n\t"
                "s_waitcnt vmcnt(0)\n\t"
                "v_cmp_gt_u32 vcc, 16, v240\n\t"
                "s_cbranch_vccz 8f\n\t"
                "s_sleep 6\n\t"
                "s_sub_u32 %[lim], %[lim], 1\n\t"
                "s_cbranch_scc0 10b\n\t"
                "s_mov_b32 %[bad], 3000\n\t"
                "s_mov_b32 %[lim], 0\n\t"
                "s_branch 8f\n\t"
                "20:\n\t"
                PAUSE(PS)
                "s_memrealtime s[84:85]\n\t"
                "s_load_dword s86, %[tb], 0\n\ts_load_dword s87, %[tb], 0x100\n\ts_load_dword s88, %[tb], 0x200\n\ts_load_dword s89, %[tb], 0x300\n\t"
                "s_load_dword s90, %[tb], 0x400\n\ts_load_dword s91, %[tb], 0x500\n\ts_load_dword s92, %[tb], 0x600\n\ts_load_dword s93, %[tb], 0x700\n\t"
                "s_waitcnt lgkmcnt(0)\n\t"
                "s_sub_u32 s86, s84, s86\n\ts_sub_u32 s87, s84, s87\n\ts_sub_u32 s88, s84, s88\n\ts_sub_u32 s89, s84, s89\n\t"
                "s_sub_u32 s90, s84, s90\n\ts_sub_u32 s91, s84, s91\n\ts_sub_u32 s92, s84, s92\n\ts_sub_u32 s93, s84, s93\n\t"
                "s_or_b32 s85, s86, s87\n\ts_min_u32 s86, s86, s87\n\ts_or_b32 s85, s85, s88\n\ts_min_u32 s86, s86, s88\n\ts_or_b32 s85, s85, s89\n\ts_min_u32 s86, s86, s89\n\t"
                "s_memrealtime s[88:89]\n\t"
                "s_or_b32 s85, s85, s90\n\ts_min_u32 s86, s86, s90\n\ts_or_b32 s85, s85, s91\n\ts_min_u32 s86, s86, s91\n\ts_or_b32 s85, s85, s92\n\ts_min_u32 s86, s86, s92\n\t"
                "s_or_b32 s85, s85, s93\n\ts_min_u32 s86, s86, s93\n\t"
                "s_cmp_gt_u32 s85, 127\n\t"
                CNS
                "s_cselect_b32 s85, " STR(TE0) ", 127\n\t"
                "s_min_u32 s86, s86, s85\n\t"
                "s_sub_u32 s84, s84, s86\n\t"
                "s_add_u32 s84, s84, %[td]\n\t"
                "s_cmp_lt_i32 %[pv], 1\n\t"
                "s_cbranch_scc1 4f\n\t"
                "v_mov_b32 v247, %[pv]\n\t"
                "s_mov_b64 exec, 1\n\t"
                "global_store_dword %[l16], v247, %[pa] sc1\n\t"
                "s_mov_b64 exec, -1\n\t"
                "4:\n\t"
                "s_waitcnt lgkmcnt(0)\n\t"
                "s_sub_i32 s84, s84, s88\n\t"
                "s_max_i32 s84, s84, 0\n\t"
                "s_min_i32 s84, s84, 40\n\t"
                "21:\n\t"
                "s_sub_u32 s84, s84, 1\n\t"
                "s_cbranch_scc0 21b\n\t"
                "s_branch 8f\n\t"
                "7:\n\t"
                PAUSE(PC)
                LD4(A0, "%[l16]", "%[sc]", "0") LD4(A1, "%[l16]", "%[sc]", "1024")
                PAUSE(PP)
                LD4(A2, "%[l16]", "%[sc]", "0") LD4(A3, "%[l16]", "%[sc]", "1024")
                "s_waitcnt vmcnt(3)\n\t"
                "v_mfma_f32_16x16x32_bf16 v[240:243], v[216:219], v[224:227], 0\n\t" CHK0("224", "225", "226", "227")
                "s_waitcnt vmcnt(2)\n\t"
                "v_mfma_f32_16x16x32_bf16 v[240:243], v[216:219], v[228:231], v[240:243]\n\t" CHK("228", "229", "230", "231")
                "v_cmp_eq_u32 vcc, " STR(SENT) ", v244\n\t"
                "s_cbranch_vccnz 30f\n\t"
                "3:\n\t"
                "s_nop 3\n\t"
                "v_cvt_pk_bf16_f32 v244, v240, v241\n\t"
                "v_cvt_pk_bf16_f32 v245, v242, v243\n\t"
                "buffer_store_dwordx2 v[244:245], v221, %[rs], %[oas] offen sc1\n\t"
                PAUSE(PB)
                "8:\n\t"
                LTA(4)
                "s_barrier\n\t"
                PAUSE(1)
                "2:\n\t"
                LD4(A0, "%[l16]", "%[sa]", "0") LD4(A1, "%[l16]", "%[sa]", "1024") LD4(A2, "%[l16]", "%[sa]", "2048") LD4(A3, "%[l16]", "%[sa]", "3072")
                S1_HEAD
                "s_cbranch_vccnz 6f\n\t"
                "5:\n\t"
                LTA(5)
                S1_TAIL("s_waitcnt lgkmcnt(0)\n\ts_barrier\n\t", "")
                "s_setprio 0\n\t"
                "s_branch 9f\n\t"
                "30:\n\t" CN(1)
                LD4(A0, "%[l16]", "%[sc]", "0") LD4(A1, "%[l16]", "%[sc]", "1024")
                "1:\n\t"
                C_TRY(A2, "232", "233", "234", "235", A3, "236", "237", "238", "239")
                C_TRY(A0, "224", "225", "226", "227", A1, "228", "229", "230", "231")
                "s_sub_u32 %[lim], %[lim], 1\n\t"
                "s_cbranch_scc0 1b\n\t"
                "s_mov_b32 %[bad], 2000\n\t"
                "s_mov_b32 %[lim], 0\n\t"
                "s_branch 3b\n\t"
                "6:\n\t" CN(65536)
                "v_mov_b32 v240, %[tg]\n\t"
                "v_mov_b32 v241, %[lf]\n\t"
                "ds_write_b32 v241, v240\n\t"
                "s_nop 0\n\t"
                "s_sub_u32 %[lim], %[lim], 1\n\t"
                "s_cbranch_scc0 2b\n\t"
                "s_mov_b32 %[bad], 1\n\t"
                "s_branch 5b\n\t"
                "9:\n\t"
                : [lim] "+s"(lim), [bad] "+s"(bad), [oas] "+s"(o_as) LTOUT CNOUT
                : U_W, [l16] "v"(l * 16), [rs] "s"(rsS), [sx] "v"(sidx), [rv] "v"(r1v), [rw] "v"(r1w),
                  [sc] "s"(soffc), [sa] "s"(soff), [uu] "s"(u), [pv] "s"(pv), [pa] "s"(pa), [vb] "s"(vb), [lv] "s"(l0 + (s + 2 >= NSEG ? C2_V + 32768 : s & 1 ? C2_K : C2_V) + (u - 1) * 16384), [td] "s"(TD), [ts] "s"(c == 0 && (u == 2 || s < NSEG - 3) ? o_tm + grp * 256 + (2 - u) * 2097152 : OOB), [tb] "s"(arena(a.slots) + o_tm + near_half(s, tau)),

                  [arm] "s"(o_arm),
                  [tg] "s"(s + 1), [lf] "s"(l0 + C2_MB + 16)
                : "memory", "vcc", "scc", "s84", "s85", "s86", "s87", "s88", "s89", "s90", "s91", "s92", "s93", U_CLOB);
            if (__builtin_expect(bad != 0, 0)) TMO_SET((unsigned)bad)
        }
        last_rows(chsm, rsS, wu, grp, c, tau);
        if (u == 3 && widx == 0 && l == 0) __hip_atomic_store((gu64*)(a.boot + 144), *(volatile unsigned long long*)(chsm + C2_MB), __ATOMIC_RELAXED, __HIP_MEMORY_SCOPE_AGENT);
        return;
    }
    const int l = opaque_tid() & 63, i = l & 15, g = l >> 4;
    const int sidx = (i & 1) ? 0xEEEE : 0x4444;
    const int ha = 4 * grp + wu;
    unsigned long long tag;
    v32u wO[2];
    {
        u16* stg = (u16*)(chsm + 122880 + wu * 10240);
        tag = boot_tag(a.boot);
        asm volatile("" ::: "memory");
        v4u rawo[16];
        const u16* src = a.Wo + (size_t)(hcol(ha) + (l >> 3)) * DM + 64 * c + 8 * (l & 7);
#pragma unroll
        for (int e = 0; e < 16; ++e) rawo[e] = *(const v4u*)(src + (size_t)8 * e * DM);
        __builtin_amdgcn_sched_barrier(0);
        if (wu == 0) {
            asm volatile("s_waitcnt vmcnt(16)" ::: "memory");
            unsigned tm[2] = {~0u, ~0u};
            const char* pb = arena(a.slots) + cal_off;
#pragma unroll
            for (int k = 0; k < 2 * NPROBE; ++k) {
                unsigned long long c0, c1; unsigned d;
                asm volatile("s_memtime %0\n\ts_waitcnt lgkmcnt(0)\n\ts_load_dword %2, %3, %4\n\ts_waitcnt lgkmcnt(0)\n\ts_memtime %1\n\ts_waitcnt lgkmcnt(0)"
                             : "=&s"(c0), "=&s"(c1), "=&s"(d) : "s"(pb), "i"((k >> 1) * 256 + (k & 1) * 8192) : "memory");
                tm[k & 1] = min(tm[k & 1], (unsigned)(c1 - c0));
            }
            const unsigned side = (__builtin_amdgcn_s_getreg(20 | (3 << 11)) >> 2) & 1u;
            if (l == 0) __hip_atomic_store((gu64*)(a.boot + widx), (tag << 2) | (((tm[0] > tm[1] ? 1u : 0u) ^ side) << 1) | side, __ATOMIC_RELAXED, __HIP_MEMORY_SCOPE_AGENT);
            PST(7)
        }
#pragma unroll
        for (int m = 0; m < 2; ++m) {
#pragma unroll
            for (int e = 0; e < 8; ++e) *(v4u*)(stg + (8 * e + (l >> 3)) * 72 + 8 * (l & 7)) = rawo[8 * m + e];
            __builtin_amdgcn_s_waitcnt(0xC07F);
#pragma unroll
            for (int ct = 0; ct < 4; ++ct) {
                const u16* p = stg + (8 * g + (i >> 2)) * 72 + 16 * ct + 4 * (i & 3);
                const v2u r0 = tr4(p), r1 = tr4(p + 4 * 72), r2 = tr4(p + 32 * 72), r3 = tr4(p + 36 * 72);
                PUT4(wO[ct >> 1], (ct & 1) * 16 + m * 8, (v4u{r0[0], r0[1], r1[0], r1[1]})) PUT4(wO[ct >> 1], (ct & 1) * 16 + m * 8 + 4, (v4u{r2[0], r2[1], r3[0], r3[1]}))
            }
            __builtin_amdgcn_s_waitcnt(0xC07F);
        }
        if (wu == 0) { PST(5) }
        if (widx == 0 && wu == 0) *(volatile unsigned long long*)(chsm + C2_MB) = tag;
    }
    if (wu >= 2) touch_sc1(arena(a.slots) + (size_t)min(l + (wu - 2) * 64, FX_OFF / 2097152 - 1) * 2097152 + 2097024 - 128, l0 + C2_MB + 256);
    unsigned long long fw = 0;
#pragma nounroll
    for (int spin = 0; spin < 100000; ++spin) {
        asm volatile("" ::: "memory");
        fw = __hip_atomic_load((gu64*)(a.boot + 256 + 16 * grp + (l & 7)), __ATOMIC_RELAXED, __HIP_MEMORY_SCOPE_AGENT);
        if (__all((fw | 255u) == ((tag << 8) | 255u))) break;
        __builtin_amdgcn_s_sleep(4);
    }
    asm volatile("" ::: "memory");
    const unsigned tau = __builtin_amdgcn_readfirstlane((unsigned)fw & 255u);
    if (wu == 3) { *(volatile unsigned*)(chsm + C2_MB + 8) = tau; *(volatile unsigned*)(chsm + C2_MB + 16) = ~0u; __builtin_amdgcn_s_waitcnt(0xC07F); }
    if (wu == 1) { PST(9) }

    const unsigned r3a = l0 + C2_R3 + wu * 2048 + g * 128 + i * 4;
    v32u yb;
    v8u kx;
    v8u sel; v2u sel3;
#pragma unroll
    for (int d = 0; d < 8; ++d) sel[d] = (i >> 1) == d ? (0x3C00u << ((i & 1) * 16)) : 0u;
#pragma unroll
    for (int d = 0; d < 2; ++d) sel3[d] = (i & 3) == 0 && (i >> 3) == d ? (0x3C00u << (((i >> 2) & 1) * 16)) : 0u;
    asm volatile("s_setreg_imm32_b32 hwreg(HW_REG_MODE, 23, 1), 1\n\ts_nop 3");
#define MF(d, a, b, c) "v_mfma_f32_16x16x32_bf16 v[" d "], v[" a "], v[" b "], " c "\n\t"
#define PX4(d0, d1, d2, d3, a, w0, w1, w2, w3) MF(d0, a, w0, "v[" d0 "]") MF(d1, a, w1, "v[" d1 "]") MF(d2, a, w2, "v[" d2 "]") MF(d3, a, w3, "v[" d3 "]")
#define PXT(d0, d1, d2, d3, a0, a1, a2, a3) MF(d0, a0, "32:35", "0") MF(d1, a0, "48:51", "0") MF(d2, a0, "64:67", "0") MF(d3, a0, "80:83", "0") \
        PX4(d0, d1, d2, d3, a1, "36:39", "52:55", "68:71", "84:87") PX4(d0, d1, d2, d3, a2, "40:43", "56:59", "72:75", "88:91") PX4(d0, d1, d2, d3, a3, "44:47", "60:63", "76:79", "92:95")
#define CVP(d, a, b) "v_cvt_pk_bf16_f32 v" d ", v" a ", v" b "\n\t"
#define PROJ_X(cnt) \
        "s_waitcnt " cnt "(8)\n\t" PXT("192:195", "196:199", "200:203", "204:207", "160:163", "164:167", "168:171", "172:175") \
        "s_waitcnt " cnt "(4)\n\t" PXT("208:211", "212:215", "216:219", "220:223", "176:179", "180:183", "184:187", "188:191") \
        CVP("128", "192", "193") CVP("129", "194", "195") CVP("136", "196", "197") CVP("137", "198", "199") CVP("144", "200", "201") CVP("145", "202", "203") CVP("152", "204", "205") CVP("153", "206", "207") \
        CVP("132", "208", "209") CVP("133", "210", "211") CVP("140", "212", "213") CVP("141", "214", "215") CVP("148", "216", "217") CVP("149", "218", "219") CVP("156", "220", "221") CVP("157", "222", "223") \
        "s_waitcnt " cnt "(0)\n\t"
#define CLOB_PX CLOB16(160, 161, 162, 163, 164, 165, 166, 167, 168, 169, 170, 171, 172, 173, 174, 175), CLOB16(176, 177, 178, 179, 180, 181, 182, 183, 184, 185, 186, 187, 188, 189, 190, 191), \
        CLOB16(192, 193, 194, 195, 196, 197, 198, 199, 200, 201, 202, 203, 204, 205, 206, 207), CLOB16(208, 209, 210, 211, 212, 213, 214, 215, 216, 217, 218, 219, 220, 221, 222, 223)
#define LDX(d, va, so, o) "buffer_load_dwordx4 v[" d "], " va ", %[rs], " so " offen offset:" o " sc1\n\t"
#define DSV(d, o) "ds_read_b128 v[" d "], %[lv] offset:" o "\n\t"
    const int voffX = ((i >> 1) & 1) * 4096 + g * 256 + (2 * (i >> 2) + (i & 1)) * 16;
    const int pk = (((i >> 1) & 3) << 1) | (i >> 3);
    const unsigned hv = wu >= 2 ? l0 + C2_R1 + 2176 + ((wu & 1) * 128 + ((l >> 4) + 4 * ((l >> 3) & 1)) * 16 + 8 * ((l >> 1) & 1) + 4 * ((l >> 2) & 1) + 2 * (l & 1)) * 4
                                : l0 + C2_R1 + (i & 1) * 544 + (i >> 1) * 64 + g * 16;
    const unsigned hk = l0 + C2_R1 + 1088 + (i & 1) * 544 + pk * 64 + g * 16;
    const int vo1 = wu >= 2 ? l * 4 : wu == 1 ? (g >> 1) * 1024 + i * 16 + (g & 1) * 8 : (i & 1) ? 1 << 30 : (i >> 1) * 16 + 4 * g;
    const int vo2 = (2 * (i & 1) + (g >> 1)) * 128 + pk * 16 + (g & 1) * 8;
    const int voffK = (g >> 1) * 256 + i * 16 + (g & 1) * 8;
#define RD4(w, ad, a, b, c, d) "ds_read_" w " v[" a "], " ad "\n\tds_read_" w " v[" b "], " ad " offset:3200\n\tds_read_" w " v[" c "], " ad " offset:6400\n\tds_read_" w " v[" d "], " ad " offset:9600\n\t"
#define AD4(d0, d1, d2, d3, a0, a1, a2, a3) ADD(d0, d0, a0) ADD(d1, d1, a1) ADD(d2, d2, a2) ADD(d3, d3, a3)
#define QSC(r) "v_mul_f32 v" r ", 0x3e0293ee, v" r "\n\t"
#define FLD "buffer_load_dword v114, %[vf], %[rs], %[fl] offen sc1\n\t"
#define DUM "buffer_store_dword v168, off, %[rs], %[ob] sc1\n\t"
#define QHEAD "s_barrier\n\t" RD4("b128", "%[hv]", "160:163", "164:167", "168:171", "172:175") FLD \
        "s_waitcnt lgkmcnt(2)\n\t" AD4("160", "161", "162", "163", "164", "165", "166", "167") \
        "s_waitcnt lgkmcnt(0)\n\t" AD4("168", "169", "170", "171", "172", "173", "174", "175") \
        "s_barrier\n\t"
#define QTAIL AD4("160", "161", "162", "163", "168", "169", "170", "171") QSC("160") QSC("161") QSC("162") QSC("163")
#define CVH(d, a, b) "v_cvt_pk_f16_f32 v" d ", v" a ", v" b "\n\t"
#define SWA(d, a, b) "v_add_f32_dpp v" d ", v" a ", v" b " quad_perm:[1,0,3,2] row_mask:0xf bank_mask:0xf\n\t"
#define KLD(so) "buffer_load_dwordx2 v[96:97], %[vk], %[rs], " so " offen sc1\n\tbuffer_load_dwordx2 v[102:103], %[vk], %[rs], " so " offen offset:512 sc1\n\t" \
        "s_add_u32 " so ", " so ", 4096\n\t" \
        "buffer_load_dwordx2 v[100:101], %[vk], %[rs], " so " offen sc1\n\tbuffer_load_dwordx2 v[98:99], %[vk], %[rs], " so " offen offset:512 sc1\n\t"
    auto hand_over = [&](const int o_1, const int o_2, const int o_f, const int tg) {
        int dmy = 0;
        asm volatile(
            "s_cmp_eq_u32 %[wu], 0\n\t"
            "s_cbranch_scc1 2f\n\t"
            "s_cmp_eq_u32 %[wu], 1\n\t"
            "s_cbranch_scc1 1f\n\t"
            "s_barrier\n\t"
            RD4("b64", "%[hv]", "160:161", "162:163", "164:165", "166:167") FLD
            "s_waitcnt lgkmcnt(2)\n\t" ADD("160", "160", "162") ADD("161", "161", "163")
            "s_waitcnt lgkmcnt(0)\n\t" ADD("164", "164", "166") ADD("165", "165", "167") ADD("160", "160", "164") ADD("161", "161", "165")
            CVP("168", "160", "161")
            "buffer_store_dword v168, %[v1], %[rs], %[o1] offen sc1\n\t"
            "s_barrier\n\t"
            DUM PAUSE(PV2)
            "s_branch 3f\n\t"
            "5:\n\t"
            "v_mov_b32 v115, %[lf]\n\t"
            "ds_read_b32 v115, v115\n\t"
            "s_waitcnt lgkmcnt(0)\n\t"
            "v_cmp_eq_u32 vcc, %[tg], v115\n\t"
            "s_cbranch_vccnz 4f\n\t" CN(256)
            PAUSE(PL)
            "s_branch 4f\n\t"
            "1:\n\t"
            Z2("176:177") Z2("178:179")
            "s_waitcnt vmcnt(0)\n\t"
            QHEAD QTAIL
            "s_mov_b64 exec, %[em]\n\t"
            CVP("176", "160", "161") CVP("177", "162", "163")
            "s_not_b64 exec, exec\n\t"
            CVP("178", "160", "161") CVP("179", "162", "163")
            "s_mov_b64 exec, -1\n\t"
            "s_nop 0\n\t"
            MF("180:183", "96:99", "176:179", "0") MF("184:187", "100:103", "176:179", "0")
            "s_nop 7\n\t"
            SWA("180", "184", "180") SWA("181", "185", "181") SWA("182", "186", "182") SWA("183", "187", "183")
            CVH("188", "180", "181") CVH("189", "182", "183")
            "buffer_store_dwordx2 v[188:189], %[v1], %[rs], %[o1] offen sc1\n\t"
            DUM PAUSE(PV1)
            "s_branch 3f\n\t"
            "2:\n\t"
            QHEAD
            RD4("b128", "%[hk]", "176:179", "180:183", "184:187", "188:191")
            QTAIL CVP("164", "160", "161") CVP("165", "162", "163")
            "s_waitcnt lgkmcnt(2)\n\t" AD4("176", "177", "178", "179", "180", "181", "182", "183")
            "s_waitcnt lgkmcnt(0)\n\t" AD4("184", "185", "186", "187", "188", "189", "190", "191") AD4("176", "177", "178", "179", "184", "185", "186", "187")
            CVP("166", "176", "177") CVP("167", "178", "179")
            "s_nop 1\n\t"
            "v_mfma_f32_16x16x16_bf16 v[168:171], v[166:167], v[164:165], 0\n\t"
            "buffer_store_dwordx2 v[166:167], %[v2], %[rs], %[o2] offen sc1\n\t"
            "s_nop 6\n\t"
            SWA("172", "169", "168") SWA("173", "171", "170")
            CVH("174", "172", "173")
            "buffer_store_dword v174, %[v1], %[rs], %[o1] offen sc1\n\t"
            "3:\n\t"
            PAUSE(PVA)
            "s_waitcnt vmcnt(2)\n\t"
            "v_cmp_ne_u32 vcc, " STR(SENT) ", v114\n\t"
            "s_cbranch_vccnz 5b\n\t"
            "4:\n\t"
            : [dm] "+s"(dmy) CNOUT : "{v[96:103]}"(kx), [fl] "s"(o_f), [vf] "v"(i * 128), [ob] "s"(1 << 30), [tg] "s"(tg), [lf] "s"(l0 + C2_MB + 16), [wu] "s"(wu), [hv] "v"(hv), [hk] "v"(hk), [v1] "v"(vo1), [v2] "v"(vo2), [rs] "s"(rsS), [o1] "s"(o_1), [o2] "s"(o_2), [em] "s"(0x5555555555555555ull)
            : "memory", "scc", "vcc", "v114", "v115", CLOB16(160, 161, 162, 163, 164, 165, 166, 167, 168, 169, 170, 171, 172, 173, 174, 175), CLOB16(176, 177, 178, 179, 180, 181, 182, 183, 184, 185, 186, 187, 188, 189, 190, 191));
    };
    auto o1_of = [&](const int s) { return wu >= 2 ? v_off(s, hs, tau) + jq * 512 + (wu & 1) * 256 : wu == 1 ? x_off(s, hs, tau) + jq * 256 : m_off(s, hs, tau) + jq * 128; };

    __builtin_amdgcn_s_setprio(1);
    int o_f = f_off(0, grp, tau);
    int o_cs = rec_base(0) + C_OFF + c * 16384 + near_half(0, tau) + wu * 2048 + grp * 256;
    int o_1 = o1_of(0), o_2 = km_off(0, hs, tau) + jq * 512, bq = x_off(0, ha, tau), bm = m_off(0, ha, tau), bb = v_off(0, ha, tau);
    {
            const int b0 = blk_off(0, ha, tau);
            int k0 = KX_OFF + blk_off(0, hs, tau) + jq * 1024;
            asm volatile(
                LDX("160:163", "%[vx]", "%[vb]", "0") LDX("164:167", "%[vx]", "%[vb]", "1024") LDX("168:171", "%[vx]", "%[vb]", "2048") LDX("172:175", "%[vx]", "%[vb]", "3072")
                LDX("176:179", "%[vx]", "%[vb]", "128") LDX("180:183", "%[vx]", "%[vb]", "1152") LDX("184:187", "%[vx]", "%[vb]", "2176") LDX("188:191", "%[vx]", "%[vb]", "3200")
                KLD("%[k0]")
                "v_mov_b32 v131, 0\n\tv_mov_b32 v135, 0\n\tv_mov_b32 v139, 0\n\tv_mov_b32 v143, 0\n\tv_mov_b32 v147, 0\n\tv_mov_b32 v151, 0\n\tv_mov_b32 v155, 0\n\tv_mov_b32 v159, 0\n\t"
                PROJ_X("vmcnt")
                : "=&{v[128:159]}"(yb), "=&{v[96:103]}"(kx), [k0] "+s"(k0)
                : "{v[32:63]}"(wO[0]), "{v[64:95]}"(wO[1]), [vx] "v"(voffX), [vk] "v"(voffK), [rs] "s"(rsS), [vb] "s"(VX_OFF + b0)
                : "memory", "scc", CLOB_PX);
        if (wu == 1) { PST(10) }
    }
    int s_first = 0;
    asm volatile("s_branch 1f\n\t.p2align 8\n1:" : "+s"(s_first));
    for (int s = s_first; s < NSEG - 1; ++s) {
        hand_over(o_1, o_2, o_f, s);
        if (s == 0 && wu == 1) { PST(8) }
        LT(0)
#define LDPA "buffer_load_dwordx2 v[160:161], v254, %[rs], %[bm] offen sc1\n\t" LDX("164:167", "%[l16]", "%[bq]", "0") LDX("168:171", "%[l16]", "%[bq]", "1024")
#define SM(d, a, b, x) "v_smfmac_f32_16x16x64_bf16 v[" d "], v[" a "], v[" b "], " x "\n\t"
#define MX2(a, b) "v_max_u32 v244, v" a ", v" b "\n\t"
#define MX3(a, b) "v_max3_u32 v244, v244, v" a ", v" b "\n\t"
#define ZAM Z2("208:209") Z2("210:211") Z2("212:213") Z2("214:215") Z2("216:217") Z2("218:219") Z2("220:221") Z2("222:223")
#define MUL(d, a, b) "v_mul_f32 v" d ", v" a ", v" b "\n\t"
#define CV0(d, a) "v_cvt_pk_bf16_f32 v" d ", v" a ", 0\n\t"
#define RDC "ds_read_b64 v[224:225], %[rc]\n\tds_read_b64 v[226:227], %[rc] offset:2048\n\tds_read_b64 v[228:229], %[rc] offset:4096\n\tds_read_b64 v[230:231], %[rc] offset:6144\n\t"
#define EXD(d, r) "v_exp_f32 v" d ", v" r "\n\t"
#define SUBM(r) "v_sub_f32 v" r ", v" r ", v250\n\t"
#define PCVM CVP("241", "180", "180") CVP("243", "180", "180")
#define PCVX CVP("242", "178", "179") CVP("240", "176", "177")
#define OUT(t0, t1, a0, a1, a2, a3, ad, o0, o1) ADD(t0, a0, a1) ADD(t1, a2, a3) MUL(t0, t0, "250") MUL(t1, t1, "251") DW2(ad, t0, t1, o0, o1)
#define TS(n)
#define TSOUT
        int lim = dead ? 0 : SPIN_LIMIT, bad = 0;
        asm volatile(
            LDX("192:195", "%[l16]", "%[bb]", "0") LDX("196:199", "%[l16]", "%[bb]", "1024")
            ZAM Z2("224:225") Z2("226:227")
            Z2("16:17") Z2("18:19") Z2("20:21") Z2("22:23") Z2("24:25") Z2("26:27") Z2("28:29") Z2("30:31")
            "v_mov_b32 v200, 0x3f803f80\n\tv_mov_b32 v201, 0x3f803f80\n\tv_mov_b32 v202, 0x3f80\n\tv_mov_b32 v203, 0\n\t"
            "v_mov_b64 v[204:205], v[200:201]\n\tv_mov_b64 v[206:207], v[202:203]\n\t"
            "v_xor_b32 v252, 0xa000, %[sx]\n\tv_add_u32 v253, 1024, %[ra]\n\tv_lshrrev_b32 v254, 1, %[l16]\n\t"
            PAUSE(PV)
            TS(8)
            LDPA
            "4:\n\t"
            "s_waitcnt vmcnt(4)\n\t"
            SM("208:211", "192:195", "32:39", "%[sx]") MX2("192", "193") SM("212:215", "192:195", "48:55", "%[sx]") MX3("194", "195")
            SM("216:219", "192:195", "64:71", "%[sx]") SM("220:223", "192:195", "80:87", "%[sx]")
            "s_waitcnt vmcnt(3)\n\t"
            SM("208:211", "196:199", "40:47", "%[sx]") MX3("196", "197") SM("212:215", "196:199", "56:63", "%[sx]") MX3("198", "199")
            SM("216:219", "196:199", "72:79", "%[sx]") "v_cmp_eq_u32 vcc, " STR(SENT) ", v244\n\t" SM("220:223", "196:199", "88:95", "%[sx]")
            "s_cbranch_vccnz 5f\n\t"
            "1:\n\t" TS(11)
            "s_waitcnt vmcnt(2)\n\t"
            "v_mfma_f32_16x16x16_f16 v[236:239], v[112:113], v[160:161], 0\n\t"
            MX2("160", "161") ADD("245", "208", "209") ADD("246", "210", "211") CV0("130", "245") CV0("134", "246")
            ADD("245", "212", "213") ADD("246", "214", "215") CV0("138", "245") CV0("142", "246")
            EXD("180", "236")
            "s_waitcnt vmcnt(1)\n\t"
            "v_mfma_f32_16x16x32_f16 v[228:231], v[104:107], v[164:167], 0\n\t"
            MX3("164", "165") MX3("166", "167") ADD("245", "216", "217") ADD("246", "218", "219") CV0("146", "245") CV0("150", "246")
            ADD("245", "220", "221") ADD("246", "222", "223") CV0("154", "245") CV0("158", "246") PCVM
            "s_waitcnt vmcnt(0)\n\t"
            "v_mfma_f32_16x16x32_f16 v[228:231], v[108:111], v[168:171], v[228:231]\n\t"
            MX3("168", "169") MX3("170", "171")
            "v_cmp_eq_u32 vcc, " STR(SENT) ", v244\n\t"
            "s_cbranch_vccnz 7f\n\t"
            "2:\n\t" TS(9)
            "s_nop 3\n\t"
            EXD("179", "231") EXD("178", "230") EXD("177", "229") EXD("176", "228")
            PCVX
            "v_max3_f32 v250, |v236|, |v231|, |v230|\n\t"
            "v_max3_f32 v250, v250, |v229|, |v228|\n\t"
            SM("224:227", "240:243", "200:207", "v252")
            "v_cmp_nge_f32 vcc, 0x41c00000, v250\n\t"
            "s_nop 0\n\t"
            SM("16:19", "240:243", "128:135", "v252")
            "s_cbranch_vccnz 10f\n\t"
            "13:\n\t"
            "s_nop 0\n\t"
            SM("20:23", "240:243", "136:143", "v252") "s_nop 1\n\t"
            ADD("250", "224", "225") ADD("251", "226", "227")
            SM("24:27", "240:243", "144:151", "v252")
            "v_rcp_f32 v250, v250\n\t"
            SM("28:31", "240:243", "152:159", "v252")
            "v_rcp_f32 v251, v251\n\t"
            OUT("245", "246", "16", "17", "18", "19", "%[ra]", "0", "16")
            OUT("247", "248", "20", "21", "22", "23", "%[ra]", "128", "144")
            OUT("245", "246", "24", "25", "26", "27", "v253", "0", "16")
            OUT("247", "248", "28", "29", "30", "31", "v253", "128", "144")
            TS(10)
            "s_waitcnt lgkmcnt(0)\n\t"
            "s_barrier\n\t"
            RDC
            "v_lshrrev_b32 v247, 2, %[l16]\n\t"
            "s_waitcnt lgkmcnt(2)\n\t"
            ADD("224", "224", "226") ADD("225", "225", "227")
            "s_waitcnt lgkmcnt(0)\n\t"
            ADD("228", "228", "230") ADD("229", "229", "231") ADD("224", "224", "228") ADD("225", "225", "229")
            "v_cvt_pk_bf16_f32 v245, v224, v225\n\t"
            "buffer_store_dword v245, v247, %[rs], %[cs] offen sc1\n\t"
            "s_branch 9f\n\t"
            "10:\n\t"
            "v_max3_f32 v250, v236, v231, v230\n\t"
            "v_max3_f32 v250, v250, v229, v228\n\t"
            "s_nop 1\n\t"
            "v_max_f32_dpp v251, v250, v250 quad_perm:[1,0,3,2] row_mask:0xf bank_mask:0xf\n\t"
            "v_max_f32_dpp v244, v250, v250 quad_perm:[1,0,3,2] row_mask:0xf bank_mask:0xf\n\t"
            "s_nop 1\n\t"
            "v_permlane16_swap_b32 v251, v244\n\t"
            "v_max_f32 v250, v251, v244\n\t"
            "v_max_f32 v251, v251, v244\n\t"
            Z2("224:225") Z2("226:227")
            "v_permlane32_swap_b32 v250, v251\n\t"
            "v_max_f32 v250, v250, v251\n\t"
            Z2("16:17") Z2("18:19")
            SUBM("228") SUBM("229") SUBM("230") SUBM("231") SUBM("236")
            EXD("180", "236") EXD("179", "231") EXD("178", "230") EXD("177", "229") EXD("176", "228")
            PCVM PCVX
            "s_nop 1\n\t"
            SM("224:227", "240:243", "200:207", "v252") "s_nop 1\n\t"
            SM("16:19", "240:243", "128:135", "v252")
            "s_branch 13b\n\t"
            "5:\n\t" CN(1)
            LDX("192:195", "%[l16]", "%[bb]", "0") LDX("196:199", "%[l16]", "%[bb]", "1024")
            LDPA
            "s_sub_u32 %[lim], %[lim], 1\n\t"
            "s_nop 1\n\t"
            ZAM
            "s_cbranch_scc0 4b\n\t"
            "s_mov_b32 %[bad], 1500\n\t"
            "s_mov_b32 %[lim], 0\n\t"
            "s_branch 1b\n\t"
            "7:\n\t" CN(65536)
            LDPA
            "s_sub_u32 %[lim], %[lim], 1\n\t"
            "s_cbranch_scc0 1b\n\t"
            "s_mov_b32 %[bad], 1000\n\t"
            "s_branch 2b\n\t"
            "9:\n\t"
            : [lim] "+s"(lim), [bad] "+s"(bad), "+{v[128:159]}"(yb) TSOUT CNOUT
            : "{v[32:63]}"(wO[0]), "{v[64:95]}"(wO[1]), "{v[104:111]}"(sel), "{v[112:113]}"(sel3), [sx] "v"(sidx), [l16] "v"(l * 16),
              [ra] "v"(r3a), [rs] "s"(rsS), [bb] "s"(bb), [bq] "s"(bq), [bm] "s"(bm), [cs] "s"(o_cs), [rc] "v"(l0 + C2_R3 + wu * 512 + l * 8)
            : "memory", "vcc", "scc", CLOB_PX, CLOB16(16, 17, 18, 19, 20, 21, 22, 23, 24, 25, 26, 27, 28, 29, 30, 31),
              CLOB16(224, 225, 226, 227, 228, 229, 230, 231, 232, 233, 234, 235, 236, 237, 238, 239), CLOB16(240, 241, 242, 243, 244, 245, 246, 247, 248, 249, 250, 251, 252, 253, 254, 254));
        if (__builtin_expect(bad != 0, 0)) TMO_SET((unsigned)bad)
        __builtin_amdgcn_s_setprio(0);
        if (s == 0 && wu == 1) { PST(6) }

        if (__builtin_expect(s <= KVS + 1, 0)) {
            if (s == 0 || __builtin_amdgcn_readfirstlane(*(volatile unsigned*)(chsm + C2_MB + 32 + ((s - 1) & 1) * 16 + 4 + (wu >> 1) * 4)) == 0u) {
                wait_ge<6>(kv_flag(a.sync, s + 1, grp, l), 1u, a.tmo, 3001u);
                const char* kp = arena(a.slots) + VX_OFF + blk_off(s + 1, ha, tau);
#pragma unroll
                for (int e = 0; e < 8; ++e) glds16h((unsigned)l * 16u, kp + e * 1024, l0 + (s & 1 ? C2_V : C2_K) + wu * 8192 + e * 1024);
                asm volatile("s_waitcnt vmcnt(0)" ::: "memory");
            }
        }
        {
            int k0 = KX_OFF + blk_off(s + 1, hs, tau) + jq * 1024;
            asm volatile(
                DSV("160:163", "0") DSV("164:167", "1024") DSV("168:171", "2048") DSV("172:175", "3072") DSV("176:179", "128") DSV("180:183", "1152") DSV("184:187", "2176") DSV("188:191", "3200")
                "s_cmp_lg_u32 %[wu], 1\n\t"
                "s_cbranch_scc1 1f\n\t"
                KLD("%[k0]")
                "1:\n\t"
                "s_waitcnt lgkmcnt(4)\n\t"
                PXT("192:195", "196:199", "200:203", "204:207", "160:163", "164:167", "168:171", "172:175")
                "s_waitcnt lgkmcnt(0)\n\t"
                PXT("208:211", "212:215", "216:219", "220:223", "176:179", "180:183", "184:187", "188:191")
                CVP("128", "192", "193") CVP("129", "194", "195") CVP("136", "196", "197") CVP("137", "198", "199") CVP("144", "200", "201") CVP("145", "202", "203") CVP("152", "204", "205") CVP("153", "206", "207")
                CVP("132", "208", "209") CVP("133", "210", "211") CVP("140", "212", "213") CVP("141", "214", "215") CVP("148", "216", "217") CVP("149", "218", "219") CVP("156", "220", "221") CVP("157", "222", "223")
                : "+{v[128:159]}"(yb), "+{v[96:103]}"(kx), [k0] "+s"(k0)
                : "{v[32:63]}"(wO[0]), "{v[64:95]}"(wO[1]), [lv] "v"(l0 + (s & 1 ? C2_V : C2_K) + wu * 8192 + voffX), [vk] "v"(voffK), [rs] "s"(rsS), [wu] "s"(wu)
                : "memory", "scc", CLOB_PX);
        }
        __builtin_amdgcn_sched_barrier(0);
        o_1 = o1_of(s + 1); o_2 = km_off(s + 1, hs, tau) + jq * 512;
        bq = x_off(s + 1, ha, tau); bm = m_off(s + 1, ha, tau); bb = v_off(s + 1, ha, tau);
        o_f = f_off(s + 1, grp, tau);
        o_cs = rec_base(s + 1) + C_OFF + c * 16384 + near_half(s + 1, tau) + wu * 2048 + grp * 256;
        asm volatile("" : "+s"(o_cs), "+s"(o_f), "+s"(o_1), "+s"(o_2), "+s"(bq), "+s"(bm), "+s"(bb));
        __builtin_amdgcn_s_barrier();
        asm volatile(
            "s_cmp_lg_u32 %[wu], 3\n\t"
            "s_cbranch_scc1 1f\n\t"
            "v_mov_b32 v160, %[lf]\n\t"
            PAUSE(PF)
            "ds_read_b32 v160, v160\n\t"
            "s_waitcnt lgkmcnt(0)\n\t"
            "v_cmp_eq_u32 vcc, %[tg], v160\n\t"
            "s_cbranch_vccz 1f\n\t"
            "s_mov_b32 exec_hi, 0\n\t"
            "buffer_store_dword v160, %[l4], %[rs], %[fo] offen sc1\n\t"
            "s_mov_b64 exec, -1\n\t"
            "1:\n\t"
            :: [wu] "s"(wu), [lf] "s"(l0 + C2_MB + 16), [tg] "s"(s + 1), [l4] "v"(l * 4), [rs] "s"(rsS), [fo] "s"(o_f + c * 128)
            : "memory", "scc", "vcc", "v160");
        __builtin_amdgcn_s_setprio(1);
    }
    {
        constexpr int s = NSEG - 1;
        const int l = lane_now(), i = l & 15, g = l >> 4;
        v4u qf[8], kf[8], pv[2], px[4];
        asm volatile("s_waitcnt vmcnt(0)" ::: "memory");
        __builtin_amdgcn_s_sleep(4);
#pragma unroll
        for (int e = 0; e < 8; ++e) kf[e] = ld_sc1s(rsS, l * 16, KX_OFF + blk_off(s, ha, tau) + e * 1024);
        hand_over(o_1, o_2, o_f, s);
#pragma unroll
        for (int e = 0; e < 8; ++e) qf[e] = *(const v4u*)(chsm + C2_V + 32768 + wu * 8192 + e * 1024 + l * 16);
        f32x4 sx[3] = {ZERO4, ZERO4, ZERO4};
        float mxx[2] = {-3.0e38f, -3.0e38f};
#pragma unroll
        for (int ks = 0; ks < 4; ++ks) {
            sx[0] = MM(kf[ks], qf[ks], sx[0]);
            sx[1] = MM(kf[ks], qf[4 + ks], sx[1]);
            sx[2] = MM(kf[4 + ks], qf[4 + ks], sx[2]);
        }
#pragma unroll
        for (int r = 0; r < 4; ++r) {
            if (4 * g + r > i) { sx[0][r] = -3.0e38f; sx[2][r] = -3.0e38f; }
            mxx[0] = fmaxf(mxx[0], sx[0][r]);
            mxx[1] = fmaxf(mxx[1], fmaxf(sx[1][r], sx[2][r]));
        }
        asm volatile("" : "+v"(sx[0]), "+v"(sx[1]), "+v"(sx[2]), "+v"(mxx[0]), "+v"(mxx[1]));
        const int bk = km_off(s, ha, tau);
#pragma nounroll
        for (int spin = 0;; ++spin) {
            asm volatile("" ::: "memory");
            pv[0] = ld_sc1s(rsS, l * 16, bb); pv[1] = ld_sc1s(rsS, l * 16, bb + 1024);
#pragma unroll
            for (int ks = 0; ks < 4; ++ks) px[ks] = ld_sc1s(rsS, g * 128 + (i >> 1) * 16, bk + ks * 512);
            if (__all(mx4(mx4(mx4(mx4(mx4(mx4(0u, pv[0]), pv[1]), px[0]), px[1]), px[2]), px[3]) != SENT)) break;
            if (dead) break;
            if (spin > SPIN_LIMIT) { TMO_SET(1700u) break; }
        }
#define SUB8(v, o) v8u{(v)[(o)], (v)[(o) + 1], (v)[(o) + 2], (v)[(o) + 3], (v)[(o) + 4], (v)[(o) + 5], (v)[(o) + 6], (v)[(o) + 7]}
#pragma unroll
        for (int ct = 0; ct < 4; ++ct) {
            f32x4 am = smm(pv[0], SUB8(wO[ct >> 1], (ct & 1) * 16), ZERO4, sidx);
            am = smm(pv[1], SUB8(wO[ct >> 1], (ct & 1) * 16 + 8), am, sidx);
            yb[8 * ct + 2] = cvt_pk_bf16(am[0] + am[1], 0.f); yb[8 * ct + 6] = cvt_pk_bf16(am[2] + am[3], 0.f);
        }
        const v8u one = {0x3F803F80u, 0x3F803F80u, 0x3F80u, 0u, 0x3F803F80u, 0x3F803F80u, 0x3F80u, 0u};
        const float SCL2E = 0.08838834764831845f * 1.4426950408889634f;
        float* xo = (float*)(chsm + C2_K) + (wu * 32 + 4 * g) * XP + i;
#pragma unroll
        for (int qt = 0; qt < 2; ++qt) {
            f32x4 sc[3] = {sx[qt], sx[2], ZERO4};
#pragma unroll
            for (int ks = 0; ks < 4; ++ks) sc[2] = MM(px[ks], qf[4 * qt + ks], sc[2]);
            float mx = fmaxf(mxx[qt], fmaxf(sc[2][1], sc[2][3]));
            mx = xor32_max(xor16_max(mx));
            const float nmx = -mx * SCL2E;
#define PE(x) __builtin_amdgcn_exp2f(fmaf(x, SCL2E, nmx))
            const float m0 = PE(sc[2][1]), m1 = PE(sc[2][3]);
            const v4u ae = {cvt_pk_bf16(PE(sc[0][0]), PE(sc[0][1])), cvt_pk_bf16(m0, m0), cvt_pk_bf16(PE(sc[0][2]), PE(sc[0][3])), 0u};
            const v4u ao = {qt ? cvt_pk_bf16(PE(sc[1][0]), PE(sc[1][1])) : 0u, 0u, qt ? cvt_pk_bf16(PE(sc[1][2]), PE(sc[1][3])) : 0u, cvt_pk_bf16(m1, m1)};
            const f32x4 sum = smm(ao, one, smm(ae, one, ZERO4, 0xE444), 0x4EEE);
#pragma unroll
            for (int ct = 0; ct < 4; ++ct) {
                const f32x4 o = smm(ao, SUB8(yb, 8 * ct), smm(ae, SUB8(yb, 8 * ct), ZERO4, 0xE444), 0x4EEE);
#pragma unroll
                for (int r = 0; r < 4; ++r) xo[(16 * qt + r) * XP + 16 * ct] = o[r] * __builtin_amdgcn_rcpf(sum[r]);
            }
        }
        __builtin_amdgcn_s_waitcnt(0xC07F);
        last_rows(chsm, rsS, wu, grp, c, tau);
        if (wu < 2) {
            v4u sel, p0, p1;
#pragma unroll
            for (int d = 0; d < 4; ++d) sel[d] = (l & 6) == 2 * d ? (0x3F80u << ((l & 1) * 16)) : 0u;
            const int xb = XB_OFF + ((c >> 1) * 8 + grp) * 16384 + (int)(tau & 1u) * 8192 + (c & 1) * 4096 + wu * 256;
            __builtin_amdgcn_s_sleep(DLY_C);
#pragma nounroll
            for (int spin = 0;; ++spin) {
                asm volatile("" ::: "memory");
                p0 = ld_sc1s(rsS, g * 512 + i * 16, xb); p1 = ld_sc1s(rsS, g * 512 + i * 16, xb + 2048);
                if (__all(mx4(mx4(0u, p0), p1) != SENT)) break;
                if (dead) break;
                if (spin > SPIN_LIMIT) { TMO_SET(1800u) break; }
            }
            const f32x4 d = MM(sel, p1, MM(sel, p0, ZERO4));
            if (g < 2) out8(a.out + (size_t)(s * SEG + 4 * grp + 2 * wu + (i >> 3)) * OUTW + 64 * c + 8 * (i & 7) + 4 * g, v2u{cvt_pk_bf16(d[0], d[1]), cvt_pk_bf16(d[2], d[3])});
        }
    }
}

struct cpx { float x, y; };
__device__ __forceinline__ cpx cmul(cpx a, cpx b) { return {a.x * b.x - a.y * b.y, a.x * b.y + a.y * b.x}; }
__device__ __forceinline__ cpx cadd(cpx a, cpx b) { return {a.x + b.x, a.y + b.y}; }
__device__ __forceinline__ cpx csub(cpx a, cpx b) { return {a.x - b.x, a.y - b.y}; }
template <int DIR> __device__ __forceinline__ cpx mul_i(cpx a) {
    return (DIR < 0) ? cpx{a.y, -a.x} : cpx{-a.y, a.x};
}
template <int DIR> __device__ __forceinline__ void r4(cpx& a, cpx& b, cpx& c, cpx& d) {
    cpx t0 = cadd(a, c), t1 = csub(a, c), t2 = cadd(b, d), t3 = mul_i<DIR>(csub(b, d));
    a = cadd(t0, t2); b = cadd(t1, t3); c = csub(t0, t2); d = csub(t1, t3);
}
template <int DIR> __device__ __forceinline__ cpx twc(float c, float s) { return cpx{c, (float)DIR * s}; }
template <int DIR> __device__ __forceinline__ void dft16(cpx (&v)[16]) {
#pragma unroll
    for (int a = 0; a < 4; ++a) r4<DIR>(v[a], v[a + 4], v[a + 8], v[a + 12]);
    const float C1 = 0.92387953251128674f, S1 = 0.38268343236508977f, R2 = 0.70710678118654752f;
    v[1 + 4] = cmul(v[1 + 4], twc<DIR>(C1, S1));
    v[1 + 8] = cmul(v[1 + 8], twc<DIR>(R2, R2));
    v[1 + 12] = cmul(v[1 + 12], twc<DIR>(S1, C1));
    v[2 + 4] = cmul(v[2 + 4], twc<DIR>(R2, R2));
    v[2 + 8] = mul_i<DIR>(v[2 + 8]);
    v[2 + 12] = cmul(v[2 + 12], twc<DIR>(-R2, R2));
    v[3 + 4] = cmul(v[3 + 4], twc<DIR>(S1, C1));
    v[3 + 8] = cmul(v[3 + 8], twc<DIR>(-R2, R2));
    v[3 + 12] = cmul(v[3 + 12], twc<DIR>(-C1, -S1));
#pragma unroll
    for (int d = 0; d < 4; ++d) r4<DIR>(v[4 * d], v[4 * d + 1], v[4 * d + 2], v[4 * d + 3]);
}
__device__ __forceinline__ constexpr int x16(int q) { return 4 * (q & 3) + (q >> 2); }
template <int DIR> __device__ __forceinline__ void dft8(cpx (&v)[8]) {
    r4<DIR>(v[0], v[2], v[4], v[6]);
    r4<DIR>(v[1], v[3], v[5], v[7]);
    const float R2 = 0.70710678118654752f;
    v[3] = cmul(v[3], twc<DIR>(R2, R2));
    v[5] = mul_i<DIR>(v[5]);
    v[7] = cmul(v[7], twc<DIR>(-R2, R2));
#pragma unroll
    for (int d = 0; d < 4; ++d) { cpx p = v[2 * d], q = v[2 * d + 1]; v[2 * d] = cadd(p, q); v[2 * d + 1] = csub(p, q); }
}
__device__ __forceinline__ int pad(int i) { return i + (i >> 4); }
__device__ __forceinline__ cpx expi(float x) { float s, c; sincospif(x, &s, &c); return cpx{c, s}; }
template <int R, int DIR> __device__ __forceinline__ void twiddle(cpx* v, cpx w1) {
    cpx w[R];
    w[1] = w1;
#pragma unroll
    for (int r = 2; r < R; ++r) w[r] = (r & 1) ? cmul(w[r - 1], w1) : cmul(w[r / 2], w[r / 2]);
#pragma unroll
    for (int r = 1; r < R; ++r) v[r] = DIR < 0 ? cmul(v[r], w[r]) : cpx{v[r].x * w[r].x + v[r].y * w[r].y, v[r].y * w[r].x - v[r].x * w[r].y};
}
struct TwBase { cpx b; cpx c[4]; };
template <int N> __device__ __forceinline__ TwBase make_tw(int t) {
    TwBase tb;
    tb.b = expi(-(float)(t & 15) * (1.0f / 128.0f));
    constexpr int NQ = (N == 1024) ? 4 : 2, T = N / 16;
#pragma unroll
    for (int q = 0; q < NQ; ++q) tb.c[q] = expi(-(float)(t + T * q) * (2.0f / (float)N));
    return tb;
}
template <int N> __device__ __forceinline__ void fft_sync() {
    if constexpr (N == 1024) { __builtin_amdgcn_fence(__ATOMIC_ACQ_REL, "wavefront"); __builtin_amdgcn_wave_barrier(); } else __syncthreads();
}
template <int N, int DIR, bool RIN, bool ROUT>
__device__ __forceinline__ void fft_reg(cpx* buf, const TwBase& tb, int t, cpx (&v)[16]) {
    constexpr int T = N / 16, TP = T + T / 16;
    cpx* const bt = buf + pad(t);
    if constexpr (!RIN) {
#pragma unroll
        for (int r = 0; r < 16; ++r) v[r] = bt[TP * r];
        fft_sync<N>();
    }
    dft16<DIR>(v);
#pragma unroll
    for (int q = 0; q < 16; ++q) buf[17 * t + x16(q)] = v[q];
    fft_sync<N>();
    {
#pragma unroll
        for (int r = 0; r < 16; ++r) v[r] = bt[TP * r];
        fft_sync<N>();
        twiddle<16, DIR>(v, tb.b);
        dft16<DIR>(v);
        cpx* const bo = buf + (t >> 4) * 272 + (t & 15);
#pragma unroll
        for (int q = 0; q < 16; ++q) bo[17 * x16(q)] = v[q];
        fft_sync<N>();
    }
    if constexpr (N == 1024) {
#pragma unroll
        for (int q = 0; q < 4; ++q)
#pragma unroll
            for (int r = 0; r < 4; ++r) v[4 * q + r] = bt[68 * q + 272 * r];
#pragma unroll
        for (int q = 0; q < 4; ++q) {
            twiddle<4, DIR>(&v[4 * q], tb.c[q]);
            r4<DIR>(v[4 * q], v[4 * q + 1], v[4 * q + 2], v[4 * q + 3]);
#pragma unroll
            for (int r = 0; r < 4; ++r) bt[68 * q + 272 * r] = v[4 * q + r];
        }
        fft_sync<N>();
    } else {
        cpx u[2][8];
#pragma unroll
        for (int q = 0; q < 2; ++q)
#pragma unroll
            for (int r = 0; r < 8; ++r) u[q][r] = bt[136 * q + 272 * r];
        if constexpr (ROUT) fft_sync<N>();
#pragma unroll
        for (int q = 0; q < 2; ++q) {
            twiddle<8, DIR>(u[q], tb.c[q]);
            dft8<DIR>(u[q]);
#pragma unroll
            for (int d = 0; d < 4; ++d) {
                if constexpr (ROUT) { v[q + 2 * d] = u[q][2 * d]; v[q + 2 * d + 8] = u[q][2 * d + 1]; }
                else { bt[136 * q + 272 * d] = u[q][2 * d]; bt[136 * q + 272 * (d + 4)] = u[q][2 * d + 1]; }
            }
        }
        if constexpr (!ROUT) fft_sync<N>();
    }
}
__device__ __forceinline__ int f_hi(int rb, int j, unsigned tw) { j = (j + rb) & 7; return rb * 65536 + (j >> 1) * 16384 + (j & 1) * 4096 + (int)((tw ^ (unsigned)(rb >> 5)) & 1u) * 8192; }
__device__ __forceinline__ int f_lo(int rb, int x) { return (x + rb * 128) & 4095; }
__device__ __forceinline__ int w_hi(int k, int j, unsigned tw) { return k * 16384 + ((j + k) & 15) * 512 + (int)((tw ^ (unsigned)(k >> 7)) & 1u) * 8192; }
__device__ __forceinline__ int w_lo(int k, int x) { return (x + k * 128) & 511; }
constexpr int PB1 = 1024 + 64, PB2 = 2048 + 128;
typedef float f32x4v __attribute__((ext_vector_type(4)));
typedef unsigned u32x4v __attribute__((ext_vector_type(4)));

__device__ __forceinline__ void fft_rows_fwd(const u16* __restrict__ x, rsrc_t rsF, const unsigned tw, int rb, char* smem, int tid, const char* wr, const char* wi, const unsigned spare) {
    cpx (*buf)[PB1] = (cpx (*)[PB1])smem;
    int f = tid >> 6, t = tid & 63;
    const u16* p0 = x + (size_t)(rb * 8 + 2 * f) * 1024 + 16 * t;
    u32x4v a0 = *(const u32x4v*)p0, a1 = *(const u32x4v*)(p0 + 8), b0 = *(const u32x4v*)(p0 + 1024), b1 = *(const u32x4v*)(p0 + 1032);
    const TwBase tb = make_tw<1024>(t);
    for (int it = 0; it < 4; ++it, rb += 64) {
        asm volatile("" : "+v"(tid));
        f = tid >> 6; t = tid & 63;
        __syncthreads();
        cpx* d = &buf[f][17 * t];
#pragma unroll
        for (int e = 0; e < 4; ++e) {
            d[2 * e] = cpx{__uint_as_float(a0[e] << 16), __uint_as_float(b0[e] << 16)};
            d[2 * e + 1] = cpx{__uint_as_float(a0[e] & 0xFFFF0000u), __uint_as_float(b0[e] & 0xFFFF0000u)};
            d[8 + 2 * e] = cpx{__uint_as_float(a1[e] << 16), __uint_as_float(b1[e] << 16)};
            d[8 + 2 * e + 1] = cpx{__uint_as_float(a1[e] & 0xFFFF0000u), __uint_as_float(b1[e] & 0xFFFF0000u)};
        }
        if (it < 3) p0 += 512 * 1024;
        touch(((it & 1) ? wi : wr) + (size_t)((rb >> 1) & 31) * 32832 + (it >> 1) * 32 * 32832 + ((rb & 1) * 128 + (tid >> 1)) * 128, spare);
        a0 = *(const u32x4v*)p0; a1 = *(const u32x4v*)(p0 + 8); b0 = *(const u32x4v*)(p0 + 1024); b1 = *(const u32x4v*)(p0 + 1032);
        fft_sync<1024>();
        cpx z[16];
        fft_reg<1024, -1, false, false>(buf[f], tb, t, z);
        __syncthreads();
        const cpx* pk = &buf[tid & 3][pad(tid >> 2)];
        const cpx* pn = &buf[tid & 3][pad(1024 - (tid >> 2))];
        const int vo = f_lo(rb, tid * 16);
#pragma unroll
        for (int m = 0; m < 8; ++m) {
            const cpx zk = pk[68 * m], zn = m == 0 && tid < 4 ? pk[544] : pn[-68 * m];
            f32x4v o = {zk.x + zn.x, zk.y - zn.y, zk.y + zn.y, zn.x - zk.x};
            if (m == 0 && tid < 4) o = f32x4v{2.f * zk.x, 2.f * zn.x, 2.f * zk.y, 2.f * zn.y};
            st_sc1s(rsF, vo, f_hi(rb, m, tw), __builtin_bit_cast(v4u, o));
        }
    }
}
__device__ __forceinline__ void filt_transpose(const u16* __restrict__ wr, const u16* __restrict__ wi, rsrc_t rsW, const unsigned tw, const int tb, char* smem, const int tid) {
    const v4u* s0 = (const v4u*)(wr + (size_t)tb * 32 * 513);
    const v4u* s1 = (const v4u*)(wi + (size_t)tb * 32 * 513);
    v4u r0[5], r1[5];
#pragma unroll
    for (int e = 0; e < 5; ++e) {
        const int idx = tid + 512 * e, ic = idx < 2052 ? idx : 2051;
        r0[e] = s0[ic]; r1[e] = s1[ic];
    }
#pragma unroll
    for (int e = 0; e < 5; ++e) {
        const int idx = tid + 512 * e;
        if (idx < 2052) { *(v4u*)(smem + idx * 16) = r0[e]; *(v4u*)(smem + 32832 + idx * 16) = r1[e]; }
    }
    __syncthreads();
    const u16* a0 = (const u16*)smem;
    const u16* a1 = (const u16*)(smem + 32832);
    for (int idx = tid; idx < 513 * 8; idx += 512) {
        const int k = idx >> 3, n4 = (idx & 7) * 4;
        v4u v;
#pragma unroll
        for (int j = 0; j < 4; ++j) v[j] = (unsigned)a0[(n4 + j) * 513 + k] | ((unsigned)a1[(n4 + j) * 513 + k] << 16);
        st_sc1(rsW, w_hi(k, tb >> 2, tw) + w_lo(k, (tb & 3) * 128 + n4 * 4), v);
    }
}

__device__ __forceinline__ void fft_cols(rsrc_t rsF, rsrc_t rsW, const unsigned tw, int blk, const int n, char* smem, int tid) {
    cpx (*buf)[PB2] = (cpx (*)[PB2])smem;
    int c = tid >> 7, t = tid & 127;
    f32x4v v[8];
    unsigned wv[16], wn[16];
    auto wld = [&](const int k, const int n) { return __builtin_amdgcn_raw_buffer_load_b32(rsW, w_hi(k, n >> 7, tw) + w_lo(k, (n & 127) * 4), 0, 0); };
    auto f_at = [&](const int bk) { return f_hi(tid >> 3, bk >> 5, tw) + f_lo(tid >> 3, (bk & 31) * 128 + (tid & 7) * 16); };
    auto fetch = [&](const int bk) {
        const int kc = bk * 2 + __builtin_amdgcn_readfirstlane(c), vo = f_at(bk), vw = w_lo(kc, t * 4);
#pragma unroll
        for (int m = 0; m < 8; ++m) v[m] = __builtin_bit_cast(f32x4v, __builtin_amdgcn_raw_buffer_load_b128(rsF, m & 1 ? vo ^ 8192 : vo, m * 2097152, 0));
#pragma unroll
        for (int m = 0; m < 16; ++m) wn[m] = __builtin_amdgcn_raw_buffer_load_b32(rsW, vw, w_hi(kc, m, tw), 0);
    };
    fetch(blk);
    const TwBase tb = make_tw<2048>(t);
    for (int it = 0; it < n; ++it, blk += 64) {
        asm volatile("" : "+v"(tid));
        c = tid >> 7; t = tid & 127;
        __syncthreads();
        cpx* const d = &buf[(tid >> 2) & 1][pad((tid >> 3) * 8 + 2 * (tid & 3))];
#pragma unroll
        for (int m = 0; m < 8; ++m) { d[272 * m] = cpx{v[m][0], v[m][1]}; d[272 * m + 1] = cpx{v[m][2], v[m][3]}; }
#pragma unroll
        for (int m = 0; m < 16; ++m) wv[m] = wn[m];
        fetch(it + 1 < n ? blk + 64 : blk);
        __syncthreads();
        cpx z[16];
        fft_reg<2048, -1, false, true>(buf[c], tb, t, z);
        auto wcx = [](const unsigned w) { return cpx{__uint_as_float(w << 16), __uint_as_float(w & 0xFFFF0000u)}; };
        if (blk < 2) {
#pragma unroll
            for (int m = 0; m < 16; ++m) buf[c][pad(t + 128 * m)] = z[m];
            __syncthreads();
            if (blk == 0 && c == 0) {
#pragma unroll
                for (int m = 0; m < 16; ++m) {
                    const int n = t + 128 * m, nn = (2048 - n) & 2047;
                    const cpx zn = buf[0][pad(nn)], a = wcx(wv[m]), b = wcx(wld(0, nn)), e = wcx(wld(512, n)), f = wcx(wld(512, nn));
                    const cpx h0 = cmul(cpx{0.5f * (z[m].x + zn.x), 0.5f * (z[m].y - zn.y)}, cpx{0.5f * (a.x + b.x), 0.5f * (a.y - b.y)});
                    const cpx h5 = cmul(cpx{0.5f * (z[m].y + zn.y), -0.5f * (z[m].x - zn.x)}, cpx{0.5f * (e.x + f.x), 0.5f * (e.y - f.y)});
                    z[m] = cpx{h0.x - h5.y, h0.y + h5.x};
                    wv[m] = 0x3F80u;
                }
            }
            __syncthreads();
        }
#pragma unroll
        for (int m = 0; m < 16; ++m) z[m] = cmul(z[m], wcx(wv[m]));
        fft_reg<2048, 1, true, false>(buf[c], tb, t, z);
        const int vo = f_at(blk);
#pragma unroll
        for (int m = 0; m < 8; ++m)
            st_sc1s(rsF, m & 1 ? vo ^ 8192 : vo, m * 2097152, __builtin_bit_cast(v4u, f32x4v{d[272 * m].x, d[272 * m].y, d[272 * m + 1].x, d[272 * m + 1].y}));
    }
}

__device__ __forceinline__ void fft_rows_inv(rsrc_t rsF, const unsigned tw, u16* __restrict__ out, int rb, const int step, const int n, char* smem, int tid) {
    cpx (*buf)[PB1] = (cpx (*)[PB1])smem;
    int f = tid >> 6, t = tid & 63;
    f32x4v v[8];
    auto fetch = [&](const int r) {
        const int vo = f_lo(r, tid * 16);
#pragma unroll
        for (int m = 0; m < 8; ++m) v[m] = __builtin_bit_cast(f32x4v, __builtin_amdgcn_raw_buffer_load_b128(rsF, vo, f_hi(r, m, tw), 0));
    };
    fetch(rb);
    const TwBase tb = make_tw<1024>(t);
    for (int it = 0; it < n; ++it, rb += step) {
        asm volatile("" : "+v"(tid));
        f = tid >> 6; t = tid & 63;
        __syncthreads();
        cpx* pk = &buf[tid & 3][pad(tid >> 2)];
        cpx* pn = &buf[tid & 3][pad(1024 - (tid >> 2))];
#pragma unroll
        for (int m = 0; m < 8; ++m) {
            if (m == 0 && tid < 4) { pk[0] = cpx{v[0][0], v[0][2]}; pk[544] = cpx{v[0][1], v[0][3]}; }
            else { pk[68 * m] = cpx{v[m][0] - v[m][3], v[m][1] + v[m][2]}; pn[-68 * m] = cpx{v[m][0] + v[m][3], v[m][2] - v[m][1]}; }
        }
        fetch(it + 1 < n ? rb + step : rb);
        __syncthreads();
        cpx z[16];
        fft_reg<1024, 1, false, false>(buf[f], tb, t, z);
        const float sc = 0.5f / (1024.0f * 2048.0f);
        const cpx* d = &buf[f][17 * t];
        u32x4v a[2], b[2];
#pragma unroll
        for (int h = 0; h < 2; ++h)
#pragma unroll
            for (int e = 0; e < 4; ++e) {
                cpx p = d[8 * h + 2 * e], q = d[8 * h + 2 * e + 1];
                a[h][e] = cvt_pk_bf16(p.x * sc, q.x * sc);
                b[h][e] = cvt_pk_bf16(p.y * sc, q.y * sc);
            }
        u16* o0 = out + (size_t)(rb * 8 + 2 * f) * OUTW + 1024 + 16 * t;
        *(u32x4v*)o0 = a[0]; *(u32x4v*)(o0 + 8) = a[1];
        *(u32x4v*)(o0 + OUTW) = b[0]; *(u32x4v*)(o0 + OUTW + 8) = b[1];
    }
}

__device__ __forceinline__ void glds16s(unsigned voff, const void* sbase, unsigned lds_dst) {
    unsigned keep;
    asm volatile("s_mov_b32 %0, m0\n\ts_mov_b32 m0, %3\n\ts_nop 0\n\tglobal_load_lds_dwordx4 %1, %2\n\ts_mov_b32 m0, %0" : "=&s"(keep) : "v"(voff), "s"(sbase), "s"(lds_dst) : "memory");
}
#define K8_BAR do { asm volatile("" ::: "memory"); __builtin_amdgcn_s_barrier(); asm volatile("" ::: "memory"); } while (0)
#define K8_WV(n) asm volatile("s_waitcnt vmcnt(" #n ")" ::: "memory")
#define K8_WL(n) asm volatile("s_waitcnt lgkmcnt(" #n ")" ::: "memory")
#define K8_SCHED __builtin_amdgcn_sched_barrier(0)
template <int BN>
__device__ __forceinline__ int bswz2(int row) {
    if (BN >= 128) return (row & 3) | (((row >> 3) & 1) << 2);
    else return ((row >> 1) & 1) | (((row >> 3) & 1) << 1);
}
template <int BM>
__device__ __forceinline__ void gemm_tile(u16* sm, const char* A, const int s0, const unsigned tau, const u16* B, u16* C) {
    constexpr int K = HD, LDB = DM, LDC = OUTW;
    constexpr int BN = 128, BK = 64, ST = BM == 128 ? 4 : 3, WN = BM == 128 ? 4 : 2;
    constexpr int ATILE = BM * BK, BTILE = BK * BN;
    constexpr int NT = 8 / WN, GA = BM / 64, GB = 2, GS = GA + GB;
    const int tid = opaque_tid(), w = tid >> 6, l = tid & 63, i = l & 15, g = l >> 4;
    const int wr = w / WN, wc = w % WN;
    const unsigned wu = __builtin_amdgcn_readfirstlane(w);
    unsigned vb;
    { const int row = l >> 4, c = l & 15; vb = (unsigned)(row * LDB + 8 * (c ^ ((row | ((wu & 1) << 2)) << 1))) * 2u; }
    const int sg = s0 + (BM == 128 ? wu >> 1 : wu);
    const char* ab = A + (BM == 128 ? (wu & 1) * 4096 : 0);
    const char* bb = (const char*)(B + (size_t)wu * 8 * LDB);
    const unsigned lds0 = (unsigned)(uintptr_t)(__attribute__((address_space(3))) u16*)sm;
    const unsigned va = (unsigned)l * 16u, va2 = va + 2048u, vb2 = vb + 4u * LDB * 2u - 1024u;
    auto req = [&](const int buf, const int kt) {
        const unsigned d = lds0 + buf * (ATILE + BTILE) * 2;
        const int wh = kt >> 1;
        const char* pa = ab + ((blk_off(sg, (wh & 7) * 4 + (wh >> 3), tau) ^ 8192) + (kt & 1) * 2048);
        const char* pb = bb + (size_t)kt * BK * LDB * 2;
        unsigned keep;
        if (BM == 128) asm volatile("s_mov_b32 %0, m0\n\ts_mov_b32 m0, %1\n\ts_nop 0\n\tglobal_load_lds_dwordx4 %3, %5\n\tglobal_load_lds_dwordx4 %3, %5 offset:1024\n\t"
                     "s_mov_b32 m0, %2\n\ts_nop 0\n\tglobal_load_lds_dwordx4 %6, %8\n\tglobal_load_lds_dwordx4 %7, %8 offset:1024\n\ts_mov_b32 m0, %0"
                     : "=&s"(keep) : "s"(d + wu * GA * 1024), "s"(d + ATILE * 2 + wu * GB * 1024), "v"(va), "v"(va2), "s"(pa), "v"(vb), "v"(vb2), "s"(pb) : "memory");
        else asm volatile("s_mov_b32 %0, m0\n\ts_mov_b32 m0, %1\n\ts_nop 0\n\tglobal_load_lds_dwordx4 %3, %5\n\tglobal_load_lds_dwordx4 %3, %5 offset:1024\n\t"
                     "global_load_lds_dwordx4 %4, %5 offset:2048\n\tglobal_load_lds_dwordx4 %4, %5 offset:3072\n\t"
                     "s_mov_b32 m0, %2\n\ts_nop 0\n\tglobal_load_lds_dwordx4 %6, %8\n\tglobal_load_lds_dwordx4 %7, %8 offset:1024\n\ts_mov_b32 m0, %0"
                     : "=&s"(keep) : "s"(d + wu * GA * 1024), "s"(d + ATILE * 2 + wu * GB * 1024), "v"(va), "v"(va2), "s"(pa), "v"(vb), "v"(vb2), "s"(pb) : "memory");
    };
    f32x4 acc[4][NT] = {};
    constexpr int nt = K / BK;
#pragma unroll
    for (int s = 0; s < ST - 1; ++s) req(s, s);
    int boff[NT];
    const int aoff = wr * 4096 + l * 8;
#pragma unroll
    for (int n = 0; n < NT; ++n) { const int row = 8 * g + (i >> 2); boff[n] = row * BN + 16 * ((wc * NT + n) ^ bswz2<BN>(row)) + 4 * (i & 3); }
    asm volatile("s_waitcnt vmcnt(%0)" :: "i"((ST - 2) * GS) : "memory");
    K8_BAR;
    if (wu >= 4) K8_BAR;
    for (int t0 = 0; t0 < nt + ST - 1 - (nt + ST - 1) % ST; t0 += ST) {
#pragma unroll
        for (int rb = 0; rb < ST; ++rb) {
            const int t = t0 + rb;
            if (nt % ST != 0 && t >= nt) break;
            const int kk = min(t + ST - 1, nt - 1);
            const u16* as = sm + rb * (ATILE + BTILE);
            const u16* bs = as + ATILE;
            bf16x8 af[2][4], bf[2][NT];
#pragma unroll
            for (int ks = 0; ks < 2; ++ks) {
#pragma unroll
                for (int n = 0; n < NT; ++n) bf[ks][n] = tr_frag(bs + boff[n] + ks * 32 * BN, bs + boff[n] + ks * 32 * BN + 4 * BN);
#pragma unroll
                for (int m = 0; m < 4; ++m) af[ks][m] = *(const bf16x8*)(as + aoff + m * 1024 + ks * 512);
            }
            K8_SCHED;
            req((rb + ST - 1) % ST, kk);
            K8_WL(0);
            if (wu >= 4) asm volatile("s_waitcnt vmcnt(%0)" :: "i"((ST - 2) * GS) : "memory");
            K8_BAR;
            __builtin_amdgcn_s_setprio(1);
#pragma unroll
            for (int q = 0; q < 8; ++q)
#pragma unroll
                for (int n = 0; n < NT; ++n) acc[q & 3][n] = __builtin_amdgcn_mfma_f32_16x16x32_bf16(bf[q >> 2][n], af[q >> 2][q & 3], acc[q & 3][n], 0, 0, 0);
            __builtin_amdgcn_s_setprio(0);
            K8_SCHED;
            asm volatile("s_waitcnt vmcnt(%0)" :: "i"((ST - 2) * GS) : "memory");
            K8_BAR;
        }
    }
    if (wu < 4) K8_BAR;
    asm volatile("s_waitcnt vmcnt(0)" ::: "memory");
#pragma unroll
    for (int m = 0; m < 4; ++m)
#pragma unroll
        for (int n = 0; n < NT; n += 2) {
            auto r0 = __builtin_amdgcn_permlane16_swap(cvt_pk_bf16(acc[m][n][0], acc[m][n][1]), cvt_pk_bf16(acc[m][n + 1][0], acc[m][n + 1][1]), false, false);
            auto r1 = __builtin_amdgcn_permlane16_swap(cvt_pk_bf16(acc[m][n][2], acc[m][n][3]), cvt_pk_bf16(acc[m][n + 1][2], acc[m][n + 1][3]), false, false);
            *(v4u*)(C + (wr * 64 + m * 16 + i) * LDC + (wc * NT + n + (g & 1)) * 16 + 8 * (g >> 1)) = v4u{r0[0], r1[0], r0[1], r1[1]};
        }
}

__device__ __forceinline__ void xattn_unit(const ChainArgs& a, rsrc_t rsQ, rsrc_t rsK, rsrc_t rsV, rsrc_t rsS, const int s, const int sn, const int h, u16 (*Vs)[VP], unsigned* mbox, const unsigned tau, int& pend, bool& ready) {
    const int l = opaque_tid() & 63, i = l & 15, g = l >> 4;
    const unsigned known = __builtin_amdgcn_readfirstlane(*(volatile unsigned*)mbox);
    const bool early = known > (unsigned)(s + 1);
    const int bq = km_off(s, h, tau), bb = v_off(s, h, tau);
    auto count = [&](const int t) { arrive(a.sync, t < SSEG ? SY_OX + (t >> 2) * 8 : SY_OXS + (t - SSEG) * 8, t < SSEG ? 128u : 32u, 8); };
    const int xk = blk_off(s, h, tau), xq = xk ^ 8192;
    v4u vv[8], vm[2], k0f[4], k1f[4], kmf[4], q0f[4], q1f[4];
#pragma unroll
    for (int e = 0; e < 8; ++e) vv[e] = ld_sc1s(rsV, l * 16 + e * 1024, xk);
#pragma unroll
    for (int ks = 0; ks < 4; ++ks) {
        k0f[ks] = ld_sc1s(rsK, l * 16 + ks * 1024, xk);
        k1f[ks] = ld_sc1s(rsK, l * 16 + ks * 1024, xk + 4096);
        q0f[ks] = ld_sc1s(rsQ, l * 16 + ks * 1024, xq);
        q1f[ks] = ld_sc1s(rsQ, l * 16 + ks * 1024, xq + 4096);
    }
#pragma unroll
    for (int e = 0; e < 2; ++e) vm[e] = ld_sc1s(rsS, l * 16 + e * 1024, early ? bb : 1 << 30);
#pragma unroll
    for (int ks = 0; ks < 4; ++ks) kmf[ks] = ld_sc1s(rsS, ks * 512 + g * 128 + (i & 7) * 16, early ? bq : 1 << 30);
    const unsigned fl = ld_flag(a.sync + (l >= 8 ? SY_PROG * 32 : (((l & 4) ? SY_Q : SY_KV) + (min(sn, NSEG - 1) >> 3) * 8) * 32 + (l & 3)) + (h >> 2) * 32);
    if (pend >= 0) { asm volatile("s_waitcnt vmcnt(31)" ::: "memory"); count(pend); }
#pragma unroll
    for (int e = 0; e < 8; ++e) *(v4u*)&Vs[16 * (e >> 2) + i][32 * (e & 3) + 8 * g] = vv[e];
    const float scale = 0.08838834764831845f;
    const float L2E = 1.4426950408889634f;
    auto round2 = [&](const f32x4 c, float* o) {
#pragma unroll
        for (int p = 0; p < 2; ++p) {
            unsigned u = cvt_pk_bf16(c[2 * p], c[2 * p + 1]);
            float lo = __uint_as_float(u << 16) * scale, hi = __uint_as_float(u & 0xFFFF0000u) * scale;
            unsigned u2 = cvt_pk_bf16(lo, hi);
            o[2 * p] = __uint_as_float(u2 << 16);
            o[2 * p + 1] = __uint_as_float(u2 & 0xFFFF0000u);
        }
    };
    float sv[2][12], mxx[2];
    {
        f32x4 sx[3] = {};
#pragma unroll
        for (int ks = 0; ks < 4; ++ks) {
            sx[0] = __builtin_amdgcn_mfma_f32_16x16x32_bf16(as_frag(k0f[ks]), as_frag(q0f[ks]), sx[0], 0, 0, 0);
            sx[1] = __builtin_amdgcn_mfma_f32_16x16x32_bf16(as_frag(k0f[ks]), as_frag(q1f[ks]), sx[1], 0, 0, 0);
            sx[2] = __builtin_amdgcn_mfma_f32_16x16x32_bf16(as_frag(k1f[ks]), as_frag(q1f[ks]), sx[2], 0, 0, 0);
        }
        round2(sx[0], &sv[0][0]); round2(sx[1], &sv[1][0]); round2(sx[2], &sv[1][4]);
        mxx[0] = mxx[1] = -3.0e38f;
#pragma unroll
        for (int r = 0; r < 4; ++r) {
            if (4 * g + r > i) { sv[0][r] = -3.0e38f; sv[1][4 + r] = -3.0e38f; }
            mxx[0] = fmaxf(mxx[0], sv[0][r]);
            mxx[1] = fmaxf(mxx[1], fmaxf(sv[1][r], sv[1][4 + r]));
        }
    }
    const u16* vb = &Vs[4 * g + (i >> 2)][4 * (i & 3)];
    bf16x8 vfx[8];
#pragma unroll
    for (int t = 0; t < 8; ++t) vfx[t] = tr_frag(vb + 16 * t, vb + 16 * VP + 16 * t);
#pragma unroll
    for (int t = 0; t < 8; ++t) asm volatile("" : "+v"(vfx[t]));
#pragma unroll
    for (int r = 0; r < 4; ++r) asm volatile("" : "+v"(sv[0][r]), "+v"(sv[1][r]), "+v"(sv[1][4 + r]));
    asm volatile("" : "+v"(mxx[0]), "+v"(mxx[1]));
    bool have = false;
    unsigned seen = __builtin_amdgcn_readlane(fl, 8);
    ready = (sn >> 3) == (s >> 3) || (__ballot(fl != 0u) & 255ull) == 255ull;
    if (early) {
        if ((h & 3) == 0 && l == 0) atomicMax(mbox, seen);
        unsigned m = mx4(mx4(0u, vm[0]), vm[1]);
#pragma unroll
        for (int ks = 0; ks < 4; ++ks) m = mx4(m, kmf[ks]);
        have = __all(m != SENT);
    } else if ((h & 3) == 0) {
        const unsigned* pw = a.sync + (SY_PROG + (h >> 2)) * 32;
        unsigned v = seen;
        if (v <= (unsigned)(s + 1)) {
            if (s < NSEG - XA_FAST) { wait_ge<32>(pw, (unsigned)(s + 1), a.tmo, 4000u); __builtin_amdgcn_s_sleep(24); }
            else { wait_ge<6>(pw, (unsigned)(s + 1), a.tmo, 4000u); __builtin_amdgcn_s_sleep(XA_D); }
            v = (unsigned)(s + 1);
        }
        if (l == 0) atomicMax(mbox, v);
    } else {
#pragma nounroll
        for (int spin = 0; spin < 4000000 && *(volatile unsigned*)mbox < (unsigned)(s + 1); ++spin) __builtin_amdgcn_s_sleep(2);
        asm volatile("" ::: "memory");
    }
#pragma nounroll
    for (int spin = 0; !have; ++spin) {
        asm volatile("" ::: "memory");
#pragma unroll
        for (int e = 0; e < 2; ++e) vm[e] = ld_sc1s(rsS, l * 16 + e * 1024, bb);
#pragma unroll
        for (int ks = 0; ks < 4; ++ks) kmf[ks] = ld_sc1s(rsS, ks * 512 + g * 128 + (i & 7) * 16, bq);
        unsigned m = mx4(mx4(0u, vm[0]), vm[1]);
#pragma unroll
        for (int ks = 0; ks < 4; ++ks) m = mx4(m, kmf[ks]);
        if (__all(m != SENT) || spin > 20000) break;
        __builtin_amdgcn_s_sleep(2);
    }
#pragma unroll
    for (int e = 0; e < 8; ++e) *(unsigned*)&Vs[32 + (i >> 2) + 4 * ((i >> 1) & 1)][64 * (e >> 2) + 16 * g + 4 * (e & 3) + 2 * (i & 1)] = vm[e >> 2][e & 3];
    f32x4 sm_[2] = {};
#pragma unroll
    for (int ks = 0; ks < 4; ++ks) {
        sm_[0] = __builtin_amdgcn_mfma_f32_16x16x32_bf16(as_frag(kmf[ks]), as_frag(q0f[ks]), sm_[0], 0, 0, 0);
        sm_[1] = __builtin_amdgcn_mfma_f32_16x16x32_bf16(as_frag(kmf[ks]), as_frag(q1f[ks]), sm_[1], 0, 0, 0);
    }
    v4u pf[2][2];
#pragma unroll
    for (int qt = 0; qt < 2; ++qt) {
        round2(sm_[qt], &sv[qt][8]);
        float mx = fmaxf(fmaxf(mxx[qt], fmaxf(sv[qt][8], sv[qt][9])), fmaxf(sv[qt][10], sv[qt][11]));
        mx = xor16_max(mx);
        mx = xor32_max(mx);
        const float nmx = -mx * L2E;
        float sum = 0.f;
#pragma unroll
        for (int e = 0; e < 12; ++e) {
            if (qt == 0 && e >= 4 && e < 8) { sv[qt][e] = 0.f; continue; }
            float ev = __builtin_amdgcn_exp2f(fmaf(sv[qt][e], L2E, nmx));
            if (e >= 8) ev = g < 2 ? ev : 0.f;
            sv[qt][e] = ev; sum += ev;
        }
        sum = xor16_sum(sum);
        sum = xor32_sum(sum);
        const float inv = 1.0f / sum;
        pf[qt][0][0] = cvt_pk_bf16(sv[qt][0] * inv, sv[qt][1] * inv);
        pf[qt][0][1] = cvt_pk_bf16(sv[qt][2] * inv, sv[qt][3] * inv);
        pf[qt][0][2] = qt ? cvt_pk_bf16(sv[qt][4] * inv, sv[qt][5] * inv) : 0u;
        pf[qt][0][3] = qt ? cvt_pk_bf16(sv[qt][6] * inv, sv[qt][7] * inv) : 0u;
        pf[qt][1][0] = cvt_pk_bf16(sv[qt][8] * inv, sv[qt][9] * inv);
        pf[qt][1][1] = cvt_pk_bf16(sv[qt][10] * inv, sv[qt][11] * inv);
        pf[qt][1][2] = 0u; pf[qt][1][3] = 0u;
    }
    v2u vmf[8];
#pragma unroll
    for (int t = 0; t < 8; ++t) vmf[t] = tr4(&Vs[32 + 4 * (g & 1) + (i >> 2)][16 * t + 4 * (i & 3)]);
#pragma unroll
    for (int t = 0; t < 8; t += 2) {
        unsigned pk[2][2][2];
#pragma unroll
        for (int tt = 0; tt < 2; ++tt) {
            const bf16x8 vf0 = vfx[t + tt];
            const bf16x8 vf1 = as_frag(v4u{vmf[t + tt][0], vmf[t + tt][1], 0u, 0u});
#pragma unroll
            for (int qt = 0; qt < 2; ++qt) {
                f32x4 o = {};
                o = __builtin_amdgcn_mfma_f32_16x16x32_bf16(vf0, as_frag(pf[qt][0]), o, 0, 0, 0);
                o = __builtin_amdgcn_mfma_f32_16x16x32_bf16(vf1, as_frag(pf[qt][1]), o, 0, 0, 0);
                pk[qt][tt][0] = cvt_pk_bf16(o[0], o[1]);
                pk[qt][tt][1] = cvt_pk_bf16(o[2], o[3]);
            }
        }
#pragma unroll
        for (int qt = 0; qt < 2; ++qt) {
            auto r0 = __builtin_amdgcn_permlane16_swap(pk[qt][0][0], pk[qt][1][0], false, false);
            auto r1 = __builtin_amdgcn_permlane16_swap(pk[qt][0][1], pk[qt][1][1], false, false);
            v4u o = {r0[0], r1[0], r0[1], r1[1]};
            st_sc1s(rsV, (t >> 1) * 1024 + ((g & 1) * 2 + (g >> 1)) * 256 + i * 16, xq + qt * 4096, o);
        }
    }
    pend = seen > (unsigned)(sn + 1) && ready ? s : -1;
    if (pend < 0) { asm volatile("s_waitcnt vmcnt(0)" ::: "memory"); count(s); }
}

__device__ __forceinline__ void tile_stream(const ChainArgs& a, const u16* X, unsigned long long todo, const int xc, unsigned& tau, const bool early, const int nw, u16* sm8 WEVP) {
    constexpr int K = DM, LDB = HD;
    const int tid = opaque_tid(), w = tid >> 6, l = tid & 63, i = l & 15, g = l >> 4;
    const int wr = w >> 2, wc = w & 3;
    const unsigned lds0 = (unsigned)(uintptr_t)(__attribute__((address_space(3))) u16*)sm8;
    const unsigned wu = __builtin_amdgcn_readfirstlane(w);
    const unsigned ldsw = lds0 + wu * 2048;
    unsigned voffA, voffB;
    { int row = 16 * w + (l >> 3), c = l & 7; voffA = (unsigned)(row * K + 8 * (c ^ (row & 7))) * 2u; }
    { int row = 8 * w + g, c = i; voffB = (unsigned)(row * LDB + 8 * (c ^ (bswz2<128>(row) << 1))) * 2u; }
    const unsigned voffA2 = voffA + 8u * K * 2u - 1024u, voffB2 = voffB + 4u * LDB * 2u - 1024u;
    auto st2 = [&](const unsigned v0, const unsigned v1, const char* p, const unsigned d) {
        unsigned keep;
        asm volatile("s_mov_b32 %0, m0\n\ts_mov_b32 m0, %1\n\ts_nop 0\n\tglobal_load_lds_dwordx4 %2, %4\n\tglobal_load_lds_dwordx4 %3, %4 offset:1024\n\ts_mov_b32 m0, %0"
                     : "=&s"(keep) : "s"(d), "v"(v0), "v"(v1), "s"(p) : "memory");
    };
    auto stA = [&](int b, int h, const char* p) {
        st2(voffA, voffA2, p + (size_t)(h * 128) * K * 2, ldsw + (2 * b + h) * 16384);
    };
    auto stB = [&](int b, int h, const char* p) {
        st2(voffB, voffB2, p + h * 2048, ldsw + 65536 + (2 * b + h) * 16384);
    };
    constexpr int SA = 128, SB = 64 * LDB * 2;
    auto a_of = [&](const unsigned t) { return (const char*)(X + (size_t)((t >> 4) & 7) * 256 * DM); };
    auto b_of = [&](const unsigned t) { const int kind = t & 7; return (const char*)((kind < 2 ? a.Wk : (kind < 4 ? a.Wv : a.Wq)) + hcol(4 * xc + (kind & 1) * 2)); };
    auto c_of = [&](const unsigned t) { const int kind = t & 7; return ((kind & 6) == 2 ? VX_OFF - KX_OFF : 0) + (int)((t >> 4) & 7) * 4194304 + blk_off(0, 4 * xc + (kind & 1) * 2, kind < 4 ? tau : tau ^ 1u); };
    auto publish = [&](const unsigned t) {
        const int kind = t & 7, ln = lane_now();
        if (wu == 0 && ln < (kind < 4 ? 1 : 2))
            __hip_atomic_store((gu32*)(a.sync + ((kind < 4 ? SY_KV : SY_Q) + ((t >> 4) & 7) * 8 + xc) * 32 + (kind < 4 ? kind : 2 * kind - 8 + ln)), 16u, __ATOMIC_RELAXED, __HIP_MEMORY_SCOPE_AGENT);
    };
    const char* smc = (const char*)sm8;
    int aoff[2], boff[2];
#pragma unroll
    for (int ks = 0; ks < 2; ++ks) aoff[ks] = (wr * 64 + i) * 128 + (((ks * 4 + g) ^ (i & 7)) << 4);
#pragma unroll
    for (int n = 0; n < 2; ++n) { int row = 8 * g + (i >> 2); boff[n] = 65536 + row * 256 + (((wc * 2 + n) ^ bswz2<128>(row)) << 5) + 8 * (i & 3); }
    f32x4 acc[2][2][4][2] = {};
    bf16x8 At[4][2], B0[2][2], B1[2][2];
#define K8_LDA(b, h) _Pragma("unroll") for (int m = 0; m < 4; ++m) _Pragma("unroll") for (int ks = 0; ks < 2; ++ks) \
        At[m][ks] = *(const bf16x8*)(smc + aoff[ks] + (2 * (b) + (h)) * 16384 + m * 2048)
#define K8_LDB(dst, b, h) _Pragma("unroll") for (int n = 0; n < 2; ++n) _Pragma("unroll") for (int ks = 0; ks < 2; ++ks) { \
        const char* p_ = smc + boff[n] + (2 * (b) + (h)) * 16384 + ks * 8192; \
        v4i16 lo_ = __builtin_amdgcn_ds_read_tr16_b64_v4i16((__attribute__((address_space(3))) v4i16*)p_); \
        v4i16 hi_ = __builtin_amdgcn_ds_read_tr16_b64_v4i16((__attribute__((address_space(3))) v4i16*)(p_ + 1024)); \
        dst[n][ks] = bf16x8{lo_[0], lo_[1], lo_[2], lo_[3], hi_[0], hi_[1], hi_[2], hi_[3]}; }
#define K8_MMA(ai, bj, Bt) do { __builtin_amdgcn_s_setprio(1); \
        _Pragma("unroll") for (int m = 0; m < 4; ++m) _Pragma("unroll") for (int n = 0; n < 2; ++n) _Pragma("unroll") for (int ks = 0; ks < 2; ++ks) \
            acc[ai][bj][m][n] = __builtin_amdgcn_mfma_f32_16x16x32_bf16(Bt[n][ks], At[m][ks], acc[ai][bj][m][n], 0, 0, 0); \
        __builtin_amdgcn_s_setprio(0); } while (0)
    const rsrc_t rsC = __builtin_amdgcn_make_buffer_rsrc((void*)(arena(a.slots) + KX_OFF), 0, FX_OFF - KX_OFF, 0x00020000);
#define K8_EPI(ai, bj, cb) do { const int ln_ = lane_now(); \
        const int vo_ = (int)(wu & 3) * 1024 + (((ln_ >> 4) & 1) * 2 + (ln_ >> 5)) * 256 + (ln_ & 15) * 16; \
        _Pragma("unroll") for (int m = 0; m < 4; ++m) { \
            const f32x4 c0 = acc[ai][bj][m][0], c1 = acc[ai][bj][m][1]; \
            auto r0 = __builtin_amdgcn_permlane16_swap(cvt_pk_bf16(c0[0], c0[1]), cvt_pk_bf16(c1[0], c1[1]), false, false); \
            auto r1 = __builtin_amdgcn_permlane16_swap(cvt_pk_bf16(c0[2], c0[3]), cvt_pk_bf16(c1[2], c1[3]), false, false); \
            const int sg_ = 4 * ai + 2 * (int)(wu >> 2) + (m >> 1); \
            st_sc1s(rsC, vo_, ((cb) ^ (ai * 8192)) + sg_ * 524288 + bj * 16384 + (m & 1) * 4096, v4u{r0[0], r1[0], r0[1], r1[1]}); \
            acc[ai][bj][m][0] = ZERO4; acc[ai][bj][m][1] = ZERO4; } } while (0)
    constexpr int nt = K / 64;
    unsigned cur = (unsigned)todo & 255u, prev = 0u;
    const char* pA = a_of(cur); const char* pB = b_of(cur);
    if (early) touch((const char*)X + 524288 + (size_t)(nw * 128 + (tid >> 2)) * 128, lds0 + 163840 - 256);
    stB(0, 0, pB); stB(0, 1, pB); stA(0, 0, pA); stA(0, 1, pA);
    stB(1, 0, pB + SB); stB(1, 1, pB + SB); stA(1, 0, pA + SA);
    K8_WV(8); K8_BAR;
    if (wu >= 4) K8_BAR;
#define K8_GO(ai) __builtin_amdgcn_s_waitcnt(0xC07F); K8_BAR; K8_MMA(ai, 0, B0); K8_MMA(ai, 1, B1); K8_SCHED; K8_BAR
    int cbp = 0;
    for (;;) {
        todo >>= 8;
        const unsigned nxt = todo != 0ull ? (unsigned)todo & 255u : cur;
        int cb = c_of(cur);
#pragma nounroll
        for (int t = 0; t < nt; t += 2) {
            if (__builtin_expect(early && prev == 0u && t == 10, 0))
                touch((const char*)X + 3 * 524288 + (size_t)(nw * 320 + min(TIDX, 319)) * 128, lds0 + 163840 - 256);
            if (__builtin_expect(early && prev == 0u && t == nt - 4, 0)) {
                tau = __builtin_amdgcn_readfirstlane((unsigned)boot_wait(a.boot) & 255u);
                cb = c_of(cur);
            }
            const bool first = t == 0 && prev != 0u, last = t == nt - 2;
            if (t == 2 && prev != 0u) { publish(prev); WEV(3) }
            K8_LDB(B0, 0, 0); K8_LDA(0, 0); K8_LDB(B1, 0, 1); K8_SCHED; stA(1, 1, pA + SA);
            pA = last ? a_of(nxt) : pA + 2 * SA; pB = last ? b_of(nxt) : pB + 2 * SB;
            if (first) { K8_EPI(1, 0, cbp); K8_EPI(1, 1, cbp); K8_WV(24); } else K8_WV(8);
            K8_GO(0);
            K8_LDA(0, 1); K8_SCHED; stB(0, 0, pB); stB(0, 1, pB); stA(0, 0, pA);
            if (first) K8_WV(24); else K8_WV(8);
            K8_GO(1);
            K8_LDB(B0, 1, 0); K8_LDA(1, 0); K8_LDB(B1, 1, 1); K8_SCHED; stA(0, 1, pA);
            if (first) K8_WV(16); else K8_WV(8);
            K8_GO(0);
            K8_LDA(1, 1); K8_SCHED; stB(1, 0, pB + SB); stB(1, 1, pB + SB); stA(1, 0, pA + SA);
            if (last) { K8_EPI(0, 0, cb); K8_EPI(0, 1, cb); K8_WV(16); } else K8_WV(8);
            K8_GO(1);
        }
        WEV(2)
        prev = cur; cbp = cb; cur = nxt;
        if (todo == 0ull) break;
    }
    K8_EPI(1, 0, cbp); K8_EPI(1, 1, cbp);
    K8_WV(0);
    if (wu < 4) K8_BAR;
    K8_BAR;
    publish(prev);
    WEV(3)
}

__device__ __forceinline__ void oproj_squad(const ChainArgs& a, u16* out, const int ct, const int rh, const int xc, const unsigned tau, const int s0, const int s1, char* smem) {
    const int tid = opaque_tid(), w = tid >> 6, l = tid & 63, i = l & 15, g = l >> 4;
    const int wu = __builtin_amdgcn_readfirstlane(w);
    bf16x8 wf[2][16];
    {
        u16* stg = (u16*)(smem + wu * 10240);
#pragma unroll
        for (int ch = 0; ch < 4; ++ch) {
            const u16* src = a.Wo + (size_t)hcol(4 * wu + ch) * DM + 32 * ct;
            v4u raw[8];
#pragma unroll
            for (int e = 0; e < 8; ++e) { int idx = l + 64 * e, r = idx >> 2, q4 = idx & 3; raw[e] = *(const v4u*)(src + (size_t)r * DM + 8 * q4); }
#pragma unroll
            for (int e = 0; e < 8; ++e) { int idx = l + 64 * e, r = idx >> 2, q4 = idx & 3; *(v4u*)(stg + r * 40 + 8 * q4) = raw[e]; }
            __builtin_amdgcn_s_waitcnt(0xC07F);
#pragma unroll
            for (int jj = 0; jj < 2; ++jj)
#pragma unroll
                for (int ks = 0; ks < 4; ++ks) {
                    const u16* p = stg + (32 * ks + 8 * g + (i >> 2)) * 40 + 16 * jj + 4 * (i & 3);
                    wf[jj][4 * ch + ks] = tr_frag(p, p + 4 * 40);
                }
            __builtin_amdgcn_s_waitcnt(0xC07F);
        }
    }
    const rsrc_t rsO = __builtin_amdgcn_make_buffer_rsrc((void*)(arena(a.slots) + VX_OFF), 0, 32 << 20, 0x00020000);
    float* red = (float*)(smem + 81920);
    const int voff = rh * 4096 + l * 16;
    volatile unsigned* rdy = (volatile unsigned*)(smem + 81920 + 2 * 8 * 16 * 36 * 4);
    unsigned ready = 0;
    bool pre = false;
    v4u xf[16];
    for (int s = s0; s < s1; ++s) {
        if (!(ready & 1u)) {
            if (wu == 0) {
                const unsigned* q = a.sync + (SY_OXS + (s - SSEG) * 8 + xc) * 32;
#pragma nounroll
                for (int spin = 0;; ++spin) {
                    asm volatile("" ::: "memory");
                    if (ld_flag(q) != 0u) break;
                    if (spin > 20000) { __hip_atomic_store((gu32*)a.tmo, 7000u, __ATOMIC_RELAXED, __HIP_MEMORY_SCOPE_AGENT); break; }
                    if (s == s0) __builtin_amdgcn_s_sleep(64); else if (s < NSEG - 5) __builtin_amdgcn_s_sleep(16); else __builtin_amdgcn_s_sleep(SQ_SF);
                }
            }
            asm volatile("" ::: "memory");
            __builtin_amdgcn_s_barrier();
            asm volatile("" ::: "memory");
        }
        if (!pre) {
#pragma unroll
            for (int ks = 0; ks < 16; ++ks) xf[ks] = ld_sc1s(rsO, voff + (ks & 3) * 1024, blk_off(s, 4 * wu + (ks >> 2), tau) ^ 8192);
        }
        unsigned nf = 0;
        if (wu == 0) nf = ld_flag(a.sync + (SY_OXS + (s + 1 - SSEG + (l & 1)) * 8 + xc) * 32);
        pre = (ready & 2u) != 0u && s + 1 < s1;
        f32x4 acc[2] = {};
        if (pre) {
#pragma unroll
            for (int ks = 0; ks < 16; ++ks) {
#pragma unroll
                for (int jj = 0; jj < 2; ++jj) acc[jj] = __builtin_amdgcn_mfma_f32_16x16x32_bf16(wf[jj][ks], as_frag(xf[ks]), acc[jj], 0, 0, 0);
                K8_SCHED;
                xf[ks] = ld_sc1s(rsO, voff + (ks & 3) * 1024, blk_off(s + 1, 4 * wu + (ks >> 2), tau) ^ 8192);
                K8_SCHED;
            }
        } else {
#pragma unroll
            for (int ks = 0; ks < 16; ++ks)
#pragma unroll
                for (int jj = 0; jj < 2; ++jj) acc[jj] = __builtin_amdgcn_mfma_f32_16x16x32_bf16(wf[jj][ks], as_frag(xf[ks]), acc[jj], 0, 0, 0);
        }
        float* rp = red + (s & 1) * (8 * 16 * 36);
#pragma unroll
        for (int jj = 0; jj < 2; ++jj) *(f32x4*)&rp[(wu * 16 + i) * 36 + 16 * jj + 4 * g] = acc[jj];
        if (wu == 0) rdy[s & 1] = (unsigned)__ballot(nf != 0u) & 3u;
        K8_WL(0); K8_BAR;
        ready = __builtin_amdgcn_readfirstlane(rdy[s & 1]) | (pre ? 1u : 0u);
        if (tid >= 64 && tid < 192) {
            const int t = tid - 64, row = t >> 3, q4 = t & 7;
            f32x4 sv_[8];
#pragma unroll
            for (int e = 0; e < 8; ++e) sv_[e] = *(const f32x4*)&rp[(e * 16 + row) * 36 + 4 * q4];
            const f32x4 s0 = ((sv_[0] + sv_[1]) + (sv_[2] + sv_[3])) + ((sv_[4] + sv_[5]) + (sv_[6] + sv_[7]));
            v2u o; o[0] = cvt_pk_bf16(s0[0], s0[1]); o[1] = cvt_pk_bf16(s0[2], s0[3]);
            out8(out + (size_t)(s * SEG + 16 * rh + row) * OUTW + 32 * ct + 4 * q4, o);
        }
    }
}

__device__ __forceinline__ void kv_stream(const ChainArgs& a, const u16* __restrict__ X, const u16* __restrict__ B, char* C, const int head, const int xc, const int wk, unsigned& tau, char* smem) {
    const int tid = opaque_tid(), w = tid >> 6, l = tid & 63, i = l & 15, g = l >> 4;
    const int wu = __builtin_amdgcn_readfirstlane(w), kh = wu >> 2, cq = wu & 3;
    const unsigned lds0 = (unsigned)(uintptr_t)(__attribute__((address_space(3))) char*)smem;
    unsigned voA, voB;
    { const int row = (wu & 1) * 8 + (l >> 3), c = l & 7; voA = (unsigned)(row * DM + 8 * (c ^ (row & 7))) * 2u + (wu >> 1) * 512; }
    { const int row = tid >> 4, c = tid & 15; voB = (unsigned)(row * HD + 8 * (c ^ (bswz2<128>(row) << 1))) * 2u; }
    auto stB = [&](int ch, int slot) {
#pragma unroll
        for (int e = 0; e < 4; ++e) glds16s(voB, (const char*)B + (size_t)(4 * ch + e) * 32 * HD * 2, lds0 + 65536 + slot * 32768 + e * 8192 + wu * 1024);
    };
    const unsigned ldA = lds0 + (wu >> 1) * 8192 + (wu & 1) * 1024;
    auto stP = [&](const char* p, const unsigned d) {
        unsigned keep;
        asm volatile("s_mov_b32 %0, m0\n\ts_mov_b32 m0, %1\n\ts_nop 0\n\tglobal_load_lds_dwordx4 %3, %2 offset:-4096\n\tglobal_load_lds_dwordx4 %4, %2 offset:-2048\n\t"
                     "global_load_lds_dwordx4 %5, %2\n\tglobal_load_lds_dwordx4 %6, %2 offset:2048\n\ts_mov_b32 m0, %0"
                     : "=&s"(keep) : "s"(d), "s"(p), "v"(voA + 8192u), "v"(voA + 6272u), "v"(voA + 4352u), "v"(voA + 2432u) : "memory");
    };
    auto srcA = [&](int h) { return (const char*)X + (size_t)min(h, 2 * KVS - 1) * 32768 - 4096; };
    auto stA = [&](int h) { stP(srcA(h), ldA + (h & 3) * 32768 + 4096); };
    stB(0, 0); stA(0); stA(1); stB(1, 1);
    touch((const char*)X + 65536 + min(wk * 512 + tid, (KVS - 1) * 512 - 1) * 128, lds0 + 131072 + wu * 1024);
    stB(2, 2);
    bf16x8 bf[16][2], af[8];
    f32x4 acc[2][2] = {}, keep[2];
    const int rb = 64 * kh + 8 * g + (i >> 2), sw = bswz2<128>(rb);
    const char* bp = smem + 65536 + rb * 256 + 8 * (i & 3);
    const char* sa0 = smem + kh * 2048 + i * 128 + 16 * (g ^ (i & 7));
    const char* sa1 = smem + kh * 2048 + i * 128 + 16 * ((4 + g) ^ (i & 7));
#define KV_AF(slot, q) *(const bf16x8*)((((q) & 1) ? sa1 : sa0) + (slot) * 32768 + ((q) >> 1) * 4096)
#pragma unroll
    for (int ch = 0; ch < 8; ++ch) {
        if (ch == 0) K8_WV(9); else if (ch < 6) K8_WV(8); else if (ch == 6) K8_WV(4); else K8_WV(0);
        K8_BAR;
#pragma unroll
        for (int e = 0; e < 4; ++e) { const char* p_ = bp + (ch % 3) * 32768 + (e >> 1) * 8192 + 32 * ((2 * cq + (e & 1)) ^ sw); bf[2 * ch + (e >> 1)][e & 1] = tr_frag(p_, p_ + 1024); }
#pragma unroll
        for (int e = 0; e < 4; ++e) af[e] = KV_AF(e >> 1, 2 * ch + (e & 1));
        K8_WL(0); K8_SCHED; K8_BAR;
        if (ch < 5) stB(ch + 3, ch % 3);
        if (ch == 7) { stA(2); stA(3); }
#pragma unroll
        for (int e = 0; e < 8; ++e) acc[e >> 2][e & 1] = __builtin_amdgcn_mfma_f32_16x16x32_bf16(bf[2 * ch + ((e >> 1) & 1)][e & 1], af[e >> 1], acc[e >> 2][e & 1], 0, 0, 0);
    }
    float* red = (float*)(smem + 131072) + l * 4;
#pragma unroll
    for (int p = 0; p < 2; ++p) { *(f32x4*)(red + (p * 8 + wu) * 256) = kh ? acc[p][0] : acc[p][1]; keep[p] = kh ? acc[p][1] : acc[p][0]; }
    unsigned long long tag = 0;
    if (wu == 4) { tag = boot_wait(a.boot); *(volatile unsigned*)(smem + 163840 - 512) = (unsigned)tag & 255u; }
    const rsrc_t rc = __builtin_amdgcn_make_buffer_rsrc((void*)C, 0, KVS * 524288, 0x00020000);
    const int vo = cq * 1024 + (2 * kh + (g >> 1)) * 256 + i * 16 + (g & 1) * 8;
    auto put = [&](const f32x4 o, const int so) {
        const v2u pk = {cvt_pk_bf16(o[0], o[1]), cvt_pk_bf16(o[2], o[3])};
        asm volatile("buffer_store_dwordx2 %0, %1, %2, %3 offen sc1" :: "v"(pk), "v"(vo), "s"(rc), "s"(so) : "memory");
    };
    K8_WL(0); K8_WV(0); K8_BAR;
    tau = __builtin_amdgcn_readfirstlane(*(volatile unsigned*)(smem + 163840 - 512));
#pragma unroll
    for (int p = 0; p < 2; ++p) put(keep[p] + *(const f32x4*)(red + (p * 8 + (wu ^ 4)) * 256), blk_off(0, head, tau) + p * 4096);
    stA(4);
    K8_WV(4); K8_BAR;
    if (tid == 256) __hip_atomic_store((gu64*)(a.boot + 256 + 16 * xc + wk), tag, __ATOMIC_RELAXED, __HIP_MEMORY_SCOPE_AGENT);
    auto stages = [&](auto kc) {
        constexpr int KH = decltype(kc)::value;
        if (KH) K8_BAR;
        f32x4 ac[2] = {ZERO4, ZERO4}, kp = ZERO4;
        int h_first = 2, so = OOB;
        asm volatile("" : "+s"(h_first));
        const char* pn = srcA(5);
        unsigned dn = ldA + 32768 + 4096;
        int r0 = (int)(sa0 - smem) + 2 * 32768, r1 = (int)(sa1 - smem) + 2 * 32768;
        for (int h = h_first; h < 2 * KVS + 4; ++h) {
            const f32x4 oth = *(const f32x4*)(red + (wu ^ 4) * 256);
            *(f32x4*)(red + wu * 256) = ac[KH ^ 1];
            bf16x8 ax[16];
#pragma unroll
            for (int q = 0; q < 16; ++q) ax[q] = *(const bf16x8*)(smem + ((q & 1) ? r1 : r0) + (q >> 1) * 4096);
            K8_SCHED;
            stP(pn, dn);
            put((KH ? ac[1] : kp) + oth, so);
            kp = ac[0];
            __builtin_amdgcn_s_waitcnt(0xC07F);
            K8_WV(10);
            if (__builtin_expect(h == 5, 0)) K8_WV(0);
            K8_BAR;
            if (KH && (0xAAA20u >> h) & 1u && tid == 256)
                __hip_atomic_store((gu32*)(a.sync + (SY_KV0 + (h / 2 - (h == 5 ? 1 : 2)) * 8 + xc) * 32 + wk), 16u, __ATOMIC_RELAXED, __HIP_MEMORY_SCOPE_AGENT);
            if (__builtin_expect(h < 2 * KVS, 1)) {
                __builtin_amdgcn_s_setprio(1);
#pragma unroll
                for (int q = 0; q < 16; ++q)
#pragma unroll
                    for (int n = 0; n < 2; ++n) ac[n] = __builtin_amdgcn_mfma_f32_16x16x32_bf16(bf[q][n], ax[q], q ? ac[n] : ZERO4, 0, 0, 0);
                __builtin_amdgcn_s_setprio(0);
            }
            {
                const int hs = h - 1 + KH;
                so = blk_off(hs >> 1, head, tau) + (hs & 1) * 4096;
                asm volatile("" : "+s"(so));
                so = hs > 1 && hs < 2 * KVS ? so : OOB;
                pn = srcA(h + 4); dn = ldA + (h & 3) * 32768 + 4096;
                r0 = (r0 + 32768) & 131071; r1 = (r1 + 32768) & 131071;
                asm volatile("" : "+s"(so), "+s"(pn), "+s"(dn), "+v"(r0), "+v"(r1));
            }
            K8_BAR;
        }
        if (!KH) K8_BAR;
    };
    if (kh) stages(std::integral_constant<int, 1>{}); else stages(std::integral_constant<int, 0>{});
    K8_WV(0);
}

struct MegaArgs {
    ChainArgs ch;
    const u16* x; u16* out;
    const u16* bx; const u16* wr; const u16* wi;
};
constexpr int MEGA_LDS = 163840;
#define WSTAMP(n)
__global__ __launch_bounds__(512, 2) void mega_kernel(MegaArgs m) {
    extern __shared__ __attribute__((aligned(256))) char chsm[];
    const int b = BIDX, xc = (b & 3) + 4 * (b >> 7), sl = (b >> 3) & 15;
    if ((b & 4) == 0) { chain_body(m.ch, xc, sl, chsm); return; }
    const ChainArgs& a = m.ch;
    const int wk = sl, tid = TIDX;
    char* ar = arena(a.slots);
    unsigned tauw = 0;
    const int hf = __builtin_amdgcn_readfirstlane(TIDX >> 8);
    WSTAMP(0)
    if (wk < 8) kv_stream(a, m.x, (wk < 4 ? a.Wk : a.Wv) + hcol(4 * xc + (wk & 3)), ar + (wk < 4 ? KX_OFF : VX_OFF), 4 * xc + (wk & 3), xc, wk, tauw, chsm);
    WSTAMP(8) WEV(1)
    {
        const int p = wk & 1, q = wk & 3;
        const unsigned long long todo = wk < 2 ? 0x84u + p : wk < 4 ? 0x74704240u + 0x01010101u * p : wk < 8 ? 0ull : wk < 10 ? 0x60503010ull + 0x01010101ull * q
                                      : wk < 12 ? 0x7060503010ull + 0x0101010101ull * q : (wk < 14 ? 0x54341420u : 0x64442422u) + 0x01010101u * p;
        __syncthreads();
        if (todo != 0ull) tile_stream(a, m.x, todo, xc, tauw, wk >= 8, xc * 8 + (wk & 7), (u16*)chsm WEVA);
    }
    WSTAMP(2)
    if (wk < 2) {
        unsigned* mbox = (unsigned*)(chsm + 131072);
        if (tid < 5) mbox[tid] = tid ? 2u : 0u;
        __syncthreads();
        const int w = __builtin_amdgcn_readfirstlane(tid >> 6), h = 4 * xc + (w & 3);
        const rsrc_t rsQ = __builtin_amdgcn_make_buffer_rsrc((void*)(ar + KX_OFF), 0, S_LEN * HD * 4, 0x00020000);
        const rsrc_t rsK = __builtin_amdgcn_make_buffer_rsrc((void*)(ar + KX_OFF), 0, S_LEN * HD * 4, 0x00020000);
        const rsrc_t rsV = __builtin_amdgcn_make_buffer_rsrc((void*)(ar + VX_OFF), 0, S_LEN * HD * 4, 0x00020000);
        const rsrc_t rsS = slot_rsrc(a.slots);
        u16 (*Vs)[VP] = (u16 (*)[VP])(chsm + w * (40 * VP * 2));
        const unsigned tau = tauw;
        int pend = -1;
        bool ready = false;
        for (int s = 2 * wk + (w >> 2), sn; s < NSEG - 1; s = sn) {
            const int band = s >> 3;
            unsigned n = 0;
            if (lane_now() == 0) n = atomicAdd(mbox + 1 + (w & 3), 1u);
            n = __builtin_amdgcn_readfirstlane(n);
            sn = 2 * wk + (int)(n & 1u) + 4 * (int)(n >> 1);
            if (sn - s > 4) __builtin_amdgcn_s_setprio(2); else __builtin_amdgcn_s_setprio(0);
            if (s < 4) wait_ge<32>(a.sync + (SY_KV0 + (KVS - 1) * 8 + xc) * 32 + (tid & 7), 16u, a.tmo, 4100u);
            if (!ready) {
                if (band > 0) wait_ge<32>(a.sync + (SY_KV + band * 8 + xc) * 32 + (tid & 3), 1u, a.tmo, 4100u);
                wait_ge<32>(a.sync + (SY_Q + band * 8 + xc) * 32 + (tid & 3), 1u, a.tmo, 4200u);
            }
            xattn_unit(a, rsQ, rsK, rsV, rsS, s, sn, h, Vs, mbox, tau, pend, ready);
        }
        return;
    }
    if (wk >= 4 && wk < 8) {
        const int fw = (wk - 4) * 8 + xc;
        const rsrc_t rsF = __builtin_amdgcn_make_buffer_rsrc((void*)(ar + FX_OFF), 0, 16 << 20, 0x00020000);
        const rsrc_t rsW = __builtin_amdgcn_make_buffer_rsrc((void*)(ar + WT_OFF), 0, 10 << 20, 0x00020000);
        const unsigned tw = tauw ^ 1u;
        {
            const int tq = opaque_tid();
            fft_rows_fwd(m.bx, rsF, tw, 2 * fw + hf, chsm + hf * 36864, tq & 255, (const char*)m.wr, (const char*)m.wi, (unsigned)(uintptr_t)(__attribute__((address_space(3))) char*)chsm + 163840 - 256);
        }
        for (int t = fw; t < 64; t += 32) {
            __syncthreads();
            filt_transpose(m.wr, m.wi, rsW, tw, t, chsm, opaque_tid());
        }
        wg_publish(a.sync, SY_DONE, 32u, 8);
        WSTAMP(3) WEV(4)
        wg_wait_acquire<16>(a.sync + (SY_DONE + xc) * 32, a.tmo, 6000u);
        {
            const int tq = opaque_tid();
            fft_cols(rsF, rsW, tw, 2 * fw + hf, 4, chsm + hf * 36864, tq & 255);
        }
        wg_publish(a.sync, SY_DONE + 8, 32u, 8);
        WSTAMP(4) WEV(5)
    }
    if (wk >= 4 && wk < 10) {
        const rsrc_t rsF = __builtin_amdgcn_make_buffer_rsrc((void*)(ar + FX_OFF), 0, 16 << 20, 0x00020000);
        wg_wait_acquire<16>(a.sync + (SY_DONE + 8 + xc) * 32, a.tmo, 6001u);
        const int tq = opaque_tid();
        fft_rows_inv(rsF, tauw ^ 1u, m.out, 2 * ((wk & 3) * 8 + xc) + hf + (wk < 8 ? 0 : 192), wk < 8 ? 64 : 32, wk < 8 ? 3 : 2, chsm + hf * 36864, tq & 255);
        WSTAMP(5) WEV(6)
    }
    if (wk >= 12) {
        const int r = 2 * (wk - 12);
        wg_wait_acquire<16>(a.sync + (SY_OX + r * 8 + xc) * 32, a.tmo, 5000u);
        wg_wait_acquire<16>(a.sync + (SY_OX + (r + 1) * 8 + xc) * 32, a.tmo, 5000u);
        WEV(7)
        gemm_tile<256>((u16*)chsm, ar + VX_OFF, 4 * r, tauw, a.Wo + xc * 128, m.out + (size_t)r * 128 * OUTW + xc * 128);
        WEV(8)
    }
    if (wk < 4) {
        const int r = wk + 6;
        wg_wait_acquire<16>(a.sync + (SY_OX + r * 8 + xc) * 32, a.tmo, 5000u);
        WEV(7)
        gemm_tile<128>((u16*)chsm, ar + VX_OFF, 4 * r, tauw, a.Wo + xc * 128, m.out + (size_t)r * 128 * OUTW + xc * 128);
        WEV(8)
        WSTAMP(9)
    }
    if (wk >= 4 && wk < 12) {
#pragma nounroll
        for (int pass = (wk >> 1) == 4 ? 0 : 1; pass < 2; ++pass) {
            __syncthreads();
            oproj_squad(a, m.out, (wk - 4 + (pass ? 0 : 2)) * 4 + (xc & 3), xc >> 2, xc, tauw, pass && wk >= 10 ? SSEG + SHLP : SSEG, pass ? NSEG - 1 : SSEG + SHLP, chsm);
        }
        WSTAMP(7)
    }
}

#if defined(__HIP_DEVICE_COMPILE__)
#pragma clang attribute pop
#endif
extern "C" void kernel_launch(void* const* d_in, const int* in_sizes, int n_in,
                              void* d_out, int out_size, void* d_ws, size_t ws_size,
                              hipStream_t stream) {
    char* ws = (char*)d_ws;
    size_t off = 0;
    auto alloc = [&](size_t bytes) { void* p = ws + off; off += (bytes + 255) & ~(size_t)255; return p; };
    char* slots = (char*)alloc((size_t)ARENA_BYTES + (2u << 20));
    static_assert((SY_WORDS + 2) * 128 <= 131072, "sync block");
    unsigned* sync = (unsigned*)alloc(131072);
    unsigned long long* boot = (unsigned long long*)alloc(4096);

    MegaArgs m;
    m.ch.mem0 = (const u16*)d_in[1]; m.ch.Wq = (const u16*)d_in[2]; m.ch.Wk = (const u16*)d_in[3]; m.ch.Wv = (const u16*)d_in[4]; m.ch.Wo = (const u16*)d_in[5];
    m.ch.out = (u16*)d_out; m.ch.slots = slots; m.ch.tmo = sync + SY_WORDS * 32;
    m.ch.stamps = (unsigned long long*)(ws + (240ull << 20));
    m.ch.sync = sync; m.ch.boot = boot;
    m.x = (const u16*)d_in[0]; m.out = (u16*)d_out;
    m.bx = (const u16*)d_in[7]; m.wr = (const u16*)d_in[8]; m.wi = (const u16*)d_in[9];
    (void)hipFuncSetAttribute((const void*)mega_kernel, hipFuncAttributeMaxDynamicSharedMemorySize, MEGA_LDS);
    mega_kernel<<<256, 512, MEGA_LDS, stream>>>(m);
}
```

```cpp
#if defined(__HIP_DEVICE_COMPILE__)
#pragma clang attribute push (__attribute__((target("no-packed-fp32-ops,no-load-store-opt"))), apply_to = function)
#endif
#include <hip/hip_runtime.h>
#include <hip/hip_bf16.h>
#include <stdint.h>
#include <type_traits>

typedef unsigned short u16;
#define TIDX ((int)__builtin_amdgcn_workitem_id_x())
#define BIDX ((int)__builtin_amdgcn_workgroup_id_x())
__device__ __forceinline__ int opaque_tid() { int t = TIDX; asm volatile("" : "+v"(t)); return t; }
__device__ __forceinline__ int lane_now() { int t; asm volatile("v_mbcnt_lo_u32_b32 %0, -1, 0\n\tv_mbcnt_hi_u32_b32 %0, -1, %0" : "=v"(t)); return t; }
typedef __attribute__((ext_vector_type(8))) short bf16x8;
typedef __attribute__((ext_vector_type(4))) float f32x4;

constexpr int S_LEN = 2048, DM = 1024, NH = 32, DH = 128, MM = 8, SEG = 32;
constexpr int NSEG = S_LEN / SEG;
constexpr int HD = NH * DH;
constexpr int OUTW = 2048;

typedef unsigned v4u __attribute__((ext_vector_type(4)));
typedef unsigned v2u __attribute__((ext_vector_type(2)));
typedef short v4i16 __attribute__((ext_vector_type(4)));
typedef __amdgpu_buffer_rsrc_t rsrc_t;
#define AUX_SC1 16
__device__ __forceinline__ v4u ld_sc1s(rsrc_t rs, int voff, int soff) { return __builtin_amdgcn_raw_buffer_load_b128(rs, voff, soff, AUX_SC1); }
__device__ __forceinline__ void st_sc1s(rsrc_t rs, int voff, int soff, v4u v) { __builtin_amdgcn_raw_buffer_store_b128(v, rs, voff, soff, AUX_SC1); asm volatile("s_nop 1" :: "v"(v) : "memory"); }
__device__ __forceinline__ void st8_sc1s(rsrc_t rs, int voff, int soff, v2u v) { __builtin_amdgcn_raw_buffer_store_b64(v, rs, voff, soff, AUX_SC1); }
__device__ __forceinline__ void st_sc1(rsrc_t rs, int off, v4u v) { __builtin_amdgcn_raw_buffer_store_b128(v, rs, off, 0, AUX_SC1); }
__device__ __forceinline__ void out8(void* p, v2u v) { asm volatile("global_store_dwordx2 %0, %1, off sc1" :: "v"(p), "v"(v) : "memory"); }
typedef __bf16 bf16x2_t __attribute__((ext_vector_type(2)));
typedef float f32x2_t __attribute__((ext_vector_type(2)));
__device__ __forceinline__ unsigned cvt_pk_bf16(float lo, float hi) {
    f32x2_t f = {lo, hi};
    return __builtin_bit_cast(unsigned, __builtin_convertvector(f, bf16x2_t));
}
#define SENT 0xFFBFFFBF
__device__ __forceinline__ unsigned umax3(unsigned a, unsigned b, unsigned c) { return max(max(a, b), c); }
__device__ __forceinline__ unsigned mx4(unsigned acc, v4u v) { return umax3(umax3(acc, v[0], v[1]), v[2], v[3]); }
__device__ __forceinline__ bf16x8 as_frag(v4u v) { return __builtin_bit_cast(bf16x8, v); }
__device__ __forceinline__ float xor16_sum(float x) {
    auto r = __builtin_amdgcn_permlane16_swap(__float_as_uint(x), __float_as_uint(x), false, false);
    return __uint_as_float(r[0]) + __uint_as_float(r[1]);
}
__device__ __forceinline__ float xor32_sum(float x) {
    auto r = __builtin_amdgcn_permlane32_swap(__float_as_uint(x), __float_as_uint(x), false, false);
    return __uint_as_float(r[0]) + __uint_as_float(r[1]);
}
__device__ __forceinline__ float xor16_max(float x) {
    auto r = __builtin_amdgcn_permlane16_swap(__float_as_uint(x), __float_as_uint(x), false, false);
    return fmaxf(__uint_as_float(r[0]), __uint_as_float(r[1]));
}
__device__ __forceinline__ float xor32_max(float x) {
    auto r = __builtin_amdgcn_permlane32_swap(__float_as_uint(x), __float_as_uint(x), false, false);
    return fmaxf(__uint_as_float(r[0]), __uint_as_float(r[1]));
}
constexpr int SPIN_LIMIT = 400000;
constexpr int OOB = 0x7FFFFF00;
#define XST 32
#define SQ_OWN 2
#define PLL 0
#define PB 14
#define TD 60
#define PS 46
#define TE0 10
#define PVA 8
#define PV 1
#define PV1 2
#define PV2 11
#define PL 20
#define PF 48
#define PC 31
#define PP 15
#define PKA 28
#define PG0 2
#define PG1 32
#define DLY_U 4
#define DLY_C 4
#define XA_FAST 6
#define XA_D 12
#define SQ_SF 4
#define LW 0
constexpr int NPROBE = 2;
constexpr int M_OFF = 32 * 16384, F_OFF = M_OFF + 2 * 16384, C_OFF = F_OFF + 2 * 16384, REC = C_OFF + 16 * 16384, PERWG = REC / 128, SLOT_BYTES = 34 * 2097152;
static_assert(PERWG == 6656, "arming shares");
__device__ __forceinline__ int rec_base(int s) { return (s >> 1) * 2097152 + (s & 1) * REC; }
__device__ __forceinline__ char* arena(char* slots) { return (char*)(((uintptr_t)slots + 2097151u) & ~(uintptr_t)2097151u); }
constexpr int KX_OFF = SLOT_BYTES, VX_OFF = KX_OFF + (32 << 20), FX_OFF = VX_OFF + (32 << 20), WT_OFF = FX_OFF + (16 << 20), ARENA_BYTES = WT_OFF + (10 << 20);
__device__ __forceinline__ rsrc_t slot_rsrc(char* slots) { return __builtin_amdgcn_make_buffer_rsrc((void*)arena(slots), 0, FX_OFF, 0x00020000); }
__device__ __forceinline__ int blk_off(int seg, int h, unsigned tau) { return seg * 524288 + (((h & 3) >> 1) * 16 + (h >> 2) * 2 + (h & 1)) * 16384 + (int)((tau ^ (unsigned)(seg >> 2)) & 1u) * 8192; }
constexpr int TM_OFF = 124 * 16384;
constexpr int CAL_OFF = 32 * 2097152;
__device__ __forceinline__ int near_half(int s, unsigned tau) { return (int)((tau ^ (unsigned)(s >> 1)) & 1u) * 8192; }
__device__ __forceinline__ int chunk_off(int s, int g, int j, unsigned tau) { return rec_base(s) + (j < 2 ? (g & 3) * 2 + j : 6 + 3 * g + j) * 16384 + near_half(s, tau); }
__device__ __forceinline__ int x_off(int s, int h, unsigned tau) { return chunk_off(s, h >> 2, 2, tau) + (h & 3) * 2048; }
__device__ __forceinline__ int km_off(int s, int h, unsigned tau) { return chunk_off(s, h >> 2, 3, tau) + (h & 3) * 2048; }
__device__ __forceinline__ int m_off(int s, int h, unsigned tau) { return rec_base(s) + M_OFF + (h >> 4) * 16384 + near_half(s, tau) + (h & 15) * 512; }
__device__ __forceinline__ int v_off(int s, int h, unsigned tau) { return chunk_off(s, h >> 2, 4, tau) + (h & 3) * 2048; }
__device__ __forceinline__ int f_off(int s, int g, unsigned tau) { return rec_base(s) + F_OFF + (g >> 2) * 16384 + near_half(s, tau) + (g & 3) * 2048; }
__device__ __forceinline__ int hcol(int h) { return ((h & 3) * 8 + (h >> 2)) * 128; }
constexpr int VP = 144;

struct ChainArgs {
    u16* out;
    const u16* mem0; const u16* Wq; const u16* Wk; const u16* Wv; const u16* Wo;
    char* slots;
    unsigned* tmo;
    unsigned long long* stamps;
    unsigned* sync;
    unsigned long long* boot;
};
constexpr int SY_KV = 0;
constexpr int SY_Q = 64;
constexpr int SY_PROG = 128;
constexpr int SY_OX = 136;
constexpr int SY_DONE = 264;
constexpr int SY_OXS = 472;
constexpr int SY_KV0 = 408;
constexpr int KVS = 8;
constexpr int SY_FLAGS = 680;
constexpr int SY_WORDS = SY_FLAGS + SY_FLAGS / 8;
#define SHLP 4
constexpr int SSEG = 40;
typedef __attribute__((address_space(1))) unsigned gu32;
typedef __attribute__((address_space(1))) unsigned long long gu64;
__device__ __forceinline__ unsigned ld_flag(const unsigned* p) { return __hip_atomic_load((gu32*)p, __ATOMIC_RELAXED, __HIP_MEMORY_SCOPE_AGENT); }
__device__ __forceinline__ unsigned long long boot_tag(const unsigned long long* boot) {
    return __hip_atomic_load((gu64*)(boot + 144), __ATOMIC_RELAXED, __HIP_MEMORY_SCOPE_AGENT) + 1ull;
}
__device__ __forceinline__ unsigned long long boot_wait(const unsigned long long* boot) {
    const int l = TIDX & 63;
    unsigned long long tag = 0, x = 0, y = 0;
#pragma nounroll
    for (int spin = 0; spin < 100000; ++spin) {
        asm volatile("" ::: "memory");
        x = __hip_atomic_load((gu64*)(boot + l), __ATOMIC_RELAXED, __HIP_MEMORY_SCOPE_AGENT);
        y = __hip_atomic_load((gu64*)(boot + 64 + l), __ATOMIC_RELAXED, __HIP_MEMORY_SCOPE_AGENT);
        tag = boot_tag(boot);
        if (__all((x | 3u) == ((tag << 2) | 3u) && (y | 3u) == ((tag << 2) | 3u))) break;
        __builtin_amdgcn_s_sleep(16);
    }
    asm volatile("" ::: "memory");
    const unsigned m = ((unsigned)__ballot(x & 1u) ^ (__popcll(__ballot(x & 2u)) + __popcll(__ballot(y & 2u)) > 64 ? 1u : 0u)) & 1u;
    return (tag << 8) | m;
}
template <int SL>
__device__ __forceinline__ void wait_ge(const unsigned* p, unsigned need, unsigned* tmo, unsigned code) {
#pragma nounroll
    for (int spin = 0;; ++spin) {
        asm volatile("" ::: "memory");
        if (__all(ld_flag(p) >= need)) break;
        if (spin > 400000 / SL) { __hip_atomic_store((gu32*)tmo, code, __ATOMIC_RELAXED, __HIP_MEMORY_SCOPE_AGENT); break; }
        __builtin_amdgcn_s_sleep(SL);
    }
    asm volatile("" ::: "memory");
}
template <int SL>
__device__ __forceinline__ void wg_wait_acquire(const unsigned* p, unsigned* tmo, unsigned code) {
    if (TIDX < 64) {
        wait_ge<SL>(p, 1u, tmo, code);
        __builtin_amdgcn_fence(__ATOMIC_ACQUIRE, "agent");
        asm volatile("s_waitcnt vmcnt(0)" ::: "memory");
    }
    __syncthreads();
}
__device__ __forceinline__ void arrive(unsigned* sync, const int f, const unsigned need, const int n) {
    const int l = lane_now();
    unsigned old = 0;
    if (l == 0) old = __hip_atomic_fetch_add((gu32*)(sync + (SY_FLAGS + (f >> 3)) * 32), 1u, __ATOMIC_RELAXED, __HIP_MEMORY_SCOPE_AGENT);
    if (__builtin_amdgcn_readfirstlane(old) == need - 1u && l < n) __hip_atomic_store((gu32*)(sync + (f + l) * 32), 16u, __ATOMIC_RELAXED, __HIP_MEMORY_SCOPE_AGENT);
}
__device__ __forceinline__ void wg_publish(unsigned* sync, const int f, const unsigned need, const int n) {
    asm volatile("s_waitcnt vmcnt(0)" ::: "memory");
    __syncthreads();
    if (TIDX < 64) arrive(sync, f, need, n);
}
#define TMO_SET(code) { dead = true; if (l == 0) __hip_atomic_store(a.tmo, (code) + s, __ATOMIC_RELAXED, __HIP_MEMORY_SCOPE_AGENT); }

__device__ __forceinline__ void glds16h(unsigned voff, const void* sbase, unsigned lds_dst) {
    unsigned keep;
    asm volatile("s_mov_b32 %0, m0\n\ts_mov_b32 m0, %3\n\ts_nop 0\n\tglobal_load_lds_dwordx4 %1, %2 sc1\n\ts_mov_b32 m0, %0" : "=&s"(keep) : "v"(voff), "s"(sbase), "s"(lds_dst) : "memory");
}
__device__ __forceinline__ void touch(const void* p, unsigned lds_dst) {
    unsigned keep;
    asm volatile("s_mov_b32 %0, m0\n\ts_mov_b32 m0, %2\n\ts_nop 0\n\tglobal_load_lds_dword %1, off\n\ts_mov_b32 m0, %0" : "=&s"(keep) : "v"(p), "s"(lds_dst) : "memory");
}
__device__ __forceinline__ void touch_sc1(const void* p, unsigned lds_dst) {
    unsigned keep;
    asm volatile("s_mov_b32 %0, m0\n\ts_mov_b32 m0, %2\n\ts_nop 0\n\tglobal_load_lds_dword %1, off sc1\n\ts_mov_b32 m0, %0" : "=&s"(keep) : "v"(p), "s"(lds_dst) : "memory");
}
#define WEV(code)
#define WEVP
#define WEVA
constexpr int C2_K = 32768, C2_V = 65536;
constexpr int C2_R1 = 0, R1S = 3200;
constexpr int C2_R3 = C2_R1 + 4 * R1S;
constexpr int C2_MB = 24576;
#define PST(n)
#define LT(n)
#define LTA(n)
#define LTOUT0
#define LTOUT
#define CN(n)
#define CNS
#define CNOUT
#define CLOB16(a, b, c, d, e, f, g, h, i, j, k, l, m, n, o, p) "v" #a, "v" #b, "v" #c, "v" #d, "v" #e, "v" #f, "v" #g, "v" #h, "v" #i, "v" #j, "v" #k, "v" #l, "v" #m, "v" #n, "v" #o, "v" #p
#define LDS3(p) ((__attribute__((address_space(3))) v4i16*)(p))
__device__ __forceinline__ bf16x8 tr_frag(const void* p0, const void* p1) {
    v4i16 lo = __builtin_amdgcn_ds_read_tr16_b64_v4i16(LDS3(p0));
    v4i16 hi = __builtin_amdgcn_ds_read_tr16_b64_v4i16(LDS3(p1));
    return bf16x8{lo[0], lo[1], lo[2], lo[3], hi[0], hi[1], hi[2], hi[3]};
}
typedef __bf16 bf16x8v __attribute__((ext_vector_type(8)));
typedef __bf16 bf16x16v __attribute__((ext_vector_type(16)));
typedef unsigned v8u __attribute__((ext_vector_type(8)));
typedef unsigned v32u __attribute__((ext_vector_type(32)));
#define PUT8(v, o, x) { const v8u x_ = (x); _Pragma("unroll") for (int e_ = 0; e_ < 8; ++e_) (v)[(o) + e_] = x_[e_]; }
#define SUB4(v, o) v4u{(v)[(o)], (v)[(o) + 1], (v)[(o) + 2], (v)[(o) + 3]}
#define PUT4(v, o, x) { const v4u x_ = (x); (v)[(o)] = x_[0]; (v)[(o) + 1] = x_[1]; (v)[(o) + 2] = x_[2]; (v)[(o) + 3] = x_[3]; }
__device__ __forceinline__ v2u tr4(const u16* p) { return __builtin_bit_cast(v2u, __builtin_amdgcn_ds_read_tr16_b64_v4i16(LDS3(p))); }
__device__ __forceinline__ f32x4 smm(v4u a, v8u b, f32x4 c, int idx) {
    return __builtin_amdgcn_smfmac_f32_16x16x64_bf16(__builtin_bit_cast(bf16x8v, a), __builtin_bit_cast(bf16x16v, b), c, idx, 0, 0);
}
__device__ __forceinline__ v8u tr_frag64(const u16* p, const int pitch) {
    const v2u r0 = tr4(p), r1 = tr4(p + 8 * pitch), r2 = tr4(p + 32 * pitch), r3 = tr4(p + 40 * pitch);
    return v8u{r0[0], r1[0], r0[1], r1[1], r2[0], r3[0], r2[1], r3[1]};
}
#define MM(a_, b_, c_) __builtin_amdgcn_mfma_f32_16x16x32_bf16(as_frag(a_), as_frag(b_), c_, 0, 0, 0)
#define STR2(x) #x
#define STR(x) STR2(x)
#define PAUSE(n) ".rept (" STR(n) ") / 4\n\ts_nop 15\n\t.endr\n\t.rept (" STR(n) ") %% 4\n\ts_nop 3\n\t.endr\n\t"
#define ZERO4 f32x4{0.f, 0.f, 0.f, 0.f}
constexpr int XB_OFF = CAL_OFF + 262144, XP = 68;
__device__ __forceinline__ void last_rows(const char* chsm, rsrc_t rsS, const int wu, const int grp, const int c, const unsigned tau) {
    const int l = lane_now();
    __builtin_amdgcn_s_barrier();
    asm volatile("" ::: "memory");
    const float* rp = (const float*)(chsm + C2_K) + (4 * wu + (l >> 4)) * XP + 4 * (l & 15);
    const f32x4 s0 = (*(const f32x4*)rp + *(const f32x4*)(rp + 32 * XP)) + (*(const f32x4*)(rp + 64 * XP) + *(const f32x4*)(rp + 96 * XP));
    v2u o;
    o[0] = cvt_pk_bf16(s0[0], s0[1]); o[1] = cvt_pk_bf16(s0[2], s0[3]);
    st8_sc1s(rsS, l * 8, XB_OFF + ((c >> 1) * 8 + wu) * 16384 + (int)(tau & 1u) * 8192 + (c & 1) * 4096 + grp * 512, o);
}
__device__ __forceinline__ const unsigned* kv_flag(const unsigned* sync, const int sq, const int grp, const int l) { return sync + (sq < KVS ? (SY_KV0 + sq * 8 + grp) * 32 + (l & 7) : (SY_KV + 8 + grp) * 32 + (l & 3)); }
__device__ __forceinline__ void chain_body(const ChainArgs& a, const int grp, const int c, char* chsm) {
    const int wu = __builtin_amdgcn_readfirstlane(TIDX >> 6);
    const int widx = grp * 16 + c;
    const int hs = 4 * grp + (c >> 2), jq = c & 3;
    const rsrc_t rsS = slot_rsrc(a.slots);
    const unsigned l0 = (unsigned)(uintptr_t)(__attribute__((address_space(3))) char*)chsm;
    bool dead = false;

    const int cal_off = CAL_OFF + grp * 32768 + (c >> 3) * 16384 + (c & 7) * 1024;
        if (wu == 0) {
            const int l = lane_now();
            const v4u ff = {SENT, SENT, SENT, SENT};
#pragma unroll
            for (int e = 0; e < 13; ++e) st_sc1s(rsS, l * 16, widx * 2 * PERWG + min(e * 1024, 2 * PERWG - 1024), ff);
            const rsrc_t rsY = __builtin_amdgcn_make_buffer_rsrc((void*)a.sync, 0, 131072, 0x00020000);
            st_sc1s(rsY, l * 16, widx * 1024, v4u{0u, 0u, 0u, 0u});
            __builtin_amdgcn_raw_buffer_store_b32(0u, rsS, (l & 3) * 256 + ((l >> 2) & 1) * 8192, cal_off, AUX_SC1);
            if (c < 2) __builtin_amdgcn_raw_buffer_store_b32(0u, rsS, (l >> 5) * 8192 + (l & 31) * 4, TM_OFF + c * 16384 + grp * 256, AUX_SC1);
        }
    asm volatile("" ::: "memory");
    if (wu >= 4) {
        __builtin_amdgcn_s_sleep(DLY_U);
        const int l = opaque_tid() & 63, i = l & 15, g = l >> 4;
        const int sidx = (i & 1) ? 0xEEEE : 0x4444;
        const int u = wu - 4;
        const int k0 = 256 * u;
        u16* stg = (u16*)(chsm + 40960 + u * 20480);
        v4u af[4];
        v32u wS[6];
        {
            v4u raw[3][16];
            {
                const size_t wo = (size_t)k0 * HD + hcol(hs) + 32 * jq;
                const rsrc_t rq = __builtin_amdgcn_make_buffer_rsrc((void*)(a.Wq + wo), 0, 256 * HD * 2, 0x00020000);
                const rsrc_t rk = __builtin_amdgcn_make_buffer_rsrc((void*)(a.Wk + wo), 0, 256 * HD * 2, 0x00020000);
                const rsrc_t rv = __builtin_amdgcn_make_buffer_rsrc((void*)(a.Wv + wo), 0, 256 * HD * 2, 0x00020000);
                const int vw = (l >> 2) * HD * 2 + (l & 3) * 16;
#pragma unroll
                for (int p = 0; p < 4; ++p) af[p] = *(const v4u*)(a.mem0 + (size_t)(i >> 1) * DM + k0 + 64 * p + 16 * g + 8 * (i & 1));
#pragma unroll
                for (int e = 0; e < 16; ++e) raw[0][e] = __builtin_amdgcn_raw_buffer_load_b128(rq, vw, e * 16 * HD * 2, 0);
#pragma unroll
                for (int e = 0; e < 16; ++e) raw[1][e] = __builtin_amdgcn_raw_buffer_load_b128(rk, vw, e * 16 * HD * 2, 0);
#pragma unroll
                for (int e = 0; e < 12; ++e) raw[2][e] = __builtin_amdgcn_raw_buffer_load_b128(rv, vw, e * 16 * HD * 2, 0);
                __builtin_amdgcn_sched_barrier(0);
#pragma unroll
                for (int e = 0; e < 16; ++e) *(v4u*)(stg + (16 * e + (l >> 2)) * 40 + 8 * (l & 3)) = raw[0][e];
                __builtin_amdgcn_sched_barrier(0);
#pragma unroll
                for (int e = 12; e < 16; ++e) raw[2][e] = __builtin_amdgcn_raw_buffer_load_b128(rv, vw, e * 16 * HD * 2, 0);
            }
            const int fr = 16 * (g >> 1) + 4 * (g & 1) + (i >> 2);
            __builtin_amdgcn_sched_barrier(0);
#pragma unroll
            for (int hx = 0; hx < 3; ++hx) {
                if (hx) {
#pragma unroll
                    for (int e = 0; e < 16; ++e) *(v4u*)(stg + (16 * e + (l >> 2)) * 40 + 8 * (l & 3)) = raw[hx][e];
                }
                __builtin_amdgcn_s_waitcnt(0xC07F);
#pragma unroll
                for (int jj = 0; jj < 2; ++jj)
#pragma unroll
                    for (int p = 0; p < 4; ++p) PUT8(wS[2 * hx + jj], 8 * p, tr_frag64(stg + (64 * p + fr) * 40 + 16 * jj + 4 * (i & 3), 40))
                __builtin_amdgcn_s_waitcnt(0xC07F);
                __builtin_amdgcn_sched_barrier(0);
            }
        }
        PST(u)
        if (u == 0) { PST(4) }
        const unsigned r1w = l0 + C2_R1 + u * R1S + (2 * g * 16 + i) * 4, r1v = l0 + C2_R1 + u * R1S + 2176 + (2 * g * 16 + ((i & 9) | ((i & 2) << 1) | ((i & 4) >> 1))) * 4;
#define LD4(d, va, so, o) "buffer_load_dwordx4 v[" d "], " va ", %[rs], " so " offen offset:" o " sc1\n\t"
#define CHK(a, b, c, d) "v_max3_u32 v244, v244, v" a ", v" b "\n\tv_max3_u32 v244, v244, v" c ", v" d "\n\t"
#define CHK0(a, b, c, d) "v_max_u32 v244, v" a ", v" b "\n\tv_max3_u32 v244, v244, v" c ", v" d "\n\t"
#define SMF(d, a, w) "v_smfmac_f32_16x16x64_bf16 v[" d "], v[" a "], v[" w "], %[sx]\n\t"
#define ADD(d, a, b) "v_add_f32 v" d ", v" a ", v" b "\n\t"
#define Z2(r) "v_mov_b64 v[" r "], 0\n\t"
#define DW2(ad, x, y, o0, o1) "ds_write2_b32 " ad ", v" x ", v" y " offset0:" o0 " offset1:" o1 "\n\t"
#define A0 "224:227"
#define A1 "228:231"
#define A2 "232:235"
#define A3 "236:239"
#define Q0 "200:203"
#define Q1 "204:207"
#define Q2 "208:211"
#define Q3 "212:215"
#define V0 "216:219"
#define V1 "220:223"
#define S1_HEAD \
            Z2("216:217") Z2("218:219") Z2("220:221") Z2("222:223") \
            "s_waitcnt vmcnt(4)\n\t" ZQ \
            "s_waitcnt vmcnt(3)\n\t" SMF(V0, A0, "136:143") CHK0("224", "225", "226", "227") SMF(V1, A0, "168:175") \
            "s_waitcnt vmcnt(2)\n\t" SMF(V0, A1, "144:151") CHK("228", "229", "230", "231") SMF(V1, A1, "176:183") \
            "s_waitcnt vmcnt(1)\n\t" SMF(V0, A2, "152:159") CHK("232", "233", "234", "235") SMF(V1, A2, "184:191") \
            "s_waitcnt vmcnt(0)\n\t" SMF(V0, A3, "160:167") CHK("236", "237", "238", "239") SMF(V1, A3, "192:199") \
            "v_cmp_eq_u32 vcc, " STR(SENT) ", v244\n\t"
#define S1_TAIL(B1, B2) \
            SMF(Q0, A0, "8:15") SMF(Q0, A1, "16:23") SMF(Q0, A2, "24:31") \
            ADD("240", "216", "217") ADD("241", "218", "219") SMF(Q0, A3, "32:39") DW2("%[rv]", "240", "241", "0", "16") \
            ADD("242", "220", "221") ADD("243", "222", "223") SMF(Q1, A0, "40:47") DW2("%[rv]", "242", "243", "128", "144") \
            SMF(Q1, A1, "48:55") SMF(Q1, A2, "56:63") SMF(Q1, A3, "64:71") \
            ADD("240", "200", "201") ADD("241", "202", "203") SMF(Q2, A0, "72:79") DW2("%[rw]", "240", "241", "0", "16") \
            SMF(Q2, A1, "80:87") SMF(Q2, A2, "88:95") "s_nop 1\n\t" \
            ADD("242", "204", "205") ADD("243", "206", "207") SMF(Q2, A3, "96:103") DW2("%[rw]", "242", "243", "136", "152") \
            SMF(Q3, A0, "104:111") SMF(Q3, A1, "112:119") \
            B1        \
            LTA(0) \
            SMF(Q3, A2, "120:127") SMF(Q3, A3, "128:135") "s_nop 0\n\t" \
            ADD("240", "208", "209") ADD("241", "210", "211") DW2("v246", "240", "241", "0", "16") \
            "s_nop 3\n\t" \
            ADD("242", "212", "213") ADD("243", "214", "215") DW2("v246", "242", "243", "136", "152") \
            "s_waitcnt lgkmcnt(0)\n\t" B2 "s_barrier\n\t"
#define ZQ Z2("200:201") Z2("202:203") Z2("204:205") Z2("206:207") Z2("208:209") Z2("210:211") Z2("212:213") Z2("214:215")
#define U_W "{v[8:39]}"(wS[0]), "{v[40:71]}"(wS[1]), "{v[72:103]}"(wS[2]), "{v[104:135]}"(wS[3]), "{v[136:167]}"(wS[4]), "{v[168:199]}"(wS[5])
#define U_CLOB CLOB16(200, 201, 202, 203, 204, 205, 206, 207, 208, 209, 210, 211, 212, 213, 214, 215), CLOB16(216, 217, 218, 219, 220, 221, 222, 223, 224, 225, 226, 227, 228, 229, 230, 231), \
            CLOB16(232, 233, 234, 235, 236, 237, 238, 239, 240, 241, 242, 243, 244, 245, 246, 247)
        __builtin_amdgcn_s_setprio(2);
        {
            asm volatile(
                "v_add_u32 v246, 1088, %[rw]\n\t"
                S1_HEAD "s_nop 0\n\t" S1_TAIL("s_nop 1\n\t", "s_barrier\n\t")
                : LTOUT0
                : U_W, "{v[224:227]}"(af[0]), "{v[228:231]}"(af[1]), "{v[232:235]}"(af[2]), "{v[236:239]}"(af[3]), [sx] "v"(sidx), [rv] "v"(r1v), [rw] "v"(r1w)
                : "memory", "vcc", CLOB16(200, 201, 202, 203, 204, 205, 206, 207, 208, 209, 210, 211, 212, 213, 214, 215), CLOB16(216, 217, 218, 219, 220, 221, 222, 223, 240, 241, 242, 243, 244, 245, 246, 247));
        }
        const unsigned tau = __builtin_amdgcn_readfirstlane(*(volatile unsigned*)(chsm + C2_MB + 8));
        const unsigned long long wmask = u == 3 ? (c == 0 ? ~0ull : 0ull) : u == 1 ? 0x0020202020202000ull : u == 2 ? 1ull << (NSEG - 3) : 0ull;
        const int pvv = u == 3 ? 2 : -1, pvm = u == 3 ? -1 : 0;
        const unsigned* pfl = a.sync + (u == 3 ? SY_PROG + grp : u == 1 ? SY_KV + 16 + grp : SY_Q + 7 * 8 + grp) * 32;
        int s_first = 0;
        asm volatile("s_branch 1f\n\t.p2align 8\n1:" : "+s"(s_first));
        for (int s = s_first; s < NSEG - 1; ++s) {
            __builtin_amdgcn_s_setprio(0);
            LT(1)
            const int pv = (wmask >> s) & 1ull ? pvv + (s & pvm) : 0;
            const unsigned* pa = pfl;
            if (pv < 0) pfl += 256;
            const int cb = grp & 3;
            int soffc = rec_base(s) + C_OFF + c * 16384 + near_half(s, tau) + cb * 2048;
            int o_as = rec_base(s + 1) + ((c >> 3) + (grp >> 2) * 4) * 16384 + near_half(s + 1, tau) + ((4 * c + cb) & 31) * 256;
            int soff = chunk_off(s + 1, grp, u >> 1, tau) + (u & 1) * 4096;
            const int o_tm = (s >> 1) * 2097152 + TM_OFF + (s & 1) * 16384;
            int lim = dead ? 0 : SPIN_LIMIT, bad = 0;
            const bool rcd = u > 0 || widx < 32;
            const int pc = u > 0 ? 3 * widx + u - 1 : rcd ? 384 + widx : s * 96 + widx - 32;
            const int o_arm = (rcd ? s + 2 < NSEG : pc < 512) ? (rcd ? rec_base(s + 2) : XB_OFF) + (pc >> 3) * 16384 + near_half(rcd ? s + 2 : 0, tau) + (pc & 7) * 1024 : OOB;
            bool there = true;
            if (__builtin_expect(s <= KVS, 0)) {
                there = __all(ld_flag(kv_flag(a.sync, s + 2, grp, l)) != 0u);
                *(volatile unsigned*)(chsm + C2_MB + 32 + (s & 1) * 16 + u * 4) = there ? 1u : 0u;
                __builtin_amdgcn_s_waitcnt(0xC07F);
            }
            const char* vb = !there ? nullptr : arena(a.slots) + (s + 2 >= NSEG ? KX_OFF : VX_OFF) + (blk_off(min(s + 2, NSEG - 1), 4 * grp + 2 * u - 2, tau) ^ (s + 2 >= NSEG ? 8192 : 0));
            asm volatile("" : "+s"(soffc), "+s"(o_as), "+s"(soff), "+s"(lim), "+s"(vb));
#define GL(o) "global_load_lds_dwordx4 %[l16], s[84:85] offset:" o " sc1\n\t"
#define GL4 GL("0") GL("1024") GL("2048") GL("3072")
#define GLN(d) "s_add_u32 m0, m0, 4096\n\ts_add_u32 s84, s84, " d "\n\ts_addc_u32 s85, s85, 0\n\t"
#define PKE PAUSE(PKA)
#define C_TRY(x, x0, x1, x2, x3, y, y0, y1, y2, y3) \
                "s_waitcnt vmcnt(3)\n\t" \
                "v_mfma_f32_16x16x32_bf16 v[240:243], v[216:219], v[" x "], 0\n\t" CHK0(x0, x1, x2, x3) \
                "s_waitcnt vmcnt(2)\n\t" \
                "v_mfma_f32_16x16x32_bf16 v[240:243], v[216:219], v[" y "], v[240:243]\n\t" CHK(y0, y1, y2, y3) \
                "v_cmp_eq_u32 vcc, " STR(SENT) ", v244\n\t" \
                "s_cbranch_vccz 3b\n\t" CN(1) \
                LD4(x, "%[l16]", "%[sc]", "0") LD4(y, "%[l16]", "%[sc]", "1024")
            asm volatile(
                "v_and_b32 v221, 16, %[l16]\n\t"
                "v_mov_b32 v222, 0x3f80\n\t"
                "v_bfe_u32 v223, %[l16], 5, 2\n\t"
                "v_lshlrev_b32 v221, v221, v222\n\t"
                "v_cmp_eq_u32 vcc, 0, v223\n\tv_and_b32 v220, 0xf0, %[l16]\n\tv_bfe_u32 v222, %[l16], 8, 1\n\tv_cndmask_b32 v216, 0, v221, vcc\n\t"
                "v_cmp_eq_u32 vcc, 1, v223\n\tv_lshl_add_u32 v220, v222, 3, v220\n\tv_lshrrev_b32 v222, 9, %[l16]\n\tv_cndmask_b32 v217, 0, v221, vcc\n\t"
                "v_cmp_eq_u32 vcc, 2, v223\n\tv_lshl_add_u32 v220, v222, 30, v220\n\ts_nop 0\n\tv_cndmask_b32 v218, 0, v221, vcc\n\t"
                "v_cmp_eq_u32 vcc, 3, v223\n\ts_nop 1\n\tv_cndmask_b32 v219, 0, v221, vcc\n\t"
                "v_mul_u32_u24 v222, 0x8000, v222\n\t"
                "v_and_b32 v221, 0xff, v220\n\t"
                "v_add_u32 v221, v221, v222\n\t"
                "v_lshrrev_b32 v247, 2, %[l16]\n\t"
                PKE
                "v_mov_b32 v240, " STR(SENT) "\n\tv_mov_b32 v241, v240\n\tv_mov_b32 v242, v240\n\tv_mov_b32 v243, v240\n\t"
                "buffer_store_dwordx4 v[240:243], %[l16], %[rs], %[arm] offen sc1\n\t"
                LTA(2)
                "s_setprio 2\n\t"
                "s_barrier\n\t"
                LTA(3)
                "v_add_u32 v246, 1088, %[rw]\n\t"
                "s_cmp_lt_u32 %[uu], 1\n\t"
                "s_cbranch_scc1 7f\n\t"
                "s_cmp_eq_u32 %[uu], 3\n\t"
                "s_cbranch_scc1 20f\n\t"
                "s_memrealtime s[84:85]\n\t"
                "v_lshrrev_b32 v241, 9, %[l16]\n\t"
                "v_and_b32 v242, 0x7c, v247\n\t"
                "v_lshl_add_u32 v241, v241, 13, v242\n\t"
                "s_waitcnt lgkmcnt(0)\n\t"
                "v_mov_b32 v240, s84\n\t"
                "buffer_store_dword v240, v241, %[rs], %[ts] offen sc1\n\t"
                "s_cmp_eq_u64 %[vb], 0\n\t"
                "s_cbranch_scc1 11f\n\t"
                "s_mov_b32 s86, m0\n\t"
                "s_mov_b64 s[84:85], %[vb]\n\t"
                "s_mov_b32 m0, %[lv]\n\t"
                PAUSE(PG0)
                "s_cmp_eq_u32 %[uu], 1\n\t"
                "s_cbranch_scc1 12f\n\t"
                PAUSE(PG1)
                "12:\n\t"
                GL4 GLN("4096") GL4 GLN("12288") GL4 GLN("4096") GL4
                "s_mov_b32 m0, s86\n\t"
                "11:\n\t"
                "s_cmp_lt_i32 %[pv], 0\n\t"
                "s_cbranch_scc0 8f\n\t"
                "v_and_b32 v241, 12, v247\n\t"
                "10:\n\t"
                "global_load_dword v240, v241, %[pa] sc1\n\t"
                "s_waitcnt vmcnt(0)\n\t"
                "v_cmp_gt_u32 vcc, 16, v240\n\t"
                "s_cbranch_vccz 8f\n\t"
                "s_sleep 6\n\t"
                "s_sub_u32 %[lim], %[lim], 1\n\t"
                "s_cbranch_scc0 10b\n\t"
                "s_mov_b32 %[bad], 3000\n\t"
                "s_mov_b32 %[lim], 0\n\t"
                "s_branch 8f\n\t"
                "20:\n\t"
                PAUSE(PS)
                "s_memrealtime s[84:85]\n\t"
                "s_load_dword s86, %[tb], 0\n\ts_load_dword s87, %[tb], 0x100\n\ts_load_dword s88, %[tb], 0x200\n\ts_load_dword s89, %[tb], 0x300\n\t"
                "s_load_dword s90, %[tb], 0x400\n\ts_load_dword s91, %[tb], 0x500\n\ts_load_dword s92, %[tb], 0x600\n\ts_load_dword s93, %[tb], 0x700\n\t"
                "s_waitcnt lgkmcnt(0)\n\t"
                "s_sub_u32 s86, s84, s86\n\ts_sub_u32 s87, s84, s87\n\ts_sub_u32 s88, s84, s88\n\ts_sub_u32 s89, s84, s89\n\t"
                "s_sub_u32 s90, s84, s90\n\ts_sub_u32 s91, s84, s91\n\ts_sub_u32 s92, s84, s92\n\ts_sub_u32 s93, s84, s93\n\t"
                "s_or_b32 s85, s86, s87\n\ts_min_u32 s86, s86, s87\n\ts_or_b32 s85, s85, s88\n\ts_min_u32 s86, s86, s88\n\ts_or_b32 s85, s85, s89\n\ts_min_u32 s86, s86, s89\n\t"
                "s_memrealtime s[88:89]\n\t"
                "s_or_b32 s85, s85, s90\n\ts_min_u32 s86, s86, s90\n\ts_or_b32 s85, s85, s91\n\ts_min_u32 s86, s86, s91\n\ts_or_b32 s85, s85, s92\n\ts_min_u32 s86, s86, s92\n\t"
                "s_or_b32 s85, s85, s93\n\ts_min_u32 s86, s86, s93\n\t"
                "s_cmp_gt_u32 s85, 127\n\t"
                CNS
                "s_cselect_b32 s85, " STR(TE0) ", 127\n\t"
                "s_min_u32 s86, s86, s85\n\t"
                "s_sub_u32 s84, s84, s86\n\t"
                "s_add_u32 s84, s84, %[td]\n\t"
                "s_cmp_lt_i32 %[pv], 1\n\t"
                "s_cbranch_scc1 4f\n\t"
                "v_mov_b32 v247, %[pv]\n\t"
                "s_mov_b64 exec, 1\n\t"
                "global_store_dword %[l16], v247, %[pa] sc1\n\t"
                "s_mov_b64 exec, -1\n\t"
                "4:\n\t"
                "s_waitcnt lgkmcnt(0)\n\t"
                "s_sub_i32 s84, s84, s88\n\t"
                "s_max_i32 s84, s84, 0\n\t"
                "s_min_i32 s84, s84, 40\n\t"
                "21:\n\t"
                "s_sub_u32 s84, s84, 1\n\t"
                "s_cbranch_scc0 21b\n\t"
                "s_branch 8f\n\t"
                "7:\n\t"
                PAUSE(PC)
                LD4(A0, "%[l16]", "%[sc]", "0") LD4(A1, "%[l16]", "%[sc]", "1024")
                PAUSE(PP)
                LD4(A2, "%[l16]", "%[sc]", "0") LD4(A3, "%[l16]", "%[sc]", "1024")
                "s_waitcnt vmcnt(3)\n\t"
                "v_mfma_f32_16x16x32_bf16 v[240:243], v[216:219], v[224:227], 0\n\t" CHK0("224", "225", "226", "227")
                "s_waitcnt vmcnt(2)\n\t"
                "v_mfma_f32_16x16x32_bf16 v[240:243], v[216:219], v[228:231], v[240:243]\n\t" CHK("228", "229", "230", "231")
                "v_cmp_eq_u32 vcc, " STR(SENT) ", v244\n\t"
                "s_cbranch_vccnz 30f\n\t"
                "3:\n\t"
                "s_nop 3\n\t"
                "v_cvt_pk_bf16_f32 v244, v240, v241\n\t"
                "v_cvt_pk_bf16_f32 v245, v242, v243\n\t"
                "buffer_store_dwordx2 v[244:245], v221, %[rs], %[oas] offen sc1\n\t"
                PAUSE(PB)
                "8:\n\t"
                LTA(4)
                "s_barrier\n\t"
                PAUSE(1)
                "2:\n\t"
                LD4(A0, "%[l16]", "%[sa]", "0") LD4(A1, "%[l16]", "%[sa]", "1024") LD4(A2, "%[l16]", "%[sa]", "2048") LD4(A3, "%[l16]", "%[sa]", "3072")
                S1_HEAD
                "s_cbranch_vccnz 6f\n\t"
                "5:\n\t"
                LTA(5)
                S1_TAIL("s_waitcnt lgkmcnt(0)\n\ts_barrier\n\t", "")
                "s_setprio 0\n\t"
                "s_branch 9f\n\t"
                "30:\n\t" CN(1)
                LD4(A0, "%[l16]", "%[sc]", "0") LD4(A1, "%[l16]", "%[sc]", "1024")
                "1:\n\t"
                C_TRY(A2, "232", "233", "234", "235", A3, "236", "237", "238", "239")
                C_TRY(A0, "224", "225", "226", "227", A1, "228", "229", "230", "231")
                "s_sub_u32 %[lim], %[lim], 1\n\t"
                "s_cbranch_scc0 1b\n\t"
                "s_mov_b32 %[bad], 2000\n\t"
                "s_mov_b32 %[lim], 0\n\t"
                "s_branch 3b\n\t"
                "6:\n\t" CN(65536)
                "v_mov_b32 v240, %[tg]\n\t"
                "v_mov_b32 v241, %[lf]\n\t"
                "ds_write_b32 v241, v240\n\t"
                "s_nop 0\n\t"
                "s_sub_u32 %[lim], %[lim], 1\n\t"
                "s_cbranch_scc0 2b\n\t"
                "s_mov_b32 %[bad], 1\n\t"
                "s_branch 5b\n\t"
                "9:\n\t"
                : [lim] "+s"(lim), [bad] "+s"(bad), [oas] "+s"(o_as) LTOUT CNOUT
                : U_W, [l16] "v"(l * 16), [rs] "s"(rsS), [sx] "v"(sidx), [rv] "v"(r1v), [rw] "v"(r1w),
                  [sc] "s"(soffc), [sa] "s"(soff), [uu] "s"(u), [pv] "s"(pv), [pa] "s"(pa), [vb] "s"(vb), [lv] "s"(l0 + (s + 2 >= NSEG ? C2_V + 32768 : s & 1 ? C2_K : C2_V) + (u - 1) * 16384), [td] "s"(TD), [ts] "s"(c == 0 && (u == 2 || s < NSEG - 3) ? o_tm + grp * 256 + (2 - u) * 2097152 : OOB), [tb] "s"(arena(a.slots) + o_tm + near_half(s, tau)),

                  [arm] "s"(o_arm),
                  [tg] "s"(s + 1), [lf] "s"(l0 + C2_MB + 16)
                : "memory", "vcc", "scc", "s84", "s85", "s86", "s87", "s88", "s89", "s90", "s91", "s92", "s93", U_CLOB);
            if (__builtin_expect(bad != 0, 0)) TMO_SET((unsigned)bad)
        }
        last_rows(chsm, rsS, wu, grp, c, tau);
        if (u == 3 && widx == 0 && l == 0) __hip_atomic_store((gu64*)(a.boot + 144), *(volatile unsigned long long*)(chsm + C2_MB), __ATOMIC_RELAXED, __HIP_MEMORY_SCOPE_AGENT);
        return;
    }
    const int l = opaque_tid() & 63, i = l & 15, g = l >> 4;
    const int sidx = (i & 1) ? 0xEEEE : 0x4444;
    const int ha = 4 * grp + wu;
    unsigned long long tag;
    v32u wO[2];
    {
        u16* stg = (u16*)(chsm + 122880 + wu * 10240);
        tag = boot_tag(a.boot);
        asm volatile("" ::: "memory");
        v4u rawo[16];
        const u16* src = a.Wo + (size_t)(hcol(ha) + (l >> 3)) * DM + 64 * c + 8 * (l & 7);
#pragma unroll
        for (int e = 0; e < 16; ++e) rawo[e] = *(const v4u*)(src + (size_t)8 * e * DM);
        __builtin_amdgcn_sched_barrier(0);
        if (wu == 0) {
            asm volatile("s_waitcnt vmcnt(16)" ::: "memory");
            unsigned tm[2] = {~0u, ~0u};
            const char* pb = arena(a.slots) + cal_off;
#pragma unroll
            for (int k = 0; k < 2 * NPROBE; ++k) {
                unsigned long long c0, c1; unsigned d;
                asm volatile("s_memtime %0\n\ts_waitcnt lgkmcnt(0)\n\ts_load_dword %2, %3, %4\n\ts_waitcnt lgkmcnt(0)\n\ts_memtime %1\n\ts_waitcnt lgkmcnt(0)"
                             : "=&s"(c0), "=&s"(c1), "=&s"(d) : "s"(pb), "i"((k >> 1) * 256 + (k & 1) * 8192) : "memory");
                tm[k & 1] = min(tm[k & 1], (unsigned)(c1 - c0));
            }
            const unsigned side = (__builtin_amdgcn_s_getreg(20 | (3 << 11)) >> 2) & 1u;
            if (l == 0) __hip_atomic_store((gu64*)(a.boot + widx), (tag << 2) | (((tm[0] > tm[1] ? 1u : 0u) ^ side) << 1) | side, __ATOMIC_RELAXED, __HIP_MEMORY_SCOPE_AGENT);
            PST(7)
        }
#pragma unroll
        for (int m = 0; m < 2; ++m) {
#pragma unroll
            for (int e = 0; e < 8; ++e) *(v4u*)(stg + (8 * e + (l >> 3)) * 72 + 8 * (l & 7)) = rawo[8 * m + e];
            __builtin_amdgcn_s_waitcnt(0xC07F);
#pragma unroll
            for (int ct = 0; ct < 4; ++ct) {
                const u16* p = stg + (8 * g + (i >> 2)) * 72 + 16 * ct + 4 * (i & 3);
                const v2u r0 = tr4(p), r1 = tr4(p + 4 * 72), r2 = tr4(p + 32 * 72), r3 = tr4(p + 36 * 72);
                PUT4(wO[ct >> 1], (ct & 1) * 16 + m * 8, (v4u{r0[0], r0[1], r1[0], r1[1]})) PUT4(wO[ct >> 1], (ct & 1) * 16 + m * 8 + 4, (v4u{r2[0], r2[1], r3[0], r3[1]}))
            }
            __builtin_amdgcn_s_waitcnt(0xC07F);
        }
        if (wu == 0) { PST(5) }
        if (widx == 0 && wu == 0) *(volatile unsigned long long*)(chsm + C2_MB) = tag;
    }
    if (wu >= 2) touch_sc1(arena(a.slots) + (size_t)min(l + (wu - 2) * 64, FX_OFF / 2097152 - 1) * 2097152 + 2097024 - 128, l0 + C2_MB + 256);
    unsigned long long fw = 0;
#pragma nounroll
    for (int spin = 0; spin < 100000; ++spin) {
        asm volatile("" ::: "memory");
        fw = __hip_atomic_load((gu64*)(a.boot + 256 + 16 * grp + (l & 7)), __ATOMIC_RELAXED, __HIP_MEMORY_SCOPE_AGENT);
        if (__all((fw | 255u) == ((tag << 8) | 255u))) break;
        __builtin_amdgcn_s_sleep(4);
    }
    asm volatile("" ::: "memory");
    const unsigned tau = __builtin_amdgcn_readfirstlane((unsigned)fw & 255u);
    if (wu == 3) { *(volatile unsigned*)(chsm + C2_MB + 8) = tau; *(volatile unsigned*)(chsm + C2_MB + 16) = ~0u; __builtin_amdgcn_s_waitcnt(0xC07F); }
    if (wu == 1) { PST(9) }

    const unsigned r3a = l0 + C2_R3 + wu * 2048 + g * 128 + i * 4;
    v32u yb;
    v8u kx;
    v8u sel; v2u sel3;
#pragma unroll
    for (int d = 0; d < 8; ++d) sel[d] = (i >> 1) == d ? (0x3C00u << ((i & 1) * 16)) : 0u;
#pragma unroll
    for (int d = 0; d < 2; ++d) sel3[d] = (i & 3) == 0 && (i >> 3) == d ? (0x3C00u << (((i >> 2) & 1) * 16)) : 0u;
    asm volatile("s_setreg_imm32_b32 hwreg(HW_REG_MODE, 23, 1), 1\n\ts_nop 3");
#define MF(d, a, b, c) "v_mfma_f32_16x16x32_bf16 v[" d "], v[" a "], v[" b "], " c "\n\t"
#define PX4(d0, d1, d2, d3, a, w0, w1, w2, w3) MF(d0, a, w0, "v[" d0 "]") MF(d1, a, w1, "v[" d1 "]") MF(d2, a, w2, "v[" d2 "]") MF(d3, a, w3, "v[" d3 "]")
#define PXT(d0, d1, d2, d3, a0, a1, a2, a3) MF(d0, a0, "32:35", "0") MF(d1, a0, "48:51", "0") MF(d2, a0, "64:67", "0") MF(d3, a0, "80:83", "0") \
        PX4(d0, d1, d2, d3, a1, "36:39", "52:55", "68:71", "84:87") PX4(d0, d1, d2, d3, a2, "40:43", "56:59", "72:75", "88:91") PX4(d0, d1, d2, d3, a3, "44:47", "60:63", "76:79", "92:95")
#define CVP(d, a, b) "v_cvt_pk_bf16_f32 v" d ", v" a ", v" b "\n\t"
#define PROJ_X(cnt) \
        "s_waitcnt " cnt "(8)\n\t" PXT("192:195", "196:199", "200:203", "204:207", "160:163", "164:167", "168:171", "172:175") \
        "s_waitcnt " cnt "(4)\n\t" PXT("208:211", "212:215", "216:219", "220:223", "176:179", "180:183", "184:187", "188:191") \
        CVP("128", "192", "193") CVP("129", "194", "195") CVP("136", "196", "197") CVP("137", "198", "199") CVP("144", "200", "201") CVP("145", "202", "203") CVP("152", "204", "205") CVP("153", "206", "207") \
        CVP("132", "208", "209") CVP("133", "210", "211") CVP("140", "212", "213") CVP("141", "214", "215") CVP("148", "216", "217") CVP("149", "218", "219") CVP("156", "220", "221") CVP("157", "222", "223") \
        "s_waitcnt " cnt "(0)\n\t"
#define CLOB_PX CLOB16(160, 161, 162, 163, 164, 165, 166, 167, 168, 169, 170, 171, 172, 173, 174, 175), CLOB16(176, 177, 178, 179, 180, 181, 182, 183, 184, 185, 186, 187, 188, 189, 190, 191), \
        CLOB16(192, 193, 194, 195, 196, 197, 198, 199, 200, 201, 202, 203, 204, 205, 206, 207), CLOB16(208, 209, 210, 211, 212, 213, 214, 215, 216, 217, 218, 219, 220, 221, 222, 223)
#define LDX(d, va, so, o) "buffer_load_dwordx4 v[" d "], " va ", %[rs], " so " offen offset:" o " sc1\n\t"
#define DSV(d, o) "ds_read_b128 v[" d "], %[lv] offset:" o "\n\t"
    const int voffX = ((i >> 1) & 1) * 4096 + g * 256 + (2 * (i >> 2) + (i & 1)) * 16;
    const int pk = (((i >> 1) & 3) << 1) | (i >> 3);
    const unsigned hv = wu >= 2 ? l0 + C2_R1 + 2176 + ((wu & 1) * 128 + ((l >> 4) + 4 * ((l >> 3) & 1)) * 16 + 8 * ((l >> 1) & 1) + 4 * ((l >> 2) & 1) + 2 * (l & 1)) * 4
                                : l0 + C2_R1 + (i & 1) * 544 + (i >> 1) * 64 + g * 16;
    const unsigned hk = l0 + C2_R1 + 1088 + (i & 1) * 544 + pk * 64 + g * 16;
    const int vo1 = wu >= 2 ? l * 4 : wu == 1 ? (g >> 1) * 1024 + i * 16 + (g & 1) * 8 : (i & 1) ? 1 << 30 : (i >> 1) * 16 + 4 * g;
    const int vo2 = (2 * (i & 1) + (g >> 1)) * 128 + pk * 16 + (g & 1) * 8;
    const int voffK = (g >> 1) * 256 + i * 16 + (g & 1) * 8;
#define RD4(w, ad, a, b, c, d) "ds_read_" w " v[" a "], " ad "\n\tds_read_" w " v[" b "], " ad " offset:3200\n\tds_read_" w " v[" c "], " ad " offset:6400\n\tds_read_" w " v[" d "], " ad " offset:9600\n\t"
#define AD4(d0, d1, d2, d3, a0, a1, a2, a3) ADD(d0, d0, a0) ADD(d1, d1, a1) ADD(d2, d2, a2) ADD(d3, d3, a3)
#define QSC(r) "v_mul_f32 v" r ", 0x3e0293ee, v" r "\n\t"
#define FLD "buffer_load_dword v114, %[vf], %[rs], %[fl] offen sc1\n\t"
#define DUM "buffer_store_dword v168, off, %[rs], %[ob] sc1\n\t"
#define QHEAD "s_barrier\n\t" RD4("b128", "%[hv]", "160:163", "164:167", "168:171", "172:175") FLD \
        "s_waitcnt lgkmcnt(2)\n\t" AD4("160", "161", "162", "163", "164", "165", "166", "167") \
        "s_waitcnt lgkmcnt(0)\n\t" AD4("168", "169", "170", "171", "172", "173", "174", "175") \
        "s_barrier\n\t"
#define QTAIL AD4("160", "161", "162", "163", "168", "169", "170", "171") QSC("160") QSC("161") QSC("162") QSC("163")
#define CVH(d, a, b) "v_cvt_pk_f16_f32 v" d ", v" a ", v" b "\n\t"
#define SWA(d, a, b) "v_add_f32_dpp v" d ", v" a ", v" b " quad_perm:[1,0,3,2] row_mask:0xf bank_mask:0xf\n\t"
#define KLD(so) "buffer_load_dwordx2 v[96:97], %[vk], %[rs], " so " offen sc1\n\tbuffer_load_dwordx2 v[102:103], %[vk], %[rs], " so " offen offset:512 sc1\n\t" \
        "s_add_u32 " so ", " so ", 4096\n\t" \
        "buffer_load_dwordx2 v[100:101], %[vk], %[rs], " so " offen sc1\n\tbuffer_load_dwordx2 v[98:99], %[vk], %[rs], " so " offen offset:512 sc1\n\t"
    auto hand_over = [&](const int o_1, const int o_2, const int o_f, const int tg) {
        int dmy = 0;
        asm volatile(
            "s_cmp_eq_u32 %[wu], 0\n\t"
            "s_cbranch_scc1 2f\n\t"
            "s_cmp_eq_u32 %[wu], 1\n\t"
            "s_cbranch_scc1 1f\n\t"
            "s_barrier\n\t"
            RD4("b64", "%[hv]", "160:161", "162:163", "164:165", "166:167") FLD
            "s_waitcnt lgkmcnt(2)\n\t" ADD("160", "160", "162") ADD("161", "161", "163")
            "s_waitcnt lgkmcnt(0)\n\t" ADD("164", "164", "166") ADD("165", "165", "167") ADD("160", "160", "164") ADD("161", "161", "165")
            CVP("168", "160", "161")
            "buffer_store_dword v168, %[v1], %[rs], %[o1] offen sc1\n\t"
            "s_barrier\n\t"
            DUM PAUSE(PV2)
            "s_branch 3f\n\t"
            "5:\n\t"
            "v_mov_b32 v115, %[lf]\n\t"
            "ds_read_b32 v115, v115\n\t"
            "s_waitcnt lgkmcnt(0)\n\t"
            "v_cmp_eq_u32 vcc, %[tg], v115\n\t"
            "s_cbranch_vccnz 4f\n\t" CN(256)
            PAUSE(PL)
            "s_branch 4f\n\t"
            "1:\n\t"
            Z2("176:177") Z2("178:179")
            "s_waitcnt vmcnt(0)\n\t"
            QHEAD QTAIL
            "s_mov_b64 exec, %[em]\n\t"
            CVP("176", "160", "161") CVP("177", "162", "163")
            "s_not_b64 exec, exec\n\t"
            CVP("178", "160", "161") CVP("179", "162", "163")
            "s_mov_b64 exec, -1\n\t"
            "s_nop 0\n\t"
            MF("180:183", "96:99", "176:179", "0") MF("184:187", "100:103", "176:179", "0")
            "s_nop 7\n\t"
            SWA("180", "184", "180") SWA("181", "185", "181") SWA("182", "186", "182") SWA("183", "187", "183")
            CVH("188", "180", "181") CVH("189", "182", "183")
            "buffer_store_dwordx2 v[188:189], %[v1], %[rs], %[o1] offen sc1\n\t"
            DUM PAUSE(PV1)
            "s_branch 3f\n\t"
            "2:\n\t"
            QHEAD
            RD4("b128", "%[hk]", "176:179", "180:183", "184:187", "188:191")
            QTAIL CVP("164", "160", "161") CVP("165", "162", "163")
            "s_waitcnt lgkmcnt(2)\n\t" AD4("176", "177", "178", "179", "180", "181", "182", "183")
            "s_waitcnt lgkmcnt(0)\n\t" AD4("184", "185", "186", "187", "188", "189", "190", "191") AD4("176", "177", "178", "179", "184", "185", "186", "187")
            CVP("166", "176", "177") CVP("167", "178", "179")
            "s_nop 1\n\t"
            "v_mfma_f32_16x16x16_bf16 v[168:171], v[166:167], v[164:165], 0\n\t"
            "buffer_store_dwordx2 v[166:167], %[v2], %[rs], %[o2] offen sc1\n\t"
            "s_nop 6\n\t"
            SWA("172", "169", "168") SWA("173", "171", "170")
            CVH("174", "172", "173")
            "buffer_store_dword v174, %[v1], %[rs], %[o1] offen sc1\n\t"
            "3:\n\t"
            PAUSE(PVA)
            "s_waitcnt vmcnt(2)\n\t"
            "v_cmp_ne_u32 vcc, " STR(SENT) ", v114\n\t"
            "s_cbranch_vccnz 5b\n\t"
            "4:\n\t"
            : [dm] "+s"(dmy) CNOUT : "{v[96:103]}"(kx), [fl] "s"(o_f), [vf] "v"(i * 128), [ob] "s"(1 << 30), [tg] "s"(tg), [lf] "s"(l0 + C2_MB + 16), [wu] "s"(wu), [hv] "v"(hv), [hk] "v"(hk), [v1] "v"(vo1), [v2] "v"(vo2), [rs] "s"(rsS), [o1] "s"(o_1), [o2] "s"(o_2), [em] "s"(0x5555555555555555ull)
            : "memory", "scc", "vcc", "v114", "v115", CLOB16(160, 161, 162, 163, 164, 165, 166, 167, 168, 169, 170, 171, 172, 173, 174, 175), CLOB16(176, 177, 178, 179, 180, 181, 182, 183, 184, 185, 186, 187, 188, 189, 190, 191));
    };
    auto o1_of = [&](const int s) { return wu >= 2 ? v_off(s, hs, tau) + jq * 512 + (wu & 1) * 256 : wu == 1 ? x_off(s, hs, tau) + jq * 256 : m_off(s, hs, tau) + jq * 128; };

    __builtin_amdgcn_s_setprio(1);
    int o_f = f_off(0, grp, tau);
    int o_cs = rec_base(0) + C_OFF + c * 16384 + near_half(0, tau) + wu * 2048 + grp * 256;
    int o_1 = o1_of(0), o_2 = km_off(0, hs, tau) + jq * 512, bq = x_off(0, ha, tau), bm = m_off(0, ha, tau), bb = v_off(0, ha, tau);
    {
            const int b0 = blk_off(0, ha, tau);
            int k0 = KX_OFF + blk_off(0, hs, tau) + jq * 1024;
            asm volatile(
                LDX("160:163", "%[vx]", "%[vb]", "0") LDX("164:167", "%[vx]", "%[vb]", "1024") LDX("168:171", "%[vx]", "%[vb]", "2048") LDX("172:175", "%[vx]", "%[vb]", "3072")
                LDX("176:179", "%[vx]", "%[vb]", "128") LDX("180:183", "%[vx]", "%[vb]", "1152") LDX("184:187", "%[vx]", "%[vb]", "2176") LDX("188:191", "%[vx]", "%[vb]", "3200")
                KLD("%[k0]")
                "v_mov_b32 v131, 0\n\tv_mov_b32 v135, 0\n\tv_mov_b32 v139, 0\n\tv_mov_b32 v143, 0\n\tv_mov_b32 v147, 0\n\tv_mov_b32 v151, 0\n\tv_mov_b32 v155, 0\n\tv_mov_b32 v159, 0\n\t"
                PROJ_X("vmcnt")
                : "=&{v[128:159]}"(yb), "=&{v[96:103]}"(kx), [k0] "+s"(k0)
                : "{v[32:63]}"(wO[0]), "{v[64:95]}"(wO[1]), [vx] "v"(voffX), [vk] "v"(voffK), [rs] "s"(rsS), [vb] "s"(VX_OFF + b0)
                : "memory", "scc", CLOB_PX);
        if (wu == 1) { PST(10) }
    }
    int s_first = 0;
    asm volatile("s_branch 1f\n\t.p2align 8\n1:" : "+s"(s_first));
    for (int s = s_first; s < NSEG - 1; ++s) {
        hand_over(o_1, o_2, o_f, s);
        if (s == 0 && wu == 1) { PST(8) }
        LT(0)
#define LDPA "buffer_load_dwordx2 v[160:161], v254, %[rs], %[bm] offen sc1\n\t" LDX("164:167", "%[l16]", "%[bq]", "0") LDX("168:171", "%[l16]", "%[bq]", "1024")
#define SM(d, a, b, x) "v_smfmac_f32_16x16x64_bf16 v[" d "], v[" a "], v[" b "], " x "\n\t"
#define MX2(a, b) "v_max_u32 v244, v" a ", v" b "\n\t"
#define MX3(a, b) "v_max3_u32 v244, v244, v" a ", v" b "\n\t"
#define ZAM Z2("208:209") Z2("210:211") Z2("212:213") Z2("214:215") Z2("216:217") Z2("218:219") Z2("220:221") Z2("222:223")
#define MUL(d, a, b) "v_mul_f32 v" d ", v" a ", v" b "\n\t"
#define CV0(d, a) "v_cvt_pk_bf16_f32 v" d ", v" a ", 0\n\t"
#define RDC "ds_read_b64 v[224:225], %[rc]\n\tds_read_b64 v[226:227], %[rc] offset:2048\n\tds_read_b64 v[228:229], %[rc] offset:4096\n\tds_read_b64 v[230:231], %[rc] offset:6144\n\t"
#define EXD(d, r) "v_exp_f32 v" d ", v" r "\n\t"
#define SUBM(r) "v_sub_f32 v" r ", v" r ", v250\n\t"
#define PCVM CVP("241", "180", "180") CVP("243", "180", "180")
#define PCVX CVP("242", "178", "179") CVP("240", "176", "177")
#define OUT(t0, t1, a0, a1, a2, a3, ad, o0, o1) ADD(t0, a0, a1) ADD(t1, a2, a3) MUL(t0, t0, "250") MUL(t1, t1, "251") DW2(ad, t0, t1, o0, o1)
#define TS(n)
#define TSOUT
        int lim = dead ? 0 : SPIN_LIMIT, bad = 0;
        asm volatile(
            LDX("192:195", "%[l16]", "%[bb]", "0") LDX("196:199", "%[l16]", "%[bb]", "1024")
            ZAM Z2("224:225") Z2("226:227")
            Z2("16:17") Z2("18:19") Z2("20:21") Z2("22:23") Z2("24:25") Z2("26:27") Z2("28:29") Z2("30:31")
            "v_mov_b32 v200, 0x3f803f80\n\tv_mov_b32 v201, 0x3f803f80\n\tv_mov_b32 v202, 0x3f80\n\tv_mov_b32 v203, 0\n\t"
            "v_mov_b64 v[204:205], v[200:201]\n\tv_mov_b64 v[206:207], v[202:203]\n\t"
            "v_xor_b32 v252, 0xa000, %[sx]\n\tv_add_u32 v253, 1024, %[ra]\n\tv_lshrrev_b32 v254, 1, %[l16]\n\t"
            PAUSE(PV)
            TS(8)
            LDPA
            "4:\n\t"
            "s_waitcnt vmcnt(4)\n\t"
            SM("208:211", "192:195", "32:39", "%[sx]") MX2("192", "193") SM("212:215", "192:195", "48:55", "%[sx]") MX3("194", "195")
            SM("216:219", "192:195", "64:71", "%[sx]") SM("220:223", "192:195", "80:87", "%[sx]")
            "s_waitcnt vmcnt(3)\n\t"
            SM("208:211", "196:199", "40:47", "%[sx]") MX3("196", "197") SM("212:215", "196:199", "56:63", "%[sx]") MX3("198", "199")
            SM("216:219", "196:199", "72:79", "%[sx]") "v_cmp_eq_u32 vcc, " STR(SENT) ", v244\n\t" SM("220:223", "196:199", "88:95", "%[sx]")
            "s_cbranch_vccnz 5f\n\t"
            "1:\n\t" TS(11)
            "s_waitcnt vmcnt(2)\n\t"
            "v_mfma_f32_16x16x16_f16 v[236:239], v[112:113], v[160:161], 0\n\t"
            MX2("160", "161") ADD("245", "208", "209") ADD("246", "210", "211") CV0("130", "245") CV0("134", "246")
            ADD("245", "212", "213") ADD("246", "214", "215") CV0("138", "245") CV0("142", "246")
            EXD("180", "236")
            "s_waitcnt vmcnt(1)\n\t"
            "v_mfma_f32_16x16x32_f16 v[228:231], v[104:107], v[164:167], 0\n\t"
            MX3("164", "165") MX3("166", "167") ADD("245", "216", "217") ADD("246", "218", "219") CV0("146", "245") CV0("150", "246")
            ADD("245", "220", "221") ADD("246", "222", "223") CV0("154", "245") CV0("158", "246") PCVM
            "s_waitcnt vmcnt(0)\n\t"
            "v_mfma_f32_16x16x32_f16 v[228:231], v[108:111], v[168:171], v[228:231]\n\t"
            MX3("168", "169") MX3("170", "171")
            "v_cmp_eq_u32 vcc, " STR(SENT) ", v244\n\t"
            "s_cbranch_vccnz 7f\n\t"
            "2:\n\t" TS(9)
            "s_nop 3\n\t"
            EXD("179", "231") EXD("178", "230") EXD("177", "229") EXD("176", "228")
            PCVX
            "v_max3_f32 v250, |v236|, |v231|, |v230|\n\t"
            "v_max3_f32 v250, v250, |v229|, |v228|\n\t"
            SM("224:227", "240:243", "200:207", "v252")
            "v_cmp_nge_f32 vcc, 0x41c00000, v250\n\t"
            "s_nop 0\n\t"
            SM("16:19", "240:243", "128:135", "v252")
            "s_cbranch_vccnz 10f\n\t"
            "13:\n\t"
            "s_nop 0\n\t"
            SM("20:23", "240:243", "136:143", "v252") "s_nop 1\n\t"
            ADD("250", "224", "225") ADD("251", "226", "227")
            SM("24:27", "240:243", "144:151", "v252")
            "v_rcp_f32 v250, v250\n\t"
            SM("28:31", "240:243", "152:159", "v252")
            "v_rcp_f32 v251, v251\n\t"
            OUT("245", "246", "16", "17", "18", "19", "%[ra]", "0", "16")
            OUT("247", "248", "20", "21", "22", "23", "%[ra]", "128", "144")
            OUT("245", "246", "24", "25", "26", "27", "v253", "0", "16")
            OUT("247", "248", "28", "29", "30", "31", "v253", "128", "144")
            TS(10)
            "s_waitcnt lgkmcnt(0)\n\t"
            "s_barrier\n\t"
            RDC
            "v_lshrrev_b32 v247, 2, %[l16]\n\t"
            "s_waitcnt lgkmcnt(2)\n\t"
            ADD("224", "224", "226") ADD("225", "225", "227")
            "s_waitcnt lgkmcnt(0)\n\t"
            ADD("228", "228", "230") ADD("229", "229", "231") ADD("224", "224", "228") ADD("225", "225", "229")
            "v_cvt_pk_bf16_f32 v245, v224, v225\n\t"
            "buffer_store_dword v245, v247, %[rs], %[cs] offen sc1\n\t"
            "s_branch 9f\n\t"
            "10:\n\t"
            "v_max3_f32 v250, v236, v231, v230\n\t"
            "v_max3_f32 v250, v250, v229, v228\n\t"
            "s_nop 1\n\t"
            "v_max_f32_dpp v251, v250, v250 quad_perm:[1,0,3,2] row_mask:0xf bank_mask:0xf\n\t"
            "v_max_f32_dpp v244, v250, v250 quad_perm:[1,0,3,2] row_mask:0xf bank_mask:0xf\n\t"
            "s_nop 1\n\t"
            "v_permlane16_swap_b32 v251, v244\n\t"
            "v_max_f32 v250, v251, v244\n\t"
            "v_max_f32 v251, v251, v244\n\t"
            Z2("224:225") Z2("226:227")
            "v_permlane32_swap_b32 v250, v251\n\t"
            "v_max_f32 v250, v250, v251\n\t"
            Z2("16:17") Z2("18:19")
            SUBM("228") SUBM("229") SUBM("230") SUBM("231") SUBM("236")
            EXD("180", "236") EXD("179", "231") EXD("178", "230") EXD("177", "229") EXD("176", "228")
            PCVM PCVX
            "s_nop 1\n\t"
            SM("224:227", "240:243", "200:207", "v252") "s_nop 1\n\t"
            SM("16:19", "240:243", "128:135", "v252")
            "s_branch 13b\n\t"
            "5:\n\t" CN(1)
            LDX("192:195", "%[l16]", "%[bb]", "0") LDX("196:199", "%[l16]", "%[bb]", "1024")
            LDPA
            "s_sub_u32 %[lim], %[lim], 1\n\t"
            "s_nop 1\n\t"
            ZAM
            "s_cbranch_scc0 4b\n\t"
            "s_mov_b32 %[bad], 1500\n\t"
            "s_mov_b32 %[lim], 0\n\t"
            "s_branch 1b\n\t"
            "7:\n\t" CN(65536)
            LDPA
            "s_sub_u32 %[lim], %[lim], 1\n\t"
            "s_cbranch_scc0 1b\n\t"
            "s_mov_b32 %[bad], 1000\n\t"
            "s_branch 2b\n\t"
            "9:\n\t"
            : [lim] "+s"(lim), [bad] "+s"(bad), "+{v[128:159]}"(yb) TSOUT CNOUT
            : "{v[32:63]}"(wO[0]), "{v[64:95]}"(wO[1]), "{v[104:111]}"(sel), "{v[112:113]}"(sel3), [sx] "v"(sidx), [l16] "v"(l * 16),
              [ra] "v"(r3a), [rs] "s"(rsS), [bb] "s"(bb), [bq] "s"(bq), [bm] "s"(bm), [cs] "s"(o_cs), [rc] "v"(l0 + C2_R3 + wu * 512 + l * 8)
            : "memory", "vcc", "scc", CLOB_PX, CLOB16(16, 17, 18, 19, 20, 21, 22, 23, 24, 25, 26, 27, 28, 29, 30, 31),
              CLOB16(224, 225, 226, 227, 228, 229, 230, 231, 232, 233, 234, 235, 236, 237, 238, 239), CLOB16(240, 241, 242, 243, 244, 245, 246, 247, 248, 249, 250, 251, 252, 253, 254, 254));
        if (__builtin_expect(bad != 0, 0)) TMO_SET((unsigned)bad)
        __builtin_amdgcn_s_setprio(0);
        if (s == 0 && wu == 1) { PST(6) }

        if (__builtin_expect(s <= KVS + 1, 0)) {
            if (s == 0 || __builtin_amdgcn_readfirstlane(*(volatile unsigned*)(chsm + C2_MB + 32 + ((s - 1) & 1) * 16 + 4 + (wu >> 1) * 4)) == 0u) {
                wait_ge<6>(kv_flag(a.sync, s + 1, grp, l), 1u, a.tmo, 3001u);
                const char* kp = arena(a.slots) + VX_OFF + blk_off(s + 1, ha, tau);
#pragma unroll
                for (int e = 0; e < 8; ++e) glds16h((unsigned)l * 16u, kp + e * 1024, l0 + (s & 1 ? C2_V : C2_K) + wu * 8192 + e * 1024);
                asm volatile("s_waitcnt vmcnt(0)" ::: "memory");
            }
        }
        {
            int k0 = KX_OFF + blk_off(s + 1, hs, tau) + jq * 1024;
            asm volatile(
                DSV("160:163", "0") DSV("164:167", "1024") DSV("168:171", "2048") DSV("172:175", "3072") DSV("176:179", "128") DSV("180:183", "1152") DSV("184:187", "2176") DSV("188:191", "3200")
                "s_cmp_lg_u32 %[wu], 1\n\t"
                "s_cbranch_scc1 1f\n\t"
                KLD("%[k0]")
                "1:\n\t"
                "s_waitcnt lgkmcnt(4)\n\t"
                PXT("192:195", "196:199", "200:203", "204:207", "160:163", "164:167", "168:171", "172:175")
                "s_waitcnt lgkmcnt(0)\n\t"
                PXT("208:211", "212:215", "216:219", "220:223", "176:179", "180:183", "184:187", "188:191")
                CVP("128", "192", "193") CVP("129", "194", "195") CVP("136", "196", "197") CVP("137", "198", "199") CVP("144", "200", "201") CVP("145", "202", "203") CVP("152", "204", "205") CVP("153", "206", "207")
                CVP("132", "208", "209") CVP("133", "210", "211") CVP("140", "212", "213") CVP("141", "214", "215") CVP("148", "216", "217") CVP("149", "218", "219") CVP("156", "220", "221") CVP("157", "222", "223")
                : "+{v[128:159]}"(yb), "+{v[96:103]}"(kx), [k0] "+s"(k0)
                : "{v[32:63]}"(wO[0]), "{v[64:95]}"(wO[1]), [lv] "v"(l0 + (s & 1 ? C2_V : C2_K) + wu * 8192 + voffX), [vk] "v"(voffK), [rs] "s"(rsS), [wu] "s"(wu)
                : "memory", "scc", CLOB_PX);
        }
        __builtin_amdgcn_sched_barrier(0);
        o_1 = o1_of(s + 1); o_2 = km_off(s + 1, hs, tau) + jq * 512;
        bq = x_off(s + 1, ha, tau); bm = m_off(s + 1, ha, tau); bb = v_off(s + 1, ha, tau);
        o_f = f_off(s + 1, grp, tau);
        o_cs = rec_base(s + 1) + C_OFF + c * 16384 + near_half(s + 1, tau) + wu * 2048 + grp * 256;
        asm volatile("" : "+s"(o_cs), "+s"(o_f), "+s"(o_1), "+s"(o_2), "+s"(bq), "+s"(bm), "+s"(bb));
        __builtin_amdgcn_s_barrier();
        asm volatile(
            "s_cmp_lg_u32 %[wu], 3\n\t"
            "s_cbranch_scc1 1f\n\t"
            "v_mov_b32 v160, %[lf]\n\t"
            PAUSE(PF)
            "ds_read_b32 v160, v160\n\t"
            "s_waitcnt lgkmcnt(0)\n\t"
            "v_cmp_eq_u32 vcc, %[tg], v160\n\t"
            "s_cbranch_vccz 1f\n\t"
            "s_mov_b32 exec_hi, 0\n\t"
            "buffer_store_dword v160, %[l4], %[rs], %[fo] offen sc1\n\t"
            "s_mov_b64 exec, -1\n\t"
            "1:\n\t"
            :: [wu] "s"(wu), [lf] "s"(l0 + C2_MB + 16), [tg] "s"(s + 1), [l4] "v"(l * 4), [rs] "s"(rsS), [fo] "s"(o_f + c * 128)
            : "memory", "scc", "vcc", "v160");
        __builtin_amdgcn_s_setprio(1);
    }
    {
        constexpr int s = NSEG - 1;
        const int l = lane_now(), i = l & 15, g = l >> 4;
        v4u qf[8], kf[8], pv[2], px[4];
        asm volatile("s_waitcnt vmcnt(0)" ::: "memory");
        __builtin_amdgcn_s_sleep(4);
#pragma unroll
        for (int e = 0; e < 8; ++e) kf[e] = ld_sc1s(rsS, l * 16, KX_OFF + blk_off(s, ha, tau) + e * 1024);
        hand_over(o_1, o_2, o_f, s);
        const int bk = km_off(s, ha, tau);
        auto ask = [&]() {
            asm volatile("" ::: "memory");
            pv[0] = ld_sc1s(rsS, l * 16, bb); pv[1] = ld_sc1s(rsS, l * 16, bb + 1024);
#pragma unroll
            for (int ks = 0; ks < 4; ++ks) px[ks] = ld_sc1s(rsS, g * 128 + (i >> 1) * 16, bk + ks * 512);
            asm volatile("" ::: "memory");
        };
        asm volatile(PAUSE(PLL) ::: "memory");
        ask();
#pragma unroll
        for (int e = 0; e < 8; ++e) qf[e] = *(const v4u*)(chsm + C2_V + 32768 + wu * 8192 + e * 1024 + l * 16);
        f32x4 sx[3] = {ZERO4, ZERO4, ZERO4};
        float mxx[2] = {-3.0e38f, -3.0e38f};
#pragma unroll
        for (int ks = 0; ks < 4; ++ks) {
            sx[0] = MM(kf[ks], qf[ks], sx[0]);
            sx[1] = MM(kf[ks], qf[4 + ks], sx[1]);
            sx[2] = MM(kf[4 + ks], qf[4 + ks], sx[2]);
        }
#pragma unroll
        for (int r = 0; r < 4; ++r) {
            if (4 * g + r > i) { sx[0][r] = -3.0e38f; sx[2][r] = -3.0e38f; }
            mxx[0] = fmaxf(mxx[0], sx[0][r]);
            mxx[1] = fmaxf(mxx[1], fmaxf(sx[1][r], sx[2][r]));
        }
        asm volatile("" : "+v"(sx[0]), "+v"(sx[1]), "+v"(sx[2]), "+v"(mxx[0]), "+v"(mxx[1]));
#pragma nounroll
        for (int spin = 0;; ++spin) {
            if (__all(mx4(mx4(mx4(mx4(mx4(mx4(0u, pv[0]), pv[1]), px[0]), px[1]), px[2]), px[3]) != SENT)) break;
            if (dead) break;
            if (spin > SPIN_LIMIT) { TMO_SET(1700u) break; }
            ask();
        }
#define SUB8(v, o) v8u{(v)[(o)], (v)[(o) + 1], (v)[(o) + 2], (v)[(o) + 3], (v)[(o) + 4], (v)[(o) + 5], (v)[(o) + 6], (v)[(o) + 7]}
#pragma unroll
        for (int ct = 0; ct < 4; ++ct) {
            f32x4 am = smm(pv[0], SUB8(wO[ct >> 1], (ct & 1) * 16), ZERO4, sidx);
            am = smm(pv[1], SUB8(wO[ct >> 1], (ct & 1) * 16 + 8), am, sidx);
            yb[8 * ct + 2] = cvt_pk_bf16(am[0] + am[1], 0.f); yb[8 * ct + 6] = cvt_pk_bf16(am[2] + am[3], 0.f);
        }
        const v8u one = {0x3F803F80u, 0x3F803F80u, 0x3F80u, 0u, 0x3F803F80u, 0x3F803F80u, 0x3F80u, 0u};
        const float SCL2E = 0.08838834764831845f * 1.4426950408889634f;
        float* xo = (float*)(chsm + C2_K) + (wu * 32 + 4 * g) * XP + i;
#pragma unroll
        for (int qt = 0; qt < 2; ++qt) {
            f32x4 sc[3] = {sx[qt], sx[2], ZERO4};
#pragma unroll
            for (int ks = 0; ks < 4; ++ks) sc[2] = MM(px[ks], qf[4 * qt + ks], sc[2]);
            float mx = fmaxf(mxx[qt], fmaxf(sc[2][1], sc[2][3]));
            mx = xor32_max(xor16_max(mx));
            const float nmx = -mx * SCL2E;
#define PE(x) __builtin_amdgcn_exp2f(fmaf(x, SCL2E, nmx))
            const float m0 = PE(sc[2][1]), m1 = PE(sc[2][3]);
            const v4u ae = {cvt_pk_bf16(PE(sc[0][0]), PE(sc[0][1])), cvt_pk_bf16(m0, m0), cvt_pk_bf16(PE(sc[0][2]), PE(sc[0][3])), 0u};
            const v4u ao = {qt ? cvt_pk_bf16(PE(sc[1][0]), PE(sc[1][1])) : 0u, 0u, qt ? cvt_pk_bf16(PE(sc[1][2]), PE(sc[1][3])) : 0u, cvt_pk_bf16(m1, m1)};
            const f32x4 sum = smm(ao, one, smm(ae, one, ZERO4, 0xE444), 0x4EEE);
#pragma unroll
            for (int ct = 0; ct < 4; ++ct) {
                const f32x4 o = smm(ao, SUB8(yb, 8 * ct), smm(ae, SUB8(yb, 8 * ct), ZERO4, 0xE444), 0x4EEE);
#pragma unroll
                for (int r = 0; r < 4; ++r) xo[(16 * qt + r) * XP + 16 * ct] = o[r] * __builtin_amdgcn_rcpf(sum[r]);
            }
        }
        __builtin_amdgcn_s_waitcnt(0xC07F);
        last_rows(chsm, rsS, wu, grp, c, tau);
        if (wu < 2) {
            v4u sel, p0, p1;
#pragma unroll
            for (int d = 0; d < 4; ++d) sel[d] = (l & 6) == 2 * d ? (0x3F80u << ((l & 1) * 16)) : 0u;
            const int xb = XB_OFF + ((c >> 1) * 8 + grp) * 16384 + (int)(tau & 1u) * 8192 + (c & 1) * 4096 + wu * 256;
            __builtin_amdgcn_s_sleep(DLY_C);
#pragma nounroll
            for (int spin = 0;; ++spin) {
                asm volatile("" ::: "memory");
                p0 = ld_sc1s(rsS, g * 512 + i * 16, xb); p1 = ld_sc1s(rsS, g * 512 + i * 16, xb + 2048);
                if (__all(mx4(mx4(0u, p0), p1) != SENT)) break;
                if (dead) break;
                if (spin > SPIN_LIMIT) { TMO_SET(1800u) break; }
            }
            const f32x4 d = MM(sel, p1, MM(sel, p0, ZERO4));
            if (g < 2) out8(a.out + (size_t)(s * SEG + 4 * grp + 2 * wu + (i >> 3)) * OUTW + 64 * c + 8 * (i & 7) + 4 * g, v2u{cvt_pk_bf16(d[0], d[1]), cvt_pk_bf16(d[2], d[3])});
        }
    }
}

struct cpx { float x, y; };
__device__ __forceinline__ cpx cmul(cpx a, cpx b) { return {a.x * b.x - a.y * b.y, a.x * b.y + a.y * b.x}; }
__device__ __forceinline__ cpx cadd(cpx a, cpx b) { return {a.x + b.x, a.y + b.y}; }
__device__ __forceinline__ cpx csub(cpx a, cpx b) { return {a.x - b.x, a.y - b.y}; }
template <int DIR> __device__ __forceinline__ cpx mul_i(cpx a) {
    return (DIR < 0) ? cpx{a.y, -a.x} : cpx{-a.y, a.x};
}
template <int DIR> __device__ __forceinline__ void r4(cpx& a, cpx& b, cpx& c, cpx& d) {
    cpx t0 = cadd(a, c), t1 = csub(a, c), t2 = cadd(b, d), t3 = mul_i<DIR>(csub(b, d));
    a = cadd(t0, t2); b = cadd(t1, t3); c = csub(t0, t2); d = csub(t1, t3);
}
template <int DIR> __device__ __forceinline__ cpx twc(float c, float s) { return cpx{c, (float)DIR * s}; }
template <int DIR> __device__ __forceinline__ void dft16(cpx (&v)[16]) {
#pragma unroll
    for (int a = 0; a < 4; ++a) r4<DIR>(v[a], v[a + 4], v[a + 8], v[a + 12]);
    const float C1 = 0.92387953251128674f, S1 = 0.38268343236508977f, R2 = 0.70710678118654752f;
    v[1 + 4] = cmul(v[1 + 4], twc<DIR>(C1, S1));
    v[1 + 8] = cmul(v[1 + 8], twc<DIR>(R2, R2));
    v[1 + 12] = cmul(v[1 + 12], twc<DIR>(S1, C1));
    v[2 + 4] = cmul(v[2 + 4], twc<DIR>(R2, R2));
    v[2 + 8] = mul_i<DIR>(v[2 + 8]);
    v[2 + 12] = cmul(v[2 + 12], twc<DIR>(-R2, R2));
    v[3 + 4] = cmul(v[3 + 4], twc<DIR>(S1, C1));
    v[3 + 8] = cmul(v[3 + 8], twc<DIR>(-R2, R2));
    v[3 + 12] = cmul(v[3 + 12], twc<DIR>(-C1, -S1));
#pragma unroll
    for (int d = 0; d < 4; ++d) r4<DIR>(v[4 * d], v[4 * d + 1], v[4 * d + 2], v[4 * d + 3]);
}
__device__ __forceinline__ constexpr int x16(int q) { return 4 * (q & 3) + (q >> 2); }
template <int DIR> __device__ __forceinline__ void dft8(cpx (&v)[8]) {
    r4<DIR>(v[0], v[2], v[4], v[6]);
    r4<DIR>(v[1], v[3], v[5], v[7]);
    const float R2 = 0.70710678118654752f;
    v[3] = cmul(v[3], twc<DIR>(R2, R2));
    v[5] = mul_i<DIR>(v[5]);
    v[7] = cmul(v[7], twc<DIR>(-R2, R2));
#pragma unroll
    for (int d = 0; d < 4; ++d) { cpx p = v[2 * d], q = v[2 * d + 1]; v[2 * d] = cadd(p, q); v[2 * d + 1] = csub(p, q); }
}
__device__ __forceinline__ int pad(int i) { return i + (i >> 4); }
__device__ __forceinline__ cpx expi(float x) { float s, c; sincospif(x, &s, &c); return cpx{c, s}; }
template <int R, int DIR> __device__ __forceinline__ void twiddle(cpx* v, cpx w1) {
    cpx w[R];
    w[1] = w1;
#pragma unroll
    for (int r = 2; r < R; ++r) w[r] = (r & 1) ? cmul(w[r - 1], w1) : cmul(w[r / 2], w[r / 2]);
#pragma unroll
    for (int r = 1; r < R; ++r) v[r] = DIR < 0 ? cmul(v[r], w[r]) : cpx{v[r].x * w[r].x + v[r].y * w[r].y, v[r].y * w[r].x - v[r].x * w[r].y};
}
struct TwBase { cpx b; cpx c[4]; };
template <int N> __device__ __forceinline__ TwBase make_tw(int t) {
    TwBase tb;
    tb.b = expi(-(float)(t & 15) * (1.0f / 128.0f));
    constexpr int NQ = (N == 1024) ? 4 : 2, T = N / 16;
#pragma unroll
    for (int q = 0; q < NQ; ++q) tb.c[q] = expi(-(float)(t + T * q) * (2.0f / (float)N));
    return tb;
}
template <int N> __device__ __forceinline__ void fft_sync() {
    if constexpr (N == 1024) { __builtin_amdgcn_fence(__ATOMIC_ACQ_REL, "wavefront"); __builtin_amdgcn_wave_barrier(); } else __syncthreads();
}
template <int N, int DIR, bool RIN, bool ROUT>
__device__ __forceinline__ void fft_reg(cpx* buf, const TwBase& tb, int t, cpx (&v)[16]) {
    constexpr int T = N / 16, TP = T + T / 16;
    cpx* const bt = buf + pad(t);
    if constexpr (!RIN) {
#pragma unroll
        for (int r = 0; r < 16; ++r) v[r] = bt[TP * r];
        fft_sync<N>();
    }
    dft16<DIR>(v);
#pragma unroll
    for (int q = 0; q < 16; ++q) buf[17 * t + x16(q)] = v[q];
    fft_sync<N>();
    {
#pragma unroll
        for (int r = 0; r < 16; ++r) v[r] = bt[TP * r];
        fft_sync<N>();
        twiddle<16, DIR>(v, tb.b);
        dft16<DIR>(v);
        cpx* const bo = buf + (t >> 4) * 272 + (t & 15);
#pragma unroll
        for (int q = 0; q < 16; ++q) bo[17 * x16(q)] = v[q];
        fft_sync<N>();
    }
    if constexpr (N == 1024) {
#pragma unroll
        for (int q = 0; q < 4; ++q)
#pragma unroll
            for (int r = 0; r < 4; ++r) v[4 * q + r] = bt[68 * q + 272 * r];
#pragma unroll
        for (int q = 0; q < 4; ++q) {
            twiddle<4, DIR>(&v[4 * q], tb.c[q]);
            r4<DIR>(v[4 * q], v[4 * q + 1], v[4 * q + 2], v[4 * q + 3]);
#pragma unroll
            for (int r = 0; r < 4; ++r) bt[68 * q + 272 * r] = v[4 * q + r];
        }
        fft_sync<N>();
    } else {
        cpx u[2][8];
#pragma unroll
        for (int q = 0; q < 2; ++q)
#pragma unroll
            for (int r = 0; r < 8; ++r) u[q][r] = bt[136 * q + 272 * r];
        if constexpr (ROUT) fft_sync<N>();
#pragma unroll
        for (int q = 0; q < 2; ++q) {
            twiddle<8, DIR>(u[q], tb.c[q]);
            dft8<DIR>(u[q]);
#pragma unroll
            for (int d = 0; d < 4; ++d) {
                if constexpr (ROUT) { v[q + 2 * d] = u[q][2 * d]; v[q + 2 * d + 8] = u[q][2 * d + 1]; }
                else { bt[136 * q + 272 * d] = u[q][2 * d]; bt[136 * q + 272 * (d + 4)] = u[q][2 * d + 1]; }
            }
        }
        if constexpr (!ROUT) fft_sync<N>();
    }
}
__device__ __forceinline__ int f_hi(int rb, int j, unsigned tw) { j = (j + rb) & 7; return rb * 65536 + (j >> 1) * 16384 + (j & 1) * 4096 + (int)((tw ^ (unsigned)(rb >> 5)) & 1u) * 8192; }
__device__ __forceinline__ int f_lo(int rb, int x) { return (x + rb * 128) & 4095; }
__device__ __forceinline__ int w_hi(int k, int j, unsigned tw) { return k * 16384 + ((j + k) & 15) * 512 + (int)((tw ^ (unsigned)(k >> 7)) & 1u) * 8192; }
__device__ __forceinline__ int w_lo(int k, int x) { return (x + k * 128) & 511; }
constexpr int PB1 = 1024 + 64, PB2 = 2048 + 128;
typedef float f32x4v __attribute__((ext_vector_type(4)));
typedef unsigned u32x4v __attribute__((ext_vector_type(4)));

__device__ __forceinline__ void fft_rows_fwd(const u16* __restrict__ x, rsrc_t rsF, const unsigned tw, int rb, char* smem, int tid, const char* wr, const char* wi, const unsigned spare) {
    cpx (*buf)[PB1] = (cpx (*)[PB1])smem;
    int f = tid >> 6, t = tid & 63;
    const u16* p0 = x + (size_t)(rb * 8 + 2 * f) * 1024 + 16 * t;
    u32x4v a0 = *(const u32x4v*)p0, a1 = *(const u32x4v*)(p0 + 8), b0 = *(const u32x4v*)(p0 + 1024), b1 = *(const u32x4v*)(p0 + 1032);
    const TwBase tb = make_tw<1024>(t);
    for (int it = 0; it < 4; ++it, rb += 64) {
        asm volatile("" : "+v"(tid));
        f = tid >> 6; t = tid & 63;
        __syncthreads();
        cpx* d = &buf[f][17 * t];
#pragma unroll
        for (int e = 0; e < 4; ++e) {
            d[2 * e] = cpx{__uint_as_float(a0[e] << 16), __uint_as_float(b0[e] << 16)};
            d[2 * e + 1] = cpx{__uint_as_float(a0[e] & 0xFFFF0000u), __uint_as_float(b0[e] & 0xFFFF0000u)};
            d[8 + 2 * e] = cpx{__uint_as_float(a1[e] << 16), __uint_as_float(b1[e] << 16)};
            d[8 + 2 * e + 1] = cpx{__uint_as_float(a1[e] & 0xFFFF0000u), __uint_as_float(b1[e] & 0xFFFF0000u)};
        }
        if (it < 3) p0 += 512 * 1024;
        touch(((it & 1) ? wi : wr) + (size_t)((rb >> 1) & 31) * 32832 + (it >> 1) * 32 * 32832 + ((rb & 1) * 128 + (tid >> 1)) * 128, spare);
        a0 = *(const u32x4v*)p0; a1 = *(const u32x4v*)(p0 + 8); b0 = *(const u32x4v*)(p0 + 1024); b1 = *(const u32x4v*)(p0 + 1032);
        fft_sync<1024>();
        cpx z[16];
        fft_reg<1024, -1, false, false>(buf[f], tb, t, z);
        __syncthreads();
        const cpx* pk = &buf[tid & 3][pad(tid >> 2)];
        const cpx* pn = &buf[tid & 3][pad(1024 - (tid >> 2))];
        const int vo = f_lo(rb, tid * 16);
#pragma unroll
        for (int m = 0; m < 8; ++m) {
            const cpx zk = pk[68 * m], zn = m == 0 && tid < 4 ? pk[544] : pn[-68 * m];
            f32x4v o = {zk.x + zn.x, zk.y - zn.y, zk.y + zn.y, zn.x - zk.x};
            if (m == 0 && tid < 4) o = f32x4v{2.f * zk.x, 2.f * zn.x, 2.f * zk.y, 2.f * zn.y};
            st_sc1s(rsF, vo, f_hi(rb, m, tw), __builtin_bit_cast(v4u, o));
        }
    }
}
__device__ __forceinline__ void filt_transpose(const u16* __restrict__ wr, const u16* __restrict__ wi, rsrc_t rsW, const unsigned tw, const int tb, char* smem, const int tid) {
    const v4u* s0 = (const v4u*)(wr + (size_t)tb * 32 * 513);
    const v4u* s1 = (const v4u*)(wi + (size_t)tb * 32 * 513);
    v4u r0[5], r1[5];
#pragma unroll
    for (int e = 0; e < 5; ++e) {
        const int idx = tid + 512 * e, ic = idx < 2052 ? idx : 2051;
        r0[e] = s0[ic]; r1[e] = s1[ic];
    }
#pragma unroll
    for (int e = 0; e < 5; ++e) {
        const int idx = tid + 512 * e;
        if (idx < 2052) { *(v4u*)(smem + idx * 16) = r0[e]; *(v4u*)(smem + 32832 + idx * 16) = r1[e]; }
    }
    __syncthreads();
    const u16* a0 = (const u16*)smem;
    const u16* a1 = (const u16*)(smem + 32832);
    for (int idx = tid; idx < 513 * 8; idx += 512) {
        const int k = idx >> 3, n4 = (idx & 7) * 4;
        v4u v;
#pragma unroll
        for (int j = 0; j < 4; ++j) v[j] = (unsigned)a0[(n4 + j) * 513 + k] | ((unsigned)a1[(n4 + j) * 513 + k] << 16);
        st_sc1(rsW, w_hi(k, tb >> 2, tw) + w_lo(k, (tb & 3) * 128 + n4 * 4), v);
    }
}

__device__ __forceinline__ void fft_cols(rsrc_t rsF, rsrc_t rsW, const unsigned tw, int blk, const int n, char* smem, int tid) {
    cpx (*buf)[PB2] = (cpx (*)[PB2])smem;
    int c = tid >> 7, t = tid & 127;
    f32x4v v[8];
    unsigned wv[16], wn[16];
    auto wld = [&](const int k, const int n) { return __builtin_amdgcn_raw_buffer_load_b32(rsW, w_hi(k, n >> 7, tw) + w_lo(k, (n & 127) * 4), 0, 0); };
    auto f_at = [&](const int bk) { return f_hi(tid >> 3, bk >> 5, tw) + f_lo(tid >> 3, (bk & 31) * 128 + (tid & 7) * 16); };
    auto fetch = [&](const int bk) {
        const int kc = bk * 2 + __builtin_amdgcn_readfirstlane(c), vo = f_at(bk), vw = w_lo(kc, t * 4);
#pragma unroll
        for (int m = 0; m < 8; ++m) v[m] = __builtin_bit_cast(f32x4v, __builtin_amdgcn_raw_buffer_load_b128(rsF, m & 1 ? vo ^ 8192 : vo, m * 2097152, 0));
#pragma unroll
        for (int m = 0; m < 16; ++m) wn[m] = __builtin_amdgcn_raw_buffer_load_b32(rsW, vw, w_hi(kc, m, tw), 0);
    };
    fetch(blk);
    const TwBase tb = make_tw<2048>(t);
    for (int it = 0; it < n; ++it, blk += 64) {
        asm volatile("" : "+v"(tid));
        c = tid >> 7; t = tid & 127;
        __syncthreads();
        cpx* const d = &buf[(tid >> 2) & 1][pad((tid >> 3) * 8 + 2 * (tid & 3))];
#pragma unroll
        for (int m = 0; m < 8; ++m) { d[272 * m] = cpx{v[m][0], v[m][1]}; d[272 * m + 1] = cpx{v[m][2], v[m][3]}; }
#pragma unroll
        for (int m = 0; m < 16; ++m) wv[m] = wn[m];
        fetch(it + 1 < n ? blk + 64 : blk);
        __syncthreads();
        cpx z[16];
        fft_reg<2048, -1, false, true>(buf[c], tb, t, z);
        auto wcx = [](const unsigned w) { return cpx{__uint_as_float(w << 16), __uint_as_float(w & 0xFFFF0000u)}; };
        if (blk < 2) {
#pragma unroll
            for (int m = 0; m < 16; ++m) buf[c][pad(t + 128 * m)] = z[m];
            __syncthreads();
            if (blk == 0 && c == 0) {
#pragma unroll
                for (int m = 0; m < 16; ++m) {
                    const int n = t + 128 * m, nn = (2048 - n) & 2047;
                    const cpx zn = buf[0][pad(nn)], a = wcx(wv[m]), b = wcx(wld(0, nn)), e = wcx(wld(512, n)), f = wcx(wld(512, nn));
                    const cpx h0 = cmul(cpx{0.5f * (z[m].x + zn.x), 0.5f * (z[m].y - zn.y)}, cpx{0.5f * (a.x + b.x), 0.5f * (a.y - b.y)});
                    const cpx h5 = cmul(cpx{0.5f * (z[m].y + zn.y), -0.5f * (z[m].x - zn.x)}, cpx{0.5f * (e.x + f.x), 0.5f * (e.y - f.y)});
                    z[m] = cpx{h0.x - h5.y, h0.y + h5.x};
                    wv[m] = 0x3F80u;
                }
            }
            __syncthreads();
        }
#pragma unroll
        for (int m = 0; m < 16; ++m) z[m] = cmul(z[m], wcx(wv[m]));
        fft_reg<2048, 1, true, false>(buf[c], tb, t, z);
        const int vo = f_at(blk);
#pragma unroll
        for (int m = 0; m < 8; ++m)
            st_sc1s(rsF, m & 1 ? vo ^ 8192 : vo, m * 2097152, __builtin_bit_cast(v4u, f32x4v{d[272 * m].x, d[272 * m].y, d[272 * m + 1].x, d[272 * m + 1].y}));
    }
}

__device__ __forceinline__ void fft_rows_inv(rsrc_t rsF, const unsigned tw, u16* __restrict__ out, int rb, const int step, const int n, char* smem, int tid) {
    cpx (*buf)[PB1] = (cpx (*)[PB1])smem;
    int f = tid >> 6, t = tid & 63;
    f32x4v v[8];
    auto fetch = [&](const int r) {
        const int vo = f_lo(r, tid * 16);
#pragma unroll
        for (int m = 0; m < 8; ++m) v[m] = __builtin_bit_cast(f32x4v, __builtin_amdgcn_raw_buffer_load_b128(rsF, vo, f_hi(r, m, tw), 0));
    };
    fetch(rb);
    const TwBase tb = make_tw<1024>(t);
    for (int it = 0; it < n; ++it, rb += step) {
        asm volatile("" : "+v"(tid));
        f = tid >> 6; t = tid & 63;
        __syncthreads();
        cpx* pk = &buf[tid & 3][pad(tid >> 2)];
        cpx* pn = &buf[tid & 3][pad(1024 - (tid >> 2))];
#pragma unroll
        for (int m = 0; m < 8; ++m) {
            if (m == 0 && tid < 4) { pk[0] = cpx{v[0][0], v[0][2]}; pk[544] = cpx{v[0][1], v[0][3]}; }
            else { pk[68 * m] = cpx{v[m][0] - v[m][3], v[m][1] + v[m][2]}; pn[-68 * m] = cpx{v[m][0] + v[m][3], v[m][2] - v[m][1]}; }
        }
        fetch(it + 1 < n ? rb + step : rb);
        __syncthreads();
        cpx z[16];
        fft_reg<1024, 1, false, false>(buf[f], tb, t, z);
        const float sc = 0.5f / (1024.0f * 2048.0f);
        const cpx* d = &buf[f][17 * t];
        u32x4v a[2], b[2];
#pragma unroll
        for (int h = 0; h < 2; ++h)
#pragma unroll
            for (int e = 0; e < 4; ++e) {
                cpx p = d[8 * h + 2 * e], q = d[8 * h + 2 * e + 1];
                a[h][e] = cvt_pk_bf16(p.x * sc, q.x * sc);
                b[h][e] = cvt_pk_bf16(p.y * sc, q.y * sc);
            }
        u16* o0 = out + (size_t)(rb * 8 + 2 * f) * OUTW + 1024 + 16 * t;
        *(u32x4v*)o0 = a[0]; *(u32x4v*)(o0 + 8) = a[1];
        *(u32x4v*)(o0 + OUTW) = b[0]; *(u32x4v*)(o0 + OUTW + 8) = b[1];
    }
}

__device__ __forceinline__ void glds16s(unsigned voff, const void* sbase, unsigned lds_dst) {
    unsigned keep;
    asm volatile("s_mov_b32 %0, m0\n\ts_mov_b32 m0, %3\n\ts_nop 0\n\tglobal_load_lds_dwordx4 %1, %2\n\ts_mov_b32 m0, %0" : "=&s"(keep) : "v"(voff), "s"(sbase), "s"(lds_dst) : "memory");
}
#define K8_BAR do { asm volatile("" ::: "memory"); __builtin_amdgcn_s_barrier(); asm volatile("" ::: "memory"); } while (0)
#define K8_WV(n) asm volatile("s_waitcnt vmcnt(" #n ")" ::: "memory")
#define K8_WL(n) asm volatile("s_waitcnt lgkmcnt(" #n ")" ::: "memory")
#define K8_SCHED __builtin_amdgcn_sched_barrier(0)
template <int BN>
__device__ __forceinline__ int bswz2(int row) {
    if (BN >= 128) return (row & 3) | (((row >> 3) & 1) << 2);
    else return ((row >> 1) & 1) | (((row >> 3) & 1) << 1);
}
template <int BM>
__device__ __forceinline__ void gemm_tile(u16* sm, const char* A, const int s0, const unsigned tau, const u16* B, u16* C) {
    constexpr int K = HD, LDB = DM, LDC = OUTW;
    constexpr int BN = 128, BK = 64, ST = BM == 128 ? 4 : 3, WN = BM == 128 ? 4 : 2;
    constexpr int ATILE = BM * BK, BTILE = BK * BN;
    constexpr int NT = 8 / WN, GA = BM / 64, GB = 2, GS = GA + GB;
    const int tid = opaque_tid(), w = tid >> 6, l = tid & 63, i = l & 15, g = l >> 4;
    const int wr = w / WN, wc = w % WN;
    const unsigned wu = __builtin_amdgcn_readfirstlane(w);
    unsigned vb;
    { const int row = l >> 4, c = l & 15; vb = (unsigned)(row * LDB + 8 * (c ^ ((row | ((wu & 1) << 2)) << 1))) * 2u; }
    const int sg = s0 + (BM == 128 ? wu >> 1 : wu);
    const char* ab = A + (BM == 128 ? (wu & 1) * 4096 : 0);
    const char* bb = (const char*)(B + (size_t)wu * 8 * LDB);
    const unsigned lds0 = (unsigned)(uintptr_t)(__attribute__((address_space(3))) u16*)sm;
    const unsigned va = (unsigned)l * 16u, va2 = va + 2048u, vb2 = vb + 4u * LDB * 2u - 1024u;
    auto req = [&](const int buf, const int kt) {
        const unsigned d = lds0 + buf * (ATILE + BTILE) * 2;
        const int wh = kt >> 1;
        const char* pa = ab + ((blk_off(sg, (wh & 7) * 4 + (wh >> 3), tau) ^ 8192) + (kt & 1) * 2048);
        const char* pb = bb + (size_t)kt * BK * LDB * 2;
        unsigned keep;
        if (BM == 128) asm volatile("s_mov_b32 %0, m0\n\ts_mov_b32 m0, %1\n\ts_nop 0\n\tglobal_load_lds_dwordx4 %3, %5\n\tglobal_load_lds_dwordx4 %3, %5 offset:1024\n\t"
                     "s_mov_b32 m0, %2\n\ts_nop 0\n\tglobal_load_lds_dwordx4 %6, %8\n\tglobal_load_lds_dwordx4 %7, %8 offset:1024\n\ts_mov_b32 m0, %0"
                     : "=&s"(keep) : "s"(d + wu * GA * 1024), "s"(d + ATILE * 2 + wu * GB * 1024), "v"(va), "v"(va2), "s"(pa), "v"(vb), "v"(vb2), "s"(pb) : "memory");
        else asm volatile("s_mov_b32 %0, m0\n\ts_mov_b32 m0, %1\n\ts_nop 0\n\tglobal_load_lds_dwordx4 %3, %5\n\tglobal_load_lds_dwordx4 %3, %5 offset:1024\n\t"
                     "global_load_lds_dwordx4 %4, %5 offset:2048\n\tglobal_load_lds_dwordx4 %4, %5 offset:3072\n\t"
                     "s_mov_b32 m0, %2\n\ts_nop 0\n\tglobal_load_lds_dwordx4 %6, %8\n\tglobal_load_lds_dwordx4 %7, %8 offset:1024\n\ts_mov_b32 m0, %0"
                     : "=&s"(keep) : "s"(d + wu * GA * 1024), "s"(d + ATILE * 2 + wu * GB * 1024), "v"(va), "v"(va2), "s"(pa), "v"(vb), "v"(vb2), "s"(pb) : "memory");
    };
    f32x4 acc[4][NT] = {};
    constexpr int nt = K / BK;
#pragma unroll
    for (int s = 0; s < ST - 1; ++s) req(s, s);
    int boff[NT];
    const int aoff = wr * 4096 + l * 8;
#pragma unroll
    for (int n = 0; n < NT; ++n) { const int row = 8 * g + (i >> 2); boff[n] = row * BN + 16 * ((wc * NT + n) ^ bswz2<BN>(row)) + 4 * (i & 3); }
    asm volatile("s_waitcnt vmcnt(%0)" :: "i"((ST - 2) * GS) : "memory");
    K8_BAR;
    if (wu >= 4) K8_BAR;
    for (int t0 = 0; t0 < nt + ST - 1 - (nt + ST - 1) % ST; t0 += ST) {
#pragma unroll
        for (int rb = 0; rb < ST; ++rb) {
            const int t = t0 + rb;
            if (nt % ST != 0 && t >= nt) break;
            const int kk = min(t + ST - 1, nt - 1);
            const u16* as = sm + rb * (ATILE + BTILE);
            const u16* bs = as + ATILE;
            bf16x8 af[2][4], bf[2][NT];
#pragma unroll
            for (int ks = 0; ks < 2; ++ks) {
#pragma unroll
                for (int n = 0; n < NT; ++n) bf[ks][n] = tr_frag(bs + boff[n] + ks * 32 * BN, bs + boff[n] + ks * 32 * BN + 4 * BN);
#pragma unroll
                for (int m = 0; m < 4; ++m) af[ks][m] = *(const bf16x8*)(as + aoff + m * 1024 + ks * 512);
            }
            K8_SCHED;
            req((rb + ST - 1) % ST, kk);
            K8_WL(0);
            if (wu >= 4) asm volatile("s_waitcnt vmcnt(%0)" :: "i"((ST - 2) * GS) : "memory");
            K8_BAR;
            __builtin_amdgcn_s_setprio(1);
#pragma unroll
            for (int q = 0; q < 8; ++q)
#pragma unroll
                for (int n = 0; n < NT; ++n) acc[q & 3][n] = __builtin_amdgcn_mfma_f32_16x16x32_bf16(bf[q >> 2][n], af[q >> 2][q & 3], acc[q & 3][n], 0, 0, 0);
            __builtin_amdgcn_s_setprio(0);
            K8_SCHED;
            asm volatile("s_waitcnt vmcnt(%0)" :: "i"((ST - 2) * GS) : "memory");
            K8_BAR;
        }
    }
    if (wu < 4) K8_BAR;
    asm volatile("s_waitcnt vmcnt(0)" ::: "memory");
#pragma unroll
    for (int m = 0; m < 4; ++m)
#pragma unroll
        for (int n = 0; n < NT; n += 2) {
            auto r0 = __builtin_amdgcn_permlane16_swap(cvt_pk_bf16(acc[m][n][0], acc[m][n][1]), cvt_pk_bf16(acc[m][n + 1][0], acc[m][n + 1][1]), false, false);
            auto r1 = __builtin_amdgcn_permlane16_swap(cvt_pk_bf16(acc[m][n][2], acc[m][n][3]), cvt_pk_bf16(acc[m][n + 1][2], acc[m][n + 1][3]), false, false);
            *(v4u*)(C + (wr * 64 + m * 16 + i) * LDC + (wc * NT + n + (g & 1)) * 16 + 8 * (g >> 1)) = v4u{r0[0], r1[0], r0[1], r1[1]};
        }
}

__device__ __forceinline__ void xattn_unit(const ChainArgs& a, rsrc_t rsQ, rsrc_t rsK, rsrc_t rsV, rsrc_t rsS, const int s, const int sn, const int h, u16 (*Vs)[VP], unsigned* mbox, const unsigned tau, int& pend, bool& ready) {
    const int l = opaque_tid() & 63, i = l & 15, g = l >> 4;
    const unsigned known = __builtin_amdgcn_readfirstlane(*(volatile unsigned*)mbox);
    const bool early = known > (unsigned)(s + 1);
    const int bq = km_off(s, h, tau), bb = v_off(s, h, tau);
    auto count = [&](const int t) {
        if (t < SSEG) { arrive(a.sync, SY_OX + (t >> 2) * 8, 128u, 8); return; }
        unsigned n = 0;
        if (l == 0) n = atomicAdd(mbox + 8 + t - SSEG, 1u);
        if (__builtin_amdgcn_readfirstlane(n) == 3u && l < 8) __hip_atomic_store((gu32*)(a.sync + (SY_OXS + (t - SSEG) * 8 + l) * 32 + (h >> 2)), 16u, __ATOMIC_RELAXED, __HIP_MEMORY_SCOPE_AGENT);
    };
    const int xk = blk_off(s, h, tau), xq = xk ^ 8192;
    v4u vv[8], vm[2], k0f[4], k1f[4], kmf[4], q0f[4], q1f[4];
#pragma unroll
    for (int e = 0; e < 8; ++e) vv[e] = ld_sc1s(rsV, l * 16 + e * 1024, xk);
#pragma unroll
    for (int ks = 0; ks < 4; ++ks) {
        k0f[ks] = ld_sc1s(rsK, l * 16 + ks * 1024, xk);
        k1f[ks] = ld_sc1s(rsK, l * 16 + ks * 1024, xk + 4096);
        q0f[ks] = ld_sc1s(rsQ, l * 16 + ks * 1024, xq);
        q1f[ks] = ld_sc1s(rsQ, l * 16 + ks * 1024, xq + 4096);
    }
#pragma unroll
    for (int e = 0; e < 2; ++e) vm[e] = ld_sc1s(rsS, l * 16 + e * 1024, early ? bb : 1 << 30);
#pragma unroll
    for (int ks = 0; ks < 4; ++ks) kmf[ks] = ld_sc1s(rsS, ks * 512 + g * 128 + (i & 7) * 16, early ? bq : 1 << 30);
    const unsigned fl = ld_flag(a.sync + (l >= 8 ? SY_PROG * 32 : (((l & 4) ? SY_Q : SY_KV) + (min(sn, NSEG - 1) >> 3) * 8) * 32 + (l & 3)) + (h >> 2) * 32);
    if (pend >= 0) { asm volatile("s_waitcnt vmcnt(31)" ::: "memory"); count(pend); }
#pragma unroll
    for (int e = 0; e < 8; ++e) *(v4u*)&Vs[16 * (e >> 2) + i][32 * (e & 3) + 8 * g] = vv[e];
    const float scale = 0.08838834764831845f;
    const float L2E = 1.4426950408889634f;
    auto round2 = [&](const f32x4 c, float* o) {
#pragma unroll
        for (int p = 0; p < 2; ++p) {
            unsigned u = cvt_pk_bf16(c[2 * p], c[2 * p + 1]);
            float lo = __uint_as_float(u << 16) * scale, hi = __uint_as_float(u & 0xFFFF0000u) * scale;
            unsigned u2 = cvt_pk_bf16(lo, hi);
            o[2 * p] = __uint_as_float(u2 << 16);
            o[2 * p + 1] = __uint_as_float(u2 & 0xFFFF0000u);
        }
    };
    float sv[2][12], mxx[2];
    {
        f32x4 sx[3] = {};
#pragma unroll
        for (int ks = 0; ks < 4; ++ks) {
            sx[0] = __builtin_amdgcn_mfma_f32_16x16x32_bf16(as_frag(k0f[ks]), as_frag(q0f[ks]), sx[0], 0, 0, 0);
            sx[1] = __builtin_amdgcn_mfma_f32_16x16x32_bf16(as_frag(k0f[ks]), as_frag(q1f[ks]), sx[1], 0, 0, 0);
            sx[2] = __builtin_amdgcn_mfma_f32_16x16x32_bf16(as_frag(k1f[ks]), as_frag(q1f[ks]), sx[2], 0, 0, 0);
        }
        round2(sx[0], &sv[0][0]); round2(sx[1], &sv[1][0]); round2(sx[2], &sv[1][4]);
        mxx[0] = mxx[1] = -3.0e38f;
#pragma unroll
        for (int r = 0; r < 4; ++r) {
            if (4 * g + r > i) { sv[0][r] = -3.0e38f; sv[1][4 + r] = -3.0e38f; }
            mxx[0] = fmaxf(mxx[0], sv[0][r]);
            mxx[1] = fmaxf(mxx[1], fmaxf(sv[1][r], sv[1][4 + r]));
        }
    }
    const u16* vb = &Vs[4 * g + (i >> 2)][4 * (i & 3)];
    bf16x8 vfx[8];
#pragma unroll
    for (int t = 0; t < 8; ++t) vfx[t] = tr_frag(vb + 16 * t, vb + 16 * VP + 16 * t);
#pragma unroll
    for (int t = 0; t < 8; ++t) asm volatile("" : "+v"(vfx[t]));
#pragma unroll
    for (int r = 0; r < 4; ++r) asm volatile("" : "+v"(sv[0][r]), "+v"(sv[1][r]), "+v"(sv[1][4 + r]));
    asm volatile("" : "+v"(mxx[0]), "+v"(mxx[1]));
    bool have = false;
    unsigned seen = __builtin_amdgcn_readlane(fl, 8);
    ready = (sn >> 3) == (s >> 3) || (__ballot(fl != 0u) & 255ull) == 255ull;
    if (early) {
        if ((h & 3) == 0 && l == 0) atomicMax(mbox, seen);
        unsigned m = mx4(mx4(0u, vm[0]), vm[1]);
#pragma unroll
        for (int ks = 0; ks < 4; ++ks) m = mx4(m, kmf[ks]);
        have = __all(m != SENT);
    } else if ((h & 3) == 0) {
        const unsigned* pw = a.sync + (SY_PROG + (h >> 2)) * 32;
        unsigned v = seen;
        if (v <= (unsigned)(s + 1)) {
            if (s < NSEG - XA_FAST) { wait_ge<32>(pw, (unsigned)(s + 1), a.tmo, 4000u); __builtin_amdgcn_s_sleep(24); }
            else { wait_ge<6>(pw, (unsigned)(s + 1), a.tmo, 4000u); __builtin_amdgcn_s_sleep(XA_D); }
            v = (unsigned)(s + 1);
        }
        if (l == 0) atomicMax(mbox, v);
    } else {
#pragma nounroll
        for (int spin = 0; spin < 4000000 && *(volatile unsigned*)mbox < (unsigned)(s + 1); ++spin) __builtin_amdgcn_s_sleep(2);
        asm volatile("" ::: "memory");
    }
#pragma nounroll
    for (int spin = 0; !have; ++spin) {
        asm volatile("" ::: "memory");
#pragma unroll
        for (int e = 0; e < 2; ++e) vm[e] = ld_sc1s(rsS, l * 16 + e * 1024, bb);
#pragma unroll
        for (int ks = 0; ks < 4; ++ks) kmf[ks] = ld_sc1s(rsS, ks * 512 + g * 128 + (i & 7) * 16, bq);
        unsigned m = mx4(mx4(0u, vm[0]), vm[1]);
#pragma unroll
        for (int ks = 0; ks < 4; ++ks) m = mx4(m, kmf[ks]);
        if (__all(m != SENT) || spin > 20000) break;
        __builtin_amdgcn_s_sleep(2);
    }
#pragma unroll
    for (int e = 0; e < 8; ++e) *(unsigned*)&Vs[32 + (i >> 2) + 4 * ((i >> 1) & 1)][64 * (e >> 2) + 16 * g + 4 * (e & 3) + 2 * (i & 1)] = vm[e >> 2][e & 3];
    f32x4 sm_[2] = {};
#pragma unroll
    for (int ks = 0; ks < 4; ++ks) {
        sm_[0] = __builtin_amdgcn_mfma_f32_16x16x32_bf16(as_frag(kmf[ks]), as_frag(q0f[ks]), sm_[0], 0, 0, 0);
        sm_[1] = __builtin_amdgcn_mfma_f32_16x16x32_bf16(as_frag(kmf[ks]), as_frag(q1f[ks]), sm_[1], 0, 0, 0);
    }
    v4u pf[2][2];
#pragma unroll
    for (int qt = 0; qt < 2; ++qt) {
        round2(sm_[qt], &sv[qt][8]);
        float mx = fmaxf(fmaxf(mxx[qt], fmaxf(sv[qt][8], sv[qt][9])), fmaxf(sv[qt][10], sv[qt][11]));
        mx = xor16_max(mx);
        mx = xor32_max(mx);
        const float nmx = -mx * L2E;
        float sum = 0.f;
#pragma unroll
        for (int e = 0; e < 12; ++e) {
            if (qt == 0 && e >= 4 && e < 8) { sv[qt][e] = 0.f; continue; }
            float ev = __builtin_amdgcn_exp2f(fmaf(sv[qt][e], L2E, nmx));
            if (e >= 8) ev = g < 2 ? ev : 0.f;
            sv[qt][e] = ev; sum += ev;
        }
        sum = xor16_sum(sum);
        sum = xor32_sum(sum);
        const float inv = 1.0f / sum;
        pf[qt][0][0] = cvt_pk_bf16(sv[qt][0] * inv, sv[qt][1] * inv);
        pf[qt][0][1] = cvt_pk_bf16(sv[qt][2] * inv, sv[qt][3] * inv);
        pf[qt][0][2] = qt ? cvt_pk_bf16(sv[qt][4] * inv, sv[qt][5] * inv) : 0u;
        pf[qt][0][3] = qt ? cvt_pk_bf16(sv[qt][6] * inv, sv[qt][7] * inv) : 0u;
        pf[qt][1][0] = cvt_pk_bf16(sv[qt][8] * inv, sv[qt][9] * inv);
        pf[qt][1][1] = cvt_pk_bf16(sv[qt][10] * inv, sv[qt][11] * inv);
        pf[qt][1][2] = 0u; pf[qt][1][3] = 0u;
    }
    v2u vmf[8];
#pragma unroll
    for (int t = 0; t < 8; ++t) vmf[t] = tr4(&Vs[32 + 4 * (g & 1) + (i >> 2)][16 * t + 4 * (i & 3)]);
#pragma unroll
    for (int t = 0; t < 8; t += 2) {
        unsigned pk[2][2][2];
#pragma unroll
        for (int tt = 0; tt < 2; ++tt) {
            const bf16x8 vf0 = vfx[t + tt];
            const bf16x8 vf1 = as_frag(v4u{vmf[t + tt][0], vmf[t + tt][1], 0u, 0u});
#pragma unroll
            for (int qt = 0; qt < 2; ++qt) {
                f32x4 o = {};
                o = __builtin_amdgcn_mfma_f32_16x16x32_bf16(vf0, as_frag(pf[qt][0]), o, 0, 0, 0);
                o = __builtin_amdgcn_mfma_f32_16x16x32_bf16(vf1, as_frag(pf[qt][1]), o, 0, 0, 0);
                pk[qt][tt][0] = cvt_pk_bf16(o[0], o[1]);
                pk[qt][tt][1] = cvt_pk_bf16(o[2], o[3]);
            }
        }
#pragma unroll
        for (int qt = 0; qt < 2; ++qt) {
            auto r0 = __builtin_amdgcn_permlane16_swap(pk[qt][0][0], pk[qt][1][0], false, false);
            auto r1 = __builtin_amdgcn_permlane16_swap(pk[qt][0][1], pk[qt][1][1], false, false);
            v4u o = {r0[0], r1[0], r0[1], r1[1]};
            st_sc1s(rsV, (t >> 1) * 1024 + ((g & 1) * 2 + (g >> 1)) * 256 + i * 16, xq + qt * 4096, o);
        }
    }
    pend = seen > (unsigned)(sn + 1) && ready ? s : -1;
    if (pend < 0) { asm volatile("s_waitcnt vmcnt(0)" ::: "memory"); count(s); }
}

__device__ __forceinline__ void tile_stream(const ChainArgs& a, const u16* X, unsigned long long todo, const int xc, unsigned& tau, const bool early, const int nw, u16* sm8 WEVP) {
    constexpr int K = DM, LDB = HD;
    const int tid = opaque_tid(), w = tid >> 6, l = tid & 63, i = l & 15, g = l >> 4;
    const int wr = w >> 2, wc = w & 3;
    const unsigned lds0 = (unsigned)(uintptr_t)(__attribute__((address_space(3))) u16*)sm8;
    const unsigned wu = __builtin_amdgcn_readfirstlane(w);
    const unsigned ldsw = lds0 + wu * 2048;
    unsigned voffA, voffB;
    { int row = 16 * w + (l >> 3), c = l & 7; voffA = (unsigned)(row * K + 8 * (c ^ (row & 7))) * 2u; }
    { int row = 8 * w + g, c = i; voffB = (unsigned)(row * LDB + 8 * (c ^ (bswz2<128>(row) << 1))) * 2u; }
    const unsigned voffA2 = voffA + 8u * K * 2u - 1024u, voffB2 = voffB + 4u * LDB * 2u - 1024u;
    auto st2 = [&](const unsigned v0, const unsigned v1, const char* p, const unsigned d) {
        unsigned keep;
        asm volatile("s_mov_b32 %0, m0\n\ts_mov_b32 m0, %1\n\ts_nop 0\n\tglobal_load_lds_dwordx4 %2, %4\n\tglobal_load_lds_dwordx4 %3, %4 offset:1024\n\ts_mov_b32 m0, %0"
                     : "=&s"(keep) : "s"(d), "v"(v0), "v"(v1), "s"(p) : "memory");
    };
    auto stA = [&](int b, int h, const char* p) {
        st2(voffA, voffA2, p + (size_t)(h * 128) * K * 2, ldsw + (2 * b + h) * 16384);
    };
    auto stB = [&](int b, int h, const char* p) {
        st2(voffB, voffB2, p + h * 2048, ldsw + 65536 + (2 * b + h) * 16384);
    };
    constexpr int SA = 128, SB = 64 * LDB * 2;
    auto a_of = [&](const unsigned t) { return (const char*)(X + (size_t)((t >> 4) & 7) * 256 * DM); };
    auto b_of = [&](const unsigned t) { const int kind = t & 7; return (const char*)((kind < 2 ? a.Wk : (kind < 4 ? a.Wv : a.Wq)) + hcol(4 * xc + (kind & 1) * 2)); };
    auto c_of = [&](const unsigned t) { const int kind = t & 7; return ((kind & 6) == 2 ? VX_OFF - KX_OFF : 0) + (int)((t >> 4) & 7) * 4194304 + blk_off(0, 4 * xc + (kind & 1) * 2, kind < 4 ? tau : tau ^ 1u); };
    auto publish = [&](const unsigned t) {
        const int kind = t & 7, ln = lane_now();
        if (wu == 0 && ln < (kind < 4 ? 1 : 2))
            __hip_atomic_store((gu32*)(a.sync + ((kind < 4 ? SY_KV : SY_Q) + ((t >> 4) & 7) * 8 + xc) * 32 + (kind < 4 ? kind : 2 * kind - 8 + ln)), 16u, __ATOMIC_RELAXED, __HIP_MEMORY_SCOPE_AGENT);
    };
    const char* smc = (const char*)sm8;
    int aoff[2], boff[2];
#pragma unroll
    for (int ks = 0; ks < 2; ++ks) aoff[ks] = (wr * 64 + i) * 128 + (((ks * 4 + g) ^ (i & 7)) << 4);
#pragma unroll
    for (int n = 0; n < 2; ++n) { int row = 8 * g + (i >> 2); boff[n] = 65536 + row * 256 + (((wc * 2 + n) ^ bswz2<128>(row)) << 5) + 8 * (i & 3); }
    f32x4 acc[2][2][4][2] = {};
    bf16x8 At[4][2], B0[2][2], B1[2][2];
#define K8_LDA(b, h) _Pragma("unroll") for (int m = 0; m < 4; ++m) _Pragma("unroll") for (int ks = 0; ks < 2; ++ks) \
        At[m][ks] = *(const bf16x8*)(smc + aoff[ks] + (2 * (b) + (h)) * 16384 + m * 2048)
#define K8_LDB(dst, b, h) _Pragma("unroll") for (int n = 0; n < 2; ++n) _Pragma("unroll") for (int ks = 0; ks < 2; ++ks) { \
        const char* p_ = smc + boff[n] + (2 * (b) + (h)) * 16384 + ks * 8192; \
        v4i16 lo_ = __builtin_amdgcn_ds_read_tr16_b64_v4i16((__attribute__((address_space(3))) v4i16*)p_); \
        v4i16 hi_ = __builtin_amdgcn_ds_read_tr16_b64_v4i16((__attribute__((address_space(3))) v4i16*)(p_ + 1024)); \
        dst[n][ks] = bf16x8{lo_[0], lo_[1], lo_[2], lo_[3], hi_[0], hi_[1], hi_[2], hi_[3]}; }
#define K8_MMA(ai, bj, Bt) do { __builtin_amdgcn_s_setprio(1); \
        _Pragma("unroll") for (int m = 0; m < 4; ++m) _Pragma("unroll") for (int n = 0; n < 2; ++n) _Pragma("unroll") for (int ks = 0; ks < 2; ++ks) \
            acc[ai][bj][m][n] = __builtin_amdgcn_mfma_f32_16x16x32_bf16(Bt[n][ks], At[m][ks], acc[ai][bj][m][n], 0, 0, 0); \
        __builtin_amdgcn_s_setprio(0); } while (0)
    const rsrc_t rsC = __builtin_amdgcn_make_buffer_rsrc((void*)(arena(a.slots) + KX_OFF), 0, FX_OFF - KX_OFF, 0x00020000);
#define K8_EPI(ai, bj, cb) do { const int ln_ = lane_now(); \
        const int vo_ = (int)(wu & 3) * 1024 + (((ln_ >> 4) & 1) * 2 + (ln_ >> 5)) * 256 + (ln_ & 15) * 16; \
        _Pragma("unroll") for (int m = 0; m < 4; ++m) { \
            const f32x4 c0 = acc[ai][bj][m][0], c1 = acc[ai][bj][m][1]; \
            auto r0 = __builtin_amdgcn_permlane16_swap(cvt_pk_bf16(c0[0], c0[1]), cvt_pk_bf16(c1[0], c1[1]), false, false); \
            auto r1 = __builtin_amdgcn_permlane16_swap(cvt_pk_bf16(c0[2], c0[3]), cvt_pk_bf16(c1[2], c1[3]), false, false); \
            const int sg_ = 4 * ai + 2 * (int)(wu >> 2) + (m >> 1); \
            st_sc1s(rsC, vo_, ((cb) ^ (ai * 8192)) + sg_ * 524288 + bj * 16384 + (m & 1) * 4096, v4u{r0[0], r1[0], r0[1], r1[1]}); \
            acc[ai][bj][m][0] = ZERO4; acc[ai][bj][m][1] = ZERO4; } } while (0)
    constexpr int nt = K / 64;
    unsigned cur = (unsigned)todo & 255u, prev = 0u;
    const char* pA = a_of(cur); const char* pB = b_of(cur);
    if (early) touch((const char*)X + 524288 + (size_t)(nw * 128 + (tid >> 2)) * 128, lds0 + 163840 - 256);
    stB(0, 0, pB); stB(0, 1, pB); stA(0, 0, pA); stA(0, 1, pA);
    stB(1, 0, pB + SB); stB(1, 1, pB + SB); stA(1, 0, pA + SA);
    K8_WV(8); K8_BAR;
    if (wu >= 4) K8_BAR;
#define K8_GO(ai) __builtin_amdgcn_s_waitcnt(0xC07F); K8_BAR; K8_MMA(ai, 0, B0); K8_MMA(ai, 1, B1); K8_SCHED; K8_BAR
    int cbp = 0;
    for (;;) {
        todo >>= 8;
        const unsigned nxt = todo != 0ull ? (unsigned)todo & 255u : cur;
        int cb = c_of(cur);
#pragma nounroll
        for (int t = 0; t < nt; t += 2) {
            if (__builtin_expect(early && prev == 0u && t == 10, 0))
                touch((const char*)X + 3 * 524288 + (size_t)(nw * 320 + min(TIDX, 319)) * 128, lds0 + 163840 - 256);
            if (__builtin_expect(early && prev == 0u && t == nt - 4, 0)) {
                tau = __builtin_amdgcn_readfirstlane((unsigned)boot_wait(a.boot) & 255u);
                cb = c_of(cur);
            }
            const bool first = t == 0 && prev != 0u, last = t == nt - 2;
            if (t == 2 && prev != 0u) { publish(prev); WEV(3) }
            K8_LDB(B0, 0, 0); K8_LDA(0, 0); K8_LDB(B1, 0, 1); K8_SCHED; stA(1, 1, pA + SA);
            pA = last ? a_of(nxt) : pA + 2 * SA; pB = last ? b_of(nxt) : pB + 2 * SB;
            if (first) { K8_EPI(1, 0, cbp); K8_EPI(1, 1, cbp); K8_WV(24); } else K8_WV(8);
            K8_GO(0);
            K8_LDA(0, 1); K8_SCHED; stB(0, 0, pB); stB(0, 1, pB); stA(0, 0, pA);
            if (first) K8_WV(24); else K8_WV(8);
            K8_GO(1);
            K8_LDB(B0, 1, 0); K8_LDA(1, 0); K8_LDB(B1, 1, 1); K8_SCHED; stA(0, 1, pA);
            if (first) K8_WV(16); else K8_WV(8);
            K8_GO(0);
            K8_LDA(1, 1); K8_SCHED; stB(1, 0, pB + SB); stB(1, 1, pB + SB); stA(1, 0, pA + SA);
            if (last) { K8_EPI(0, 0, cb); K8_EPI(0, 1, cb); K8_WV(16); } else K8_WV(8);
            K8_GO(1);
        }
        WEV(2)
        prev = cur; cbp = cb; cur = nxt;
        if (todo == 0ull) break;
    }
    K8_EPI(1, 0, cbp); K8_EPI(1, 1, cbp);
    K8_WV(0);
    if (wu < 4) K8_BAR;
    K8_BAR;
    publish(prev);
    WEV(3)
}

__device__ __forceinline__ void oproj_squad(const ChainArgs& a, u16* out, const int ct, const int rh, const int xc, const unsigned tau, const int s0, const int s1, char* smem) {
    const int tid = opaque_tid(), w = tid >> 6, l = tid & 63, i = l & 15, g = l >> 4;
    const int wu = __builtin_amdgcn_readfirstlane(w);
    bf16x8 wf[2][16];
    {
        u16* stg = (u16*)(smem + wu * 10240);
#pragma unroll
        for (int ch = 0; ch < 4; ++ch) {
            const u16* src = a.Wo + (size_t)hcol(4 * wu + ch) * DM + 32 * ct;
            v4u raw[8];
#pragma unroll
            for (int e = 0; e < 8; ++e) { int idx = l + 64 * e, r = idx >> 2, q4 = idx & 3; raw[e] = *(const v4u*)(src + (size_t)r * DM + 8 * q4); }
#pragma unroll
            for (int e = 0; e < 8; ++e) { int idx = l + 64 * e, r = idx >> 2, q4 = idx & 3; *(v4u*)(stg + r * 40 + 8 * q4) = raw[e]; }
            __builtin_amdgcn_s_waitcnt(0xC07F);
#pragma unroll
            for (int jj = 0; jj < 2; ++jj)
#pragma unroll
                for (int ks = 0; ks < 4; ++ks) {
                    const u16* p = stg + (32 * ks + 8 * g + (i >> 2)) * 40 + 16 * jj + 4 * (i & 3);
                    wf[jj][4 * ch + ks] = tr_frag(p, p + 4 * 40);
                }
            __builtin_amdgcn_s_waitcnt(0xC07F);
        }
    }
    const rsrc_t rsO = __builtin_amdgcn_make_buffer_rsrc((void*)(arena(a.slots) + VX_OFF), 0, 32 << 20, 0x00020000);
    float* red = (float*)(smem + 81920);
    const int voff = rh * 4096 + l * 16;
    volatile unsigned* rdy = (volatile unsigned*)(smem + 81920 + 2 * 8 * 16 * 36 * 4);
    unsigned ready = 0;
    bool pre = false;
    v4u xf[16];
    for (int s = s0; s < s1; ++s) {
        if (!(ready & 1u)) {
            const bool own = s >= NSEG - SQ_OWN;
            if (wu == 0 || own) {
                const unsigned* q = a.sync + (SY_OXS + (s - SSEG) * 8 + xc) * 32 + (own ? wu : l & 7);
#pragma nounroll
                for (int spin = 0;; ++spin) {
                    asm volatile("" ::: "memory");
                    if (__all(ld_flag(q) != 0u)) break;
                    if (spin > 20000) { __hip_atomic_store((gu32*)a.tmo, 7000u, __ATOMIC_RELAXED, __HIP_MEMORY_SCOPE_AGENT); break; }
                    if (s == s0) __builtin_amdgcn_s_sleep(64); else if (s < NSEG - 5) __builtin_amdgcn_s_sleep(16); else __builtin_amdgcn_s_sleep(SQ_SF);
                }
            }
            asm volatile("" ::: "memory");
            if (!own) __builtin_amdgcn_s_barrier();
            asm volatile("" ::: "memory");
        }
        if (!pre) {
#pragma unroll
            for (int ks = 0; ks < 16; ++ks) xf[ks] = ld_sc1s(rsO, voff + (ks & 3) * 1024, blk_off(s, 4 * wu + (ks >> 2), tau) ^ 8192);
        }
        unsigned nf = 0;
        if (wu == 0) nf = ld_flag(a.sync + (SY_OXS + (s + 1 - SSEG + ((l >> 3) & 1)) * 8 + xc) * 32 + (l & 7));
        pre = (ready & 2u) != 0u && s + 1 < s1;
        f32x4 acc[2] = {};
        if (pre) {
#pragma unroll
            for (int ks = 0; ks < 16; ++ks) {
#pragma unroll
                for (int jj = 0; jj < 2; ++jj) acc[jj] = __builtin_amdgcn_mfma_f32_16x16x32_bf16(wf[jj][ks], as_frag(xf[ks]), acc[jj], 0, 0, 0);
                K8_SCHED;
                xf[ks] = ld_sc1s(rsO, voff + (ks & 3) * 1024, blk_off(s + 1, 4 * wu + (ks >> 2), tau) ^ 8192);
                K8_SCHED;
            }
        } else {
#pragma unroll
            for (int ks = 0; ks < 16; ++ks)
#pragma unroll
                for (int jj = 0; jj < 2; ++jj) acc[jj] = __builtin_amdgcn_mfma_f32_16x16x32_bf16(wf[jj][ks], as_frag(xf[ks]), acc[jj], 0, 0, 0);
        }
        float* rp = red + (s & 1) * (8 * 16 * 36);
#pragma unroll
        for (int jj = 0; jj < 2; ++jj) *(f32x4*)&rp[(wu * 16 + i) * 36 + 16 * jj + 4 * g] = acc[jj];
        if (wu == 0) { const unsigned bl = (unsigned)__ballot(nf != 0u); rdy[s & 1] = ((bl & 255u) == 255u ? 1u : 0u) | ((bl & 0xFF00u) == 0xFF00u ? 2u : 0u); }
        K8_WL(0); K8_BAR;
        ready = __builtin_amdgcn_readfirstlane(rdy[s & 1]) | (pre ? 1u : 0u);
        if (tid >= 64 && tid < 192) {
            const int t = tid - 64, row = t >> 3, q4 = t & 7;
            f32x4 sv_[8];
#pragma unroll
            for (int e = 0; e < 8; ++e) sv_[e] = *(const f32x4*)&rp[(e * 16 + row) * 36 + 4 * q4];
            const f32x4 s0 = ((sv_[0] + sv_[1]) + (sv_[2] + sv_[3])) + ((sv_[4] + sv_[5]) + (sv_[6] + sv_[7]));
            v2u o; o[0] = cvt_pk_bf16(s0[0], s0[1]); o[1] = cvt_pk_bf16(s0[2], s0[3]);
            out8(out + (size_t)(s * SEG + 16 * rh + row) * OUTW + 32 * ct + 4 * q4, o);
        }
    }
}

__device__ __forceinline__ void kv_stream(const ChainArgs& a, const u16* __restrict__ X, const u16* __restrict__ B, char* C, const int head, const int xc, const int wk, unsigned& tau, char* smem) {
    const int tid = opaque_tid(), w = tid >> 6, l = tid & 63, i = l & 15, g = l >> 4;
    const int wu = __builtin_amdgcn_readfirstlane(w), kh = wu >> 2, cq = wu & 3;
    const unsigned lds0 = (unsigned)(uintptr_t)(__attribute__((address_space(3))) char*)smem;
    unsigned voA, voB;
    { const int row = (wu & 1) * 8 + (l >> 3), c = l & 7; voA = (unsigned)(row * DM + 8 * (c ^ (row & 7))) * 2u + (wu >> 1) * 512; }
    { const int row = tid >> 4, c = tid & 15; voB = (unsigned)(row * HD + 8 * (c ^ (bswz2<128>(row) << 1))) * 2u; }
    auto stB = [&](int ch, int slot) {
#pragma unroll
        for (int e = 0; e < 4; ++e) glds16s(voB, (const char*)B + (size_t)(4 * ch + e) * 32 * HD * 2, lds0 + 65536 + slot * 32768 + e * 8192 + wu * 1024);
    };
    const unsigned ldA = lds0 + (wu >> 1) * 8192 + (wu & 1) * 1024;
    auto stP = [&](const char* p, const unsigned d) {
        unsigned keep;
        asm volatile("s_mov_b32 %0, m0\n\ts_mov_b32 m0, %1\n\ts_nop 0\n\tglobal_load_lds_dwordx4 %3, %2 offset:-4096\n\tglobal_load_lds_dwordx4 %4, %2 offset:-2048\n\t"
                     "global_load_lds_dwordx4 %5, %2\n\tglobal_load_lds_dwordx4 %6, %2 offset:2048\n\ts_mov_b32 m0, %0"
                     : "=&s"(keep) : "s"(d), "s"(p), "v"(voA + 8192u), "v"(voA + 6272u), "v"(voA + 4352u), "v"(voA + 2432u) : "memory");
    };
    auto srcA = [&](int h) { return (const char*)X + (size_t)min(h, 2 * KVS - 1) * 32768 - 4096; };
    auto stA = [&](int h) { stP(srcA(h), ldA + (h & 3) * 32768 + 4096); };
    stB(0, 0); stA(0); stA(1); stB(1, 1);
    touch((const char*)X + 65536 + min(wk * 512 + tid, (KVS - 1) * 512 - 1) * 128, lds0 + 131072 + wu * 1024);
    stB(2, 2);
    bf16x8 bf[16][2], af[8];
    f32x4 acc[2][2] = {}, keep[2];
    const int rb = 64 * kh + 8 * g + (i >> 2), sw = bswz2<128>(rb);
    const char* bp = smem + 65536 + rb * 256 + 8 * (i & 3);
    const char* sa0 = smem + kh * 2048 + i * 128 + 16 * (g ^ (i & 7));
    const char* sa1 = smem + kh * 2048 + i * 128 + 16 * ((4 + g) ^ (i & 7));
#define KV_AF(slot, q) *(const bf16x8*)((((q) & 1) ? sa1 : sa0) + (slot) * 32768 + ((q) >> 1) * 4096)
#pragma unroll
    for (int ch = 0; ch < 8; ++ch) {
        if (ch == 0) K8_WV(9); else if (ch < 6) K8_WV(8); else if (ch == 6) K8_WV(4); else K8_WV(0);
        K8_BAR;
#pragma unroll
        for (int e = 0; e < 4; ++e) { const char* p_ = bp + (ch % 3) * 32768 + (e >> 1) * 8192 + 32 * ((2 * cq + (e & 1)) ^ sw); bf[2 * ch + (e >> 1)][e & 1] = tr_frag(p_, p_ + 1024); }
#pragma unroll
        for (int e = 0; e < 4; ++e) af[e] = KV_AF(e >> 1, 2 * ch + (e & 1));
        K8_WL(0); K8_SCHED; K8_BAR;
        if (ch < 5) stB(ch + 3, ch % 3);
        if (ch == 7) { stA(2); stA(3); }
#pragma unroll
        for (int e = 0; e < 8; ++e) acc[e >> 2][e & 1] = __builtin_amdgcn_mfma_f32_16x16x32_bf16(bf[2 * ch + ((e >> 1) & 1)][e & 1], af[e >> 1], acc[e >> 2][e & 1], 0, 0, 0);
    }
    float* red = (float*)(smem + 131072) + l * 4;
#pragma unroll
    for (int p = 0; p < 2; ++p) { *(f32x4*)(red + (p * 8 + wu) * 256) = kh ? acc[p][0] : acc[p][1]; keep[p] = kh ? acc[p][1] : acc[p][0]; }
    unsigned long long tag = 0;
    if (wu == 4) { tag = boot_wait(a.boot); *(volatile unsigned*)(smem + 163840 - 512) = (unsigned)tag & 255u; }
    const rsrc_t rc = __builtin_amdgcn_make_buffer_rsrc((void*)C, 0, KVS * 524288, 0x00020000);
    const int vo = cq * 1024 + (2 * kh + (g >> 1)) * 256 + i * 16 + (g & 1) * 8;
    auto put = [&](const f32x4 o, const int so) {
        const v2u pk = {cvt_pk_bf16(o[0], o[1]), cvt_pk_bf16(o[2], o[3])};
        asm volatile("buffer_store_dwordx2 %0, %1, %2, %3 offen sc1" :: "v"(pk), "v"(vo), "s"(rc), "s"(so) : "memory");
    };
    K8_WL(0); K8_WV(0); K8_BAR;
    tau = __builtin_amdgcn_readfirstlane(*(volatile unsigned*)(smem + 163840 - 512));
#pragma unroll
    for (int p = 0; p < 2; ++p) put(keep[p] + *(const f32x4*)(red + (p * 8 + (wu ^ 4)) * 256), blk_off(0, head, tau) + p * 4096);
    stA(4);
    K8_WV(4); K8_BAR;
    if (tid == 256) __hip_atomic_store((gu64*)(a.boot + 256 + 16 * xc + wk), tag, __ATOMIC_RELAXED, __HIP_MEMORY_SCOPE_AGENT);
    auto stages = [&](auto kc) {
        constexpr int KH = decltype(kc)::value;
        if (KH) K8_BAR;
        f32x4 ac[2] = {ZERO4, ZERO4}, kp = ZERO4;
        int h_first = 2, so = OOB;
        asm volatile("" : "+s"(h_first));
        const char* pn = srcA(5);
        unsigned dn = ldA + 32768 + 4096;
        int r0 = (int)(sa0 - smem) + 2 * 32768, r1 = (int)(sa1 - smem) + 2 * 32768;
        for (int h = h_first; h < 2 * KVS + 4; ++h) {
            const f32x4 oth = *(const f32x4*)(red + (wu ^ 4) * 256);
            *(f32x4*)(red + wu * 256) = ac[KH ^ 1];
            bf16x8 ax[16];
#pragma unroll
            for (int q = 0; q < 16; ++q) ax[q] = *(const bf16x8*)(smem + ((q & 1) ? r1 : r0) + (q >> 1) * 4096);
            K8_SCHED;
            stP(pn, dn);
            put((KH ? ac[1] : kp) + oth, so);
            kp = ac[0];
            __builtin_amdgcn_s_waitcnt(0xC07F);
            K8_WV(10);
            if (__builtin_expect(h == 5, 0)) K8_WV(0);
            K8_BAR;
            if (KH && (0xAAA20u >> h) & 1u && tid == 256)
                __hip_atomic_store((gu32*)(a.sync + (SY_KV0 + (h / 2 - (h == 5 ? 1 : 2)) * 8 + xc) * 32 + wk), 16u, __ATOMIC_RELAXED, __HIP_MEMORY_SCOPE_AGENT);
            if (__builtin_expect(h < 2 * KVS, 1)) {
                __builtin_amdgcn_s_setprio(1);
#pragma unroll
                for (int q = 0; q < 16; ++q)
#pragma unroll
                    for (int n = 0; n < 2; ++n) ac[n] = __builtin_amdgcn_mfma_f32_16x16x32_bf16(bf[q][n], ax[q], q ? ac[n] : ZERO4, 0, 0, 0);
                __builtin_amdgcn_s_setprio(0);
            }
            {
                const int hs = h - 1 + KH;
                so = blk_off(hs >> 1, head, tau) + (hs & 1) * 4096;
                asm volatile("" : "+s"(so));
                so = hs > 1 && hs < 2 * KVS ? so : OOB;
                pn = srcA(h + 4); dn = ldA + (h & 3) * 32768 + 4096;
                r0 = (r0 + 32768) & 131071; r1 = (r1 + 32768) & 131071;
                asm volatile("" : "+s"(so), "+s"(pn), "+s"(dn), "+v"(r0), "+v"(r1));
            }
            K8_BAR;
        }
        if (!KH) K8_BAR;
    };
    if (kh) stages(std::integral_constant<int, 1>{}); else stages(std::integral_constant<int, 0>{});
    K8_WV(0);
}

struct MegaArgs {
    ChainArgs ch;
    const u16* x; u16* out;
    const u16* bx; const u16* wr; const u16* wi;
};
constexpr int MEGA_LDS = 163840;
#define WSTAMP(n)
__global__ __launch_bounds__(512, 2) void mega_kernel(MegaArgs m) {
    extern __shared__ __attribute__((aligned(256))) char chsm[];
    const int b = BIDX, xc = (b & 3) + 4 * (b >> 7), sl = (b >> 3) & 15;
    if ((b & 4) == 0) { chain_body(m.ch, xc, sl, chsm); return; }
    const ChainArgs& a = m.ch;
    const int wk = sl, tid = TIDX;
    char* ar = arena(a.slots);
    unsigned tauw = 0;
    const int hf = __builtin_amdgcn_readfirstlane(TIDX >> 8);
    WSTAMP(0)
    if (wk < 8) kv_stream(a, m.x, (wk < 4 ? a.Wk : a.Wv) + hcol(4 * xc + (wk & 3)), ar + (wk < 4 ? KX_OFF : VX_OFF), 4 * xc + (wk & 3), xc, wk, tauw, chsm);
    WSTAMP(8) WEV(1)
    {
        const int p = wk & 1, q = wk & 3;
        const unsigned long long todo = wk < 2 ? 0x84u + p : wk < 4 ? 0x74704240u + 0x01010101u * p : wk < 8 ? 0ull : wk < 10 ? 0x60503010ull + 0x01010101ull * q
                                      : wk < 12 ? 0x7060503010ull + 0x0101010101ull * q : (wk < 14 ? 0x54341420u : 0x64442422u) + 0x01010101u * p;
        __syncthreads();
        if (todo != 0ull) tile_stream(a, m.x, todo, xc, tauw, wk >= 8, xc * 8 + (wk & 7), (u16*)chsm WEVA);
    }
    WSTAMP(2)
    if (wk < 2) {
        unsigned* mbox = (unsigned*)(chsm + 131072);
        if (tid < 40) mbox[tid] = tid >= 1 && tid < 5 ? 2u : 0u;
        __syncthreads();
        const int w = __builtin_amdgcn_readfirstlane(tid >> 6), h = 4 * xc + (w & 3);
        const rsrc_t rsQ = __builtin_amdgcn_make_buffer_rsrc((void*)(ar + KX_OFF), 0, S_LEN * HD * 4, 0x00020000);
        const rsrc_t rsK = __builtin_amdgcn_make_buffer_rsrc((void*)(ar + KX_OFF), 0, S_LEN * HD * 4, 0x00020000);
        const rsrc_t rsV = __builtin_amdgcn_make_buffer_rsrc((void*)(ar + VX_OFF), 0, S_LEN * HD * 4, 0x00020000);
        const rsrc_t rsS = slot_rsrc(a.slots);
        u16 (*Vs)[VP] = (u16 (*)[VP])(chsm + w * (40 * VP * 2));
        const unsigned tau = tauw;
        int pend = -1;
        bool ready = false;
        for (int s = 2 * wk + (w >> 2), sn; s < NSEG - 1; s = sn) {
            const int band = s >> 3;
            sn = s + 4;
            if (s < XST) {
                unsigned n = 0;
                if (lane_now() == 0) n = atomicAdd(mbox + 1 + (w & 3), 1u);
                n = __builtin_amdgcn_readfirstlane(n);
                sn = 2 * wk + (n < XST / 2 ? (int)(n & 1u) + 4 * (int)(n >> 1) : XST + (w >> 2));
            }
            if (sn - s > 4) __builtin_amdgcn_s_setprio(2); else __builtin_amdgcn_s_setprio(0);
            if (s < 4) wait_ge<32>(a.sync + (SY_KV0 + (KVS - 1) * 8 + xc) * 32 + (tid & 7), 16u, a.tmo, 4100u);
            if (!ready) {
                if (band > 0) wait_ge<32>(a.sync + (SY_KV + band * 8 + xc) * 32 + (tid & 3), 1u, a.tmo, 4100u);
                wait_ge<32>(a.sync + (SY_Q + band * 8 + xc) * 32 + (tid & 3), 1u, a.tmo, 4200u);
            }
            xattn_unit(a, rsQ, rsK, rsV, rsS, s, sn, h, Vs, mbox, tau, pend, ready);
        }
        return;
    }
    if (wk >= 4 && wk < 8) {
        const int fw = (wk - 4) * 8 + xc;
        const rsrc_t rsF = __builtin_amdgcn_make_buffer_rsrc((void*)(ar + FX_OFF), 0, 16 << 20, 0x00020000);
        const rsrc_t rsW = __builtin_amdgcn_make_buffer_rsrc((void*)(ar + WT_OFF), 0, 10 << 20, 0x00020000);
        const unsigned tw = tauw ^ 1u;
        {
            const int tq = opaque_tid();
            fft_rows_fwd(m.bx, rsF, tw, 2 * fw + hf, chsm + hf * 36864, tq & 255, (const char*)m.wr, (const char*)m.wi, (unsigned)(uintptr_t)(__attribute__((address_space(3))) char*)chsm + 163840 - 256);
        }
        for (int t = fw; t < 64; t += 32) {
            __syncthreads();
            filt_transpose(m.wr, m.wi, rsW, tw, t, chsm, opaque_tid());
        }
        wg_publish(a.sync, SY_DONE, 32u, 8);
        WSTAMP(3) WEV(4)
        wg_wait_acquire<16>(a.sync + (SY_DONE + xc) * 32, a.tmo, 6000u);
        {
            const int tq = opaque_tid();
            fft_cols(rsF, rsW, tw, 2 * fw + hf, 4, chsm + hf * 36864, tq & 255);
        }
        wg_publish(a.sync, SY_DONE + 8, 32u, 8);
        WSTAMP(4) WEV(5)
    }
    if (wk >= 4 && wk < 10) {
        const rsrc_t rsF = __builtin_amdgcn_make_buffer_rsrc((void*)(ar + FX_OFF), 0, 16 << 20, 0x00020000);
        wg_wait_acquire<16>(a.sync + (SY_DONE + 8 + xc) * 32, a.tmo, 6001u);
        const int tq = opaque_tid();
        fft_rows_inv(rsF, tauw ^ 1u, m.out, 2 * ((wk & 3) * 8 + xc) + hf + (wk < 8 ? 0 : 192), wk < 8 ? 64 : 32, wk < 8 ? 3 : 2, chsm + hf * 36864, tq & 255);
        WSTAMP(5) WEV(6)
    }
    if (wk >= 12) {
        const int r = 2 * (wk - 12);
        wg_wait_acquire<16>(a.sync + (SY_OX + r * 8 + xc) * 32, a.tmo, 5000u);
        wg_wait_acquire<16>(a.sync + (SY_OX + (r + 1) * 8 + xc) * 32, a.tmo, 5000u);
        WEV(7)
        gemm_tile<256>((u16*)chsm, ar + VX_OFF, 4 * r, tauw, a.Wo + xc * 128, m.out + (size_t)r * 128 * OUTW + xc * 128);
        WEV(8)
    }
    if (wk < 4) {
        const int r = wk + 6;
        wg_wait_acquire<16>(a.sync + (SY_OX + r * 8 + xc) * 32, a.tmo, 5000u);
        WEV(7)
        gemm_tile<128>((u16*)chsm, ar + VX_OFF, 4 * r, tauw, a.Wo + xc * 128, m.out + (size_t)r * 128 * OUTW + xc * 128);
        WEV(8)
        WSTAMP(9)
    }
    if (wk >= 4 && wk < 12) {
#pragma nounroll
        for (int pass = (wk >> 1) == 4 ? 0 : 1; pass < 2; ++pass) {
            __syncthreads();
            oproj_squad(a, m.out, (wk - 4 + (pass ? 0 : 2)) * 4 + (xc & 3), xc >> 2, xc, tauw, pass && wk >= 10 ? SSEG + SHLP : SSEG, pass ? NSEG - 1 : SSEG + SHLP, chsm);
        }
        WSTAMP(7)
    }
}

#if defined(__HIP_DEVICE_COMPILE__)
#pragma clang attribute pop
#endif
extern "C" void kernel_launch(void* const* d_in, const int* in_sizes, int n_in,
                              void* d_out, int out_size, void* d_ws, size_t ws_size,
                              hipStream_t stream) {
    char* ws = (char*)d_ws;
    size_t off = 0;
    auto alloc = [&](size_t bytes) { void* p = ws + off; off += (bytes + 255) & ~(size_t)255; return p; };
    char* slots = (char*)alloc((size_t)ARENA_BYTES + (2u << 20));
    static_assert((SY_WORDS + 2) * 128 <= 131072, "sync block");
    unsigned* sync = (unsigned*)alloc(131072);
    unsigned long long* boot = (unsigned long long*)alloc(4096);

    MegaArgs m;
    m.ch.mem0 = (const u16*)d_in[1]; m.ch.Wq = (const u16*)d_in[2]; m.ch.Wk = (const u16*)d_in[3]; m.ch.Wv = (const u16*)d_in[4]; m.ch.Wo = (const u16*)d_in[5];
    m.ch.out = (u16*)d_out; m.ch.slots = slots; m.ch.tmo = sync + SY_WORDS * 32;
    m.ch.stamps = (unsigned long long*)(ws + (240ull << 20));
    m.ch.sync = sync; m.ch.boot = boot;
    m.x = (const u16*)d_in[0]; m.out = (u16*)d_out;
    m.bx = (const u16*)d_in[7]; m.wr = (const u16*)d_in[8]; m.wi = (const u16*)d_in[9];
    (void)hipFuncSetAttribute((const void*)mega_kernel, hipFuncAttributeMaxDynamicSharedMemorySize, MEGA_LDS);
    mega_kernel<<<256, 512, MEGA_LDS, stream>>>(m);
}
```

```cpp
#if defined(__HIP_DEVICE_COMPILE__)
#pragma clang attribute push (__attribute__((target("no-packed-fp32-ops,no-load-store-opt"))), apply_to = function)
#endif
#include <hip/hip_runtime.h>
#include <hip/hip_bf16.h>
#include <stdint.h>
#include <type_traits>

typedef unsigned short u16;
#define TIDX ((int)__builtin_amdgcn_workitem_id_x())
#define BIDX ((int)__builtin_amdgcn_workgroup_id_x())
__device__ __forceinline__ int opaque_tid() { int t = TIDX; asm volatile("" : "+v"(t)); return t; }
__device__ __forceinline__ int lane_now() { int t; asm volatile("v_mbcnt_lo_u32_b32 %0, -1, 0\n\tv_mbcnt_hi_u32_b32 %0, -1, %0" : "=v"(t)); return t; }
typedef __attribute__((ext_vector_type(8))) short bf16x8;
typedef __attribute__((ext_vector_type(4))) float f32x4;

constexpr int S_LEN = 2048, DM = 1024, NH = 32, DH = 128, MM = 8, SEG = 32;
constexpr int NSEG = S_LEN / SEG;
constexpr int HD = NH * DH;
constexpr int OUTW = 2048;

typedef unsigned v4u __attribute__((ext_vector_type(4)));
typedef unsigned v2u __attribute__((ext_vector_type(2)));
typedef short v4i16 __attribute__((ext_vector_type(4)));
typedef __amdgpu_buffer_rsrc_t rsrc_t;
#define AUX_SC1 16
__device__ __forceinline__ v4u ld_sc1s(rsrc_t rs, int voff, int soff) { return __builtin_amdgcn_raw_buffer_load_b128(rs, voff, soff, AUX_SC1); }
__device__ __forceinline__ void st_sc1s(rsrc_t rs, int voff, int soff, v4u v) { __builtin_amdgcn_raw_buffer_store_b128(v, rs, voff, soff, AUX_SC1); asm volatile("s_nop 1" :: "v"(v) : "memory"); }
__device__ __forceinline__ void st8_sc1s(rsrc_t rs, int voff, int soff, v2u v) { __builtin_amdgcn_raw_buffer_store_b64(v, rs, voff, soff, AUX_SC1); }
__device__ __forceinline__ void st_sc1(rsrc_t rs, int off, v4u v) { __builtin_amdgcn_raw_buffer_store_b128(v, rs, off, 0, AUX_SC1); }
__device__ __forceinline__ void out8(void* p, v2u v) { asm volatile("global_store_dwordx2 %0, %1, off sc1" :: "v"(p), "v"(v) : "memory"); }
typedef __bf16 bf16x2_t __attribute__((ext_vector_type(2)));
typedef float f32x2_t __attribute__((ext_vector_type(2)));
__device__ __forceinline__ unsigned cvt_pk_bf16(float lo, float hi) {
    f32x2_t f = {lo, hi};
    return __builtin_bit_cast(unsigned, __builtin_convertvector(f, bf16x2_t));
}
#define SENT 0xFFBFFFBF
__device__ __forceinline__ unsigned umax3(unsigned a, unsigned b, unsigned c) { return max(max(a, b), c); }
__device__ __forceinline__ unsigned mx4(unsigned acc, v4u v) { return umax3(umax3(acc, v[0], v[1]), v[2], v[3]); }
__device__ __forceinline__ bf16x8 as_frag(v4u v) { return __builtin_bit_cast(bf16x8, v); }
__device__ __forceinline__ float xor16_sum(float x) {
    auto r = __builtin_amdgcn_permlane16_swap(__float_as_uint(x), __float_as_uint(x), false, false);
    return __uint_as_float(r[0]) + __uint_as_float(r[1]);
}
__device__ __forceinline__ float xor32_sum(float x) {
    auto r = __builtin_amdgcn_permlane32_swap(__float_as_uint(x), __float_as_uint(x), false, false);
    return __uint_as_float(r[0]) + __uint_as_float(r[1]);
}
__device__ __forceinline__ float xor16_max(float x) {
    auto r = __builtin_amdgcn_permlane16_swap(__float_as_uint(x), __float_as_uint(x), false, false);
    return fmaxf(__uint_as_float(r[0]), __uint_as_float(r[1]));
}
__device__ __forceinline__ float xor32_max(float x) {
    auto r = __builtin_amdgcn_permlane32_swap(__float_as_uint(x), __float_as_uint(x), false, false);
    return fmaxf(__uint_as_float(r[0]), __uint_as_float(r[1]));
}
constexpr int SPIN_LIMIT = 400000;
constexpr int OOB = 0x7FFFFF00;
#define XST 32
#define SQ_SL 16
#define PLL 0
#define SE 6
#define TDE 72
#define PB 14
#define TD 60
#define PS 46
#define TE0 10
#define PVA 8
#define PV 1
#define PV1 2
#define PV2 11
#define PL 20
#define PF 48
#define PC 31
#define PP 15
#define PKA 28
#define PG0 2
#define PG1 32
#define DLY_U 4
#define DLY_C 4
#define XA_FAST 6
#define XA_D 12
#define SQ_SF 4
#define LW 0
constexpr int NPROBE = 2;
constexpr int M_OFF = 32 * 16384, F_OFF = M_OFF + 2 * 16384, C_OFF = F_OFF + 2 * 16384, REC = C_OFF + 16 * 16384, PERWG = REC / 128, SLOT_BYTES = 34 * 2097152;
static_assert(PERWG == 6656, "arming shares");
__device__ __forceinline__ int rec_base(int s) { return (s >> 1) * 2097152 + (s & 1) * REC; }
__device__ __forceinline__ char* arena(char* slots) { return (char*)(((uintptr_t)slots + 2097151u) & ~(uintptr_t)2097151u); }
constexpr int KX_OFF = SLOT_BYTES, VX_OFF = KX_OFF + (32 << 20), FX_OFF = VX_OFF + (32 << 20), WT_OFF = FX_OFF + (16 << 20), ARENA_BYTES = WT_OFF + (10 << 20);
__device__ __forceinline__ rsrc_t slot_rsrc(char* slots) { return __builtin_amdgcn_make_buffer_rsrc((void*)arena(slots), 0, FX_OFF, 0x00020000); }
__device__ __forceinline__ int blk_off(int seg, int h, unsigned tau) { return seg * 524288 + (((h & 3) >> 1) * 16 + (h >> 2) * 2 + (h & 1)) * 16384 + (int)((tau ^ (unsigned)(seg >> 2)) & 1u) * 8192; }
constexpr int TM_OFF = 124 * 16384;
constexpr int CAL_OFF = 32 * 2097152;
__device__ __forceinline__ int near_half(int s, unsigned tau) { return (int)((tau ^ (unsigned)(s >> 1)) & 1u) * 8192; }
__device__ __forceinline__ int chunk_off(int s, int g, int j, unsigned tau) { return rec_base(s) + (j < 2 ? (g & 3) * 2 + j : 6 + 3 * g + j) * 16384 + near_half(s, tau); }
__device__ __forceinline__ int x_off(int s, int h, unsigned tau) { return chunk_off(s, h >> 2, 2, tau) + (h & 3) * 2048; }
__device__ __forceinline__ int km_off(int s, int h, unsigned tau) { return chunk_off(s, h >> 2, 3, tau) + (h & 3) * 2048; }
__device__ __forceinline__ int m_off(int s, int h, unsigned tau) { return rec_base(s) + M_OFF + (h >> 4) * 16384 + near_half(s, tau) + (h & 15) * 512; }
__device__ __forceinline__ int v_off(int s, int h, unsigned tau) { return chunk_off(s, h >> 2, 4, tau) + (h & 3) * 2048; }
__device__ __forceinline__ int f_off(int s, int g, unsigned tau) { return rec_base(s) + F_OFF + (g >> 2) * 16384 + near_half(s, tau) + (g & 3) * 2048; }
__device__ __forceinline__ int hcol(int h) { return ((h & 3) * 8 + (h >> 2)) * 128; }
constexpr int VP = 144;

struct ChainArgs {
    u16* out;
    const u16* mem0; const u16* Wq; const u16* Wk; const u16* Wv; const u16* Wo;
    char* slots;
    unsigned* tmo;
    unsigned long long* stamps;
    unsigned* sync;
    unsigned long long* boot;
};
constexpr int SY_KV = 0;
constexpr int SY_Q = 64;
constexpr int SY_PROG = 128;
constexpr int SY_OX = 136;
constexpr int SY_DONE = 264;
constexpr int SY_OXS = 472;
constexpr int SY_KV0 = 408;
constexpr int KVS = 8;
constexpr int SY_FLAGS = 680;
constexpr int SY_WORDS = SY_FLAGS + SY_FLAGS / 8;
constexpr int SSEG = 44;
typedef __attribute__((address_space(1))) unsigned gu32;
typedef __attribute__((address_space(1))) unsigned long long gu64;
__device__ __forceinline__ unsigned ld_flag(const unsigned* p) { return __hip_atomic_load((gu32*)p, __ATOMIC_RELAXED, __HIP_MEMORY_SCOPE_AGENT); }
__device__ __forceinline__ unsigned long long boot_tag(const unsigned long long* boot) {
    return __hip_atomic_load((gu64*)(boot + 144), __ATOMIC_RELAXED, __HIP_MEMORY_SCOPE_AGENT) + 1ull;
}
__device__ __forceinline__ unsigned long long boot_wait(const unsigned long long* boot) {
    const int l = TIDX & 63;
    unsigned long long tag = 0, x = 0, y = 0;
#pragma nounroll
    for (int spin = 0; spin < 100000; ++spin) {
        asm volatile("" ::: "memory");
        x = __hip_atomic_load((gu64*)(boot + l), __ATOMIC_RELAXED, __HIP_MEMORY_SCOPE_AGENT);
        y = __hip_atomic_load((gu64*)(boot + 64 + l), __ATOMIC_RELAXED, __HIP_MEMORY_SCOPE_AGENT);
        tag = boot_tag(boot);
        if (__all((x | 3u) == ((tag << 2) | 3u) && (y | 3u) == ((tag << 2) | 3u))) break;
        __builtin_amdgcn_s_sleep(16);
    }
    asm volatile("" ::: "memory");
    const unsigned m = ((unsigned)__ballot(x & 1u) ^ (__popcll(__ballot(x & 2u)) + __popcll(__ballot(y & 2u)) > 64 ? 1u : 0u)) & 1u;
    return (tag << 8) | m;
}
template <int SL>
__device__ __forceinline__ void wait_ge(const unsigned* p, unsigned need, unsigned* tmo, unsigned code) {
#pragma nounroll
    for (int spin = 0;; ++spin) {
        asm volatile("" ::: "memory");
        if (__all(ld_flag(p) >= need)) break;
        if (spin > 400000 / SL) { __hip_atomic_store((gu32*)tmo, code, __ATOMIC_RELAXED, __HIP_MEMORY_SCOPE_AGENT); break; }
        __builtin_amdgcn_s_sleep(SL);
    }
    asm volatile("" ::: "memory");
}
template <int SL>
__device__ __forceinline__ void wg_wait_acquire(const unsigned* p, unsigned* tmo, unsigned code) {
    if (TIDX < 64) {
        wait_ge<SL>(p, 1u, tmo, code);
        __builtin_amdgcn_fence(__ATOMIC_ACQUIRE, "agent");
        asm volatile("s_waitcnt vmcnt(0)" ::: "memory");
    }
    __syncthreads();
}
__device__ __forceinline__ void arrive(unsigned* sync, const int f, const unsigned need, const int n) {
    const int l = lane_now();
    unsigned old = 0;
    if (l == 0) old = __hip_atomic_fetch_add((gu32*)(sync + (SY_FLAGS + (f >> 3)) * 32), 1u, __ATOMIC_RELAXED, __HIP_MEMORY_SCOPE_AGENT);
    if (__builtin_amdgcn_readfirstlane(old) == need - 1u && l < n) __hip_atomic_store((gu32*)(sync + (f + l) * 32), 16u, __ATOMIC_RELAXED, __HIP_MEMORY_SCOPE_AGENT);
}
__device__ __forceinline__ void wg_publish(unsigned* sync, const int f, const unsigned need, const int n) {
    asm volatile("s_waitcnt vmcnt(0)" ::: "memory");
    __syncthreads();
    if (TIDX < 64) arrive(sync, f, need, n);
}
#define TMO_SET(code) { dead = true; if (l == 0) __hip_atomic_store(a.tmo, (code) + s, __ATOMIC_RELAXED, __HIP_MEMORY_SCOPE_AGENT); }

__device__ __forceinline__ void glds16h(unsigned voff, const void* sbase, unsigned lds_dst) {
    unsigned keep;
    asm volatile("s_mov_b32 %0, m0\n\ts_mov_b32 m0, %3\n\ts_nop 0\n\tglobal_load_lds_dwordx4 %1, %2 sc1\n\ts_mov_b32 m0, %0" : "=&s"(keep) : "v"(voff), "s"(sbase), "s"(lds_dst) : "memory");
}
__device__ __forceinline__ void touch(const void* p, unsigned lds_dst) {
    unsigned keep;
    asm volatile("s_mov_b32 %0, m0\n\ts_mov_b32 m0, %2\n\ts_nop 0\n\tglobal_load_lds_dword %1, off\n\ts_mov_b32 m0, %0" : "=&s"(keep) : "v"(p), "s"(lds_dst) : "memory");
}
__device__ __forceinline__ void touch_sc1(const void* p, unsigned lds_dst) {
    unsigned keep;
    asm volatile("s_mov_b32 %0, m0\n\ts_mov_b32 m0, %2\n\ts_nop 0\n\tglobal_load_lds_dword %1, off sc1\n\ts_mov_b32 m0, %0" : "=&s"(keep) : "v"(p), "s"(lds_dst) : "memory");
}
#define WEV(code)
#define WEVP
#define WEVA
constexpr int C2_K = 32768, C2_V = 65536;
constexpr int C2_R1 = 0, R1S = 3200;
constexpr int C2_R3 = C2_R1 + 4 * R1S;
constexpr int C2_MB = 24576;
#define PST(n)
#define LT(n)
#define LTA(n)
#define LTOUT0
#define LTOUT
#define CN(n)
#define CNS
#define CNOUT
#define CLOB16(a, b, c, d, e, f, g, h, i, j, k, l, m, n, o, p) "v" #a, "v" #b, "v" #c, "v" #d, "v" #e, "v" #f, "v" #g, "v" #h, "v" #i, "v" #j, "v" #k, "v" #l, "v" #m, "v" #n, "v" #o, "v" #p
#define LDS3(p) ((__attribute__((address_space(3))) v4i16*)(p))
__device__ __forceinline__ bf16x8 tr_frag(const void* p0, const void* p1) {
    v4i16 lo = __builtin_amdgcn_ds_read_tr16_b64_v4i16(LDS3(p0));
    v4i16 hi = __builtin_amdgcn_ds_read_tr16_b64_v4i16(LDS3(p1));
    return bf16x8{lo[0], lo[1], lo[2], lo[3], hi[0], hi[1], hi[2], hi[3]};
}
typedef __bf16 bf16x8v __attribute__((ext_vector_type(8)));
typedef __bf16 bf16x16v __attribute__((ext_vector_type(16)));
typedef unsigned v8u __attribute__((ext_vector_type(8)));
typedef unsigned v32u __attribute__((ext_vector_type(32)));
#define PUT8(v, o, x) { const v8u x_ = (x); _Pragma("unroll") for (int e_ = 0; e_ < 8; ++e_) (v)[(o) + e_] = x_[e_]; }
#define SUB4(v, o) v4u{(v)[(o)], (v)[(o) + 1], (v)[(o) + 2], (v)[(o) + 3]}
#define PUT4(v, o, x) { const v4u x_ = (x); (v)[(o)] = x_[0]; (v)[(o) + 1] = x_[1]; (v)[(o) + 2] = x_[2]; (v)[(o) + 3] = x_[3]; }
__device__ __forceinline__ v2u tr4(const u16* p) { return __builtin_bit_cast(v2u, __builtin_amdgcn_ds_read_tr16_b64_v4i16(LDS3(p))); }
__device__ __forceinline__ f32x4 smm(v4u a, v8u b, f32x4 c, int idx) {
    return __builtin_amdgcn_smfmac_f32_16x16x64_bf16(__builtin_bit_cast(bf16x8v, a), __builtin_bit_cast(bf16x16v, b), c, idx, 0, 0);
}
__device__ __forceinline__ v8u tr_frag64(const u16* p, const int pitch) {
    const v2u r0 = tr4(p), r1 = tr4(p + 8 * pitch), r2 = tr4(p + 32 * pitch), r3 = tr4(p + 40 * pitch);
    return v8u{r0[0], r1[0], r0[1], r1[1], r2[0], r3[0], r2[1], r3[1]};
}
#define MM(a_, b_, c_) __builtin_amdgcn_mfma_f32_16x16x32_bf16(as_frag(a_), as_frag(b_), c_, 0, 0, 0)
#define STR2(x) #x
#define STR(x) STR2(x)
#define PAUSE(n) ".rept (" STR(n) ") / 4\n\ts_nop 15\n\t.endr\n\t.rept (" STR(n) ") %% 4\n\ts_nop 3\n\t.endr\n\t"
#define ZERO4 f32x4{0.f, 0.f, 0.f, 0.f}
constexpr int XB_OFF = CAL_OFF + 262144, XP = 68;
__device__ __forceinline__ void last_rows(const char* chsm, rsrc_t rsS, const int wu, const int grp, const int c, const unsigned tau) {
    const int l = lane_now();
    __builtin_amdgcn_s_barrier();
    asm volatile("" ::: "memory");
    const float* rp = (const float*)(chsm + C2_K) + (4 * wu + (l >> 4)) * XP + 4 * (l & 15);
    const f32x4 s0 = (*(const f32x4*)rp + *(const f32x4*)(rp + 32 * XP)) + (*(const f32x4*)(rp + 64 * XP) + *(const f32x4*)(rp + 96 * XP));
    v2u o;
    o[0] = cvt_pk_bf16(s0[0], s0[1]); o[1] = cvt_pk_bf16(s0[2], s0[3]);
    st8_sc1s(rsS, l * 8, XB_OFF + ((c >> 1) * 8 + wu) * 16384 + (int)(tau & 1u) * 8192 + (c & 1) * 4096 + grp * 512, o);
}
__device__ __forceinline__ const unsigned* kv_flag(const unsigned* sync, const int sq, const int grp, const int l) { return sync + (sq < KVS ? (SY_KV0 + sq * 8 + grp) * 32 + (l & 7) : (SY_KV + 8 + grp) * 32 + (l & 3)); }
__device__ __forceinline__ void chain_body(const ChainArgs& a, const int grp, const int c, char* chsm) {
    const int wu = __builtin_amdgcn_readfirstlane(TIDX >> 6);
    const int widx = grp * 16 + c;
    const int hs = 4 * grp + (c >> 2), jq = c & 3;
    const rsrc_t rsS = slot_rsrc(a.slots);
    const unsigned l0 = (unsigned)(uintptr_t)(__attribute__((address_space(3))) char*)chsm;
    bool dead = false;

    const int cal_off = CAL_OFF + grp * 32768 + (c >> 3) * 16384 + (c & 7) * 1024;
        if (wu == 0) {
            const int l = lane_now();
            const v4u ff = {SENT, SENT, SENT, SENT};
#pragma unroll
            for (int e = 0; e < 13; ++e) st_sc1s(rsS, l * 16, widx * 2 * PERWG + min(e * 1024, 2 * PERWG - 1024), ff);
            const rsrc_t rsY = __builtin_amdgcn_make_buffer_rsrc((void*)a.sync, 0, 131072, 0x00020000);
            st_sc1s(rsY, l * 16, widx * 1024, v4u{0u, 0u, 0u, 0u});
            __builtin_amdgcn_raw_buffer_store_b32(0u, rsS, (l & 3) * 256 + ((l >> 2) & 1) * 8192, cal_off, AUX_SC1);
            if (c < 2) __builtin_amdgcn_raw_buffer_store_b32(0u, rsS, (l >> 5) * 8192 + (l & 31) * 4, TM_OFF + c * 16384 + grp * 256, AUX_SC1);
        }
    asm volatile("" ::: "memory");
    if (wu >= 4) {
        __builtin_amdgcn_s_sleep(DLY_U);
        const int l = opaque_tid() & 63, i = l & 15, g = l >> 4;
        const int sidx = (i & 1) ? 0xEEEE : 0x4444;
        const int u = wu - 4;
        const int k0 = 256 * u;
        u16* stg = (u16*)(chsm + 40960 + u * 20480);
        v4u af[4];
        v32u wS[6];
        {
            v4u raw[3][16];
            {
                const size_t wo = (size_t)k0 * HD + hcol(hs) + 32 * jq;
                const rsrc_t rq = __builtin_amdgcn_make_buffer_rsrc((void*)(a.Wq + wo), 0, 256 * HD * 2, 0x00020000);
                const rsrc_t rk = __builtin_amdgcn_make_buffer_rsrc((void*)(a.Wk + wo), 0, 256 * HD * 2, 0x00020000);
                const rsrc_t rv = __builtin_amdgcn_make_buffer_rsrc((void*)(a.Wv + wo), 0, 256 * HD * 2, 0x00020000);
                const int vw = (l >> 2) * HD * 2 + (l & 3) * 16;
#pragma unroll
                for (int p = 0; p < 4; ++p) af[p] = *(const v4u*)(a.mem0 + (size_t)(i >> 1) * DM + k0 + 64 * p + 16 * g + 8 * (i & 1));
#pragma unroll
                for (int e = 0; e < 16; ++e) raw[0][e] = __builtin_amdgcn_raw_buffer_load_b128(rq, vw, e * 16 * HD * 2, 0);
#pragma unroll
                for (int e = 0; e < 16; ++e) raw[1][e] = __builtin_amdgcn_raw_buffer_load_b128(rk, vw, e * 16 * HD * 2, 0);
#pragma unroll
                for (int e = 0; e < 12; ++e) raw[2][e] = __builtin_amdgcn_raw_buffer_load_b128(rv, vw, e * 16 * HD * 2, 0);
                __builtin_amdgcn_sched_barrier(0);
#pragma unroll
                for (int e = 0; e < 16; ++e) *(v4u*)(stg + (16 * e + (l >> 2)) * 40 + 8 * (l & 3)) = raw[0][e];
                __builtin_amdgcn_sched_barrier(0);
#pragma unroll
                for (int e = 12; e < 16; ++e) raw[2][e] = __builtin_amdgcn_raw_buffer_load_b128(rv, vw, e * 16 * HD * 2, 0);
            }
            const int fr = 16 * (g >> 1) + 4 * (g & 1) + (i >> 2);
            __builtin_amdgcn_sched_barrier(0);
#pragma unroll
            for (int hx = 0; hx < 3; ++hx) {
                if (hx) {
#pragma unroll
                    for (int e = 0; e < 16; ++e) *(v4u*)(stg + (16 * e + (l >> 2)) * 40 + 8 * (l & 3)) = raw[hx][e];
                }
                __builtin_amdgcn_s_waitcnt(0xC07F);
#pragma unroll
                for (int jj = 0; jj < 2; ++jj)
#pragma unroll
                    for (int p = 0; p < 4; ++p) PUT8(wS[2 * hx + jj], 8 * p, tr_frag64(stg + (64 * p + fr) * 40 + 16 * jj + 4 * (i & 3), 40))
                __builtin_amdgcn_s_waitcnt(0xC07F);
                __builtin_amdgcn_sched_barrier(0);
            }
        }
        PST(u)
        if (u == 0) { PST(4) }
        const unsigned r1w = l0 + C2_R1 + u * R1S + (2 * g * 16 + i) * 4, r1v = l0 + C2_R1 + u * R1S + 2176 + (2 * g * 16 + ((i & 9) | ((i & 2) << 1) | ((i & 4) >> 1))) * 4;
#define LD4(d, va, so, o) "buffer_load_dwordx4 v[" d "], " va ", %[rs], " so " offen offset:" o " sc1\n\t"
#define CHK(a, b, c, d) "v_max3_u32 v244, v244, v" a ", v" b "\n\tv_max3_u32 v244, v244, v" c ", v" d "\n\t"
#define CHK0(a, b, c, d) "v_max_u32 v244, v" a ", v" b "\n\tv_max3_u32 v244, v244, v" c ", v" d "\n\t"
#define SMF(d, a, w) "v_smfmac_f32_16x16x64_bf16 v[" d "], v[" a "], v[" w "], %[sx]\n\t"
#define ADD(d, a, b) "v_add_f32 v" d ", v" a ", v" b "\n\t"
#define Z2(r) "v_mov_b64 v[" r "], 0\n\t"
#define DW2(ad, x, y, o0, o1) "ds_write2_b32 " ad ", v" x ", v" y " offset0:" o0 " offset1:" o1 "\n\t"
#define A0 "224:227"
#define A1 "228:231"
#define A2 "232:235"
#define A3 "236:239"
#define Q0 "200:203"
#define Q1 "204:207"
#define Q2 "208:211"
#define Q3 "212:215"
#define V0 "216:219"
#define V1 "220:223"
#define S1_HEAD \
            Z2("216:217") Z2("218:219") Z2("220:221") Z2("222:223") \
            "s_waitcnt vmcnt(4)\n\t" ZQ \
            "s_waitcnt vmcnt(3)\n\t" SMF(V0, A0, "136:143") CHK0("224", "225", "226", "227") SMF(V1, A0, "168:175") \
            "s_waitcnt vmcnt(2)\n\t" SMF(V0, A1, "144:151") CHK("228", "229", "230", "231") SMF(V1, A1, "176:183") \
            "s_waitcnt vmcnt(1)\n\t" SMF(V0, A2, "152:159") CHK("232", "233", "234", "235") SMF(V1, A2, "184:191") \
            "s_waitcnt vmcnt(0)\n\t" SMF(V0, A3, "160:167") CHK("236", "237", "238", "239") SMF(V1, A3, "192:199") \
            "v_cmp_eq_u32 vcc, " STR(SENT) ", v244\n\t"
#define S1_TAIL(B1, B2) \
            SMF(Q0, A0, "8:15") SMF(Q0, A1, "16:23") SMF(Q0, A2, "24:31") \
            ADD("240", "216", "217") ADD("241", "218", "219") SMF(Q0, A3, "32:39") DW2("%[rv]", "240", "241", "0", "16") \
            ADD("242", "220", "221") ADD("243", "222", "223") SMF(Q1, A0, "40:47") DW2("%[rv]", "242", "243", "128", "144") \
            SMF(Q1, A1, "48:55") SMF(Q1, A2, "56:63") SMF(Q1, A3, "64:71") \
            ADD("240", "200", "201") ADD("241", "202", "203") SMF(Q2, A0, "72:79") DW2("%[rw]", "240", "241", "0", "16") \
            SMF(Q2, A1, "80:87") SMF(Q2, A2, "88:95") "s_nop 1\n\t" \
            ADD("242", "204", "205") ADD("243", "206", "207") SMF(Q2, A3, "96:103") DW2("%[rw]", "242", "243", "136", "152") \
            SMF(Q3, A0, "104:111") SMF(Q3, A1, "112:119") \
            B1        \
            LTA(0) \
            SMF(Q3, A2, "120:127") SMF(Q3, A3, "128:135") "s_nop 0\n\t" \
            ADD("240", "208", "209") ADD("241", "210", "211") DW2("v246", "240", "241", "0", "16") \
            "s_nop 3\n\t" \
            ADD("242", "212", "213") ADD("243", "214", "215") DW2("v246", "242", "243", "136", "152") \
            "s_waitcnt lgkmcnt(0)\n\t" B2 "s_barrier\n\t"
#define ZQ Z2("200:201") Z2("202:203") Z2("204:205") Z2("206:207") Z2("208:209") Z2("210:211") Z2("212:213") Z2("214:215")
#define U_W "{v[8:39]}"(wS[0]), "{v[40:71]}"(wS[1]), "{v[72:103]}"(wS[2]), "{v[104:135]}"(wS[3]), "{v[136:167]}"(wS[4]), "{v[168:199]}"(wS[5])
#define U_CLOB CLOB16(200, 201, 202, 203, 204, 205, 206, 207, 208, 209, 210, 211, 212, 213, 214, 215), CLOB16(216, 217, 218, 219, 220, 221, 222, 223, 224, 225, 226, 227, 228, 229, 230, 231), \
            CLOB16(232, 233, 234, 235, 236, 237, 238, 239, 240, 241, 242, 243, 244, 245, 246, 247)
        __builtin_amdgcn_s_setprio(2);
        {
            asm volatile(
                "v_add_u32 v246, 1088, %[rw]\n\t"
                S1_HEAD "s_nop 0\n\t" S1_TAIL("s_nop 1\n\t", "s_barrier\n\t")
                : LTOUT0
                : U_W, "{v[224:227]}"(af[0]), "{v[228:231]}"(af[1]), "{v[232:235]}"(af[2]), "{v[236:239]}"(af[3]), [sx] "v"(sidx), [rv] "v"(r1v), [rw] "v"(r1w)
                : "memory", "vcc", CLOB16(200, 201, 202, 203, 204, 205, 206, 207, 208, 209, 210, 211, 212, 213, 214, 215), CLOB16(216, 217, 218, 219, 220, 221, 222, 223, 240, 241, 242, 243, 244, 245, 246, 247));
        }
        const unsigned tau = __builtin_amdgcn_readfirstlane(*(volatile unsigned*)(chsm + C2_MB + 8));
        const unsigned long long wmask = u == 3 ? (c == 0 ? ~0ull : 0ull) : u == 1 ? 0x0020202020202000ull : u == 2 ? 1ull << (NSEG - 3) : 0ull;
        const int pvv = u == 3 ? 2 : -1, pvm = u == 3 ? -1 : 0;
        const unsigned* pfl = a.sync + (u == 3 ? SY_PROG + grp : u == 1 ? SY_KV + 16 + grp : SY_Q + 7 * 8 + grp) * 32;
        int s_first = 0;
        asm volatile("s_branch 1f\n\t.p2align 8\n1:" : "+s"(s_first));
        for (int s = s_first; s < NSEG - 1; ++s) {
            __builtin_amdgcn_s_setprio(0);
            LT(1)
            const int pv = (wmask >> s) & 1ull ? pvv + (s & pvm) : 0;
            const unsigned* pa = pfl;
            if (pv < 0) pfl += 256;
            const int cb = grp & 3;
            int soffc = rec_base(s) + C_OFF + c * 16384 + near_half(s, tau) + cb * 2048;
            int o_as = rec_base(s + 1) + ((c >> 3) + (grp >> 2) * 4) * 16384 + near_half(s + 1, tau) + ((4 * c + cb) & 31) * 256;
            int soff = chunk_off(s + 1, grp, u >> 1, tau) + (u & 1) * 4096;
            const int o_tm = (s >> 1) * 2097152 + TM_OFF + (s & 1) * 16384;
            int lim = dead ? 0 : SPIN_LIMIT, bad = 0;
            const bool rcd = u > 0 || widx < 32;
            const int pc = u > 0 ? 3 * widx + u - 1 : rcd ? 384 + widx : s * 96 + widx - 32;
            const int o_arm = (rcd ? s + 2 < NSEG : pc < 512) ? (rcd ? rec_base(s + 2) : XB_OFF) + (pc >> 3) * 16384 + near_half(rcd ? s + 2 : 0, tau) + (pc & 7) * 1024 : OOB;
            bool there = true;
            if (__builtin_expect(s <= KVS, 0)) {
                there = __all(ld_flag(kv_flag(a.sync, s + 2, grp, l)) != 0u);
                *(volatile unsigned*)(chsm + C2_MB + 32 + (s & 1) * 16 + u * 4) = there ? 1u : 0u;
                __builtin_amdgcn_s_waitcnt(0xC07F);
            }
            const char* vb = !there ? nullptr : arena(a.slots) + (s + 2 >= NSEG ? KX_OFF : VX_OFF) + (blk_off(min(s + 2, NSEG - 1), 4 * grp + 2 * u - 2, tau) ^ (s + 2 >= NSEG ? 8192 : 0));
            asm volatile("" : "+s"(soffc), "+s"(o_as), "+s"(soff), "+s"(lim), "+s"(vb));
#define GL(o) "global_load_lds_dwordx4 %[l16], s[84:85] offset:" o " sc1\n\t"
#define GL4 GL("0") GL("1024") GL("2048") GL("3072")
#define GLN(d) "s_add_u32 m0, m0, 4096\n\ts_add_u32 s84, s84, " d "\n\ts_addc_u32 s85, s85, 0\n\t"
#define PKE PAUSE(PKA)
#define C_TRY(x, x0, x1, x2, x3, y, y0, y1, y2, y3) \
                "s_waitcnt vmcnt(3)\n\t" \
                "v_mfma_f32_16x16x32_bf16 v[240:243], v[216:219], v[" x "], 0\n\t" CHK0(x0, x1, x2, x3) \
                "s_waitcnt vmcnt(2)\n\t" \
                "v_mfma_f32_16x16x32_bf16 v[240:243], v[216:219], v[" y "], v[240:243]\n\t" CHK(y0, y1, y2, y3) \
                "v_cmp_eq_u32 vcc, " STR(SENT) ", v244\n\t" \
                "s_cbranch_vccz 3b\n\t" CN(1) \
                LD4(x, "%[l16]", "%[sc]", "0") LD4(y, "%[l16]", "%[sc]", "1024")
            asm volatile(
                "v_and_b32 v221, 16, %[l16]\n\t"
                "v_mov_b32 v222, 0x3f80\n\t"
                "v_bfe_u32 v223, %[l16], 5, 2\n\t"
                "v_lshlrev_b32 v221, v221, v222\n\t"
                "v_cmp_eq_u32 vcc, 0, v223\n\tv_and_b32 v220, 0xf0, %[l16]\n\tv_bfe_u32 v222, %[l16], 8, 1\n\tv_cndmask_b32 v216, 0, v221, vcc\n\t"
                "v_cmp_eq_u32 vcc, 1, v223\n\tv_lshl_add_u32 v220, v222, 3, v220\n\tv_lshrrev_b32 v222, 9, %[l16]\n\tv_cndmask_b32 v217, 0, v221, vcc\n\t"
                "v_cmp_eq_u32 vcc, 2, v223\n\tv_lshl_add_u32 v220, v222, 30, v220\n\ts_nop 0\n\tv_cndmask_b32 v218, 0, v221, vcc\n\t"
                "v_cmp_eq_u32 vcc, 3, v223\n\ts_nop 1\n\tv_cndmask_b32 v219, 0, v221, vcc\n\t"
                "v_mul_u32_u24 v222, 0x8000, v222\n\t"
                "v_and_b32 v221, 0xff, v220\n\t"
                "v_add_u32 v221, v221, v222\n\t"
                "v_lshrrev_b32 v247, 2, %[l16]\n\t"
                PKE
                "v_mov_b32 v240, " STR(SENT) "\n\tv_mov_b32 v241, v240\n\tv_mov_b32 v242, v240\n\tv_mov_b32 v243, v240\n\t"
                "buffer_store_dwordx4 v[240:243], %[l16], %[rs], %[arm] offen sc1\n\t"
                LTA(2)
                "s_setprio 2\n\t"
                "s_barrier\n\t"
                LTA(3)
                "v_add_u32 v246, 1088, %[rw]\n\t"
                "s_cmp_lt_u32 %[uu], 1\n\t"
                "s_cbranch_scc1 7f\n\t"
                "s_cmp_eq_u32 %[uu], 3\n\t"
                "s_cbranch_scc1 20f\n\t"
                "s_memrealtime s[84:85]\n\t"
                "v_lshrrev_b32 v241, 9, %[l16]\n\t"
                "v_and_b32 v242, 0x7c, v247\n\t"
                "v_lshl_add_u32 v241, v241, 13, v242\n\t"
                "s_waitcnt lgkmcnt(0)\n\t"
                "v_mov_b32 v240, s84\n\t"
                "buffer_store_dword v240, v241, %[rs], %[ts] offen sc1\n\t"
                "s_cmp_eq_u64 %[vb], 0\n\t"
                "s_cbranch_scc1 11f\n\t"
                "s_mov_b32 s86, m0\n\t"
                "s_mov_b64 s[84:85], %[vb]\n\t"
                "s_mov_b32 m0, %[lv]\n\t"
                PAUSE(PG0)
                "s_cmp_eq_u32 %[uu], 1\n\t"
                "s_cbranch_scc1 12f\n\t"
                PAUSE(PG1)
                "12:\n\t"
                GL4 GLN("4096") GL4 GLN("12288") GL4 GLN("4096") GL4
                "s_mov_b32 m0, s86\n\t"
                "11:\n\t"
                "s_cmp_lt_i32 %[pv], 0\n\t"
                "s_cbranch_scc0 8f\n\t"
                "v_and_b32 v241, 12, v247\n\t"
                "10:\n\t"
                "global_load_dword v240, v241, %[pa] sc1\n\t"
                "s_waitcnt vmcnt(0)\n\t"
                "v_cmp_gt_u32 vcc, 16, v240\n\t"
                "s_cbranch_vccz 8f\n\t"
                "s_sleep 6\n\t"
                "s_sub_u32 %[lim], %[lim], 1\n\t"
                "s_cbranch_scc0 10b\n\t"
                "s_mov_b32 %[bad], 3000\n\t"
                "s_mov_b32 %[lim], 0\n\t"
                "s_branch 8f\n\t"
                "20:\n\t"
                PAUSE(PS)
                "s_memrealtime s[84:85]\n\t"
                "s_load_dword s86, %[tb], 0\n\ts_load_dword s87, %[tb], 0x100\n\ts_load_dword s88, %[tb], 0x200\n\ts_load_dword s89, %[tb], 0x300\n\t"
                "s_load_dword s90, %[tb], 0x400\n\ts_load_dword s91, %[tb], 0x500\n\ts_load_dword s92, %[tb], 0x600\n\ts_load_dword s93, %[tb], 0x700\n\t"
                "s_waitcnt lgkmcnt(0)\n\t"
                "s_sub_u32 s86, s84, s86\n\ts_sub_u32 s87, s84, s87\n\ts_sub_u32 s88, s84, s88\n\ts_sub_u32 s89, s84, s89\n\t"
                "s_sub_u32 s90, s84, s90\n\ts_sub_u32 s91, s84, s91\n\ts_sub_u32 s92, s84, s92\n\ts_sub_u32 s93, s84, s93\n\t"
                "s_or_b32 s85, s86, s87\n\ts_min_u32 s86, s86, s87\n\ts_or_b32 s85, s85, s88\n\ts_min_u32 s86, s86, s88\n\ts_or_b32 s85, s85, s89\n\ts_min_u32 s86, s86, s89\n\t"
                "s_memrealtime s[88:89]\n\t"
                "s_or_b32 s85, s85, s90\n\ts_min_u32 s86, s86, s90\n\ts_or_b32 s85, s85, s91\n\ts_min_u32 s86, s86, s91\n\ts_or_b32 s85, s85, s92\n\ts_min_u32 s86, s86, s92\n\t"
                "s_or_b32 s85, s85, s93\n\ts_min_u32 s86, s86, s93\n\t"
                "s_cmp_gt_u32 s85, 127\n\t"
                CNS
                "s_cselect_b32 s85, " STR(TE0) ", 127\n\t"
                "s_min_u32 s86, s86, s85\n\t"
                "s_sub_u32 s84, s84, s86\n\t"
                "s_add_u32 s84, s84, %[td]\n\t"
                "s_cmp_lt_i32 %[pv], 1\n\t"
                "s_cbranch_scc1 4f\n\t"
                "v_mov_b32 v247, %[pv]\n\t"
                "s_mov_b64 exec, 1\n\t"
                "global_store_dword %[l16], v247, %[pa] sc1\n\t"
                "s_mov_b64 exec, -1\n\t"
                "4:\n\t"
                "s_waitcnt lgkmcnt(0)\n\t"
                "s_sub_i32 s84, s84, s88\n\t"
                "s_max_i32 s84, s84, 0\n\t"
                "s_min_i32 s84, s84, 40\n\t"
                "21:\n\t"
                "s_sub_u32 s84, s84, 1\n\t"
                "s_cbranch_scc0 21b\n\t"
                "s_branch 8f\n\t"
                "7:\n\t"
                PAUSE(PC)
                LD4(A0, "%[l16]", "%[sc]", "0") LD4(A1, "%[l16]", "%[sc]", "1024")
                PAUSE(PP)
                LD4(A2, "%[l16]", "%[sc]", "0") LD4(A3, "%[l16]", "%[sc]", "1024")
                "s_waitcnt vmcnt(3)\n\t"
                "v_mfma_f32_16x16x32_bf16 v[240:243], v[216:219], v[224:227], 0\n\t" CHK0("224", "225", "226", "227")
                "s_waitcnt vmcnt(2)\n\t"
                "v_mfma_f32_16x16x32_bf16 v[240:243], v[216:219], v[228:231], v[240:243]\n\t" CHK("228", "229", "230", "231")
                "v_cmp_eq_u32 vcc, " STR(SENT) ", v244\n\t"
                "s_cbranch_vccnz 30f\n\t"
                "3:\n\t"
                "s_nop 3\n\t"
                "v_cvt_pk_bf16_f32 v244, v240, v241\n\t"
                "v_cvt_pk_bf16_f32 v245, v242, v243\n\t"
                "buffer_store_dwordx2 v[244:245], v221, %[rs], %[oas] offen sc1\n\t"
                PAUSE(PB)
                "8:\n\t"
                LTA(4)
                "s_barrier\n\t"
                PAUSE(1)
                "2:\n\t"
                LD4(A0, "%[l16]", "%[sa]", "0") LD4(A1, "%[l16]", "%[sa]", "1024") LD4(A2, "%[l16]", "%[sa]", "2048") LD4(A3, "%[l16]", "%[sa]", "3072")
                S1_HEAD
                "s_cbranch_vccnz 6f\n\t"
                "5:\n\t"
                LTA(5)
                S1_TAIL("s_waitcnt lgkmcnt(0)\n\ts_barrier\n\t", "")
                "s_setprio 0\n\t"
                "s_branch 9f\n\t"
                "30:\n\t" CN(1)
                LD4(A0, "%[l16]", "%[sc]", "0") LD4(A1, "%[l16]", "%[sc]", "1024")
                "1:\n\t"
                C_TRY(A2, "232", "233", "234", "235", A3, "236", "237", "238", "239")
                C_TRY(A0, "224", "225", "226", "227", A1, "228", "229", "230", "231")
                "s_sub_u32 %[lim], %[lim], 1\n\t"
                "s_cbranch_scc0 1b\n\t"
                "s_mov_b32 %[bad], 2000\n\t"
                "s_mov_b32 %[lim], 0\n\t"
                "s_branch 3b\n\t"
                "6:\n\t" CN(65536)
                "v_mov_b32 v240, %[tg]\n\t"
                "v_mov_b32 v241, %[lf]\n\t"
                "ds_write_b32 v241, v240\n\t"
                "s_nop 0\n\t"
                "s_sub_u32 %[lim], %[lim], 1\n\t"
                "s_cbranch_scc0 2b\n\t"
                "s_mov_b32 %[bad], 1\n\t"
                "s_branch 5b\n\t"
                "9:\n\t"
                : [lim] "+s"(lim), [bad] "+s"(bad), [oas] "+s"(o_as) LTOUT CNOUT
                : U_W, [l16] "v"(l * 16), [rs] "s"(rsS), [sx] "v"(sidx), [rv] "v"(r1v), [rw] "v"(r1w),
                  [sc] "s"(soffc), [sa] "s"(soff), [uu] "s"(u), [pv] "s"(pv), [pa] "s"(pa), [vb] "s"(vb), [lv] "s"(l0 + (s + 2 >= NSEG ? C2_V + 32768 : s & 1 ? C2_K : C2_V) + (u - 1) * 16384), [td] "s"(s < SE ? TDE : TD), [ts] "s"(c == 0 && (u == 2 || s < NSEG - 3) ? o_tm + grp * 256 + (2 - u) * 2097152 : OOB), [tb] "s"(arena(a.slots) + o_tm + near_half(s, tau)),

                  [arm] "s"(o_arm),
                  [tg] "s"(s + 1), [lf] "s"(l0 + C2_MB + 16)
                : "memory", "vcc", "scc", "s84", "s85", "s86", "s87", "s88", "s89", "s90", "s91", "s92", "s93", U_CLOB);
            if (__builtin_expect(bad != 0, 0)) TMO_SET((unsigned)bad)
        }
        last_rows(chsm, rsS, wu, grp, c, tau);
        if (u == 3 && widx == 0 && l == 0) __hip_atomic_store((gu64*)(a.boot + 144), *(volatile unsigned long long*)(chsm + C2_MB), __ATOMIC_RELAXED, __HIP_MEMORY_SCOPE_AGENT);
        return;
    }
    const int l = opaque_tid() & 63, i = l & 15, g = l >> 4;
    const int sidx = (i & 1) ? 0xEEEE : 0x4444;
    const int ha = 4 * grp + wu;
    unsigned long long tag;
    v32u wO[2];
    {
        u16* stg = (u16*)(chsm + 122880 + wu * 10240);
        tag = boot_tag(a.boot);
        asm volatile("" ::: "memory");
        v4u rawo[16];
        const u16* src = a.Wo + (size_t)(hcol(ha) + (l >> 3)) * DM + 64 * c + 8 * (l & 7);
#pragma unroll
        for (int e = 0; e < 16; ++e) rawo[e] = *(const v4u*)(src + (size_t)8 * e * DM);
        __builtin_amdgcn_sched_barrier(0);
        if (wu == 0) {
            asm volatile("s_waitcnt vmcnt(16)" ::: "memory");
            unsigned tm[2] = {~0u, ~0u};
            const char* pb = arena(a.slots) + cal_off;
#pragma unroll
            for (int k = 0; k < 2 * NPROBE; ++k) {
                unsigned long long c0, c1; unsigned d;
                asm volatile("s_memtime %0\n\ts_waitcnt lgkmcnt(0)\n\ts_load_dword %2, %3, %4\n\ts_waitcnt lgkmcnt(0)\n\ts_memtime %1\n\ts_waitcnt lgkmcnt(0)"
                             : "=&s"(c0), "=&s"(c1), "=&s"(d) : "s"(pb), "i"((k >> 1) * 256 + (k & 1) * 8192) : "memory");
                tm[k & 1] = min(tm[k & 1], (unsigned)(c1 - c0));
            }
            const unsigned side = (__builtin_amdgcn_s_getreg(20 | (3 << 11)) >> 2) & 1u;
            if (l == 0) __hip_atomic_store((gu64*)(a.boot + widx), (tag << 2) | (((tm[0] > tm[1] ? 1u : 0u) ^ side) << 1) | side, __ATOMIC_RELAXED, __HIP_MEMORY_SCOPE_AGENT);
            PST(7)
        }
#pragma unroll
        for (int m = 0; m < 2; ++m) {
#pragma unroll
            for (int e = 0; e < 8; ++e) *(v4u*)(stg + (8 * e + (l >> 3)) * 72 + 8 * (l & 7)) = rawo[8 * m + e];
            __builtin_amdgcn_s_waitcnt(0xC07F);
#pragma unroll
            for (int ct = 0; ct < 4; ++ct) {
                const u16* p = stg + (8 * g + (i >> 2)) * 72 + 16 * ct + 4 * (i & 3);
                const v2u r0 = tr4(p), r1 = tr4(p + 4 * 72), r2 = tr4(p + 32 * 72), r3 = tr4(p + 36 * 72);
                PUT4(wO[ct >> 1], (ct & 1) * 16 + m * 8, (v4u{r0[0], r0[1], r1[0], r1[1]})) PUT4(wO[ct >> 1], (ct & 1) * 16 + m * 8 + 4, (v4u{r2[0], r2[1], r3[0], r3[1]}))
            }
            __builtin_amdgcn_s_waitcnt(0xC07F);
        }
        if (wu == 0) { PST(5) }
        if (widx == 0 && wu == 0) *(volatile unsigned long long*)(chsm + C2_MB) = tag;
    }
    if (wu >= 2) touch_sc1(arena(a.slots) + (size_t)min(l + (wu - 2) * 64, FX_OFF / 2097152 - 1) * 2097152 + 2097024 - 128, l0 + C2_MB + 256);
    unsigned long long fw = 0;
#pragma nounroll
    for (int spin = 0; spin < 100000; ++spin) {
        asm volatile("" ::: "memory");
        fw = __hip_atomic_load((gu64*)(a.boot + 256 + 16 * grp + (l & 7)), __ATOMIC_RELAXED, __HIP_MEMORY_SCOPE_AGENT);
        if (__all((fw | 255u) == ((tag << 8) | 255u))) break;
        __builtin_amdgcn_s_sleep(4);
    }
    asm volatile("" ::: "memory");
    const unsigned tau = __builtin_amdgcn_readfirstlane((unsigned)fw & 255u);
    if (wu == 3) { *(volatile unsigned*)(chsm + C2_MB + 8) = tau; *(volatile unsigned*)(chsm + C2_MB + 16) = ~0u; __builtin_amdgcn_s_waitcnt(0xC07F); }
    if (wu == 1) { PST(9) }

    const unsigned r3a = l0 + C2_R3 + wu * 2048 + g * 128 + i * 4;
    v32u yb;
    v8u kx;
    v8u sel; v2u sel3;
#pragma unroll
    for (int d = 0; d < 8; ++d) sel[d] = (i >> 1) == d ? (0x3C00u << ((i & 1) * 16)) : 0u;
#pragma unroll
    for (int d = 0; d < 2; ++d) sel3[d] = (i & 3) == 0 && (i >> 3) == d ? (0x3C00u << (((i >> 2) & 1) * 16)) : 0u;
    asm volatile("s_setreg_imm32_b32 hwreg(HW_REG_MODE, 23, 1), 1\n\ts_nop 3");
#define MF(d, a, b, c) "v_mfma_f32_16x16x32_bf16 v[" d "], v[" a "], v[" b "], " c "\n\t"
#define PX4(d0, d1, d2, d3, a, w0, w1, w2, w3) MF(d0, a, w0, "v[" d0 "]") MF(d1, a, w1, "v[" d1 "]") MF(d2, a, w2, "v[" d2 "]") MF(d3, a, w3, "v[" d3 "]")
#define PXT(d0, d1, d2, d3, a0, a1, a2, a3) MF(d0, a0, "32:35", "0") MF(d1, a0, "48:51", "0") MF(d2, a0, "64:67", "0") MF(d3, a0, "80:83", "0") \
        PX4(d0, d1, d2, d3, a1, "36:39", "52:55", "68:71", "84:87") PX4(d0, d1, d2, d3, a2, "40:43", "56:59", "72:75", "88:91") PX4(d0, d1, d2, d3, a3, "44:47", "60:63", "76:79", "92:95")
#define CVP(d, a, b) "v_cvt_pk_bf16_f32 v" d ", v" a ", v" b "\n\t"
#define PROJ_X(cnt) \
        "s_waitcnt " cnt "(8)\n\t" PXT("192:195", "196:199", "200:203", "204:207", "160:163", "164:167", "168:171", "172:175") \
        "s_waitcnt " cnt "(4)\n\t" PXT("208:211", "212:215", "216:219", "220:223", "176:179", "180:183", "184:187", "188:191") \
        CVP("128", "192", "193") CVP("129", "194", "195") CVP("136", "196", "197") CVP("137", "198", "199") CVP("144", "200", "201") CVP("145", "202", "203") CVP("152", "204", "205") CVP("153", "206", "207") \
        CVP("132", "208", "209") CVP("133", "210", "211") CVP("140", "212", "213") CVP("141", "214", "215") CVP("148", "216", "217") CVP("149", "218", "219") CVP("156", "220", "221") CVP("157", "222", "223") \
        "s_waitcnt " cnt "(0)\n\t"
#define CLOB_PX CLOB16(160, 161, 162, 163, 164, 165, 166, 167, 168, 169, 170, 171, 172, 173, 174, 175), CLOB16(176, 177, 178, 179, 180, 181, 182, 183, 184, 185, 186, 187, 188, 189, 190, 191), \
        CLOB16(192, 193, 194, 195, 196, 197, 198, 199, 200, 201, 202, 203, 204, 205, 206, 207), CLOB16(208, 209, 210, 211, 212, 213, 214, 215, 216, 217, 218, 219, 220, 221, 222, 223)
#define LDX(d, va, so, o) "buffer_load_dwordx4 v[" d "], " va ", %[rs], " so " offen offset:" o " sc1\n\t"
#define DSV(d, o) "ds_read_b128 v[" d "], %[lv] offset:" o "\n\t"
    const int voffX = ((i >> 1) & 1) * 4096 + g * 256 + (2 * (i >> 2) + (i & 1)) * 16;
    const int pk = (((i >> 1) & 3) << 1) | (i >> 3);
    const unsigned hv = wu >= 2 ? l0 + C2_R1 + 2176 + ((wu & 1) * 128 + ((l >> 4) + 4 * ((l >> 3) & 1)) * 16 + 8 * ((l >> 1) & 1) + 4 * ((l >> 2) & 1) + 2 * (l & 1)) * 4
                                : l0 + C2_R1 + (i & 1) * 544 + (i >> 1) * 64 + g * 16;
    const unsigned hk = l0 + C2_R1 + 1088 + (i & 1) * 544 + pk * 64 + g * 16;
    const int vo1 = wu >= 2 ? l * 4 : wu == 1 ? (g >> 1) * 1024 + i * 16 + (g & 1) * 8 : (i & 1) ? 1 << 30 : (i >> 1) * 16 + 4 * g;
    const int vo2 = (2 * (i & 1) + (g >> 1)) * 128 + pk * 16 + (g & 1) * 8;
    const int voffK = (g >> 1) * 256 + i * 16 + (g & 1) * 8;
#define RD4(w, ad, a, b, c, d) "ds_read_" w " v[" a "], " ad "\n\tds_read_" w " v[" b "], " ad " offset:3200\n\tds_read_" w " v[" c "], " ad " offset:6400\n\tds_read_" w " v[" d "], " ad " offset:9600\n\t"
#define AD4(d0, d1, d2, d3, a0, a1, a2, a3) ADD(d0, d0, a0) ADD(d1, d1, a1) ADD(d2, d2, a2) ADD(d3, d3, a3)
#define QSC(r) "v_mul_f32 v" r ", 0x3e0293ee, v" r "\n\t"
#define FLD "buffer_load_dword v114, %[vf], %[rs], %[fl] offen sc1\n\t"
#define DUM "buffer_store_dword v168, off, %[rs], %[ob] sc1\n\t"
#define QHEAD "s_barrier\n\t" RD4("b128", "%[hv]", "160:163", "164:167", "168:171", "172:175") FLD \
        "s_waitcnt lgkmcnt(2)\n\t" AD4("160", "161", "162", "163", "164", "165", "166", "167") \
        "s_waitcnt lgkmcnt(0)\n\t" AD4("168", "169", "170", "171", "172", "173", "174", "175") \
        "s_barrier\n\t"
#define QTAIL AD4("160", "161", "162", "163", "168", "169", "170", "171") QSC("160") QSC("161") QSC("162") QSC("163")
#define CVH(d, a, b) "v_cvt_pk_f16_f32 v" d ", v" a ", v" b "\n\t"
#define SWA(d, a, b) "v_add_f32_dpp v" d ", v" a ", v" b " quad_perm:[1,0,3,2] row_mask:0xf bank_mask:0xf\n\t"
#define KLD(so) "buffer_load_dwordx2 v[96:97], %[vk], %[rs], " so " offen sc1\n\tbuffer_load_dwordx2 v[102:103], %[vk], %[rs], " so " offen offset:512 sc1\n\t" \
        "s_add_u32 " so ", " so ", 4096\n\t" \
        "buffer_load_dwordx2 v[100:101], %[vk], %[rs], " so " offen sc1\n\tbuffer_load_dwordx2 v[98:99], %[vk], %[rs], " so " offen offset:512 sc1\n\t"
    auto hand_over = [&](const int o_1, const int o_2, const int o_f, const int tg) {
        int dmy = 0;
        asm volatile(
            "s_cmp_eq_u32 %[wu], 0\n\t"
            "s_cbranch_scc1 2f\n\t"
            "s_cmp_eq_u32 %[wu], 1\n\t"
            "s_cbranch_scc1 1f\n\t"
            "s_barrier\n\t"
            RD4("b64", "%[hv]", "160:161", "162:163", "164:165", "166:167") FLD
            "s_waitcnt lgkmcnt(2)\n\t" ADD("160", "160", "162") ADD("161", "161", "163")
            "s_waitcnt lgkmcnt(0)\n\t" ADD("164", "164", "166") ADD("165", "165", "167") ADD("160", "160", "164") ADD("161", "161", "165")
            CVP("168", "160", "161")
            "buffer_store_dword v168, %[v1], %[rs], %[o1] offen sc1\n\t"
            "s_barrier\n\t"
            DUM PAUSE(PV2)
            "s_branch 3f\n\t"
            "5:\n\t"
            "v_mov_b32 v115, %[lf]\n\t"
            "ds_read_b32 v115, v115\n\t"
            "s_waitcnt lgkmcnt(0)\n\t"
            "v_cmp_eq_u32 vcc, %[tg], v115\n\t"
            "s_cbranch_vccnz 4f\n\t" CN(256)
            PAUSE(PL)
            "s_branch 4f\n\t"
            "1:\n\t"
            Z2("176:177") Z2("178:179")
            "s_waitcnt vmcnt(0)\n\t"
            QHEAD QTAIL
            "s_mov_b64 exec, %[em]\n\t"
            CVP("176", "160", "161") CVP("177", "162", "163")
            "s_not_b64 exec, exec\n\t"
            CVP("178", "160", "161") CVP("179", "162", "163")
            "s_mov_b64 exec, -1\n\t"
            "s_nop 0\n\t"
            MF("180:183", "96:99", "176:179", "0") MF("184:187", "100:103", "176:179", "0")
            "s_nop 7\n\t"
            SWA("180", "184", "180") SWA("181", "185", "181") SWA("182", "186", "182") SWA("183", "187", "183")
            CVH("188", "180", "181") CVH("189", "182", "183")
            "buffer_store_dwordx2 v[188:189], %[v1], %[rs], %[o1] offen sc1\n\t"
            DUM PAUSE(PV1)
            "s_branch 3f\n\t"
            "2:\n\t"
            QHEAD
            RD4("b128", "%[hk]", "176:179", "180:183", "184:187", "188:191")
            QTAIL CVP("164", "160", "161") CVP("165", "162", "163")
            "s_waitcnt lgkmcnt(2)\n\t" AD4("176", "177", "178", "179", "180", "181", "182", "183")
            "s_waitcnt lgkmcnt(0)\n\t" AD4("184", "185", "186", "187", "188", "189", "190", "191") AD4("176", "177", "178", "179", "184", "185", "186", "187")
            CVP("166", "176", "177") CVP("167", "178", "179")
            "s_nop 1\n\t"
            "v_mfma_f32_16x16x16_bf16 v[168:171], v[166:167], v[164:165], 0\n\t"
            "buffer_store_dwordx2 v[166:167], %[v2], %[rs], %[o2] offen sc1\n\t"
            "s_nop 6\n\t"
            SWA("172", "169", "168") SWA("173", "171", "170")
            CVH("174", "172", "173")
            "buffer_store_dword v174, %[v1], %[rs], %[o1] offen sc1\n\t"
            "3:\n\t"
            PAUSE(PVA)
            "s_waitcnt vmcnt(2)\n\t"
            "v_cmp_ne_u32 vcc, " STR(SENT) ", v114\n\t"
            "s_cbranch_vccnz 5b\n\t"
            "4:\n\t"
            : [dm] "+s"(dmy) CNOUT : "{v[96:103]}"(kx), [fl] "s"(o_f), [vf] "v"(i * 128), [ob] "s"(1 << 30), [tg] "s"(tg), [lf] "s"(l0 + C2_MB + 16), [wu] "s"(wu), [hv] "v"(hv), [hk] "v"(hk), [v1] "v"(vo1), [v2] "v"(vo2), [rs] "s"(rsS), [o1] "s"(o_1), [o2] "s"(o_2), [em] "s"(0x5555555555555555ull)
            : "memory", "scc", "vcc", "v114", "v115", CLOB16(160, 161, 162, 163, 164, 165, 166, 167, 168, 169, 170, 171, 172, 173, 174, 175), CLOB16(176, 177, 178, 179, 180, 181, 182, 183, 184, 185, 186, 187, 188, 189, 190, 191));
    };
    auto o1_of = [&](const int s) { return wu >= 2 ? v_off(s, hs, tau) + jq * 512 + (wu & 1) * 256 : wu == 1 ? x_off(s, hs, tau) + jq * 256 : m_off(s, hs, tau) + jq * 128; };

    __builtin_amdgcn_s_setprio(1);
    int o_f = f_off(0, grp, tau);
    int o_cs = rec_base(0) + C_OFF + c * 16384 + near_half(0, tau) + wu * 2048 + grp * 256;
    int o_1 = o1_of(0), o_2 = km_off(0, hs, tau) + jq * 512, bq = x_off(0, ha, tau), bm = m_off(0, ha, tau), bb = v_off(0, ha, tau);
    {
            const int b0 = blk_off(0, ha, tau);
            int k0 = KX_OFF + blk_off(0, hs, tau) + jq * 1024;
            asm volatile(
                LDX("160:163", "%[vx]", "%[vb]", "0") LDX("164:167", "%[vx]", "%[vb]", "1024") LDX("168:171", "%[vx]", "%[vb]", "2048") LDX("172:175", "%[vx]", "%[vb]", "3072")
                LDX("176:179", "%[vx]", "%[vb]", "128") LDX("180:183", "%[vx]", "%[vb]", "1152") LDX("184:187", "%[vx]", "%[vb]", "2176") LDX("188:191", "%[vx]", "%[vb]", "3200")
                KLD("%[k0]")
                "v_mov_b32 v131, 0\n\tv_mov_b32 v135, 0\n\tv_mov_b32 v139, 0\n\tv_mov_b32 v143, 0\n\tv_mov_b32 v147, 0\n\tv_mov_b32 v151, 0\n\tv_mov_b32 v155, 0\n\tv_mov_b32 v159, 0\n\t"
                PROJ_X("vmcnt")
                : "=&{v[128:159]}"(yb), "=&{v[96:103]}"(kx), [k0] "+s"(k0)
                : "{v[32:63]}"(wO[0]), "{v[64:95]}"(wO[1]), [vx] "v"(voffX), [vk] "v"(voffK), [rs] "s"(rsS), [vb] "s"(VX_OFF + b0)
                : "memory", "scc", CLOB_PX);
        if (wu == 1) { PST(10) }
    }
    int s_first = 0;
    asm volatile("s_branch 1f\n\t.p2align 8\n1:" : "+s"(s_first));
    for (int s = s_first; s < NSEG - 1; ++s) {
        hand_over(o_1, o_2, o_f, s);
        if (s == 0 && wu == 1) { PST(8) }
        LT(0)
#define LDPA "buffer_load_dwordx2 v[160:161], v254, %[rs], %[bm] offen sc1\n\t" LDX("164:167", "%[l16]", "%[bq]", "0") LDX("168:171", "%[l16]", "%[bq]", "1024")
#define SM(d, a, b, x) "v_smfmac_f32_16x16x64_bf16 v[" d "], v[" a "], v[" b "], " x "\n\t"
#define MX2(a, b) "v_max_u32 v244, v" a ", v" b "\n\t"
#define MX3(a, b) "v_max3_u32 v244, v244, v" a ", v" b "\n\t"
#define ZAM Z2("208:209") Z2("210:211") Z2("212:213") Z2("214:215") Z2("216:217") Z2("218:219") Z2("220:221") Z2("222:223")
#define MUL(d, a, b) "v_mul_f32 v" d ", v" a ", v" b "\n\t"
#define CV0(d, a) "v_cvt_pk_bf16_f32 v" d ", v" a ", 0\n\t"
#define RDC "ds_read_b64 v[224:225], %[rc]\n\tds_read_b64 v[226:227], %[rc] offset:2048\n\tds_read_b64 v[228:229], %[rc] offset:4096\n\tds_read_b64 v[230:231], %[rc] offset:6144\n\t"
#define EXD(d, r) "v_exp_f32 v" d ", v" r "\n\t"
#define SUBM(r) "v_sub_f32 v" r ", v" r ", v250\n\t"
#define PCVM CVP("241", "180", "180") CVP("243", "180", "180")
#define PCVX CVP("242", "178", "179") CVP("240", "176", "177")
#define OUT(t0, t1, a0, a1, a2, a3, ad, o0, o1) ADD(t0, a0, a1) ADD(t1, a2, a3) MUL(t0, t0, "250") MUL(t1, t1, "251") DW2(ad, t0, t1, o0, o1)
#define TS(n)
#define TSOUT
        int lim = dead ? 0 : SPIN_LIMIT, bad = 0;
        asm volatile(
            LDX("192:195", "%[l16]", "%[bb]", "0") LDX("196:199", "%[l16]", "%[bb]", "1024")
            ZAM Z2("224:225") Z2("226:227")
            Z2("16:17") Z2("18:19") Z2("20:21") Z2("22:23") Z2("24:25") Z2("26:27") Z2("28:29") Z2("30:31")
            "v_mov_b32 v200, 0x3f803f80\n\tv_mov_b32 v201, 0x3f803f80\n\tv_mov_b32 v202, 0x3f80\n\tv_mov_b32 v203, 0\n\t"
            "v_mov_b64 v[204:205], v[200:201]\n\tv_mov_b64 v[206:207], v[202:203]\n\t"
            "v_xor_b32 v252, 0xa000, %[sx]\n\tv_add_u32 v253, 1024, %[ra]\n\tv_lshrrev_b32 v254, 1, %[l16]\n\t"
            PAUSE(PV)
            TS(8)
            LDPA
            "4:\n\t"
            "s_waitcnt vmcnt(4)\n\t"
            SM("208:211", "192:195", "32:39", "%[sx]") MX2("192", "193") SM("212:215", "192:195", "48:55", "%[sx]") MX3("194", "195")
            SM("216:219", "192:195", "64:71", "%[sx]") SM("220:223", "192:195", "80:87", "%[sx]")
            "s_waitcnt vmcnt(3)\n\t"
            SM("208:211", "196:199", "40:47", "%[sx]") MX3("196", "197") SM("212:215", "196:199", "56:63", "%[sx]") MX3("198", "199")
            SM("216:219", "196:199", "72:79", "%[sx]") "v_cmp_eq_u32 vcc, " STR(SENT) ", v244\n\t" SM("220:223", "196:199", "88:95", "%[sx]")
            "s_cbranch_vccnz 5f\n\t"
            "1:\n\t" TS(11)
            "s_waitcnt vmcnt(2)\n\t"
            "v_mfma_f32_16x16x16_f16 v[236:239], v[112:113], v[160:161], 0\n\t"
            MX2("160", "161") ADD("245", "208", "209") ADD("246", "210", "211") CV0("130", "245") CV0("134", "246")
            ADD("245", "212", "213") ADD("246", "214", "215") CV0("138", "245") CV0("142", "246")
            EXD("180", "236")
            "s_waitcnt vmcnt(1)\n\t"
            "v_mfma_f32_16x16x32_f16 v[228:231], v[104:107], v[164:167], 0\n\t"
            MX3("164", "165") MX3("166", "167") ADD("245", "216", "217") ADD("246", "218", "219") CV0("146", "245") CV0("150", "246")
            ADD("245", "220", "221") ADD("246", "222", "223") CV0("154", "245") CV0("158", "246") PCVM
            "s_waitcnt vmcnt(0)\n\t"
            "v_mfma_f32_16x16x32_f16 v[228:231], v[108:111], v[168:171], v[228:231]\n\t"
            MX3("168", "169") MX3("170", "171")
            "v_cmp_eq_u32 vcc, " STR(SENT) ", v244\n\t"
            "s_cbranch_vccnz 7f\n\t"
            "2:\n\t" TS(9)
            "s_nop 3\n\t"
            EXD("179", "231") EXD("178", "230") EXD("177", "229") EXD("176", "228")
            PCVX
            "v_max3_f32 v250, |v236|, |v231|, |v230|\n\t"
            "v_max3_f32 v250, v250, |v229|, |v228|\n\t"
            SM("224:227", "240:243", "200:207", "v252")
            "v_cmp_nge_f32 vcc, 0x41c00000, v250\n\t"
            "s_nop 0\n\t"
            SM("16:19", "240:243", "128:135", "v252")
            "s_cbranch_vccnz 10f\n\t"
            "13:\n\t"
            "s_nop 0\n\t"
            SM("20:23", "240:243", "136:143", "v252") "s_nop 1\n\t"
            ADD("250", "224", "225") ADD("251", "226", "227")
            SM("24:27", "240:243", "144:151", "v252")
            "v_rcp_f32 v250, v250\n\t"
            SM("28:31", "240:243", "152:159", "v252")
            "v_rcp_f32 v251, v251\n\t"
            OUT("245", "246", "16", "17", "18", "19", "%[ra]", "0", "16")
            OUT("247", "248", "20", "21", "22", "23", "%[ra]", "128", "144")
            OUT("245", "246", "24", "25", "26", "27", "v253", "0", "16")
            OUT("247", "248", "28", "29", "30", "31", "v253", "128", "144")
            TS(10)
            "s_waitcnt lgkmcnt(0)\n\t"
            "s_barrier\n\t"
            RDC
            "v_lshrrev_b32 v247, 2, %[l16]\n\t"
            "s_waitcnt lgkmcnt(2)\n\t"
            ADD("224", "224", "226") ADD("225", "225", "227")
            "s_waitcnt lgkmcnt(0)\n\t"
            ADD("228", "228", "230") ADD("229", "229", "231") ADD("224", "224", "228") ADD("225", "225", "229")
            "v_cvt_pk_bf16_f32 v245, v224, v225\n\t"
            "buffer_store_dword v245, v247, %[rs], %[cs] offen sc1\n\t"
            "s_branch 9f\n\t"
            "10:\n\t"
            "v_max3_f32 v250, v236, v231, v230\n\t"
            "v_max3_f32 v250, v250, v229, v228\n\t"
            "s_nop 1\n\t"
            "v_max_f32_dpp v251, v250, v250 quad_perm:[1,0,3,2] row_mask:0xf bank_mask:0xf\n\t"
            "v_max_f32_dpp v244, v250, v250 quad_perm:[1,0,3,2] row_mask:0xf bank_mask:0xf\n\t"
            "s_nop 1\n\t"
            "v_permlane16_swap_b32 v251, v244\n\t"
            "v_max_f32 v250, v251, v244\n\t"
            "v_max_f32 v251, v251, v244\n\t"
            Z2("224:225") Z2("226:227")
            "v_permlane32_swap_b32 v250, v251\n\t"
            "v_max_f32 v250, v250, v251\n\t"
            Z2("16:17") Z2("18:19")
            SUBM("228") SUBM("229") SUBM("230") SUBM("231") SUBM("236")
            EXD("180", "236") EXD("179", "231") EXD("178", "230") EXD("177", "229") EXD("176", "228")
            PCVM PCVX
            "s_nop 1\n\t"
            SM("224:227", "240:243", "200:207", "v252") "s_nop 1\n\t"
            SM("16:19", "240:243", "128:135", "v252")
            "s_branch 13b\n\t"
            "5:\n\t" CN(1)
            LDX("192:195", "%[l16]", "%[bb]", "0") LDX("196:199", "%[l16]", "%[bb]", "1024")
            LDPA
            "s_sub_u32 %[lim], %[lim], 1\n\t"
            "s_nop 1\n\t"
            ZAM
            "s_cbranch_scc0 4b\n\t"
            "s_mov_b32 %[bad], 1500\n\t"
            "s_mov_b32 %[lim], 0\n\t"
            "s_branch 1b\n\t"
            "7:\n\t" CN(65536)
            LDPA
            "s_sub_u32 %[lim], %[lim], 1\n\t"
            "s_cbranch_scc0 1b\n\t"
            "s_mov_b32 %[bad], 1000\n\t"
            "s_branch 2b\n\t"
            "9:\n\t"
            : [lim] "+s"(lim), [bad] "+s"(bad), "+{v[128:159]}"(yb) TSOUT CNOUT
            : "{v[32:63]}"(wO[0]), "{v[64:95]}"(wO[1]), "{v[104:111]}"(sel), "{v[112:113]}"(sel3), [sx] "v"(sidx), [l16] "v"(l * 16),
              [ra] "v"(r3a), [rs] "s"(rsS), [bb] "s"(bb), [bq] "s"(bq), [bm] "s"(bm), [cs] "s"(o_cs), [rc] "v"(l0 + C2_R3 + wu * 512 + l * 8)
            : "memory", "vcc", "scc", CLOB_PX, CLOB16(16, 17, 18, 19, 20, 21, 22, 23, 24, 25, 26, 27, 28, 29, 30, 31),
              CLOB16(224, 225, 226, 227, 228, 229, 230, 231, 232, 233, 234, 235, 236, 237, 238, 239), CLOB16(240, 241, 242, 243, 244, 245, 246, 247, 248, 249, 250, 251, 252, 253, 254, 254));
        if (__builtin_expect(bad != 0, 0)) TMO_SET((unsigned)bad)
        __builtin_amdgcn_s_setprio(0);
        if (s == 0 && wu == 1) { PST(6) }

        if (__builtin_expect(s <= KVS + 1, 0)) {
            if (s == 0 || __builtin_amdgcn_readfirstlane(*(volatile unsigned*)(chsm + C2_MB + 32 + ((s - 1) & 1) * 16 + 4 + (wu >> 1) * 4)) == 0u) {
                wait_ge<6>(kv_flag(a.sync, s + 1, grp, l), 1u, a.tmo, 3001u);
                const char* kp = arena(a.slots) + VX_OFF + blk_off(s + 1, ha, tau);
#pragma unroll
                for (int e = 0; e < 8; ++e) glds16h((unsigned)l * 16u, kp + e * 1024, l0 + (s & 1 ? C2_V : C2_K) + wu * 8192 + e * 1024);
                asm volatile("s_waitcnt vmcnt(0)" ::: "memory");
            }
        }
        {
            int k0 = KX_OFF + blk_off(s + 1, hs, tau) + jq * 1024;
            asm volatile(
                DSV("160:163", "0") DSV("164:167", "1024") DSV("168:171", "2048") DSV("172:175", "3072") DSV("176:179", "128") DSV("180:183", "1152") DSV("184:187", "2176") DSV("188:191", "3200")
                "s_cmp_lg_u32 %[wu], 1\n\t"
                "s_cbranch_scc1 1f\n\t"
                KLD("%[k0]")
                "1:\n\t"
                "s_waitcnt lgkmcnt(4)\n\t"
                PXT("192:195", "196:199", "200:203", "204:207", "160:163", "164:167", "168:171", "172:175")
                "s_waitcnt lgkmcnt(0)\n\t"
                PXT("208:211", "212:215", "216:219", "220:223", "176:179", "180:183", "184:187", "188:191")
                CVP("128", "192", "193") CVP("129", "194", "195") CVP("136", "196", "197") CVP("137", "198", "199") CVP("144", "200", "201") CVP("145", "202", "203") CVP("152", "204", "205") CVP("153", "206", "207")
                CVP("132", "208", "209") CVP("133", "210", "211") CVP("140", "212", "213") CVP("141", "214", "215") CVP("148", "216", "217") CVP("149", "218", "219") CVP("156", "220", "221") CVP("157", "222", "223")
                : "+{v[128:159]}"(yb), "+{v[96:103]}"(kx), [k0] "+s"(k0)
                : "{v[32:63]}"(wO[0]), "{v[64:95]}"(wO[1]), [lv] "v"(l0 + (s & 1 ? C2_V : C2_K) + wu * 8192 + voffX), [vk] "v"(voffK), [rs] "s"(rsS), [wu] "s"(wu)
                : "memory", "scc", CLOB_PX);
        }
        __builtin_amdgcn_sched_barrier(0);
        o_1 = o1_of(s + 1); o_2 = km_off(s + 1, hs, tau) + jq * 512;
        bq = x_off(s + 1, ha, tau); bm = m_off(s + 1, ha, tau); bb = v_off(s + 1, ha, tau);
        o_f = f_off(s + 1, grp, tau);
        o_cs = rec_base(s + 1) + C_OFF + c * 16384 + near_half(s + 1, tau) + wu * 2048 + grp * 256;
        asm volatile("" : "+s"(o_cs), "+s"(o_f), "+s"(o_1), "+s"(o_2), "+s"(bq), "+s"(bm), "+s"(bb));
        __builtin_amdgcn_s_barrier();
        asm volatile(
            "s_cmp_lg_u32 %[wu], 3\n\t"
            "s_cbranch_scc1 1f\n\t"
            "v_mov_b32 v160, %[lf]\n\t"
            PAUSE(PF)
            "ds_read_b32 v160, v160\n\t"
            "s_waitcnt lgkmcnt(0)\n\t"
            "v_cmp_eq_u32 vcc, %[tg], v160\n\t"
            "s_cbranch_vccz 1f\n\t"
            "s_mov_b32 exec_hi, 0\n\t"
            "buffer_store_dword v160, %[l4], %[rs], %[fo] offen sc1\n\t"
            "s_mov_b64 exec, -1\n\t"
            "1:\n\t"
            :: [wu] "s"(wu), [lf] "s"(l0 + C2_MB + 16), [tg] "s"(s + 1), [l4] "v"(l * 4), [rs] "s"(rsS), [fo] "s"(o_f + c * 128)
            : "memory", "scc", "vcc", "v160");
        __builtin_amdgcn_s_setprio(1);
    }
    {
        constexpr int s = NSEG - 1;
        const int l = lane_now(), i = l & 15, g = l >> 4;
        v4u qf[8], kf[8], pv[2], px[4];
        asm volatile("s_waitcnt vmcnt(0)" ::: "memory");
        __builtin_amdgcn_s_sleep(4);
#pragma unroll
        for (int e = 0; e < 8; ++e) kf[e] = ld_sc1s(rsS, l * 16, KX_OFF + blk_off(s, ha, tau) + e * 1024);
        hand_over(o_1, o_2, o_f, s);
        const int bk = km_off(s, ha, tau);
        auto ask = [&]() {
            asm volatile("" ::: "memory");
            pv[0] = ld_sc1s(rsS, l * 16, bb); pv[1] = ld_sc1s(rsS, l * 16, bb + 1024);
#pragma unroll
            for (int ks = 0; ks < 4; ++ks) px[ks] = ld_sc1s(rsS, g * 128 + (i >> 1) * 16, bk + ks * 512);
            asm volatile("" ::: "memory");
        };
        asm volatile(PAUSE(PLL) ::: "memory");
        ask();
#pragma unroll
        for (int e = 0; e < 8; ++e) qf[e] = *(const v4u*)(chsm + C2_V + 32768 + wu * 8192 + e * 1024 + l * 16);
        f32x4 sx[3] = {ZERO4, ZERO4, ZERO4};
        float mxx[2] = {-3.0e38f, -3.0e38f};
#pragma unroll
        for (int ks = 0; ks < 4; ++ks) {
            sx[0] = MM(kf[ks], qf[ks], sx[0]);
            sx[1] = MM(kf[ks], qf[4 + ks], sx[1]);
            sx[2] = MM(kf[4 + ks], qf[4 + ks], sx[2]);
        }
#pragma unroll
        for (int r = 0; r < 4; ++r) {
            if (4 * g + r > i) { sx[0][r] = -3.0e38f; sx[2][r] = -3.0e38f; }
            mxx[0] = fmaxf(mxx[0], sx[0][r]);
            mxx[1] = fmaxf(mxx[1], fmaxf(sx[1][r], sx[2][r]));
        }
        asm volatile("" : "+v"(sx[0]), "+v"(sx[1]), "+v"(sx[2]), "+v"(mxx[0]), "+v"(mxx[1]));
#pragma nounroll
        for (int spin = 0;; ++spin) {
            if (__all(mx4(mx4(mx4(mx4(mx4(mx4(0u, pv[0]), pv[1]), px[0]), px[1]), px[2]), px[3]) != SENT)) break;
            if (dead) break;
            if (spin > SPIN_LIMIT) { TMO_SET(1700u) break; }
            ask();
        }
#define SUB8(v, o) v8u{(v)[(o)], (v)[(o) + 1], (v)[(o) + 2], (v)[(o) + 3], (v)[(o) + 4], (v)[(o) + 5], (v)[(o) + 6], (v)[(o) + 7]}
#pragma unroll
        for (int ct = 0; ct < 4; ++ct) {
            f32x4 am = smm(pv[0], SUB8(wO[ct >> 1], (ct & 1) * 16), ZERO4, sidx);
            am = smm(pv[1], SUB8(wO[ct >> 1], (ct & 1) * 16 + 8), am, sidx);
            yb[8 * ct + 2] = cvt_pk_bf16(am[0] + am[1], 0.f); yb[8 * ct + 6] = cvt_pk_bf16(am[2] + am[3], 0.f);
        }
        const v8u one = {0x3F803F80u, 0x3F803F80u, 0x3F80u, 0u, 0x3F803F80u, 0x3F803F80u, 0x3F80u, 0u};
        const float SCL2E = 0.08838834764831845f * 1.4426950408889634f;
        float* xo = (float*)(chsm + C2_K) + (wu * 32 + 4 * g) * XP + i;
#pragma unroll
        for (int qt = 0; qt < 2; ++qt) {
            f32x4 sc[3] = {sx[qt], sx[2], ZERO4};
#pragma unroll
            for (int ks = 0; ks < 4; ++ks) sc[2] = MM(px[ks], qf[4 * qt + ks], sc[2]);
            float mx = fmaxf(mxx[qt], fmaxf(sc[2][1], sc[2][3]));
            mx = xor32_max(xor16_max(mx));
            const float nmx = -mx * SCL2E;
#define PE(x) __builtin_amdgcn_exp2f(fmaf(x, SCL2E, nmx))
            const float m0 = PE(sc[2][1]), m1 = PE(sc[2][3]);
            const v4u ae = {cvt_pk_bf16(PE(sc[0][0]), PE(sc[0][1])), cvt_pk_bf16(m0, m0), cvt_pk_bf16(PE(sc[0][2]), PE(sc[0][3])), 0u};
            const v4u ao = {qt ? cvt_pk_bf16(PE(sc[1][0]), PE(sc[1][1])) : 0u, 0u, qt ? cvt_pk_bf16(PE(sc[1][2]), PE(sc[1][3])) : 0u, cvt_pk_bf16(m1, m1)};
            const f32x4 sum = smm(ao, one, smm(ae, one, ZERO4, 0xE444), 0x4EEE);
#pragma unroll
            for (int ct = 0; ct < 4; ++ct) {
                const f32x4 o = smm(ao, SUB8(yb, 8 * ct), smm(ae, SUB8(yb, 8 * ct), ZERO4, 0xE444), 0x4EEE);
#pragma unroll
                for (int r = 0; r < 4; ++r) xo[(16 * qt + r) * XP + 16 * ct] = o[r] * __builtin_amdgcn_rcpf(sum[r]);
            }
        }
        __builtin_amdgcn_s_waitcnt(0xC07F);
        last_rows(chsm, rsS, wu, grp, c, tau);
        if (wu < 2) {
            v4u sel, p0, p1;
#pragma unroll
            for (int d = 0; d < 4; ++d) sel[d] = (l & 6) == 2 * d ? (0x3F80u << ((l & 1) * 16)) : 0u;
            const int xb = XB_OFF + ((c >> 1) * 8 + grp) * 16384 + (int)(tau & 1u) * 8192 + (c & 1) * 4096 + wu * 256;
            __builtin_amdgcn_s_sleep(DLY_C);
#pragma nounroll
            for (int spin = 0;; ++spin) {
                asm volatile("" ::: "memory");
                p0 = ld_sc1s(rsS, g * 512 + i * 16, xb); p1 = ld_sc1s(rsS, g * 512 + i * 16, xb + 2048);
                if (__all(mx4(mx4(0u, p0), p1) != SENT)) break;
                if (dead) break;
                if (spin > SPIN_LIMIT) { TMO_SET(1800u) break; }
            }
            const f32x4 d = MM(sel, p1, MM(sel, p0, ZERO4));
            if (g < 2) out8(a.out + (size_t)(s * SEG + 4 * grp + 2 * wu + (i >> 3)) * OUTW + 64 * c + 8 * (i & 7) + 4 * g, v2u{cvt_pk_bf16(d[0], d[1]), cvt_pk_bf16(d[2], d[3])});
        }
    }
}

struct cpx { float x, y; };
__device__ __forceinline__ cpx cmul(cpx a, cpx b) { return {a.x * b.x - a.y * b.y, a.x * b.y + a.y * b.x}; }
__device__ __forceinline__ cpx cadd(cpx a, cpx b) { return {a.x + b.x, a.y + b.y}; }
__device__ __forceinline__ cpx csub(cpx a, cpx b) { return {a.x - b.x, a.y - b.y}; }
template <int DIR> __device__ __forceinline__ cpx mul_i(cpx a) {
    return (DIR < 0) ? cpx{a.y, -a.x} : cpx{-a.y, a.x};
}
template <int DIR> __device__ __forceinline__ void r4(cpx& a, cpx& b, cpx& c, cpx& d) {
    cpx t0 = cadd(a, c), t1 = csub(a, c), t2 = cadd(b, d), t3 = mul_i<DIR>(csub(b, d));
    a = cadd(t0, t2); b = cadd(t1, t3); c = csub(t0, t2); d = csub(t1, t3);
}
template <int DIR> __device__ __forceinline__ cpx twc(float c, float s) { return cpx{c, (float)DIR * s}; }
template <int DIR> __device__ __forceinline__ void dft16(cpx (&v)[16]) {
#pragma unroll
    for (int a = 0; a < 4; ++a) r4<DIR>(v[a], v[a + 4], v[a + 8], v[a + 12]);
    const float C1 = 0.92387953251128674f, S1 = 0.38268343236508977f, R2 = 0.70710678118654752f;
    v[1 + 4] = cmul(v[1 + 4], twc<DIR>(C1, S1));
    v[1 + 8] = cmul(v[1 + 8], twc<DIR>(R2, R2));
    v[1 + 12] = cmul(v[1 + 12], twc<DIR>(S1, C1));
    v[2 + 4] = cmul(v[2 + 4], twc<DIR>(R2, R2));
    v[2 + 8] = mul_i<DIR>(v[2 + 8]);
    v[2 + 12] = cmul(v[2 + 12], twc<DIR>(-R2, R2));
    v[3 + 4] = cmul(v[3 + 4], twc<DIR>(S1, C1));
    v[3 + 8] = cmul(v[3 + 8], twc<DIR>(-R2, R2));
    v[3 + 12] = cmul(v[3 + 12], twc<DIR>(-C1, -S1));
#pragma unroll
    for (int d = 0; d < 4; ++d) r4<DIR>(v[4 * d], v[4 * d + 1], v[4 * d + 2], v[4 * d + 3]);
}
__device__ __forceinline__ constexpr int x16(int q) { return 4 * (q & 3) + (q >> 2); }
template <int DIR> __device__ __forceinline__ void dft8(cpx (&v)[8]) {
    r4<DIR>(v[0], v[2], v[4], v[6]);
    r4<DIR>(v[1], v[3], v[5], v[7]);
    const float R2 = 0.70710678118654752f;
    v[3] = cmul(v[3], twc<DIR>(R2, R2));
    v[5] = mul_i<DIR>(v[5]);
    v[7] = cmul(v[7], twc<DIR>(-R2, R2));
#pragma unroll
    for (int d = 0; d < 4; ++d) { cpx p = v[2 * d], q = v[2 * d + 1]; v[2 * d] = cadd(p, q); v[2 * d + 1] = csub(p, q); }
}
__device__ __forceinline__ int pad(int i) { return i + (i >> 4); }
__device__ __forceinline__ cpx expi(float x) { float s, c; sincospif(x, &s, &c); return cpx{c, s}; }
template <int R, int DIR> __device__ __forceinline__ void twiddle(cpx* v, cpx w1) {
    cpx w[R];
    w[1] = w1;
#pragma unroll
    for (int r = 2; r < R; ++r) w[r] = (r & 1) ? cmul(w[r - 1], w1) : cmul(w[r / 2], w[r / 2]);
#pragma unroll
    for (int r = 1; r < R; ++r) v[r] = DIR < 0 ? cmul(v[r], w[r]) : cpx{v[r].x * w[r].x + v[r].y * w[r].y, v[r].y * w[r].x - v[r].x * w[r].y};
}
struct TwBase { cpx b; cpx c[4]; };
template <int N> __device__ __forceinline__ TwBase make_tw(int t) {
    TwBase tb;
    tb.b = expi(-(float)(t & 15) * (1.0f / 128.0f));
    constexpr int NQ = (N == 1024) ? 4 : 2, T = N / 16;
#pragma unroll
    for (int q = 0; q < NQ; ++q) tb.c[q] = expi(-(float)(t + T * q) * (2.0f / (float)N));
    return tb;
}
template <int N> __device__ __forceinline__ void fft_sync() {
    if constexpr (N == 1024) { __builtin_amdgcn_fence(__ATOMIC_ACQ_REL, "wavefront"); __builtin_amdgcn_wave_barrier(); } else __syncthreads();
}
template <int N, int DIR, bool RIN, bool ROUT>
__device__ __forceinline__ void fft_reg(cpx* buf, const TwBase& tb, int t, cpx (&v)[16]) {
    constexpr int T = N / 16, TP = T + T / 16;
    cpx* const bt = buf + pad(t);
    if constexpr (!RIN) {
#pragma unroll
        for (int r = 0; r < 16; ++r) v[r] = bt[TP * r];
        fft_sync<N>();
    }
    dft16<DIR>(v);
#pragma unroll
    for (int q = 0; q < 16; ++q) buf[17 * t + x16(q)] = v[q];
    fft_sync<N>();
    {
#pragma unroll
        for (int r = 0; r < 16; ++r) v[r] = bt[TP * r];
        fft_sync<N>();
        twiddle<16, DIR>(v, tb.b);
        dft16<DIR>(v);
        cpx* const bo = buf + (t >> 4) * 272 + (t & 15);
#pragma unroll
        for (int q = 0; q < 16; ++q) bo[17 * x16(q)] = v[q];
        fft_sync<N>();
    }
    if constexpr (N == 1024) {
#pragma unroll
        for (int q = 0; q < 4; ++q)
#pragma unroll
            for (int r = 0; r < 4; ++r) v[4 * q + r] = bt[68 * q + 272 * r];
#pragma unroll
        for (int q = 0; q < 4; ++q) {
            twiddle<4, DIR>(&v[4 * q], tb.c[q]);
            r4<DIR>(v[4 * q], v[4 * q + 1], v[4 * q + 2], v[4 * q + 3]);
#pragma unroll
            for (int r = 0; r < 4; ++r) bt[68 * q + 272 * r] = v[4 * q + r];
        }
        fft_sync<N>();
    } else {
        cpx u[2][8];
#pragma unroll
        for (int q = 0; q < 2; ++q)
#pragma unroll
            for (int r = 0; r < 8; ++r) u[q][r] = bt[136 * q + 272 * r];
        if constexpr (ROUT) fft_sync<N>();
#pragma unroll
        for (int q = 0; q < 2; ++q) {
            twiddle<8, DIR>(u[q], tb.c[q]);
            dft8<DIR>(u[q]);
#pragma unroll
            for (int d = 0; d < 4; ++d) {
                if constexpr (ROUT) { v[q + 2 * d] = u[q][2 * d]; v[q + 2 * d + 8] = u[q][2 * d + 1]; }
                else { bt[136 * q + 272 * d] = u[q][2 * d]; bt[136 * q + 272 * (d + 4)] = u[q][2 * d + 1]; }
            }
        }
        if constexpr (!ROUT) fft_sync<N>();
    }
}
__device__ __forceinline__ int f_hi(int rb, int j, unsigned tw) { j = (j + rb) & 7; return rb * 65536 + (j >> 1) * 16384 + (j & 1) * 4096 + (int)((tw ^ (unsigned)(rb >> 5)) & 1u) * 8192; }
__device__ __forceinline__ int f_lo(int rb, int x) { return (x + rb * 128) & 4095; }
__device__ __forceinline__ int w_hi(int k, int j, unsigned tw) { return k * 16384 + ((j + k) & 15) * 512 + (int)((tw ^ (unsigned)(k >> 7)) & 1u) * 8192; }
__device__ __forceinline__ int w_lo(int k, int x) { return (x + k * 128) & 511; }
constexpr int PB1 = 1024 + 64, PB2 = 2048 + 128;
typedef float f32x4v __attribute__((ext_vector_type(4)));
typedef unsigned u32x4v __attribute__((ext_vector_type(4)));

__device__ __forceinline__ void fft_rows_fwd(const u16* __restrict__ x, rsrc_t rsF, const unsigned tw, int rb, char* smem, int tid, const char* wr, const char* wi, const unsigned spare) {
    cpx (*buf)[PB1] = (cpx (*)[PB1])smem;
    int f = tid >> 6, t = tid & 63;
    const u16* p0 = x + (size_t)(rb * 8 + 2 * f) * 1024 + 16 * t;
    u32x4v a0 = *(const u32x4v*)p0, a1 = *(const u32x4v*)(p0 + 8), b0 = *(const u32x4v*)(p0 + 1024), b1 = *(const u32x4v*)(p0 + 1032);
    const TwBase tb = make_tw<1024>(t);
    for (int it = 0; it < 4; ++it, rb += 64) {
        asm volatile("" : "+v"(tid));
        f = tid >> 6; t = tid & 63;
        __syncthreads();
        cpx* d = &buf[f][17 * t];
#pragma unroll
        for (int e = 0; e < 4; ++e) {
            d[2 * e] = cpx{__uint_as_float(a0[e] << 16), __uint_as_float(b0[e] << 16)};
            d[2 * e + 1] = cpx{__uint_as_float(a0[e] & 0xFFFF0000u), __uint_as_float(b0[e] & 0xFFFF0000u)};
            d[8 + 2 * e] = cpx{__uint_as_float(a1[e] << 16), __uint_as_float(b1[e] << 16)};
            d[8 + 2 * e + 1] = cpx{__uint_as_float(a1[e] & 0xFFFF0000u), __uint_as_float(b1[e] & 0xFFFF0000u)};
        }
        if (it < 3) p0 += 512 * 1024;
        touch(((it & 1) ? wi : wr) + (size_t)((rb >> 1) & 31) * 32832 + (it >> 1) * 32 * 32832 + ((rb & 1) * 128 + (tid >> 1)) * 128, spare);
        a0 = *(const u32x4v*)p0; a1 = *(const u32x4v*)(p0 + 8); b0 = *(const u32x4v*)(p0 + 1024); b1 = *(const u32x4v*)(p0 + 1032);
        fft_sync<1024>();
        cpx z[16];
        fft_reg<1024, -1, false, false>(buf[f], tb, t, z);
        __syncthreads();
        const cpx* pk = &buf[tid & 3][pad(tid >> 2)];
        const cpx* pn = &buf[tid & 3][pad(1024 - (tid >> 2))];
        const int vo = f_lo(rb, tid * 16);
#pragma unroll
        for (int m = 0; m < 8; ++m) {
            const cpx zk = pk[68 * m], zn = m == 0 && tid < 4 ? pk[544] : pn[-68 * m];
            f32x4v o = {zk.x + zn.x, zk.y - zn.y, zk.y + zn.y, zn.x - zk.x};
            if (m == 0 && tid < 4) o = f32x4v{2.f * zk.x, 2.f * zn.x, 2.f * zk.y, 2.f * zn.y};
            st_sc1s(rsF, vo, f_hi(rb, m, tw), __builtin_bit_cast(v4u, o));
        }
    }
}
__device__ __forceinline__ void filt_transpose(const u16* __restrict__ wr, const u16* __restrict__ wi, rsrc_t rsW, const unsigned tw, const int tb, char* smem, const int tid) {
    const v4u* s0 = (const v4u*)(wr + (size_t)tb * 32 * 513);
    const v4u* s1 = (const v4u*)(wi + (size_t)tb * 32 * 513);
    v4u r0[5], r1[5];
#pragma unroll
    for (int e = 0; e < 5; ++e) {
        const int idx = tid + 512 * e, ic = idx < 2052 ? idx : 2051;
        r0[e] = s0[ic]; r1[e] = s1[ic];
    }
#pragma unroll
    for (int e = 0; e < 5; ++e) {
        const int idx = tid + 512 * e;
        if (idx < 2052) { *(v4u*)(smem + idx * 16) = r0[e]; *(v4u*)(smem + 32832 + idx * 16) = r1[e]; }
    }
    __syncthreads();
    const u16* a0 = (const u16*)smem;
    const u16* a1 = (const u16*)(smem + 32832);
    for (int idx = tid; idx < 513 * 8; idx += 512) {
        const int k = idx >> 3, n4 = (idx & 7) * 4;
        v4u v;
#pragma unroll
        for (int j = 0; j < 4; ++j) v[j] = (unsigned)a0[(n4 + j) * 513 + k] | ((unsigned)a1[(n4 + j) * 513 + k] << 16);
        st_sc1(rsW, w_hi(k, tb >> 2, tw) + w_lo(k, (tb & 3) * 128 + n4 * 4), v);
    }
}

__device__ __forceinline__ void fft_cols(rsrc_t rsF, rsrc_t rsW, const unsigned tw, int blk, const int n, char* smem, int tid) {
    cpx (*buf)[PB2] = (cpx (*)[PB2])smem;
    int c = tid >> 7, t = tid & 127;
    f32x4v v[8];
    unsigned wv[16], wn[16];
    auto wld = [&](const int k, const int n) { return __builtin_amdgcn_raw_buffer_load_b32(rsW, w_hi(k, n >> 7, tw) + w_lo(k, (n & 127) * 4), 0, 0); };
    auto f_at = [&](const int bk) { return f_hi(tid >> 3, bk >> 5, tw) + f_lo(tid >> 3, (bk & 31) * 128 + (tid & 7) * 16); };
    auto fetch = [&](const int bk) {
        const int kc = bk * 2 + __builtin_amdgcn_readfirstlane(c), vo = f_at(bk), vw = w_lo(kc, t * 4);
#pragma unroll
        for (int m = 0; m < 8; ++m) v[m] = __builtin_bit_cast(f32x4v, __builtin_amdgcn_raw_buffer_load_b128(rsF, m & 1 ? vo ^ 8192 : vo, m * 2097152, 0));
#pragma unroll
        for (int m = 0; m < 16; ++m) wn[m] = __builtin_amdgcn_raw_buffer_load_b32(rsW, vw, w_hi(kc, m, tw), 0);
    };
    fetch(blk);
    const TwBase tb = make_tw<2048>(t);
    for (int it = 0; it < n; ++it, blk += 64) {
        asm volatile("" : "+v"(tid));
        c = tid >> 7; t = tid & 127;
        __syncthreads();
        cpx* const d = &buf[(tid >> 2) & 1][pad((tid >> 3) * 8 + 2 * (tid & 3))];
#pragma unroll
        for (int m = 0; m < 8; ++m) { d[272 * m] = cpx{v[m][0], v[m][1]}; d[272 * m + 1] = cpx{v[m][2], v[m][3]}; }
#pragma unroll
        for (int m = 0; m < 16; ++m) wv[m] = wn[m];
        fetch(it + 1 < n ? blk + 64 : blk);
        __syncthreads();
        cpx z[16];
        fft_reg<2048, -1, false, true>(buf[c], tb, t, z);
        auto wcx = [](const unsigned w) { return cpx{__uint_as_float(w << 16), __uint_as_float(w & 0xFFFF0000u)}; };
        if (blk < 2) {
#pragma unroll
            for (int m = 0; m < 16; ++m) buf[c][pad(t + 128 * m)] = z[m];
            __syncthreads();
            if (blk == 0 && c == 0) {
#pragma unroll
                for (int m = 0; m < 16; ++m) {
                    const int n = t + 128 * m, nn = (2048 - n) & 2047;
                    const cpx zn = buf[0][pad(nn)], a = wcx(wv[m]), b = wcx(wld(0, nn)), e = wcx(wld(512, n)), f = wcx(wld(512, nn));
                    const cpx h0 = cmul(cpx{0.5f * (z[m].x + zn.x), 0.5f * (z[m].y - zn.y)}, cpx{0.5f * (a.x + b.x), 0.5f * (a.y - b.y)});
                    const cpx h5 = cmul(cpx{0.5f * (z[m].y + zn.y), -0.5f * (z[m].x - zn.x)}, cpx{0.5f * (e.x + f.x), 0.5f * (e.y - f.y)});
                    z[m] = cpx{h0.x - h5.y, h0.y + h5.x};
                    wv[m] = 0x3F80u;
                }
            }
            __syncthreads();
        }
#pragma unroll
        for (int m = 0; m < 16; ++m) z[m] = cmul(z[m], wcx(wv[m]));
        fft_reg<2048, 1, true, false>(buf[c], tb, t, z);
        const int vo = f_at(blk);
#pragma unroll
        for (int m = 0; m < 8; ++m)
            st_sc1s(rsF, m & 1 ? vo ^ 8192 : vo, m * 2097152, __builtin_bit_cast(v4u, f32x4v{d[272 * m].x, d[272 * m].y, d[272 * m + 1].x, d[272 * m + 1].y}));
    }
}

__device__ __forceinline__ void fft_rows_inv(rsrc_t rsF, const unsigned tw, u16* __restrict__ out, int rb, const int step, const int n, char* smem, int tid) {
    cpx (*buf)[PB1] = (cpx (*)[PB1])smem;
    int f = tid >> 6, t = tid & 63;
    f32x4v v[8];
    auto fetch = [&](const int r) {
        const int vo = f_lo(r, tid * 16);
#pragma unroll
        for (int m = 0; m < 8; ++m) v[m] = __builtin_bit_cast(f32x4v, __builtin_amdgcn_raw_buffer_load_b128(rsF, vo, f_hi(r, m, tw), 0));
    };
    fetch(rb);
    const TwBase tb = make_tw<1024>(t);
    for (int it = 0; it < n; ++it, rb += step) {
        asm volatile("" : "+v"(tid));
        f = tid >> 6; t = tid & 63;
        __syncthreads();
        cpx* pk = &buf[tid & 3][pad(tid >> 2)];
        cpx* pn = &buf[tid & 3][pad(1024 - (tid >> 2))];
#pragma unroll
        for (int m = 0; m < 8; ++m) {
            if (m == 0 && tid < 4) { pk[0] = cpx{v[0][0], v[0][2]}; pk[544] = cpx{v[0][1], v[0][3]}; }
            else { pk[68 * m] = cpx{v[m][0] - v[m][3], v[m][1] + v[m][2]}; pn[-68 * m] = cpx{v[m][0] + v[m][3], v[m][2] - v[m][1]}; }
        }
        fetch(it + 1 < n ? rb + step : rb);
        __syncthreads();
        cpx z[16];
        fft_reg<1024, 1, false, false>(buf[f], tb, t, z);
        const float sc = 0.5f / (1024.0f * 2048.0f);
        const cpx* d = &buf[f][17 * t];
        u32x4v a[2], b[2];
#pragma unroll
        for (int h = 0; h < 2; ++h)
#pragma unroll
            for (int e = 0; e < 4; ++e) {
                cpx p = d[8 * h + 2 * e], q = d[8 * h + 2 * e + 1];
                a[h][e] = cvt_pk_bf16(p.x * sc, q.x * sc);
                b[h][e] = cvt_pk_bf16(p.y * sc, q.y * sc);
            }
        u16* o0 = out + (size_t)(rb * 8 + 2 * f) * OUTW + 1024 + 16 * t;
        *(u32x4v*)o0 = a[0]; *(u32x4v*)(o0 + 8) = a[1];
        *(u32x4v*)(o0 + OUTW) = b[0]; *(u32x4v*)(o0 + OUTW + 8) = b[1];
    }
}

__device__ __forceinline__ void glds16s(unsigned voff, const void* sbase, unsigned lds_dst) {
    unsigned keep;
    asm volatile("s_mov_b32 %0, m0\n\ts_mov_b32 m0, %3\n\ts_nop 0\n\tglobal_load_lds_dwordx4 %1, %2\n\ts_mov_b32 m0, %0" : "=&s"(keep) : "v"(voff), "s"(sbase), "s"(lds_dst) : "memory");
}
#define K8_BAR do { asm volatile("" ::: "memory"); __builtin_amdgcn_s_barrier(); asm volatile("" ::: "memory"); } while (0)
#define K8_WV(n) asm volatile("s_waitcnt vmcnt(" #n ")" ::: "memory")
#define K8_WL(n) asm volatile("s_waitcnt lgkmcnt(" #n ")" ::: "memory")
#define K8_SCHED __builtin_amdgcn_sched_barrier(0)
template <int BN>
__device__ __forceinline__ int bswz2(int row) {
    if (BN >= 128) return (row & 3) | (((row >> 3) & 1) << 2);
    else return ((row >> 1) & 1) | (((row >> 3) & 1) << 1);
}
template <int BM>
__device__ __forceinline__ void gemm_tile(u16* sm, const char* A, const int s0, const unsigned tau, const u16* B, u16* C) {
    constexpr int K = HD, LDB = DM, LDC = OUTW;
    constexpr int BN = 128, BK = 64, ST = BM == 128 ? 4 : 3, WN = BM == 128 ? 4 : 2;
    constexpr int ATILE = BM * BK, BTILE = BK * BN;
    constexpr int NT = 8 / WN, GA = BM / 64, GB = 2, GS = GA + GB;
    const int tid = opaque_tid(), w = tid >> 6, l = tid & 63, i = l & 15, g = l >> 4;
    const int wr = w / WN, wc = w % WN;
    const unsigned wu = __builtin_amdgcn_readfirstlane(w);
    unsigned vb;
    { const int row = l >> 4, c = l & 15; vb = (unsigned)(row * LDB + 8 * (c ^ ((row | ((wu & 1) << 2)) << 1))) * 2u; }
    const int sg = s0 + (BM == 128 ? wu >> 1 : wu);
    const char* ab = A + (BM == 128 ? (wu & 1) * 4096 : 0);
    const char* bb = (const char*)(B + (size_t)wu * 8 * LDB);
    const unsigned lds0 = (unsigned)(uintptr_t)(__attribute__((address_space(3))) u16*)sm;
    const unsigned va = (unsigned)l * 16u, va2 = va + 2048u, vb2 = vb + 4u * LDB * 2u - 1024u;
    auto req = [&](const int buf, const int kt) {
        const unsigned d = lds0 + buf * (ATILE + BTILE) * 2;
        const int wh = kt >> 1;
        const char* pa = ab + ((blk_off(sg, (wh & 7) * 4 + (wh >> 3), tau) ^ 8192) + (kt & 1) * 2048);
        const char* pb = bb + (size_t)kt * BK * LDB * 2;
        unsigned keep;
        if (BM == 128) asm volatile("s_mov_b32 %0, m0\n\ts_mov_b32 m0, %1\n\ts_nop 0\n\tglobal_load_lds_dwordx4 %3, %5\n\tglobal_load_lds_dwordx4 %3, %5 offset:1024\n\t"
                     "s_mov_b32 m0, %2\n\ts_nop 0\n\tglobal_load_lds_dwordx4 %6, %8\n\tglobal_load_lds_dwordx4 %7, %8 offset:1024\n\ts_mov_b32 m0, %0"
                     : "=&s"(keep) : "s"(d + wu * GA * 1024), "s"(d + ATILE * 2 + wu * GB * 1024), "v"(va), "v"(va2), "s"(pa), "v"(vb), "v"(vb2), "s"(pb) : "memory");
        else asm volatile("s_mov_b32 %0, m0\n\ts_mov_b32 m0, %1\n\ts_nop 0\n\tglobal_load_lds_dwordx4 %3, %5\n\tglobal_load_lds_dwordx4 %3, %5 offset:1024\n\t"
                     "global_load_lds_dwordx4 %4, %5 offset:2048\n\tglobal_load_lds_dwordx4 %4, %5 offset:3072\n\t"
                     "s_mov_b32 m0, %2\n\ts_nop 0\n\tglobal_load_lds_dwordx4 %6, %8\n\tglobal_load_lds_dwordx4 %7, %8 offset:1024\n\ts_mov_b32 m0, %0"
                     : "=&s"(keep) : "s"(d + wu * GA * 1024), "s"(d + ATILE * 2 + wu * GB * 1024), "v"(va), "v"(va2), "s"(pa), "v"(vb), "v"(vb2), "s"(pb) : "memory");
    };
    f32x4 acc[4][NT] = {};
    constexpr int nt = K / BK;
#pragma unroll
    for (int s = 0; s < ST - 1; ++s) req(s, s);
    int boff[NT];
    const int aoff = wr * 4096 + l * 8;
#pragma unroll
    for (int n = 0; n < NT; ++n) { const int row = 8 * g + (i >> 2); boff[n] = row * BN + 16 * ((wc * NT + n) ^ bswz2<BN>(row)) + 4 * (i & 3); }
    asm volatile("s_waitcnt vmcnt(%0)" :: "i"((ST - 2) * GS) : "memory");
    K8_BAR;
    if (wu >= 4) K8_BAR;
    for (int t0 = 0; t0 < nt + ST - 1 - (nt + ST - 1) % ST; t0 += ST) {
#pragma unroll
        for (int rb = 0; rb < ST; ++rb) {
            const int t = t0 + rb;
            if (nt % ST != 0 && t >= nt) break;
            const int kk = min(t + ST - 1, nt - 1);
            const u16* as = sm + rb * (ATILE + BTILE);
            const u16* bs = as + ATILE;
            bf16x8 af[2][4], bf[2][NT];
#pragma unroll
            for (int ks = 0; ks < 2; ++ks) {
#pragma unroll
                for (int n = 0; n < NT; ++n) bf[ks][n] = tr_frag(bs + boff[n] + ks * 32 * BN, bs + boff[n] + ks * 32 * BN + 4 * BN);
#pragma unroll
                for (int m = 0; m < 4; ++m) af[ks][m] = *(const bf16x8*)(as + aoff + m * 1024 + ks * 512);
            }
            K8_SCHED;
            req((rb + ST - 1) % ST, kk);
            K8_WL(0);
            if (wu >= 4) asm volatile("s_waitcnt vmcnt(%0)" :: "i"((ST - 2) * GS) : "memory");
            K8_BAR;
            __builtin_amdgcn_s_setprio(1);
#pragma unroll
            for (int q = 0; q < 8; ++q)
#pragma unroll
                for (int n = 0; n < NT; ++n) acc[q & 3][n] = __builtin_amdgcn_mfma_f32_16x16x32_bf16(bf[q >> 2][n], af[q >> 2][q & 3], acc[q & 3][n], 0, 0, 0);
            __builtin_amdgcn_s_setprio(0);
            K8_SCHED;
            asm volatile("s_waitcnt vmcnt(%0)" :: "i"((ST - 2) * GS) : "memory");
            K8_BAR;
        }
    }
    if (wu < 4) K8_BAR;
    asm volatile("s_waitcnt vmcnt(0)" ::: "memory");
#pragma unroll
    for (int m = 0; m < 4; ++m)
#pragma unroll
        for (int n = 0; n < NT; n += 2) {
            auto r0 = __builtin_amdgcn_permlane16_swap(cvt_pk_bf16(acc[m][n][0], acc[m][n][1]), cvt_pk_bf16(acc[m][n + 1][0], acc[m][n + 1][1]), false, false);
            auto r1 = __builtin_amdgcn_permlane16_swap(cvt_pk_bf16(acc[m][n][2], acc[m][n][3]), cvt_pk_bf16(acc[m][n + 1][2], acc[m][n + 1][3]), false, false);
            *(v4u*)(C + (wr * 64 + m * 16 + i) * LDC + (wc * NT + n + (g & 1)) * 16 + 8 * (g >> 1)) = v4u{r0[0], r1[0], r0[1], r1[1]};
        }
}

__device__ __forceinline__ void xattn_unit(const ChainArgs& a, rsrc_t rsQ, rsrc_t rsK, rsrc_t rsV, rsrc_t rsS, const int s, const int sn, const int h, u16 (*Vs)[VP], unsigned* mbox, const unsigned tau, int& pend, bool& ready) {
    const int l = opaque_tid() & 63, i = l & 15, g = l >> 4;
    const unsigned known = __builtin_amdgcn_readfirstlane(*(volatile unsigned*)mbox);
    const bool early = known > (unsigned)(s + 1);
    const int bq = km_off(s, h, tau), bb = v_off(s, h, tau);
    auto count = [&](const int t) {
        if (t < SSEG) { arrive(a.sync, SY_OX + (t >> 2) * 8, 128u, 8); return; }
        unsigned n = 0;
        if (l == 0) n = atomicAdd(mbox + 8 + t - SSEG, 1u);
        if (__builtin_amdgcn_readfirstlane(n) == 3u && l < 8) __hip_atomic_store((gu32*)(a.sync + (SY_OXS + (t - SSEG) * 8 + l) * 32 + (h >> 2)), 16u, __ATOMIC_RELAXED, __HIP_MEMORY_SCOPE_AGENT);
    };
    const int xk = blk_off(s, h, tau), xq = xk ^ 8192;
    v4u vv[8], vm[2], k0f[4], k1f[4], kmf[4], q0f[4], q1f[4];
#pragma unroll
    for (int e = 0; e < 8; ++e) vv[e] = ld_sc1s(rsV, l * 16 + e * 1024, xk);
#pragma unroll
    for (int ks = 0; ks < 4; ++ks) {
        k0f[ks] = ld_sc1s(rsK, l * 16 + ks * 1024, xk);
        k1f[ks] = ld_sc1s(rsK, l * 16 + ks * 1024, xk + 4096);
        q0f[ks] = ld_sc1s(rsQ, l * 16 + ks * 1024, xq);
        q1f[ks] = ld_sc1s(rsQ, l * 16 + ks * 1024, xq + 4096);
    }
#pragma unroll
    for (int e = 0; e < 2; ++e) vm[e] = ld_sc1s(rsS, l * 16 + e * 1024, early ? bb : 1 << 30);
#pragma unroll
    for (int ks = 0; ks < 4; ++ks) kmf[ks] = ld_sc1s(rsS, ks * 512 + g * 128 + (i & 7) * 16, early ? bq : 1 << 30);
    const unsigned fl = ld_flag(a.sync + (l >= 8 ? SY_PROG * 32 : (((l & 4) ? SY_Q : SY_KV) + (min(sn, NSEG - 1) >> 3) * 8) * 32 + (l & 3)) + (h >> 2) * 32);
    if (pend >= 0) { asm volatile("s_waitcnt vmcnt(31)" ::: "memory"); count(pend); }
#pragma unroll
    for (int e = 0; e < 8; ++e) *(v4u*)&Vs[16 * (e >> 2) + i][32 * (e & 3) + 8 * g] = vv[e];
    const float scale = 0.08838834764831845f;
    const float L2E = 1.4426950408889634f;
    auto round2 = [&](const f32x4 c, float* o) {
#pragma unroll
        for (int p = 0; p < 2; ++p) {
            unsigned u = cvt_pk_bf16(c[2 * p], c[2 * p + 1]);
            float lo = __uint_as_float(u << 16) * scale, hi = __uint_as_float(u & 0xFFFF0000u) * scale;
            unsigned u2 = cvt_pk_bf16(lo, hi);
            o[2 * p] = __uint_as_float(u2 << 16);
            o[2 * p + 1] = __uint_as_float(u2 & 0xFFFF0000u);
        }
    };
    float sv[2][12], mxx[2];
    {
        f32x4 sx[3] = {};
#pragma unroll
        for (int ks = 0; ks < 4; ++ks) {
            sx[0] = __builtin_amdgcn_mfma_f32_16x16x32_bf16(as_frag(k0f[ks]), as_frag(q0f[ks]), sx[0], 0, 0, 0);
            sx[1] = __builtin_amdgcn_mfma_f32_16x16x32_bf16(as_frag(k0f[ks]), as_frag(q1f[ks]), sx[1], 0, 0, 0);
            sx[2] = __builtin_amdgcn_mfma_f32_16x16x32_bf16(as_frag(k1f[ks]), as_frag(q1f[ks]), sx[2], 0, 0, 0);
        }
        round2(sx[0], &sv[0][0]); round2(sx[1], &sv[1][0]); round2(sx[2], &sv[1][4]);
        mxx[0] = mxx[1] = -3.0e38f;
#pragma unroll
        for (int r = 0; r < 4; ++r) {
            if (4 * g + r > i) { sv[0][r] = -3.0e38f; sv[1][4 + r] = -3.0e38f; }
            mxx[0] = fmaxf(mxx[0], sv[0][r]);
            mxx[1] = fmaxf(mxx[1], fmaxf(sv[1][r], sv[1][4 + r]));
        }
    }
    const u16* vb = &Vs[4 * g + (i >> 2)][4 * (i & 3)];
    bf16x8 vfx[8];
#pragma unroll
    for (int t = 0; t < 8; ++t) vfx[t] = tr_frag(vb + 16 * t, vb + 16 * VP + 16 * t);
#pragma unroll
    for (int t = 0; t < 8; ++t) asm volatile("" : "+v"(vfx[t]));
#pragma unroll
    for (int r = 0; r < 4; ++r) asm volatile("" : "+v"(sv[0][r]), "+v"(sv[1][r]), "+v"(sv[1][4 + r]));
    asm volatile("" : "+v"(mxx[0]), "+v"(mxx[1]));
    bool have = false;
    unsigned seen = __builtin_amdgcn_readlane(fl, 8);
    ready = (sn >> 3) == (s >> 3) || (__ballot(fl != 0u) & 255ull) == 255ull;
    if (early) {
        if ((h & 3) == 0 && l == 0) atomicMax(mbox, seen);
        unsigned m = mx4(mx4(0u, vm[0]), vm[1]);
#pragma unroll
        for (int ks = 0; ks < 4; ++ks) m = mx4(m, kmf[ks]);
        have = __all(m != SENT);
    } else if ((h & 3) == 0) {
        const unsigned* pw = a.sync + (SY_PROG + (h >> 2)) * 32;
        unsigned v = seen;
        if (v <= (unsigned)(s + 1)) {
            if (s < NSEG - XA_FAST) { wait_ge<32>(pw, (unsigned)(s + 1), a.tmo, 4000u); __builtin_amdgcn_s_sleep(24); }
            else { wait_ge<6>(pw, (unsigned)(s + 1), a.tmo, 4000u); __builtin_amdgcn_s_sleep(XA_D); }
            v = (unsigned)(s + 1);
        }
        if (l == 0) atomicMax(mbox, v);
    } else {
#pragma nounroll
        for (int spin = 0; spin < 4000000 && *(volatile unsigned*)mbox < (unsigned)(s + 1); ++spin) __builtin_amdgcn_s_sleep(2);
        asm volatile("" ::: "memory");
    }
#pragma nounroll
    for (int spin = 0; !have; ++spin) {
        asm volatile("" ::: "memory");
#pragma unroll
        for (int e = 0; e < 2; ++e) vm[e] = ld_sc1s(rsS, l * 16 + e * 1024, bb);
#pragma unroll
        for (int ks = 0; ks < 4; ++ks) kmf[ks] = ld_sc1s(rsS, ks * 512 + g * 128 + (i & 7) * 16, bq);
        unsigned m = mx4(mx4(0u, vm[0]), vm[1]);
#pragma unroll
        for (int ks = 0; ks < 4; ++ks) m = mx4(m, kmf[ks]);
        if (__all(m != SENT) || spin > 20000) break;
        __builtin_amdgcn_s_sleep(2);
    }
#pragma unroll
    for (int e = 0; e < 8; ++e) *(unsigned*)&Vs[32 + (i >> 2) + 4 * ((i >> 1) & 1)][64 * (e >> 2) + 16 * g + 4 * (e & 3) + 2 * (i & 1)] = vm[e >> 2][e & 3];
    f32x4 sm_[2] = {};
#pragma unroll
    for (int ks = 0; ks < 4; ++ks) {
        sm_[0] = __builtin_amdgcn_mfma_f32_16x16x32_bf16(as_frag(kmf[ks]), as_frag(q0f[ks]), sm_[0], 0, 0, 0);
        sm_[1] = __builtin_amdgcn_mfma_f32_16x16x32_bf16(as_frag(kmf[ks]), as_frag(q1f[ks]), sm_[1], 0, 0, 0);
    }
    v4u pf[2][2];
#pragma unroll
    for (int qt = 0; qt < 2; ++qt) {
        round2(sm_[qt], &sv[qt][8]);
        float mx = fmaxf(fmaxf(mxx[qt], fmaxf(sv[qt][8], sv[qt][9])), fmaxf(sv[qt][10], sv[qt][11]));
        mx = xor16_max(mx);
        mx = xor32_max(mx);
        const float nmx = -mx * L2E;
        float sum = 0.f;
#pragma unroll
        for (int e = 0; e < 12; ++e) {
            if (qt == 0 && e >= 4 && e < 8) { sv[qt][e] = 0.f; continue; }
            float ev = __builtin_amdgcn_exp2f(fmaf(sv[qt][e], L2E, nmx));
            if (e >= 8) ev = g < 2 ? ev : 0.f;
            sv[qt][e] = ev; sum += ev;
        }
        sum = xor16_sum(sum);
        sum = xor32_sum(sum);
        const float inv = 1.0f / sum;
        pf[qt][0][0] = cvt_pk_bf16(sv[qt][0] * inv, sv[qt][1] * inv);
        pf[qt][0][1] = cvt_pk_bf16(sv[qt][2] * inv, sv[qt][3] * inv);
        pf[qt][0][2] = qt ? cvt_pk_bf16(sv[qt][4] * inv, sv[qt][5] * inv) : 0u;
        pf[qt][0][3] = qt ? cvt_pk_bf16(sv[qt][6] * inv, sv[qt][7] * inv) : 0u;
        pf[qt][1][0] = cvt_pk_bf16(sv[qt][8] * inv, sv[qt][9] * inv);
        pf[qt][1][1] = cvt_pk_bf16(sv[qt][10] * inv, sv[qt][11] * inv);
        pf[qt][1][2] = 0u; pf[qt][1][3] = 0u;
    }
    v2u vmf[8];
#pragma unroll
    for (int t = 0; t < 8; ++t) vmf[t] = tr4(&Vs[32 + 4 * (g & 1) + (i >> 2)][16 * t + 4 * (i & 3)]);
#pragma unroll
    for (int t = 0; t < 8; t += 2) {
        unsigned pk[2][2][2];
#pragma unroll
        for (int tt = 0; tt < 2; ++tt) {
            const bf16x8 vf0 = vfx[t + tt];
            const bf16x8 vf1 = as_frag(v4u{vmf[t + tt][0], vmf[t + tt][1], 0u, 0u});
#pragma unroll
            for (int qt = 0; qt < 2; ++qt) {
                f32x4 o = {};
                o = __builtin_amdgcn_mfma_f32_16x16x32_bf16(vf0, as_frag(pf[qt][0]), o, 0, 0, 0);
                o = __builtin_amdgcn_mfma_f32_16x16x32_bf16(vf1, as_frag(pf[qt][1]), o, 0, 0, 0);
                pk[qt][tt][0] = cvt_pk_bf16(o[0], o[1]);
                pk[qt][tt][1] = cvt_pk_bf16(o[2], o[3]);
            }
        }
#pragma unroll
        for (int qt = 0; qt < 2; ++qt) {
            auto r0 = __builtin_amdgcn_permlane16_swap(pk[qt][0][0], pk[qt][1][0], false, false);
            auto r1 = __builtin_amdgcn_permlane16_swap(pk[qt][0][1], pk[qt][1][1], false, false);
            v4u o = {r0[0], r1[0], r0[1], r1[1]};
            st_sc1s(rsV, (t >> 1) * 1024 + ((g & 1) * 2 + (g >> 1)) * 256 + i * 16, xq + qt * 4096, o);
        }
    }
    pend = seen > (unsigned)(sn + 1) && ready ? s : -1;
    if (pend < 0) { asm volatile("s_waitcnt vmcnt(0)" ::: "memory"); count(s); }
}

__device__ __forceinline__ void tile_stream(const ChainArgs& a, const u16* X, unsigned long long todo, const int xc, unsigned& tau, const bool early, const int nw, u16* sm8 WEVP) {
    constexpr int K = DM, LDB = HD;
    const int tid = opaque_tid(), w = tid >> 6, l = tid & 63, i = l & 15, g = l >> 4;
    const int wr = w >> 2, wc = w & 3;
    const unsigned lds0 = (unsigned)(uintptr_t)(__attribute__((address_space(3))) u16*)sm8;
    const unsigned wu = __builtin_amdgcn_readfirstlane(w);
    const unsigned ldsw = lds0 + wu * 2048;
    unsigned voffA, voffB;
    { int row = 16 * w + (l >> 3), c = l & 7; voffA = (unsigned)(row * K + 8 * (c ^ (row & 7))) * 2u; }
    { int row = 8 * w + g, c = i; voffB = (unsigned)(row * LDB + 8 * (c ^ (bswz2<128>(row) << 1))) * 2u; }
    const unsigned voffA2 = voffA + 8u * K * 2u - 1024u, voffB2 = voffB + 4u * LDB * 2u - 1024u;
    auto st2 = [&](const unsigned v0, const unsigned v1, const char* p, const unsigned d) {
        unsigned keep;
        asm volatile("s_mov_b32 %0, m0\n\ts_mov_b32 m0, %1\n\ts_nop 0\n\tglobal_load_lds_dwordx4 %2, %4\n\tglobal_load_lds_dwordx4 %3, %4 offset:1024\n\ts_mov_b32 m0, %0"
                     : "=&s"(keep) : "s"(d), "v"(v0), "v"(v1), "s"(p) : "memory");
    };
    auto stA = [&](int b, int h, const char* p) {
        st2(voffA, voffA2, p + (size_t)(h * 128) * K * 2, ldsw + (2 * b + h) * 16384);
    };
    auto stB = [&](int b, int h, const char* p) {
        st2(voffB, voffB2, p + h * 2048, ldsw + 65536 + (2 * b + h) * 16384);
    };
    constexpr int SA = 128, SB = 64 * LDB * 2;
    auto a_of = [&](const unsigned t) { return (const char*)(X + (size_t)((t >> 4) & 7) * 256 * DM); };
    auto b_of = [&](const unsigned t) { const int kind = t & 7; return (const char*)((kind < 2 ? a.Wk : (kind < 4 ? a.Wv : a.Wq)) + hcol(4 * xc + (kind & 1) * 2)); };
    auto c_of = [&](const unsigned t) { const int kind = t & 7; return ((kind & 6) == 2 ? VX_OFF - KX_OFF : 0) + (int)((t >> 4) & 7) * 4194304 + blk_off(0, 4 * xc + (kind & 1) * 2, kind < 4 ? tau : tau ^ 1u); };
    auto publish = [&](const unsigned t) {
        const int kind = t & 7, ln = lane_now();
        if (wu == 0 && ln < (kind < 4 ? 1 : 2))
            __hip_atomic_store((gu32*)(a.sync + ((kind < 4 ? SY_KV : SY_Q) + ((t >> 4) & 7) * 8 + xc) * 32 + (kind < 4 ? kind : 2 * kind - 8 + ln)), 16u, __ATOMIC_RELAXED, __HIP_MEMORY_SCOPE_AGENT);
    };
    const char* smc = (const char*)sm8;
    int aoff[2], boff[2];
#pragma unroll
    for (int ks = 0; ks < 2; ++ks) aoff[ks] = (wr * 64 + i) * 128 + (((ks * 4 + g) ^ (i & 7)) << 4);
#pragma unroll
    for (int n = 0; n < 2; ++n) { int row = 8 * g + (i >> 2); boff[n] = 65536 + row * 256 + (((wc * 2 + n) ^ bswz2<128>(row)) << 5) + 8 * (i & 3); }
    f32x4 acc[2][2][4][2] = {};
    bf16x8 At[4][2], B0[2][2], B1[2][2];
#define K8_LDA(b, h) _Pragma("unroll") for (int m = 0; m < 4; ++m) _Pragma("unroll") for (int ks = 0; ks < 2; ++ks) \
        At[m][ks] = *(const bf16x8*)(smc + aoff[ks] + (2 * (b) + (h)) * 16384 + m * 2048)
#define K8_LDB(dst, b, h) _Pragma("unroll") for (int n = 0; n < 2; ++n) _Pragma("unroll") for (int ks = 0; ks < 2; ++ks) { \
        const char* p_ = smc + boff[n] + (2 * (b) + (h)) * 16384 + ks * 8192; \
        v4i16 lo_ = __builtin_amdgcn_ds_read_tr16_b64_v4i16((__attribute__((address_space(3))) v4i16*)p_); \
        v4i16 hi_ = __builtin_amdgcn_ds_read_tr16_b64_v4i16((__attribute__((address_space(3))) v4i16*)(p_ + 1024)); \
        dst[n][ks] = bf16x8{lo_[0], lo_[1], lo_[2], lo_[3], hi_[0], hi_[1], hi_[2], hi_[3]}; }
#define K8_MMA(ai, bj, Bt) do { __builtin_amdgcn_s_setprio(1); \
        _Pragma("unroll") for (int m = 0; m < 4; ++m) _Pragma("unroll") for (int n = 0; n < 2; ++n) _Pragma("unroll") for (int ks = 0; ks < 2; ++ks) \
            acc[ai][bj][m][n] = __builtin_amdgcn_mfma_f32_16x16x32_bf16(Bt[n][ks], At[m][ks], acc[ai][bj][m][n], 0, 0, 0); \
        __builtin_amdgcn_s_setprio(0); } while (0)
    const rsrc_t rsC = __builtin_amdgcn_make_buffer_rsrc((void*)(arena(a.slots) + KX_OFF), 0, FX_OFF - KX_OFF, 0x00020000);
#define K8_EPI(ai, bj, cb) do { const int ln_ = lane_now(); \
        const int vo_ = (int)(wu & 3) * 1024 + (((ln_ >> 4) & 1) * 2 + (ln_ >> 5)) * 256 + (ln_ & 15) * 16; \
        _Pragma("unroll") for (int m = 0; m < 4; ++m) { \
            const f32x4 c0 = acc[ai][bj][m][0], c1 = acc[ai][bj][m][1]; \
            auto r0 = __builtin_amdgcn_permlane16_swap(cvt_pk_bf16(c0[0], c0[1]), cvt_pk_bf16(c1[0], c1[1]), false, false); \
            auto r1 = __builtin_amdgcn_permlane16_swap(cvt_pk_bf16(c0[2], c0[3]), cvt_pk_bf16(c1[2], c1[3]), false, false); \
            const int sg_ = 4 * ai + 2 * (int)(wu >> 2) + (m >> 1); \
            st_sc1s(rsC, vo_, ((cb) ^ (ai * 8192)) + sg_ * 524288 + bj * 16384 + (m & 1) * 4096, v4u{r0[0], r1[0], r0[1], r1[1]}); \
            acc[ai][bj][m][0] = ZERO4; acc[ai][bj][m][1] = ZERO4; } } while (0)
    constexpr int nt = K / 64;
    unsigned cur = (unsigned)todo & 255u, prev = 0u;
    const char* pA = a_of(cur); const char* pB = b_of(cur);
    if (early) touch((const char*)X + 524288 + (size_t)(nw * 128 + (tid >> 2)) * 128, lds0 + 163840 - 256);
    stB(0, 0, pB); stB(0, 1, pB); stA(0, 0, pA); stA(0, 1, pA);
    stB(1, 0, pB + SB); stB(1, 1, pB + SB); stA(1, 0, pA + SA);
    K8_WV(8); K8_BAR;
    if (wu >= 4) K8_BAR;
#define K8_GO(ai) __builtin_amdgcn_s_waitcnt(0xC07F); K8_BAR; K8_MMA(ai, 0, B0); K8_MMA(ai, 1, B1); K8_SCHED; K8_BAR
    int cbp = 0;
    for (;;) {
        todo >>= 8;
        const unsigned nxt = todo != 0ull ? (unsigned)todo & 255u : cur;
        int cb = c_of(cur);
#pragma nounroll
        for (int t = 0; t < nt; t += 2) {
            if (__builtin_expect(early && prev == 0u && t == 10, 0))
                touch((const char*)X + 3 * 524288 + (size_t)(nw * 320 + min(TIDX, 319)) * 128, lds0 + 163840 - 256);
            if (__builtin_expect(early && prev == 0u && t == nt - 4, 0)) {
                tau = __builtin_amdgcn_readfirstlane((unsigned)boot_wait(a.boot) & 255u);
                cb = c_of(cur);
            }
            const bool first = t == 0 && prev != 0u, last = t == nt - 2;
            if (t == 2 && prev != 0u) { publish(prev); WEV(3) }
            K8_LDB(B0, 0, 0); K8_LDA(0, 0); K8_LDB(B1, 0, 1); K8_SCHED; stA(1, 1, pA + SA);
            pA = last ? a_of(nxt) : pA + 2 * SA; pB = last ? b_of(nxt) : pB + 2 * SB;
            if (first) { K8_EPI(1, 0, cbp); K8_EPI(1, 1, cbp); K8_WV(24); } else K8_WV(8);
            K8_GO(0);
            K8_LDA(0, 1); K8_SCHED; stB(0, 0, pB); stB(0, 1, pB); stA(0, 0, pA);
            if (first) K8_WV(24); else K8_WV(8);
            K8_GO(1);
            K8_LDB(B0, 1, 0); K8_LDA(1, 0); K8_LDB(B1, 1, 1); K8_SCHED; stA(0, 1, pA);
            if (first) K8_WV(16); else K8_WV(8);
            K8_GO(0);
            K8_LDA(1, 1); K8_SCHED; stB(1, 0, pB + SB); stB(1, 1, pB + SB); stA(1, 0, pA + SA);
            if (last) { K8_EPI(0, 0, cb); K8_EPI(0, 1, cb); K8_WV(16); } else K8_WV(8);
            K8_GO(1);
        }
        WEV(2)
        prev = cur; cbp = cb; cur = nxt;
        if (todo == 0ull) break;
    }
    K8_EPI(1, 0, cbp); K8_EPI(1, 1, cbp);
    K8_WV(0);
    if (wu < 4) K8_BAR;
    K8_BAR;
    publish(prev);
    WEV(3)
}

__device__ __forceinline__ void oproj_squad(const ChainArgs& a, u16* out, const int ct, const int rh, const int xc, const unsigned tau, char* smem) {
    const int tid = opaque_tid(), w = tid >> 6, l = tid & 63, i = l & 15, g = l >> 4;
    const int wu = __builtin_amdgcn_readfirstlane(w);
    const int c0 = (ct / 3) * 128 + (ct % 3) * 48, nq = ct % 3 == 2 ? 8 : 12;
    bf16x8 wf[2][16];
    char* w3 = smem + wu * 16384 + l * 16;
    {
        u16* stg = (u16*)(smem + wu * 16384);
        bf16x8 wt[16];
#pragma unroll
        for (int ch = 0; ch < 4; ++ch) {
            const u16* src = a.Wo + (size_t)hcol(4 * wu + ch) * DM;
            v4u raw[12];
#pragma unroll
            for (int e = 0; e < 12; ++e) { const int idx = l + 64 * e, r = idx / 6, q = idx % 6; raw[e] = *(const v4u*)(src + (size_t)r * DM + min(c0 + 8 * q, DM - 8)); }
#pragma unroll
            for (int e = 0; e < 12; ++e) { const int idx = l + 64 * e, r = idx / 6, q = idx % 6; *(v4u*)(stg + r * 56 + 8 * q) = raw[e]; }
            __builtin_amdgcn_s_waitcnt(0xC07F);
#pragma unroll
            for (int jj = 0; jj < 3; ++jj)
#pragma unroll
                for (int ks = 0; ks < 4; ++ks) {
                    const u16* p = stg + (32 * ks + 8 * g + (i >> 2)) * 56 + 16 * jj + 4 * (i & 3);
                    (jj < 2 ? wf[jj & 1] : wt)[4 * ch + ks] = tr_frag(p, p + 4 * 56);
                }
            __builtin_amdgcn_s_waitcnt(0xC07F);
        }
#pragma unroll
        for (int ks = 0; ks < 16; ++ks) *(bf16x8*)(w3 + ks * 1024) = wt[ks];
    }
    const rsrc_t rsO = __builtin_amdgcn_make_buffer_rsrc((void*)(arena(a.slots) + VX_OFF), 0, 32 << 20, 0x00020000);
    float* rp = (float*)(smem + 131072);
    const int voff = rh * 4096 + l * 16;
    bool pre = false;
    v4u xf[16];
    const unsigned* fq = a.sync + (SY_OXS + xc) * 32 + wu;
    for (int s = SSEG; s < NSEG - 1; ++s, fq += 256) {
        if (!pre) {
#pragma nounroll
            for (int spin = 0;; ++spin) {
                asm volatile("" ::: "memory");
                if (ld_flag(fq) != 0u) break;
                if (spin > 20000) { __hip_atomic_store((gu32*)a.tmo, 7000u, __ATOMIC_RELAXED, __HIP_MEMORY_SCOPE_AGENT); break; }
                if (s == SSEG) __builtin_amdgcn_s_sleep(64); else if (s < NSEG - 3) __builtin_amdgcn_s_sleep(SQ_SL); else __builtin_amdgcn_s_sleep(SQ_SF);
            }
            asm volatile("" ::: "memory");
        }
        const unsigned nf = ld_flag(fq + 256);
        if (!pre) {
#pragma unroll
            for (int ks = 0; ks < 16; ++ks) xf[ks] = ld_sc1s(rsO, voff + (ks & 3) * 1024, blk_off(s, 4 * wu + (ks >> 2), tau) ^ 8192);
        }
        pre = __builtin_amdgcn_readfirstlane(nf) != 0u;
        f32x4 acc[3] = {};
        if (pre) {
            bf16x8 wn = *(const bf16x8*)w3;
#pragma unroll
            for (int ks = 0; ks < 16; ++ks) {
#pragma unroll
                for (int jj = 0; jj < 2; ++jj) acc[jj] = __builtin_amdgcn_mfma_f32_16x16x32_bf16(wf[jj][ks], as_frag(xf[ks]), acc[jj], 0, 0, 0);
                acc[2] = __builtin_amdgcn_mfma_f32_16x16x32_bf16(wn, as_frag(xf[ks]), acc[2], 0, 0, 0);
                wn = *(const bf16x8*)(w3 + ((ks + 1) & 15) * 1024);
                K8_SCHED;
                xf[ks] = ld_sc1s(rsO, voff + (ks & 3) * 1024, blk_off(s + 1, 4 * wu + (ks >> 2), tau) ^ 8192);
                K8_SCHED;
            }
        } else {
#pragma unroll
            for (int ks = 0; ks < 16; ++ks) {
#pragma unroll
                for (int jj = 0; jj < 2; ++jj) acc[jj] = __builtin_amdgcn_mfma_f32_16x16x32_bf16(wf[jj][ks], as_frag(xf[ks]), acc[jj], 0, 0, 0);
                acc[2] = __builtin_amdgcn_mfma_f32_16x16x32_bf16(*(const bf16x8*)(w3 + ks * 1024), as_frag(xf[ks]), acc[2], 0, 0, 0);
            }
        }
        K8_BAR;
#pragma unroll
        for (int jj = 0; jj < 3; ++jj) *(f32x4*)&rp[(wu * 16 + i) * 52 + 16 * jj + 4 * g] = acc[jj];
        K8_WL(0); K8_BAR;
        if (tid >= 256 && (tid & 15) < nq) {
            const int row = (tid >> 4) & 15, q4 = tid & 15;
            f32x4 sv_[8];
#pragma unroll
            for (int e = 0; e < 8; ++e) sv_[e] = *(const f32x4*)&rp[(e * 16 + row) * 52 + 4 * q4];
            const f32x4 s0 = ((sv_[0] + sv_[1]) + (sv_[2] + sv_[3])) + ((sv_[4] + sv_[5]) + (sv_[6] + sv_[7]));
            v2u o; o[0] = cvt_pk_bf16(s0[0], s0[1]); o[1] = cvt_pk_bf16(s0[2], s0[3]);
            out8(out + (size_t)(s * SEG + 16 * rh + row) * OUTW + c0 + 4 * q4, o);
        }
    }
}

__device__ __forceinline__ void kv_stream(const ChainArgs& a, const u16* __restrict__ X, const u16* __restrict__ B, char* C, const int head, const int xc, const int wk, unsigned& tau, char* smem) {
    const int tid = opaque_tid(), w = tid >> 6, l = tid & 63, i = l & 15, g = l >> 4;
    const int wu = __builtin_amdgcn_readfirstlane(w), kh = wu >> 2, cq = wu & 3;
    const unsigned lds0 = (unsigned)(uintptr_t)(__attribute__((address_space(3))) char*)smem;
    unsigned voA, voB;
    { const int row = (wu & 1) * 8 + (l >> 3), c = l & 7; voA = (unsigned)(row * DM + 8 * (c ^ (row & 7))) * 2u + (wu >> 1) * 512; }
    { const int row = tid >> 4, c = tid & 15; voB = (unsigned)(row * HD + 8 * (c ^ (bswz2<128>(row) << 1))) * 2u; }
    auto stB = [&](int ch, int slot) {
#pragma unroll
        for (int e = 0; e < 4; ++e) glds16s(voB, (const char*)B + (size_t)(4 * ch + e) * 32 * HD * 2, lds0 + 65536 + slot * 32768 + e * 8192 + wu * 1024);
    };
    const unsigned ldA = lds0 + (wu >> 1) * 8192 + (wu & 1) * 1024;
    auto stP = [&](const char* p, const unsigned d) {
        unsigned keep;
        asm volatile("s_mov_b32 %0, m0\n\ts_mov_b32 m0, %1\n\ts_nop 0\n\tglobal_load_lds_dwordx4 %3, %2 offset:-4096\n\tglobal_load_lds_dwordx4 %4, %2 offset:-2048\n\t"
                     "global_load_lds_dwordx4 %5, %2\n\tglobal_load_lds_dwordx4 %6, %2 offset:2048\n\ts_mov_b32 m0, %0"
                     : "=&s"(keep) : "s"(d), "s"(p), "v"(voA + 8192u), "v"(voA + 6272u), "v"(voA + 4352u), "v"(voA + 2432u) : "memory");
    };
    auto srcA = [&](int h) { return (const char*)X + (size_t)min(h, 2 * KVS - 1) * 32768 - 4096; };
    auto stA = [&](int h) { stP(srcA(h), ldA + (h & 3) * 32768 + 4096); };
    stB(0, 0); stA(0); stA(1); stB(1, 1);
    touch((const char*)X + 65536 + min(wk * 512 + tid, (KVS - 1) * 512 - 1) * 128, lds0 + 131072 + wu * 1024);
    stB(2, 2);
    bf16x8 bf[16][2], af[8];
    f32x4 acc[2][2] = {}, keep[2];
    const int rb = 64 * kh + 8 * g + (i >> 2), sw = bswz2<128>(rb);
    const char* bp = smem + 65536 + rb * 256 + 8 * (i & 3);
    const char* sa0 = smem + kh * 2048 + i * 128 + 16 * (g ^ (i & 7));
    const char* sa1 = smem + kh * 2048 + i * 128 + 16 * ((4 + g) ^ (i & 7));
#define KV_AF(slot, q) *(const bf16x8*)((((q) & 1) ? sa1 : sa0) + (slot) * 32768 + ((q) >> 1) * 4096)
#pragma unroll
    for (int ch = 0; ch < 8; ++ch) {
        if (ch == 0) K8_WV(9); else if (ch < 6) K8_WV(8); else if (ch == 6) K8_WV(4); else K8_WV(0);
        K8_BAR;
#pragma unroll
        for (int e = 0; e < 4; ++e) { const char* p_ = bp + (ch % 3) * 32768 + (e >> 1) * 8192 + 32 * ((2 * cq + (e & 1)) ^ sw); bf[2 * ch + (e >> 1)][e & 1] = tr_frag(p_, p_ + 1024); }
#pragma unroll
        for (int e = 0; e < 4; ++e) af[e] = KV_AF(e >> 1, 2 * ch + (e & 1));
        K8_WL(0); K8_SCHED; K8_BAR;
        if (ch < 5) stB(ch + 3, ch % 3);
        if (ch == 7) { stA(2); stA(3); }
#pragma unroll
        for (int e = 0; e < 8; ++e) acc[e >> 2][e & 1] = __builtin_amdgcn_mfma_f32_16x16x32_bf16(bf[2 * ch + ((e >> 1) & 1)][e & 1], af[e >> 1], acc[e >> 2][e & 1], 0, 0, 0);
    }
    float* red = (float*)(smem + 131072) + l * 4;
#pragma unroll
    for (int p = 0; p < 2; ++p) { *(f32x4*)(red + (p * 8 + wu) * 256) = kh ? acc[p][0] : acc[p][1]; keep[p] = kh ? acc[p][1] : acc[p][0]; }
    unsigned long long tag = 0;
    if (wu == 4) { tag = boot_wait(a.boot); *(volatile unsigned*)(smem + 163840 - 512) = (unsigned)tag & 255u; }
    const rsrc_t rc = __builtin_amdgcn_make_buffer_rsrc((void*)C, 0, KVS * 524288, 0x00020000);
    const int vo = cq * 1024 + (2 * kh + (g >> 1)) * 256 + i * 16 + (g & 1) * 8;
    auto put = [&](const f32x4 o, const int so) {
        const v2u pk = {cvt_pk_bf16(o[0], o[1]), cvt_pk_bf16(o[2], o[3])};
        asm volatile("buffer_store_dwordx2 %0, %1, %2, %3 offen sc1" :: "v"(pk), "v"(vo), "s"(rc), "s"(so) : "memory");
    };
    K8_WL(0); K8_WV(0); K8_BAR;
    tau = __builtin_amdgcn_readfirstlane(*(volatile unsigned*)(smem + 163840 - 512));
#pragma unroll
    for (int p = 0; p < 2; ++p) put(keep[p] + *(const f32x4*)(red + (p * 8 + (wu ^ 4)) * 256), blk_off(0, head, tau) + p * 4096);
    stA(4);
    K8_WV(4); K8_BAR;
    if (tid == 256) __hip_atomic_store((gu64*)(a.boot + 256 + 16 * xc + wk), tag, __ATOMIC_RELAXED, __HIP_MEMORY_SCOPE_AGENT);
    auto stages = [&](auto kc) {
        constexpr int KH = decltype(kc)::value;
        if (KH) K8_BAR;
        f32x4 ac[2] = {ZERO4, ZERO4}, kp = ZERO4;
        int h_first = 2, so = OOB;
        asm volatile("" : "+s"(h_first));
        const char* pn = srcA(5);
        unsigned dn = ldA + 32768 + 4096;
        int r0 = (int)(sa0 - smem) + 2 * 32768, r1 = (int)(sa1 - smem) + 2 * 32768;
        for (int h = h_first; h < 2 * KVS + 4; ++h) {
            const f32x4 oth = *(const f32x4*)(red + (wu ^ 4) * 256);
            *(f32x4*)(red + wu * 256) = ac[KH ^ 1];
            bf16x8 ax[16];
#pragma unroll
            for (int q = 0; q < 16; ++q) ax[q] = *(const bf16x8*)(smem + ((q & 1) ? r1 : r0) + (q >> 1) * 4096);
            K8_SCHED;
            stP(pn, dn);
            put((KH ? ac[1] : kp) + oth, so);
            kp = ac[0];
            __builtin_amdgcn_s_waitcnt(0xC07F);
            K8_WV(10);
            if (__builtin_expect(h == 5, 0)) K8_WV(0);
            K8_BAR;
            if (KH && (0xAAA20u >> h) & 1u && tid == 256)
                __hip_atomic_store((gu32*)(a.sync + (SY_KV0 + (h / 2 - (h == 5 ? 1 : 2)) * 8 + xc) * 32 + wk), 16u, __ATOMIC_RELAXED, __HIP_MEMORY_SCOPE_AGENT);
            if (__builtin_expect(h < 2 * KVS, 1)) {
                __builtin_amdgcn_s_setprio(1);
#pragma unroll
                for (int q = 0; q < 16; ++q)
#pragma unroll
                    for (int n = 0; n < 2; ++n) ac[n] = __builtin_amdgcn_mfma_f32_16x16x32_bf16(bf[q][n], ax[q], q ? ac[n] : ZERO4, 0, 0, 0);
                __builtin_amdgcn_s_setprio(0);
            }
            {
                const int hs = h - 1 + KH;
                so = blk_off(hs >> 1, head, tau) + (hs & 1) * 4096;
                asm volatile("" : "+s"(so));
                so = hs > 1 && hs < 2 * KVS ? so : OOB;
                pn = srcA(h + 4); dn = ldA + (h & 3) * 32768 + 4096;
                r0 = (r0 + 32768) & 131071; r1 = (r1 + 32768) & 131071;
                asm volatile("" : "+s"(so), "+s"(pn), "+s"(dn), "+v"(r0), "+v"(r1));
            }
            K8_BAR;
        }
        if (!KH) K8_BAR;
    };
    if (kh) stages(std::integral_constant<int, 1>{}); else stages(std::integral_constant<int, 0>{});
    K8_WV(0);
}

struct MegaArgs {
    ChainArgs ch;
    const u16* x; u16* out;
    const u16* bx; const u16* wr; const u16* wi;
};
constexpr int MEGA_LDS = 163840;
#define WSTAMP(n)
__global__ __launch_bounds__(512, 2) void mega_kernel(MegaArgs m) {
    extern __shared__ __attribute__((aligned(256))) char chsm[];
    const int b = BIDX, xc = (b & 3) + 4 * (b >> 7), sl = (b >> 3) & 15;
    if ((b & 4) == 0) { chain_body(m.ch, xc, sl, chsm); return; }
    const ChainArgs& a = m.ch;
    const int wk = sl, tid = TIDX;
    char* ar = arena(a.slots);
    unsigned tauw = 0;
    const int hf = __builtin_amdgcn_readfirstlane(TIDX >> 8);
    WSTAMP(0)
    if (wk < 8) kv_stream(a, m.x, (wk < 4 ? a.Wk : a.Wv) + hcol(4 * xc + (wk & 3)), ar + (wk < 4 ? KX_OFF : VX_OFF), 4 * xc + (wk & 3), xc, wk, tauw, chsm);
    WSTAMP(8) WEV(1)
    {
        const int p = wk & 1, q = wk & 3;
        const unsigned long long todo = wk < 2 ? 0x84u + p : wk < 4 ? 0x74704240u + 0x01010101u * p : wk < 8 ? 0ull : wk < 10 ? 0x60503010ull + 0x01010101ull * q
                                      : wk < 12 ? 0x7060503010ull + 0x0101010101ull * q : (wk < 14 ? 0x54341420u : 0x64442422u) + 0x01010101u * p;
        __syncthreads();
        if (todo != 0ull) tile_stream(a, m.x, todo, xc, tauw, wk >= 8, xc * 8 + (wk & 7), (u16*)chsm WEVA);
    }
    WSTAMP(2)
    if (wk < 2) {
        unsigned* mbox = (unsigned*)(chsm + 131072);
        if (tid < 40) mbox[tid] = tid >= 1 && tid < 5 ? 2u : 0u;
        __syncthreads();
        const int w = __builtin_amdgcn_readfirstlane(tid >> 6), h = 4 * xc + (w & 3);
        const rsrc_t rsQ = __builtin_amdgcn_make_buffer_rsrc((void*)(ar + KX_OFF), 0, S_LEN * HD * 4, 0x00020000);
        const rsrc_t rsK = __builtin_amdgcn_make_buffer_rsrc((void*)(ar + KX_OFF), 0, S_LEN * HD * 4, 0x00020000);
        const rsrc_t rsV = __builtin_amdgcn_make_buffer_rsrc((void*)(ar + VX_OFF), 0, S_LEN * HD * 4, 0x00020000);
        const rsrc_t rsS = slot_rsrc(a.slots);
        u16 (*Vs)[VP] = (u16 (*)[VP])(chsm + w * (40 * VP * 2));
        const unsigned tau = tauw;
        int pend = -1;
        bool ready = false;
        for (int s = 2 * wk + (w >> 2), sn; s < NSEG - 1; s = sn) {
            const int band = s >> 3;
            sn = s + 4;
            if (s < XST) {
                unsigned n = 0;
                if (lane_now() == 0) n = atomicAdd(mbox + 1 + (w & 3), 1u);
                n = __builtin_amdgcn_readfirstlane(n);
                sn = 2 * wk + (n < XST / 2 ? (int)(n & 1u) + 4 * (int)(n >> 1) : XST + (w >> 2));
            }
            if (sn - s > 4) __builtin_amdgcn_s_setprio(2); else __builtin_amdgcn_s_setprio(0);
            if (s < 4) wait_ge<32>(a.sync + (SY_KV0 + (KVS - 1) * 8 + xc) * 32 + (tid & 7), 16u, a.tmo, 4100u);
            if (!ready) {
                if (band > 0) wait_ge<32>(a.sync + (SY_KV + band * 8 + xc) * 32 + (tid & 3), 1u, a.tmo, 4100u);
                wait_ge<32>(a.sync + (SY_Q + band * 8 + xc) * 32 + (tid & 3), 1u, a.tmo, 4200u);
            }
            xattn_unit(a, rsQ, rsK, rsV, rsS, s, sn, h, Vs, mbox, tau, pend, ready);
        }
        return;
    }
    if (wk >= 4 && wk < 8) {
        const int fw = (wk - 4) * 8 + xc;
        const rsrc_t rsF = __builtin_amdgcn_make_buffer_rsrc((void*)(ar + FX_OFF), 0, 16 << 20, 0x00020000);
        const rsrc_t rsW = __builtin_amdgcn_make_buffer_rsrc((void*)(ar + WT_OFF), 0, 10 << 20, 0x00020000);
        const unsigned tw = tauw ^ 1u;
        {
            const int tq = opaque_tid();
            fft_rows_fwd(m.bx, rsF, tw, 2 * fw + hf, chsm + hf * 36864, tq & 255, (const char*)m.wr, (const char*)m.wi, (unsigned)(uintptr_t)(__attribute__((address_space(3))) char*)chsm + 163840 - 256);
        }
        for (int t = fw; t < 64; t += 32) {
            __syncthreads();
            filt_transpose(m.wr, m.wi, rsW, tw, t, chsm, opaque_tid());
        }
        wg_publish(a.sync, SY_DONE, 32u, 8);
        WSTAMP(3) WEV(4)
        wg_wait_acquire<16>(a.sync + (SY_DONE + xc) * 32, a.tmo, 6000u);
        {
            const int tq = opaque_tid();
            fft_cols(rsF, rsW, tw, 2 * fw + hf, 4, chsm + hf * 36864, tq & 255);
        }
        wg_publish(a.sync, SY_DONE + 8, 32u, 8);
        WSTAMP(4) WEV(5)
    }
    if (wk >= 4 && wk < 10) {
        const rsrc_t rsF = __builtin_amdgcn_make_buffer_rsrc((void*)(ar + FX_OFF), 0, 16 << 20, 0x00020000);
        wg_wait_acquire<16>(a.sync + (SY_DONE + 8 + xc) * 32, a.tmo, 6001u);
        const int tq = opaque_tid();
        fft_rows_inv(rsF, tauw ^ 1u, m.out, 2 * ((wk & 3) * 8 + xc) + hf + (wk < 8 ? 0 : 192), wk < 8 ? 64 : 32, wk < 8 ? 3 : 2, chsm + hf * 36864, tq & 255);
        WSTAMP(5) WEV(6)
    }
    if (wk >= 12) {
        const int r = 2 * (wk - 12);
        wg_wait_acquire<16>(a.sync + (SY_OX + r * 8 + xc) * 32, a.tmo, 5000u);
        wg_wait_acquire<16>(a.sync + (SY_OX + (r + 1) * 8 + xc) * 32, a.tmo, 5000u);
        WEV(7)
        gemm_tile<256>((u16*)chsm, ar + VX_OFF, 4 * r, tauw, a.Wo + xc * 128, m.out + (size_t)r * 128 * OUTW + xc * 128);
        WEV(8)
    }
    if (wk < 4 || wk == 10) {
        const int r = wk < 4 ? wk + 6 : 10;
        wg_wait_acquire<16>(a.sync + (SY_OX + r * 8 + xc) * 32, a.tmo, 5000u);
        WEV(7)
        gemm_tile<128>((u16*)chsm, ar + VX_OFF, 4 * r, tauw, a.Wo + xc * 128, m.out + (size_t)r * 128 * OUTW + xc * 128);
        WEV(8)
        WSTAMP(9)
    }
    if (wk >= 4 && wk < 10) {
        __syncthreads();
        oproj_squad(a, m.out, (wk - 4) * 4 + (xc & 3), xc >> 2, xc, tauw, chsm);
        WSTAMP(7)
    }
}

#if defined(__HIP_DEVICE_COMPILE__)
#pragma clang attribute pop
#endif
extern "C" void kernel_launch(void* const* d_in, const int* in_sizes, int n_in,
                              void* d_out, int out_size, void* d_ws, size_t ws_size,
                              hipStream_t stream) {
    char* ws = (char*)d_ws;
    size_t off = 0;
    auto alloc = [&](size_t bytes) { void* p = ws + off; off += (bytes + 255) & ~(size_t)255; return p; };
    char* slots = (char*)alloc((size_t)ARENA_BYTES + (2u << 20));
    static_assert((SY_WORDS + 2) * 128 <= 131072, "sync block");
    unsigned* sync = (unsigned*)alloc(131072);
    unsigned long long* boot = (unsigned long long*)alloc(4096);

    MegaArgs m;
    m.ch.mem0 = (const u16*)d_in[1]; m.ch.Wq = (const u16*)d_in[2]; m.ch.Wk = (const u16*)d_in[3]; m.ch.Wv = (const u16*)d_in[4]; m.ch.Wo = (const u16*)d_in[5];
    m.ch.out = (u16*)d_out; m.ch.slots = slots; m.ch.tmo = sync + SY_WORDS * 32;
    m.ch.stamps = (unsigned long long*)(ws + (240ull << 20));
    m.ch.sync = sync; m.ch.boot = boot;
    m.x = (const u16*)d_in[0]; m.out = (u16*)d_out;
    m.bx = (const u16*)d_in[7]; m.wr = (const u16*)d_in[8]; m.wi = (const u16*)d_in[9];
    (void)hipFuncSetAttribute((const void*)mega_kernel, hipFuncAttributeMaxDynamicSharedMemorySize, MEGA_LDS);
    mega_kernel<<<256, 512, MEGA_LDS, stream>>>(m);
}
```

```cpp
#if defined(__HIP_DEVICE_COMPILE__)
#pragma clang attribute push (__attribute__((target("no-packed-fp32-ops,no-load-store-opt"))), apply_to = function)
#endif
#include <hip/hip_runtime.h>
#include <hip/hip_bf16.h>
#include <stdint.h>
#include <type_traits>

typedef unsigned short u16;
#define TIDX ((int)__builtin_amdgcn_workitem_id_x())
#define BIDX ((int)__builtin_amdgcn_workgroup_id_x())
__device__ __forceinline__ int opaque_tid() { int t = TIDX; asm volatile("" : "+v"(t)); return t; }
__device__ __forceinline__ int lane_now() { int t; asm volatile("v_mbcnt_lo_u32_b32 %0, -1, 0\n\tv_mbcnt_hi_u32_b32 %0, -1, %0" : "=v"(t)); return t; }
typedef __attribute__((ext_vector_type(8))) short bf16x8;
typedef __attribute__((ext_vector_type(4))) float f32x4;

constexpr int S_LEN = 2048, DM = 1024, NH = 32, DH = 128, MM = 8, SEG = 32;
constexpr int NSEG = S_LEN / SEG;
constexpr int HD = NH * DH;
constexpr int OUTW = 2048;

typedef unsigned v4u __attribute__((ext_vector_type(4)));
typedef unsigned v2u __attribute__((ext_vector_type(2)));
typedef short v4i16 __attribute__((ext_vector_type(4)));
typedef __amdgpu_buffer_rsrc_t rsrc_t;
#define AUX_SC1 16
__device__ __forceinline__ v4u ld_sc1s(rsrc_t rs, int voff, int soff) { return __builtin_amdgcn_raw_buffer_load_b128(rs, voff, soff, AUX_SC1); }
__device__ __forceinline__ void st_sc1s(rsrc_t rs, int voff, int soff, v4u v) { __builtin_amdgcn_raw_buffer_store_b128(v, rs, voff, soff, AUX_SC1); asm volatile("s_nop 1" :: "v"(v) : "memory"); }
__device__ __forceinline__ void st8_sc1s(rsrc_t rs, int voff, int soff, v2u v) { __builtin_amdgcn_raw_buffer_store_b64(v, rs, voff, soff, AUX_SC1); }
__device__ __forceinline__ void st_sc1(rsrc_t rs, int off, v4u v) { __builtin_amdgcn_raw_buffer_store_b128(v, rs, off, 0, AUX_SC1); }
__device__ __forceinline__ void out8(void* p, v2u v) { asm volatile("global_store_dwordx2 %0, %1, off sc1" :: "v"(p), "v"(v) : "memory"); }
typedef __bf16 bf16x2_t __attribute__((ext_vector_type(2)));
typedef float f32x2_t __attribute__((ext_vector_type(2)));
__device__ __forceinline__ unsigned cvt_pk_bf16(float lo, float hi) {
    f32x2_t f = {lo, hi};
    return __builtin_bit_cast(unsigned, __builtin_convertvector(f, bf16x2_t));
}
#define SENT 0xFFBFFFBF
__device__ __forceinline__ unsigned umax3(unsigned a, unsigned b, unsigned c) { return max(max(a, b), c); }
__device__ __forceinline__ unsigned mx4(unsigned acc, v4u v) { return umax3(umax3(acc, v[0], v[1]), v[2], v[3]); }
__device__ __forceinline__ bf16x8 as_frag(v4u v) { return __builtin_bit_cast(bf16x8, v); }
__device__ __forceinline__ float xor16_sum(float x) {
    auto r = __builtin_amdgcn_permlane16_swap(__float_as_uint(x), __float_as_uint(x), false, false);
    return __uint_as_float(r[0]) + __uint_as_float(r[1]);
}
__device__ __forceinline__ float xor32_sum(float x) {
    auto r = __builtin_amdgcn_permlane32_swap(__float_as_uint(x), __float_as_uint(x), false, false);
    return __uint_as_float(r[0]) + __uint_as_float(r[1]);
}
__device__ __forceinline__ float xor16_max(float x) {
    auto r = __builtin_amdgcn_permlane16_swap(__float_as_uint(x), __float_as_uint(x), false, false);
    return fmaxf(__uint_as_float(r[0]), __uint_as_float(r[1]));
}
__device__ __forceinline__ float xor32_max(float x) {
    auto r = __builtin_amdgcn_permlane32_swap(__float_as_uint(x), __float_as_uint(x), false, false);
    return fmaxf(__uint_as_float(r[0]), __uint_as_float(r[1]));
}
constexpr int SPIN_LIMIT = 400000;
constexpr int OOB = 0x7FFFFF00;
#define XST 32
#define SQ_SL 16
#define PLL 0
#define SE 6
#define TDE 72
#define PB 14
#define TD 60
#define PS 46
#define TE0 10
#define PVA 8
#define PV 1
#define PV1 2
#define PV2 11
#define PL 20
#define PF 48
#define PC 31
#define PP 15
#define PKA 28
#define PG0 2
#define PG1 32
#define DLY_U 4
#define DLY_C 4
#define XA_FAST 6
#define XA_D 12
#define SQ_SF 4
#define LW 0
constexpr int NPROBE = 2;
constexpr int M_OFF = 32 * 16384, F_OFF = M_OFF + 2 * 16384, C_OFF = F_OFF + 2 * 16384, REC = C_OFF + 16 * 16384, PERWG = REC / 128, SLOT_BYTES = 34 * 2097152;
static_assert(PERWG == 6656, "arming shares");
__device__ __forceinline__ int rec_base(int s) { return (s >> 1) * 2097152 + (s & 1) * REC; }
__device__ __forceinline__ char* arena(char* slots) { return (char*)(((uintptr_t)slots + 2097151u) & ~(uintptr_t)2097151u); }
constexpr int KX_OFF = SLOT_BYTES, VX_OFF = KX_OFF + (32 << 20), FX_OFF = VX_OFF + (32 << 20), WT_OFF = FX_OFF + (16 << 20), ARENA_BYTES = WT_OFF + (10 << 20);
__device__ __forceinline__ rsrc_t slot_rsrc(char* slots) { return __builtin_amdgcn_make_buffer_rsrc((void*)arena(slots), 0, FX_OFF, 0x00020000); }
__device__ __forceinline__ int blk_off(int seg, int h, unsigned tau) { return seg * 524288 + (((h & 3) >> 1) * 16 + (h >> 2) * 2 + (h & 1)) * 16384 + (int)((tau ^ (unsigned)(seg >> 2)) & 1u) * 8192; }
constexpr int TM_OFF = 124 * 16384;
constexpr int CAL_OFF = 32 * 2097152;
__device__ __forceinline__ int near_half(int s, unsigned tau) { return (int)((tau ^ (unsigned)(s >> 1)) & 1u) * 8192; }
__device__ __forceinline__ int chunk_off(int s, int g, int j, unsigned tau) { return rec_base(s) + (j < 2 ? (g & 3) * 2 + j : 6 + 3 * g + j) * 16384 + near_half(s, tau); }
__device__ __forceinline__ int x_off(int s, int h, unsigned tau) { return chunk_off(s, h >> 2, 2, tau) + (h & 3) * 2048; }
__device__ __forceinline__ int km_off(int s, int h, unsigned tau) { return chunk_off(s, h >> 2, 3, tau) + (h & 3) * 2048; }
__device__ __forceinline__ int m_off(int s, int h, unsigned tau) { return rec_base(s) + M_OFF + (h >> 4) * 16384 + near_half(s, tau) + (h & 15) * 512; }
__device__ __forceinline__ int v_off(int s, int h, unsigned tau) { return chunk_off(s, h >> 2, 4, tau) + (h & 3) * 2048; }
__device__ __forceinline__ int f_off(int s, int g, unsigned tau) { return rec_base(s) + F_OFF + (g >> 2) * 16384 + near_half(s, tau) + (g & 3) * 2048; }
__device__ __forceinline__ int hcol(int h) { return ((h & 3) * 8 + (h >> 2)) * 128; }
constexpr int VP = 144;

struct ChainArgs {
    u16* out;
    const u16* mem0; const u16* Wq; const u16* Wk; const u16* Wv; const u16* Wo;
    char* slots;
    unsigned* tmo;
    unsigned long long* stamps;
    unsigned* sync;
    unsigned long long* boot;
};
constexpr int SY_KV = 0;
constexpr int SY_Q = 64;
constexpr int SY_PROG = 128;
constexpr int SY_OX = 136;
constexpr int SY_DONE = 264;
constexpr int SY_OXS = 472;
constexpr int SY_KV0 = 408;
constexpr int KVS = 8;
constexpr int SY_FLAGS = 680;
constexpr int SY_WORDS = SY_FLAGS + SY_FLAGS / 8;
constexpr int SSEG = 44;
typedef __attribute__((address_space(1))) unsigned gu32;
typedef __attribute__((address_space(1))) unsigned long long gu64;
__device__ __forceinline__ unsigned ld_flag(const unsigned* p) { return __hip_atomic_load((gu32*)p, __ATOMIC_RELAXED, __HIP_MEMORY_SCOPE_AGENT); }
__device__ __forceinline__ unsigned long long boot_tag(const unsigned long long* boot) {
    return __hip_atomic_load((gu64*)(boot + 144), __ATOMIC_RELAXED, __HIP_MEMORY_SCOPE_AGENT) + 1ull;
}
__device__ __forceinline__ unsigned long long boot_wait(const unsigned long long* boot) {
    const int l = TIDX & 63;
    unsigned long long tag = 0, x = 0, y = 0;
#pragma nounroll
    for (int spin = 0; spin < 100000; ++spin) {
        asm volatile("" ::: "memory");
        x = __hip_atomic_load((gu64*)(boot + l), __ATOMIC_RELAXED, __HIP_MEMORY_SCOPE_AGENT);
        y = __hip_atomic_load((gu64*)(boot + 64 + l), __ATOMIC_RELAXED, __HIP_MEMORY_SCOPE_AGENT);
        tag = boot_tag(boot);
        if (__all((x | 3u) == ((tag << 2) | 3u) && (y | 3u) == ((tag << 2) | 3u))) break;
        __builtin_amdgcn_s_sleep(16);
    }
    asm volatile("" ::: "memory");
    const unsigned m = ((unsigned)__ballot(x & 1u) ^ (__popcll(__ballot(x & 2u)) + __popcll(__ballot(y & 2u)) > 64 ? 1u : 0u)) & 1u;
    return (tag << 8) | m;
}
template <int SL>
__device__ __forceinline__ void wait_ge(const unsigned* p, unsigned need, unsigned* tmo, unsigned code) {
#pragma nounroll
    for (int spin = 0;; ++spin) {
        asm volatile("" ::: "memory");
        if (__all(ld_flag(p) >= need)) break;
        if (spin > 400000 / SL) { __hip_atomic_store((gu32*)tmo, code, __ATOMIC_RELAXED, __HIP_MEMORY_SCOPE_AGENT); break; }
        __builtin_amdgcn_s_sleep(SL);
    }
    asm volatile("" ::: "memory");
}
template <int SL>
__device__ __forceinline__ void wg_wait_acquire(const unsigned* p, unsigned* tmo, unsigned code) {
    if (TIDX < 64) {
        wait_ge<SL>(p, 1u, tmo, code);
        __builtin_amdgcn_fence(__ATOMIC_ACQUIRE, "agent");
        asm volatile("s_waitcnt vmcnt(0)" ::: "memory");
    }
    __syncthreads();
}
__device__ __forceinline__ void arrive(unsigned* sync, const int f, const unsigned need, const int n) {
    const int l = lane_now();
    unsigned old = 0;
    if (l == 0) old = __hip_atomic_fetch_add((gu32*)(sync + (SY_FLAGS + (f >> 3)) * 32), 1u, __ATOMIC_RELAXED, __HIP_MEMORY_SCOPE_AGENT);
    if (__builtin_amdgcn_readfirstlane(old) == need - 1u && l < n) __hip_atomic_store((gu32*)(sync + (f + l) * 32), 16u, __ATOMIC_RELAXED, __HIP_MEMORY_SCOPE_AGENT);
}
__device__ __forceinline__ void wg_publish(unsigned* sync, const int f, const unsigned need, const int n) {
    asm volatile("s_waitcnt vmcnt(0)" ::: "memory");
    __syncthreads();
    if (TIDX < 64) arrive(sync, f, need, n);
}
#define TMO_SET(code) { dead = true; if (l == 0) __hip_atomic_store(a.tmo, (code) + s, __ATOMIC_RELAXED, __HIP_MEMORY_SCOPE_AGENT); }

__device__ __forceinline__ void glds16h(unsigned voff, const void* sbase, unsigned lds_dst) {
    unsigned keep;
    asm volatile("s_mov_b32 %0, m0\n\ts_mov_b32 m0, %3\n\ts_nop 0\n\tglobal_load_lds_dwordx4 %1, %2 sc1\n\ts_mov_b32 m0, %0" : "=&s"(keep) : "v"(voff), "s"(sbase), "s"(lds_dst) : "memory");
}
__device__ __forceinline__ void touch(const void* p, unsigned lds_dst) {
    unsigned keep;
    asm volatile("s_mov_b32 %0, m0\n\ts_mov_b32 m0, %2\n\ts_nop 0\n\tglobal_load_lds_dword %1, off\n\ts_mov_b32 m0, %0" : "=&s"(keep) : "v"(p), "s"(lds_dst) : "memory");
}
__device__ __forceinline__ void touch_sc1(const void* p, unsigned lds_dst) {
    unsigned keep;
    asm volatile("s_mov_b32 %0, m0\n\ts_mov_b32 m0, %2\n\ts_nop 0\n\tglobal_load_lds_dword %1, off sc1\n\ts_mov_b32 m0, %0" : "=&s"(keep) : "v"(p), "s"(lds_dst) : "memory");
}
#define WEV(code)
#define WEVP
#define WEVA
constexpr int C2_K = 32768, C2_V = 65536;
constexpr int C2_R1 = 0, R1S = 3200;
constexpr int C2_R3 = C2_R1 + 4 * R1S;
constexpr int C2_MB = 24576;
#define PST(n)
#define LT(n)
#define LTA(n)
#define LTOUT0
#define LTOUT
#define CN(n)
#define CNS
#define CNOUT
#define CLOB16(a, b, c, d, e, f, g, h, i, j, k, l, m, n, o, p) "v" #a, "v" #b, "v" #c, "v" #d, "v" #e, "v" #f, "v" #g, "v" #h, "v" #i, "v" #j, "v" #k, "v" #l, "v" #m, "v" #n, "v" #o, "v" #p
#define LDS3(p) ((__attribute__((address_space(3))) v4i16*)(p))
__device__ __forceinline__ bf16x8 tr_frag(const void* p0, const void* p1) {
    v4i16 lo = __builtin_amdgcn_ds_read_tr16_b64_v4i16(LDS3(p0));
    v4i16 hi = __builtin_amdgcn_ds_read_tr16_b64_v4i16(LDS3(p1));
    return bf16x8{lo[0], lo[1], lo[2], lo[3], hi[0], hi[1], hi[2], hi[3]};
}
typedef __bf16 bf16x8v __attribute__((ext_vector_type(8)));
typedef __bf16 bf16x16v __attribute__((ext_vector_type(16)));
typedef unsigned v8u __attribute__((ext_vector_type(8)));
typedef unsigned v32u __attribute__((ext_vector_type(32)));
#define PUT8(v, o, x) { const v8u x_ = (x); _Pragma("unroll") for (int e_ = 0; e_ < 8; ++e_) (v)[(o) + e_] = x_[e_]; }
#define SUB4(v, o) v4u{(v)[(o)], (v)[(o) + 1], (v)[(o) + 2], (v)[(o) + 3]}
#define PUT4(v, o, x) { const v4u x_ = (x); (v)[(o)] = x_[0]; (v)[(o) + 1] = x_[1]; (v)[(o) + 2] = x_[2]; (v)[(o) + 3] = x_[3]; }
__device__ __forceinline__ v2u tr4(const u16* p) { return __builtin_bit_cast(v2u, __builtin_amdgcn_ds_read_tr16_b64_v4i16(LDS3(p))); }
__device__ __forceinline__ f32x4 smm(v4u a, v8u b, f32x4 c, int idx) {
    return __builtin_amdgcn_smfmac_f32_16x16x64_bf16(__builtin_bit_cast(bf16x8v, a), __builtin_bit_cast(bf16x16v, b), c, idx, 0, 0);
}
__device__ __forceinline__ v8u tr_frag64(const u16* p, const int pitch) {
    const v2u r0 = tr4(p), r1 = tr4(p + 8 * pitch), r2 = tr4(p + 32 * pitch), r3 = tr4(p + 40 * pitch);
    return v8u{r0[0], r1[0], r0[1], r1[1], r2[0], r3[0], r2[1], r3[1]};
}
#define MM(a_, b_, c_) __builtin_amdgcn_mfma_f32_16x16x32_bf16(as_frag(a_), as_frag(b_), c_, 0, 0, 0)
#define STR2(x) #x
#define STR(x) STR2(x)
#define PAUSE(n) ".rept (" STR(n) ") / 4\n\ts_nop 15\n\t.endr\n\t.rept (" STR(n) ") %% 4\n\ts_nop 3\n\t.endr\n\t"
#define ZERO4 f32x4{0.f, 0.f, 0.f, 0.f}
constexpr int XB_OFF = CAL_OFF + 262144, XP = 68;
__device__ __forceinline__ void last_rows(const char* chsm, rsrc_t rsS, const int wu, const int grp, const int c, const unsigned tau) {
    const int l = lane_now();
    __builtin_amdgcn_s_barrier();
    asm volatile("" ::: "memory");
    const float* rp = (const float*)(chsm + C2_K) + (4 * wu + (l >> 4)) * XP + 4 * (l & 15);
    const f32x4 s0 = (*(const f32x4*)rp + *(const f32x4*)(rp + 32 * XP)) + (*(const f32x4*)(rp + 64 * XP) + *(const f32x4*)(rp + 96 * XP));
    v2u o;
    o[0] = cvt_pk_bf16(s0[0], s0[1]); o[1] = cvt_pk_bf16(s0[2], s0[3]);
    st8_sc1s(rsS, l * 8, XB_OFF + ((c >> 1) * 8 + wu) * 16384 + (int)(tau & 1u) * 8192 + (c & 1) * 4096 + grp * 512, o);
}
__device__ __forceinline__ const unsigned* kv_flag(const unsigned* sync, const int sq, const int grp, const int l) { return sync + (sq < KVS ? (SY_KV0 + sq * 8 + grp) * 32 + (l & 7) : (SY_KV + 8 + grp) * 32 + (l & 3)); }
__device__ __forceinline__ void chain_body(const ChainArgs& a, const int grp, const int c, char* chsm) {
    const int wu = __builtin_amdgcn_readfirstlane(TIDX >> 6);
    const int widx = grp * 16 + c;
    const int hs = 4 * grp + (c >> 2), jq = c & 3;
    const rsrc_t rsS = slot_rsrc(a.slots);
    const unsigned l0 = (unsigned)(uintptr_t)(__attribute__((address_space(3))) char*)chsm;
    bool dead = false;

    const int cal_off = CAL_OFF + grp * 32768 + (c >> 3) * 16384 + (c & 7) * 1024;
        if (wu == 0) {
            const int l = lane_now();
            const v4u ff = {SENT, SENT, SENT, SENT};
#pragma unroll
            for (int e = 0; e < 13; ++e) st_sc1s(rsS, l * 16, widx * 2 * PERWG + min(e * 1024, 2 * PERWG - 1024), ff);
            const rsrc_t rsY = __builtin_amdgcn_make_buffer_rsrc((void*)a.sync, 0, 131072, 0x00020000);
            st_sc1s(rsY, l * 16, widx * 1024, v4u{0u, 0u, 0u, 0u});
            __builtin_amdgcn_raw_buffer_store_b32(0u, rsS, (l & 3) * 256 + ((l >> 2) & 1) * 8192, cal_off, AUX_SC1);
            if (c < 2) __builtin_amdgcn_raw_buffer_store_b32(0u, rsS, (l >> 5) * 8192 + (l & 31) * 4, TM_OFF + c * 16384 + grp * 256, AUX_SC1);
        }
    asm volatile("" ::: "memory");
    if (wu >= 4) {
        __builtin_amdgcn_s_sleep(DLY_U);
        const int l = opaque_tid() & 63, i = l & 15, g = l >> 4;
        const int sidx = (i & 1) ? 0xEEEE : 0x4444;
        const int u = wu - 4;
        const int k0 = 256 * u;
        u16* stg = (u16*)(chsm + 40960 + u * 20480);
        v4u af[4];
        v32u wS[6];
        {
            v4u raw[3][16];
            {
                const size_t wo = (size_t)k0 * HD + hcol(hs) + 32 * jq;
                const rsrc_t rq = __builtin_amdgcn_make_buffer_rsrc((void*)(a.Wq + wo), 0, 256 * HD * 2, 0x00020000);
                const rsrc_t rk = __builtin_amdgcn_make_buffer_rsrc((void*)(a.Wk + wo), 0, 256 * HD * 2, 0x00020000);
                const rsrc_t rv = __builtin_amdgcn_make_buffer_rsrc((void*)(a.Wv + wo), 0, 256 * HD * 2, 0x00020000);
                const int vw = (l >> 2) * HD * 2 + (l & 3) * 16;
#pragma unroll
                for (int p = 0; p < 4; ++p) af[p] = *(const v4u*)(a.mem0 + (size_t)(i >> 1) * DM + k0 + 64 * p + 16 * g + 8 * (i & 1));
#pragma unroll
                for (int e = 0; e < 16; ++e) raw[0][e] = __builtin_amdgcn_raw_buffer_load_b128(rq, vw, e * 16 * HD * 2, 0);
#pragma unroll
                for (int e = 0; e < 16; ++e) raw[1][e] = __builtin_amdgcn_raw_buffer_load_b128(rk, vw, e * 16 * HD * 2, 0);
#pragma unroll
                for (int e = 0; e < 12; ++e) raw[2][e] = __builtin_amdgcn_raw_buffer_load_b128(rv, vw, e * 16 * HD * 2, 0);
                __builtin_amdgcn_sched_barrier(0);
#pragma unroll
                for (int e = 0; e < 16; ++e) *(v4u*)(stg + (16 * e + (l >> 2)) * 40 + 8 * (l & 3)) = raw[0][e];
                __builtin_amdgcn_sched_barrier(0);
#pragma unroll
                for (int e = 12; e < 16; ++e) raw[2][e] = __builtin_amdgcn_raw_buffer_load_b128(rv, vw, e * 16 * HD * 2, 0);
            }
            const int fr = 16 * (g >> 1) + 4 * (g & 1) + (i >> 2);
            __builtin_amdgcn_sched_barrier(0);
#pragma unroll
            for (int hx = 0; hx < 3; ++hx) {
                if (hx) {
#pragma unroll
                    for (int e = 0; e < 16; ++e) *(v4u*)(stg + (16 * e + (l >> 2)) * 40 + 8 * (l & 3)) = raw[hx][e];
                }
                __builtin_amdgcn_s_waitcnt(0xC07F);
#pragma unroll
                for (int jj = 0; jj < 2; ++jj)
#pragma unroll
                    for (int p = 0; p < 4; ++p) PUT8(wS[2 * hx + jj], 8 * p, tr_frag64(stg + (64 * p + fr) * 40 + 16 * jj + 4 * (i & 3), 40))
                __builtin_amdgcn_s_waitcnt(0xC07F);
                __builtin_amdgcn_sched_barrier(0);
            }
        }
        PST(u)
        if (u == 0) { PST(4) }
        const unsigned r1w = l0 + C2_R1 + u * R1S + (2 * g * 16 + i) * 4, r1v = l0 + C2_R1 + u * R1S + 2176 + (2 * g * 16 + ((i & 9) | ((i & 2) << 1) | ((i & 4) >> 1))) * 4;
#define LD4(d, va, so, o) "buffer_load_dwordx4 v[" d "], " va ", %[rs], " so " offen offset:" o " sc1\n\t"
#define CHK(a, b, c, d) "v_max3_u32 v244, v244, v" a ", v" b "\n\tv_max3_u32 v244, v244, v" c ", v" d "\n\t"
#define CHK0(a, b, c, d) "v_max_u32 v244, v" a ", v" b "\n\tv_max3_u32 v244, v244, v" c ", v" d "\n\t"
#define SMF(d, a, w) "v_smfmac_f32_16x16x64_bf16 v[" d "], v[" a "], v[" w "], %[sx]\n\t"
#define ADD(d, a, b) "v_add_f32 v" d ", v" a ", v" b "\n\t"
#define Z2(r) "v_mov_b64 v[" r "], 0\n\t"
#define DW2(ad, x, y, o0, o1) "ds_write2_b32 " ad ", v" x ", v" y " offset0:" o0 " offset1:" o1 "\n\t"
#define A0 "224:227"
#define A1 "228:231"
#define A2 "232:235"
#define A3 "236:239"
#define Q0 "200:203"
#define Q1 "204:207"
#define Q2 "208:211"
#define Q3 "212:215"
#define V0 "216:219"
#define V1 "220:223"
#define S1_HEAD \
            Z2("216:217") Z2("218:219") Z2("220:221") Z2("222:223") \
            "s_waitcnt vmcnt(4)\n\t" ZQ \
            "s_waitcnt vmcnt(3)\n\t" SMF(V0, A0, "136:143") CHK0("224", "225", "226", "227") SMF(V1, A0, "168:175") \
            "s_waitcnt vmcnt(2)\n\t" SMF(V0, A1, "144:151") CHK("228", "229", "230", "231") SMF(V1, A1, "176:183") \
            "s_waitcnt vmcnt(1)\n\t" SMF(V0, A2, "152:159") CHK("232", "233", "234", "235") SMF(V1, A2, "184:191") \
            "s_waitcnt vmcnt(0)\n\t" SMF(V0, A3, "160:167") CHK("236", "237", "238", "239") SMF(V1, A3, "192:199") \
            "v_cmp_eq_u32 vcc, " STR(SENT) ", v244\n\t"
#define S1_TAIL(B1, B2) \
            SMF(Q0, A0, "8:15") SMF(Q0, A1, "16:23") SMF(Q0, A2, "24:31") \
            ADD("240", "216", "217") ADD("241", "218", "219") SMF(Q0, A3, "32:39") DW2("%[rv]", "240", "241", "0", "16") \
            ADD("242", "220", "221") ADD("243", "222", "223") SMF(Q1, A0, "40:47") DW2("%[rv]", "242", "243", "128", "144") \
            SMF(Q1, A1, "48:55") SMF(Q1, A2, "56:63") SMF(Q1, A3, "64:71") \
            ADD("240", "200", "201") ADD("241", "202", "203") SMF(Q2, A0, "72:79") DW2("%[rw]", "240", "241", "0", "16") \
            SMF(Q2, A1, "80:87") SMF(Q2, A2, "88:95") "s_nop 1\n\t" \
            ADD("242", "204", "205") ADD("243", "206", "207") SMF(Q2, A3, "96:103") DW2("%[rw]", "242", "243", "136", "152") \
            SMF(Q3, A0, "104:111") SMF(Q3, A1, "112:119") \
            B1        \
            LTA(0) \
            SMF(Q3, A2, "120:127") SMF(Q3, A3, "128:135") "s_nop 0\n\t" \
            ADD("240", "208", "209") ADD("241", "210", "211") DW2("v246", "240", "241", "0", "16") \
            "s_nop 3\n\t" \
            ADD("242", "212", "213") ADD("243", "214", "215") DW2("v246", "242", "243", "136", "152") \
            "s_waitcnt lgkmcnt(0)\n\t" B2 "s_barrier\n\t"
#define ZQ Z2("200:201") Z2("202:203") Z2("204:205") Z2("206:207") Z2("208:209") Z2("210:211") Z2("212:213") Z2("214:215")
#define U_W "{v[8:39]}"(wS[0]), "{v[40:71]}"(wS[1]), "{v[72:103]}"(wS[2]), "{v[104:135]}"(wS[3]), "{v[136:167]}"(wS[4]), "{v[168:199]}"(wS[5])
#define U_CLOB CLOB16(200, 201, 202, 203, 204, 205, 206, 207, 208, 209, 210, 211, 212, 213, 214, 215), CLOB16(216, 217, 218, 219, 220, 221, 222, 223, 224, 225, 226, 227, 228, 229, 230, 231), \
            CLOB16(232, 233, 234, 235, 236, 237, 238, 239, 240, 241, 242, 243, 244, 245, 246, 247)
        __builtin_amdgcn_s_setprio(2);
        {
            asm volatile(
                "v_add_u32 v246, 1088, %[rw]\n\t"
                S1_HEAD "s_nop 0\n\t" S1_TAIL("s_nop 1\n\t", "s_barrier\n\t")
                : LTOUT0
                : U_W, "{v[224:227]}"(af[0]), "{v[228:231]}"(af[1]), "{v[232:235]}"(af[2]), "{v[236:239]}"(af[3]), [sx] "v"(sidx), [rv] "v"(r1v), [rw] "v"(r1w)
                : "memory", "vcc", CLOB16(200, 201, 202, 203, 204, 205, 206, 207, 208, 209, 210, 211, 212, 213, 214, 215), CLOB16(216, 217, 218, 219, 220, 221, 222, 223, 240, 241, 242, 243, 244, 245, 246, 247));
        }
        const unsigned tau = __builtin_amdgcn_readfirstlane(*(volatile unsigned*)(chsm + C2_MB + 8));
        const unsigned long long wmask = u == 3 ? (c == 0 ? ~0ull : 0ull) : u == 1 ? 0x0020202020202000ull : u == 2 ? 1ull << (NSEG - 3) : 0ull;
        const int pvv = u == 3 ? 2 : -1, pvm = u == 3 ? -1 : 0;
        const unsigned* pfl = a.sync + (u == 3 ? SY_PROG + grp : u == 1 ? SY_KV + 16 + grp : SY_Q + 7 * 8 + grp) * 32;
        int s_first = 0;
        asm volatile("s_branch 1f\n\t.p2align 8\n1:" : "+s"(s_first));
        for (int s = s_first; s < NSEG - 1; ++s) {
            __builtin_amdgcn_s_setprio(0);
            LT(1)
            const int pv = (wmask >> s) & 1ull ? pvv + (s & pvm) : 0;
            const unsigned* pa = pfl;
            if (pv < 0) pfl += 256;
            const int cb = grp & 3;
            int soffc = rec_base(s) + C_OFF + c * 16384 + near_half(s, tau) + cb * 2048;
            int o_as = rec_base(s + 1) + ((c >> 3) + (grp >> 2) * 4) * 16384 + near_half(s + 1, tau) + ((4 * c + cb) & 31) * 256;
            int soff = chunk_off(s + 1, grp, u >> 1, tau) + (u & 1) * 4096;
            const int o_tm = (s >> 1) * 2097152 + TM_OFF + (s & 1) * 16384;
            int lim = dead ? 0 : SPIN_LIMIT, bad = 0;
            const bool rcd = u > 0 || widx < 32;
            const int pc = u > 0 ? 3 * widx + u - 1 : rcd ? 384 + widx : s * 96 + widx - 32;
            const int o_arm = (rcd ? s + 2 < NSEG : pc < 512) ? (rcd ? rec_base(s + 2) : XB_OFF) + (pc >> 3) * 16384 + near_half(rcd ? s + 2 : 0, tau) + (pc & 7) * 1024 : OOB;
            bool there = true;
            if (__builtin_expect(s <= KVS, 0)) {
                there = __all(ld_flag(kv_flag(a.sync, s + 2, grp, l)) != 0u);
                *(volatile unsigned*)(chsm + C2_MB + 32 + (s & 1) * 16 + u * 4) = there ? 1u : 0u;
                __builtin_amdgcn_s_waitcnt(0xC07F);
            }
            const char* vb = !there ? nullptr : arena(a.slots) + (s + 2 >= NSEG ? KX_OFF : VX_OFF) + (blk_off(min(s + 2, NSEG - 1), 4 * grp + 2 * u - 2, tau) ^ (s + 2 >= NSEG ? 8192 : 0));
            asm volatile("" : "+s"(soffc), "+s"(o_as), "+s"(soff), "+s"(lim), "+s"(vb));
#define GL(o) "global_load_lds_dwordx4 %[l16], s[84:85] offset:" o " sc1\n\t"
#define GL4 GL("0") GL("1024") GL("2048") GL("3072")
#define GLN(d) "s_add_u32 m0, m0, 4096\n\ts_add_u32 s84, s84, " d "\n\ts_addc_u32 s85, s85, 0\n\t"
#define PKE PAUSE(PKA)
#define C_TRY(x, x0, x1, x2, x3, y, y0, y1, y2, y3) \
                "s_waitcnt vmcnt(3)\n\t" \
                "v_mfma_f32_16x16x32_bf16 v[240:243], v[216:219], v[" x "], 0\n\t" CHK0(x0, x1, x2, x3) \
                "s_waitcnt vmcnt(2)\n\t" \
                "v_mfma_f32_16x16x32_bf16 v[240:243], v[216:219], v[" y "], v[240:243]\n\t" CHK(y0, y1, y2, y3) \
                "v_cmp_eq_u32 vcc, " STR(SENT) ", v244\n\t" \
                "s_cbranch_vccz 3b\n\t" CN(1) \
                LD4(x, "%[l16]", "%[sc]", "0") LD4(y, "%[l16]", "%[sc]", "1024")
            asm volatile(
                "v_and_b32 v221, 16, %[l16]\n\t"
                "v_mov_b32 v222, 0x3f80\n\t"
                "v_bfe_u32 v223, %[l16], 5, 2\n\t"
                "v_lshlrev_b32 v221, v221, v222\n\t"
                "v_cmp_eq_u32 vcc, 0, v223\n\tv_and_b32 v220, 0xf0, %[l16]\n\tv_bfe_u32 v222, %[l16], 8, 1\n\tv_cndmask_b32 v216, 0, v221, vcc\n\t"
                "v_cmp_eq_u32 vcc, 1, v223\n\tv_lshl_add_u32 v220, v222, 3, v220\n\tv_lshrrev_b32 v222, 9, %[l16]\n\tv_cndmask_b32 v217, 0, v221, vcc\n\t"
                "v_cmp_eq_u32 vcc, 2, v223\n\tv_lshl_add_u32 v220, v222, 30, v220\n\ts_nop 0\n\tv_cndmask_b32 v218, 0, v221, vcc\n\t"
                "v_cmp_eq_u32 vcc, 3, v223\n\ts_nop 1\n\tv_cndmask_b32 v219, 0, v221, vcc\n\t"
                "v_mul_u32_u24 v222, 0x8000, v222\n\t"
                "v_and_b32 v221, 0xff, v220\n\t"
                "v_add_u32 v221, v221, v222\n\t"
                "v_lshrrev_b32 v247, 2, %[l16]\n\t"
                PKE
                "v_mov_b32 v240, " STR(SENT) "\n\tv_mov_b32 v241, v240\n\tv_mov_b32 v242, v240\n\tv_mov_b32 v243, v240\n\t"
                "buffer_store_dwordx4 v[240:243], %[l16], %[rs], %[arm] offen sc1\n\t"
                LTA(2)
                "s_setprio 2\n\t"
                "s_barrier\n\t"
                LTA(3)
                "v_add_u32 v246, 1088, %[rw]\n\t"
                "s_cmp_lt_u32 %[uu], 1\n\t"
                "s_cbranch_scc1 7f\n\t"
                "s_cmp_eq_u32 %[uu], 3\n\t"
                "s_cbranch_scc1 20f\n\t"
                "s_memrealtime s[84:85]\n\t"
                "v_lshrrev_b32 v241, 9, %[l16]\n\t"
                "v_and_b32 v242, 0x7c, v247\n\t"
                "v_lshl_add_u32 v241, v241, 13, v242\n\t"
                "s_waitcnt lgkmcnt(0)\n\t"
                "v_mov_b32 v240, s84\n\t"
                "buffer_store_dword v240, v241, %[rs], %[ts] offen sc1\n\t"
                "s_cmp_eq_u64 %[vb], 0\n\t"
                "s_cbranch_scc1 11f\n\t"
                "s_mov_b32 s86, m0\n\t"
                "s_mov_b64 s[84:85], %[vb]\n\t"
                "s_mov_b32 m0, %[lv]\n\t"
                PAUSE(PG0)
                "s_cmp_eq_u32 %[uu], 1\n\t"
                "s_cbranch_scc1 12f\n\t"
                PAUSE(PG1)
                "12:\n\t"
                GL4 GLN("4096") GL4 GLN("12288") GL4 GLN("4096") GL4
                "s_mov_b32 m0, s86\n\t"
                "11:\n\t"
                "s_cmp_lt_i32 %[pv], 0\n\t"
                "s_cbranch_scc0 8f\n\t"
                "v_and_b32 v241, 12, v247\n\t"
                "10:\n\t"
                "global_load_dword v240, v241, %[pa] sc1\n\t"
                "s_waitcnt vmcnt(0)\n\t"
                "v_cmp_gt_u32 vcc, 16, v240\n\t"
                "s_cbranch_vccz 8f\n\t"
                "s_sleep 6\n\t"
                "s_sub_u32 %[lim], %[lim], 1\n\t"
                "s_cbranch_scc0 10b\n\t"
                "s_mov_b32 %[bad], 3000\n\t"
                "s_mov_b32 %[lim], 0\n\t"
                "s_branch 8f\n\t"
                "20:\n\t"
                PAUSE(PS)
                "s_memrealtime s[84:85]\n\t"
                "s_load_dword s86, %[tb], 0\n\ts_load_dword s87, %[tb], 0x100\n\ts_load_dword s88, %[tb], 0x200\n\ts_load_dword s89, %[tb], 0x300\n\t"
                "s_load_dword s90, %[tb], 0x400\n\ts_load_dword s91, %[tb], 0x500\n\ts_load_dword s92, %[tb], 0x600\n\ts_load_dword s93, %[tb], 0x700\n\t"
                "s_waitcnt lgkmcnt(0)\n\t"
                "s_sub_u32 s86, s84, s86\n\ts_sub_u32 s87, s84, s87\n\ts_sub_u32 s88, s84, s88\n\ts_sub_u32 s89, s84, s89\n\t"
                "s_sub_u32 s90, s84, s90\n\ts_sub_u32 s91, s84, s91\n\ts_sub_u32 s92, s84, s92\n\ts_sub_u32 s93, s84, s93\n\t"
                "s_or_b32 s85, s86, s87\n\ts_min_u32 s86, s86, s87\n\ts_or_b32 s85, s85, s88\n\ts_min_u32 s86, s86, s88\n\ts_or_b32 s85, s85, s89\n\ts_min_u32 s86, s86, s89\n\t"
                "s_memrealtime s[88:89]\n\t"
                "s_or_b32 s85, s85, s90\n\ts_min_u32 s86, s86, s90\n\ts_or_b32 s85, s85, s91\n\ts_min_u32 s86, s86, s91\n\ts_or_b32 s85, s85, s92\n\ts_min_u32 s86, s86, s92\n\t"
                "s_or_b32 s85, s85, s93\n\ts_min_u32 s86, s86, s93\n\t"
                "s_cmp_gt_u32 s85, 127\n\t"
                CNS
                "s_cselect_b32 s85, " STR(TE0) ", 127\n\t"
                "s_min_u32 s86, s86, s85\n\t"
                "s_sub_u32 s84, s84, s86\n\t"
                "s_add_u32 s84, s84, %[td]\n\t"
                "s_cmp_lt_i32 %[pv], 1\n\t"
                "s_cbranch_scc1 4f\n\t"
                "v_mov_b32 v247, %[pv]\n\t"
                "s_mov_b64 exec, 1\n\t"
                "global_store_dword %[l16], v247, %[pa] sc1\n\t"
                "s_mov_b64 exec, -1\n\t"
                "4:\n\t"
                "s_waitcnt lgkmcnt(0)\n\t"
                "s_sub_i32 s84, s84, s88\n\t"
                "s_max_i32 s84, s84, 0\n\t"
                "s_min_i32 s84, s84, 40\n\t"
                "21:\n\t"
                "s_sub_u32 s84, s84, 1\n\t"
                "s_cbranch_scc0 21b\n\t"
                "s_branch 8f\n\t"
                "7:\n\t"
                PAUSE(PC)
                LD4(A0, "%[l16]", "%[sc]", "0") LD4(A1, "%[l16]", "%[sc]", "1024")
                PAUSE(PP)
                LD4(A2, "%[l16]", "%[sc]", "0") LD4(A3, "%[l16]", "%[sc]", "1024")
                "s_waitcnt vmcnt(3)\n\t"
                "v_mfma_f32_16x16x32_bf16 v[240:243], v[216:219], v[224:227], 0\n\t" CHK0("224", "225", "226", "227")
                "s_waitcnt vmcnt(2)\n\t"
                "v_mfma_f32_16x16x32_bf16 v[240:243], v[216:219], v[228:231], v[240:243]\n\t" CHK("228", "229", "230", "231")
                "v_cmp_eq_u32 vcc, " STR(SENT) ", v244\n\t"
                "s_cbranch_vccnz 30f\n\t"
                "3:\n\t"
                "s_nop 3\n\t"
                "v_cvt_pk_bf16_f32 v244, v240, v241\n\t"
                "v_cvt_pk_bf16_f32 v245, v242, v243\n\t"
                "buffer_store_dwordx2 v[244:245], v221, %[rs], %[oas] offen sc1\n\t"
                PAUSE(PB)
                "8:\n\t"
                LTA(4)
                "s_barrier\n\t"
                PAUSE(1)
                "2:\n\t"
                LD4(A0, "%[l16]", "%[sa]", "0") LD4(A1, "%[l16]", "%[sa]", "1024") LD4(A2, "%[l16]", "%[sa]", "2048") LD4(A3, "%[l16]", "%[sa]", "3072")
                S1_HEAD
                "s_cbranch_vccnz 6f\n\t"
                "5:\n\t"
                LTA(5)
                S1_TAIL("s_waitcnt lgkmcnt(0)\n\ts_barrier\n\t", "")
                "s_setprio 0\n\t"
                "s_branch 9f\n\t"
                "30:\n\t" CN(1)
                LD4(A0, "%[l16]", "%[sc]", "0") LD4(A1, "%[l16]", "%[sc]", "1024")
                "1:\n\t"
                C_TRY(A2, "232", "233", "234", "235", A3, "236", "237", "238", "239")
                C_TRY(A0, "224", "225", "226", "227", A1, "228", "229", "230", "231")
                "s_sub_u32 %[lim], %[lim], 1\n\t"
                "s_cbranch_scc0 1b\n\t"
                "s_mov_b32 %[bad], 2000\n\t"
                "s_mov_b32 %[lim], 0\n\t"
                "s_branch 3b\n\t"
                "6:\n\t" CN(65536)
                "v_mov_b32 v240, %[tg]\n\t"
                "v_mov_b32 v241, %[lf]\n\t"
                "ds_write_b32 v241, v240\n\t"
                "s_nop 0\n\t"
                "s_sub_u32 %[lim], %[lim], 1\n\t"
                "s_cbranch_scc0 2b\n\t"
                "s_mov_b32 %[bad], 1\n\t"
                "s_branch 5b\n\t"
                "9:\n\t"
                : [lim] "+s"(lim), [bad] "+s"(bad), [oas] "+s"(o_as) LTOUT CNOUT
                : U_W, [l16] "v"(l * 16), [rs] "s"(rsS), [sx] "v"(sidx), [rv] "v"(r1v), [rw] "v"(r1w),
                  [sc] "s"(soffc), [sa] "s"(soff), [uu] "s"(u), [pv] "s"(pv), [pa] "s"(pa), [vb] "s"(vb), [lv] "s"(l0 + (s + 2 >= NSEG ? C2_V + 32768 : s & 1 ? C2_K : C2_V) + (u - 1) * 16384), [td] "s"(s < SE ? TDE : TD), [ts] "s"(c == 0 && (u == 2 || s < NSEG - 3) ? o_tm + grp * 256 + (2 - u) * 2097152 : OOB), [tb] "s"(arena(a.slots) + o_tm + near_half(s, tau)),

                  [arm] "s"(o_arm),
                  [tg] "s"(s + 1), [lf] "s"(l0 + C2_MB + 16)
                : "memory", "vcc", "scc", "s84", "s85", "s86", "s87", "s88", "s89", "s90", "s91", "s92", "s93", U_CLOB);
            if (__builtin_expect(bad != 0, 0)) TMO_SET((unsigned)bad)
        }
        last_rows(chsm, rsS, wu, grp, c, tau);
        if (u == 3 && widx == 0 && l == 0) __hip_atomic_store((gu64*)(a.boot + 144), *(volatile unsigned long long*)(chsm + C2_MB), __ATOMIC_RELAXED, __HIP_MEMORY_SCOPE_AGENT);
        return;
    }
    const int l = opaque_tid() & 63, i = l & 15, g = l >> 4;
    const int sidx = (i & 1) ? 0xEEEE : 0x4444;
    const int ha = 4 * grp + wu;
    unsigned long long tag;
    v32u wO[2];
    {
        u16* stg = (u16*)(chsm + 122880 + wu * 10240);
        tag = boot_tag(a.boot);
        asm volatile("" ::: "memory");
        v4u rawo[16];
        const u16* src = a.Wo + (size_t)(hcol(ha) + (l >> 3)) * DM + 64 * c + 8 * (l & 7);
#pragma unroll
        for (int e = 0; e < 16; ++e) rawo[e] = *(const v4u*)(src + (size_t)8 * e * DM);
        __builtin_amdgcn_sched_barrier(0);
        if (wu == 0) {
            asm volatile("s_waitcnt vmcnt(16)" ::: "memory");
            unsigned tm[2] = {~0u, ~0u};
            const char* pb = arena(a.slots) + cal_off;
#pragma unroll
            for (int k = 0; k < 2 * NPROBE; ++k) {
                unsigned long long c0, c1; unsigned d;
                asm volatile("s_memtime %0\n\ts_waitcnt lgkmcnt(0)\n\ts_load_dword %2, %3, %4\n\ts_waitcnt lgkmcnt(0)\n\ts_memtime %1\n\ts_waitcnt lgkmcnt(0)"
                             : "=&s"(c0), "=&s"(c1), "=&s"(d) : "s"(pb), "i"((k >> 1) * 256 + (k & 1) * 8192) : "memory");
                tm[k & 1] = min(tm[k & 1], (unsigned)(c1 - c0));
            }
            const unsigned side = (__builtin_amdgcn_s_getreg(20 | (3 << 11)) >> 2) & 1u;
            if (l == 0) __hip_atomic_store((gu64*)(a.boot + widx), (tag << 2) | (((tm[0] > tm[1] ? 1u : 0u) ^ side) << 1) | side, __ATOMIC_RELAXED, __HIP_MEMORY_SCOPE_AGENT);
            PST(7)
        }
#pragma unroll
        for (int m = 0; m < 2; ++m) {
#pragma unroll
            for (int e = 0; e < 8; ++e) *(v4u*)(stg + (8 * e + (l >> 3)) * 72 + 8 * (l & 7)) = rawo[8 * m + e];
            __builtin_amdgcn_s_waitcnt(0xC07F);
#pragma unroll
            for (int ct = 0; ct < 4; ++ct) {
                const u16* p = stg + (8 * g + (i >> 2)) * 72 + 16 * ct + 4 * (i & 3);
                const v2u r0 = tr4(p), r1 = tr4(p + 4 * 72), r2 = tr4(p + 32 * 72), r3 = tr4(p + 36 * 72);
                PUT4(wO[ct >> 1], (ct & 1) * 16 + m * 8, (v4u{r0[0], r0[1], r1[0], r1[1]})) PUT4(wO[ct >> 1], (ct & 1) * 16 + m * 8 + 4, (v4u{r2[0], r2[1], r3[0], r3[1]}))
            }
            __builtin_amdgcn_s_waitcnt(0xC07F);
        }
        if (wu == 0) { PST(5) }
        if (widx == 0 && wu == 0) *(volatile unsigned long long*)(chsm + C2_MB) = tag;
    }
    if (wu >= 2) touch_sc1(arena(a.slots) + (size_t)min(l + (wu - 2) * 64, FX_OFF / 2097152 - 1) * 2097152 + 2097024 - 128, l0 + C2_MB + 256);
    unsigned long long fw = 0;
#pragma nounroll
    for (int spin = 0; spin < 100000; ++spin) {
        asm volatile("" ::: "memory");
        fw = __hip_atomic_load((gu64*)(a.boot + 256 + 16 * grp + (l & 7)), __ATOMIC_RELAXED, __HIP_MEMORY_SCOPE_AGENT);
        if (__all((fw | 255u) == ((tag << 8) | 255u))) break;
        __builtin_amdgcn_s_sleep(4);
    }
    asm volatile("" ::: "memory");
    const unsigned tau = __builtin_amdgcn_readfirstlane((unsigned)fw & 255u);
    if (wu == 3) { *(volatile unsigned*)(chsm + C2_MB + 8) = tau; *(volatile unsigned*)(chsm + C2_MB + 16) = ~0u; __builtin_amdgcn_s_waitcnt(0xC07F); }
    if (wu == 1) { PST(9) }

    const unsigned r3a = l0 + C2_R3 + wu * 2048 + g * 128 + i * 4;
    v32u yb;
    v8u kx;
    v8u sel; v2u sel3;
#pragma unroll
    for (int d = 0; d < 8; ++d) sel[d] = (i >> 1) == d ? (0x3C00u << ((i & 1) * 16)) : 0u;
#pragma unroll
    for (int d = 0; d < 2; ++d) sel3[d] = (i & 3) == 0 && (i >> 3) == d ? (0x3C00u << (((i >> 2) & 1) * 16)) : 0u;
    asm volatile("s_setreg_imm32_b32 hwreg(HW_REG_MODE, 23, 1), 1\n\ts_nop 3");
#define MF(d, a, b, c) "v_mfma_f32_16x16x32_bf16 v[" d "], v[" a "], v[" b "], " c "\n\t"
#define PX4(d0, d1, d2, d3, a, w0, w1, w2, w3) MF(d0, a, w0, "v[" d0 "]") MF(d1, a, w1, "v[" d1 "]") MF(d2, a, w2, "v[" d2 "]") MF(d3, a, w3, "v[" d3 "]")
#define PXT(d0, d1, d2, d3, a0, a1, a2, a3) MF(d0, a0, "32:35", "0") MF(d1, a0, "48:51", "0") MF(d2, a0, "64:67", "0") MF(d3, a0, "80:83", "0") \
        PX4(d0, d1, d2, d3, a1, "36:39", "52:55", "68:71", "84:87") PX4(d0, d1, d2, d3, a2, "40:43", "56:59", "72:75", "88:91") PX4(d0, d1, d2, d3, a3, "44:47", "60:63", "76:79", "92:95")
#define CVP(d, a, b) "v_cvt_pk_bf16_f32 v" d ", v" a ", v" b "\n\t"
#define PROJ_X(cnt) \
        "s_waitcnt " cnt "(8)\n\t" PXT("192:195", "196:199", "200:203", "204:207", "160:163", "164:167", "168:171", "172:175") \
        "s_waitcnt " cnt "(4)\n\t" PXT("208:211", "212:215", "216:219", "220:223", "176:179", "180:183", "184:187", "188:191") \
        CVP("128", "192", "193") CVP("129", "194", "195") CVP("136", "196", "197") CVP("137", "198", "199") CVP("144", "200", "201") CVP("145", "202", "203") CVP("152", "204", "205") CVP("153", "206", "207") \
        CVP("132", "208", "209") CVP("133", "210", "211") CVP("140", "212", "213") CVP("141", "214", "215") CVP("148", "216", "217") CVP("149", "218", "219") CVP("156", "220", "221") CVP("157", "222", "223") \
        "s_waitcnt " cnt "(0)\n\t"
#define CLOB_PX CLOB16(160, 161, 162, 163, 164, 165, 166, 167, 168, 169, 170, 171, 172, 173, 174, 175), CLOB16(176, 177, 178, 179, 180, 181, 182, 183, 184, 185, 186, 187, 188, 189, 190, 191), \
        CLOB16(192, 193, 194, 195, 196, 197, 198, 199, 200, 201, 202, 203, 204, 205, 206, 207), CLOB16(208, 209, 210, 211, 212, 213, 214, 215, 216, 217, 218, 219, 220, 221, 222, 223)
#define LDX(d, va, so, o) "buffer_load_dwordx4 v[" d "], " va ", %[rs], " so " offen offset:" o " sc1\n\t"
#define DSV(d, o) "ds_read_b128 v[" d "], %[lv] offset:" o "\n\t"
    const int voffX = ((i >> 1) & 1) * 4096 + g * 256 + (2 * (i >> 2) + (i & 1)) * 16;
    const int pk = (((i >> 1) & 3) << 1) | (i >> 3);
    const unsigned hv = wu >= 2 ? l0 + C2_R1 + 2176 + ((wu & 1) * 128 + ((l >> 4) + 4 * ((l >> 3) & 1)) * 16 + 8 * ((l >> 1) & 1) + 4 * ((l >> 2) & 1) + 2 * (l & 1)) * 4
                                : l0 + C2_R1 + (i & 1) * 544 + (i >> 1) * 64 + g * 16;
    const unsigned hk = l0 + C2_R1 + 1088 + (i & 1) * 544 + pk * 64 + g * 16;
    const int vo1 = wu >= 2 ? l * 4 : wu == 1 ? (g >> 1) * 1024 + i * 16 + (g & 1) * 8 : (i & 1) ? 1 << 30 : (i >> 1) * 16 + 4 * g;
    const int vo2 = (2 * (i & 1) + (g >> 1)) * 128 + pk * 16 + (g & 1) * 8;
    const int voffK = (g >> 1) * 256 + i * 16 + (g & 1) * 8;
#define RD4(w, ad, a, b, c, d) "ds_read_" w " v[" a "], " ad "\n\tds_read_" w " v[" b "], " ad " offset:3200\n\tds_read_" w " v[" c "], " ad " offset:6400\n\tds_read_" w " v[" d "], " ad " offset:9600\n\t"
#define AD4(d0, d1, d2, d3, a0, a1, a2, a3) ADD(d0, d0, a0) ADD(d1, d1, a1) ADD(d2, d2, a2) ADD(d3, d3, a3)
#define QSC(r) "v_mul_f32 v" r ", 0x3e0293ee, v" r "\n\t"
#define FLD "buffer_load_dword v114, %[vf], %[rs], %[fl] offen sc1\n\t"
#define DUM "buffer_store_dword v168, off, %[rs], %[ob] sc1\n\t"
#define QHEAD "s_barrier\n\t" RD4("b128", "%[hv]", "160:163", "164:167", "168:171", "172:175") FLD \
        "s_waitcnt lgkmcnt(2)\n\t" AD4("160", "161", "162", "163", "164", "165", "166", "167") \
        "s_waitcnt lgkmcnt(0)\n\t" AD4("168", "169", "170", "171", "172", "173", "174", "175") \
        "s_barrier\n\t"
#define QTAIL AD4("160", "161", "162", "163", "168", "169", "170", "171") QSC("160") QSC("161") QSC("162") QSC("163")
#define CVH(d, a, b) "v_cvt_pk_f16_f32 v" d ", v" a ", v" b "\n\t"
#define SWA(d, a, b) "v_add_f32_dpp v" d ", v" a ", v" b " quad_perm:[1,0,3,2] row_mask:0xf bank_mask:0xf\n\t"
#define KLD(so) "buffer_load_dwordx2 v[96:97], %[vk], %[rs], " so " offen sc1\n\tbuffer_load_dwordx2 v[102:103], %[vk], %[rs], " so " offen offset:512 sc1\n\t" \
        "s_add_u32 " so ", " so ", 4096\n\t" \
        "buffer_load_dwordx2 v[100:101], %[vk], %[rs], " so " offen sc1\n\tbuffer_load_dwordx2 v[98:99], %[vk], %[rs], " so " offen offset:512 sc1\n\t"
    auto hand_over = [&](const int o_1, const int o_2, const int o_f, const int tg) {
        int dmy = 0;
        asm volatile(
            "s_cmp_eq_u32 %[wu], 0\n\t"
            "s_cbranch_scc1 2f\n\t"
            "s_cmp_eq_u32 %[wu], 1\n\t"
            "s_cbranch_scc1 1f\n\t"
            "s_barrier\n\t"
            RD4("b64", "%[hv]", "160:161", "162:163", "164:165", "166:167") FLD
            "s_waitcnt lgkmcnt(2)\n\t" ADD("160", "160", "162") ADD("161", "161", "163")
            "s_waitcnt lgkmcnt(0)\n\t" ADD("164", "164", "166") ADD("165", "165", "167") ADD("160", "160", "164") ADD("161", "161", "165")
            CVP("168", "160", "161")
            "buffer_store_dword v168, %[v1], %[rs], %[o1] offen sc1\n\t"
            "s_barrier\n\t"
            DUM PAUSE(PV2)
            "s_branch 3f\n\t"
            "5:\n\t"
            "v_mov_b32 v115, %[lf]\n\t"
            "ds_read_b32 v115, v115\n\t"
            "s_waitcnt lgkmcnt(0)\n\t"
            "v_cmp_eq_u32 vcc, %[tg], v115\n\t"
            "s_cbranch_vccnz 4f\n\t" CN(256)
            PAUSE(PL)
            "s_branch 4f\n\t"
            "1:\n\t"
            Z2("176:177") Z2("178:179")
            "s_waitcnt vmcnt(0)\n\t"
            QHEAD QTAIL
            "s_mov_b64 exec, %[em]\n\t"
            CVP("176", "160", "161") CVP("177", "162", "163")
            "s_not_b64 exec, exec\n\t"
            CVP("178", "160", "161") CVP("179", "162", "163")
            "s_mov_b64 exec, -1\n\t"
            "s_nop 0\n\t"
            MF("180:183", "96:99", "176:179", "0") MF("184:187", "100:103", "176:179", "0")
            "s_nop 7\n\t"
            SWA("180", "184", "180") SWA("181", "185", "181") SWA("182", "186", "182") SWA("183", "187", "183")
            CVH("188", "180", "181") CVH("189", "182", "183")
            "buffer_store_dwordx2 v[188:189], %[v1], %[rs], %[o1] offen sc1\n\t"
            DUM PAUSE(PV1)
            "s_branch 3f\n\t"
            "2:\n\t"
            QHEAD
            RD4("b128", "%[hk]", "176:179", "180:183", "184:187", "188:191")
            QTAIL CVP("164", "160", "161") CVP("165", "162", "163")
            "s_waitcnt lgkmcnt(2)\n\t" AD4("176", "177", "178", "179", "180", "181", "182", "183")
            "s_waitcnt lgkmcnt(0)\n\t" AD4("184", "185", "186", "187", "188", "189", "190", "191") AD4("176", "177", "178", "179", "184", "185", "186", "187")
            CVP("166", "176", "177") CVP("167", "178", "179")
            "s_nop 1\n\t"
            "v_mfma_f32_16x16x16_bf16 v[168:171], v[166:167], v[164:165], 0\n\t"
            "buffer_store_dwordx2 v[166:167], %[v2], %[rs], %[o2] offen sc1\n\t"
            "s_nop 6\n\t"
            SWA("172", "169", "168") SWA("173", "171", "170")
            CVH("174", "172", "173")
            "buffer_store_dword v174, %[v1], %[rs], %[o1] offen sc1\n\t"
            "3:\n\t"
            PAUSE(PVA)
            "s_waitcnt vmcnt(2)\n\t"
            "v_cmp_ne_u32 vcc, " STR(SENT) ", v114\n\t"
            "s_cbranch_vccnz 5b\n\t"
            "4:\n\t"
            : [dm] "+s"(dmy) CNOUT : "{v[96:103]}"(kx), [fl] "s"(o_f), [vf] "v"(i * 128), [ob] "s"(1 << 30), [tg] "s"(tg), [lf] "s"(l0 + C2_MB + 16), [wu] "s"(wu), [hv] "v"(hv), [hk] "v"(hk), [v1] "v"(vo1), [v2] "v"(vo2), [rs] "s"(rsS), [o1] "s"(o_1), [o2] "s"(o_2), [em] "s"(0x5555555555555555ull)
            : "memory", "scc", "vcc", "v114", "v115", CLOB16(160, 161, 162, 163, 164, 165, 166, 167, 168, 169, 170, 171, 172, 173, 174, 175), CLOB16(176, 177, 178, 179, 180, 181, 182, 183, 184, 185, 186, 187, 188, 189, 190, 191));
    };
    auto o1_of = [&](const int s) { return wu >= 2 ? v_off(s, hs, tau) + jq * 512 + (wu & 1) * 256 : wu == 1 ? x_off(s, hs, tau) + jq * 256 : m_off(s, hs, tau) + jq * 128; };

    __builtin_amdgcn_s_setprio(1);
    int o_f = f_off(0, grp, tau);
    int o_cs = rec_base(0) + C_OFF + c * 16384 + near_half(0, tau) + wu * 2048 + grp * 256;
    int o_1 = o1_of(0), o_2 = km_off(0, hs, tau) + jq * 512, bq = x_off(0, ha, tau), bm = m_off(0, ha, tau), bb = v_off(0, ha, tau);
    {
            const int b0 = blk_off(0, ha, tau);
            int k0 = KX_OFF + blk_off(0, hs, tau) + jq * 1024;
            asm volatile(
                LDX("160:163", "%[vx]", "%[vb]", "0") LDX("164:167", "%[vx]", "%[vb]", "1024") LDX("168:171", "%[vx]", "%[vb]", "2048") LDX("172:175", "%[vx]", "%[vb]", "3072")
                LDX("176:179", "%[vx]", "%[vb]", "128") LDX("180:183", "%[vx]", "%[vb]", "1152") LDX("184:187", "%[vx]", "%[vb]", "2176") LDX("188:191", "%[vx]", "%[vb]", "3200")
                KLD("%[k0]")
                "v_mov_b32 v131, 0\n\tv_mov_b32 v135, 0\n\tv_mov_b32 v139, 0\n\tv_mov_b32 v143, 0\n\tv_mov_b32 v147, 0\n\tv_mov_b32 v151, 0\n\tv_mov_b32 v155, 0\n\tv_mov_b32 v159, 0\n\t"
                PROJ_X("vmcnt")
                : "=&{v[128:159]}"(yb), "=&{v[96:103]}"(kx), [k0] "+s"(k0)
                : "{v[32:63]}"(wO[0]), "{v[64:95]}"(wO[1]), [vx] "v"(voffX), [vk] "v"(voffK), [rs] "s"(rsS), [vb] "s"(VX_OFF + b0)
                : "memory", "scc", CLOB_PX);
        if (wu == 1) { PST(10) }
    }
    int s_first = 0;
    asm volatile("s_branch 1f\n\t.p2align 8\n1:" : "+s"(s_first));
    for (int s = s_first; s < NSEG - 1; ++s) {
        hand_over(o_1, o_2, o_f, s);
        if (s == 0 && wu == 1) { PST(8) }
        LT(0)
#define LDPA "buffer_load_dwordx2 v[160:161], v254, %[rs], %[bm] offen sc1\n\t" LDX("164:167", "%[l16]", "%[bq]", "0") LDX("168:171", "%[l16]", "%[bq]", "1024")
#define SM(d, a, b, x) "v_smfmac_f32_16x16x64_bf16 v[" d "], v[" a "], v[" b "], " x "\n\t"
#define MX2(a, b) "v_max_u32 v244, v" a ", v" b "\n\t"
#define MX3(a, b) "v_max3_u32 v244, v244, v" a ", v" b "\n\t"
#define ZAM Z2("208:209") Z2("210:211") Z2("212:213") Z2("214:215") Z2("216:217") Z2("218:219") Z2("220:221") Z2("222:223")
#define MUL(d, a, b) "v_mul_f32 v" d ", v" a ", v" b "\n\t"
#define CV0(d, a) "v_cvt_pk_bf16_f32 v" d ", v" a ", 0\n\t"
#define RDC "ds_read_b64 v[224:225], %[rc]\n\tds_read_b64 v[226:227], %[rc] offset:2048\n\tds_read_b64 v[228:229], %[rc] offset:4096\n\tds_read_b64 v[230:231], %[rc] offset:6144\n\t"
#define EXD(d, r) "v_exp_f32 v" d ", v" r "\n\t"
#define SUBM(r) "v_sub_f32 v" r ", v" r ", v250\n\t"
#define PCVM CVP("241", "180", "180") CVP("243", "180", "180")
#define PCVX CVP("242", "178", "179") CVP("240", "176", "177")
#define OUT(t0, t1, a0, a1, a2, a3, ad, o0, o1) ADD(t0, a0, a1) ADD(t1, a2, a3) MUL(t0, t0, "250") MUL(t1, t1, "251") DW2(ad, t0, t1, o0, o1)
#define TS(n)
#define TSOUT
        int lim = dead ? 0 : SPIN_LIMIT, bad = 0;
        asm volatile(
            LDX("192:195", "%[l16]", "%[bb]", "0") LDX("196:199", "%[l16]", "%[bb]", "1024")
            ZAM Z2("224:225") Z2("226:227")
            Z2("16:17") Z2("18:19") Z2("20:21") Z2("22:23") Z2("24:25") Z2("26:27") Z2("28:29") Z2("30:31")
            "v_mov_b32 v200, 0x3f803f80\n\tv_mov_b32 v201, 0x3f803f80\n\tv_mov_b32 v202, 0x3f80\n\tv_mov_b32 v203, 0\n\t"
            "v_mov_b64 v[204:205], v[200:201]\n\tv_mov_b64 v[206:207], v[202:203]\n\t"
            "v_xor_b32 v252, 0xa000, %[sx]\n\tv_add_u32 v253, 1024, %[ra]\n\tv_lshrrev_b32 v254, 1, %[l16]\n\t"
            PAUSE(PV)
            TS(8)
            LDPA
            "4:\n\t"
            "s_waitcnt vmcnt(4)\n\t"
            SM("208:211", "192:195", "32:39", "%[sx]") MX2("192", "193") SM("212:215", "192:195", "48:55", "%[sx]") MX3("194", "195")
            SM("216:219", "192:195", "64:71", "%[sx]") SM("220:223", "192:195", "80:87", "%[sx]")
            "s_waitcnt vmcnt(3)\n\t"
            SM("208:211", "196:199", "40:47", "%[sx]") MX3("196", "197") SM("212:215", "196:199", "56:63", "%[sx]") MX3("198", "199")
            SM("216:219", "196:199", "72:79", "%[sx]") "v_cmp_eq_u32 vcc, " STR(SENT) ", v244\n\t" SM("220:223", "196:199", "88:95", "%[sx]")
            "s_cbranch_vccnz 5f\n\t"
            "1:\n\t" TS(11)
            "s_waitcnt vmcnt(2)\n\t"
            "v_mfma_f32_16x16x16_f16 v[236:239], v[112:113], v[160:161], 0\n\t"
            MX2("160", "161") ADD("245", "208", "209") ADD("246", "210", "211") CV0("130", "245") CV0("134", "246")
            ADD("245", "212", "213") ADD("246", "214", "215") CV0("138", "245") CV0("142", "246")
            EXD("180", "236")
            "s_waitcnt vmcnt(1)\n\t"
            "v_mfma_f32_16x16x32_f16 v[228:231], v[104:107], v[164:167], 0\n\t"
            MX3("164", "165") MX3("166", "167") ADD("245", "216", "217") ADD("246", "218", "219") CV0("146", "245") CV0("150", "246")
            ADD("245", "220", "221") ADD("246", "222", "223") CV0("154", "245") CV0("158", "246") PCVM
            "s_waitcnt vmcnt(0)\n\t"
            "v_mfma_f32_16x16x32_f16 v[228:231], v[108:111], v[168:171], v[228:231]\n\t"
            MX3("168", "169") MX3("170", "171")
            "v_cmp_eq_u32 vcc, " STR(SENT) ", v244\n\t"
            "s_cbranch_vccnz 7f\n\t"
            "2:\n\t" TS(9)
            "s_nop 3\n\t"
            EXD("179", "231") EXD("178", "230") EXD("177", "229") EXD("176", "228")
            PCVX
            "v_max3_f32 v250, |v236|, |v231|, |v230|\n\t"
            "v_max3_f32 v250, v250, |v229|, |v228|\n\t"
            SM("224:227", "240:243", "200:207", "v252")
            "v_cmp_nge_f32 vcc, 0x41c00000, v250\n\t"
            "s_nop 0\n\t"
            SM("16:19", "240:243", "128:135", "v252")
            "s_cbranch_vccnz 10f\n\t"
            "13:\n\t"
            "s_nop 0\n\t"
            SM("20:23", "240:243", "136:143", "v252") "s_nop 1\n\t"
            ADD("250", "224", "225") ADD("251", "226", "227")
            SM("24:27", "240:243", "144:151", "v252")
            "v_rcp_f32 v250, v250\n\t"
            SM("28:31", "240:243", "152:159", "v252")
            "v_rcp_f32 v251, v251\n\t"
            OUT("245", "246", "16", "17", "18", "19", "%[ra]", "0", "16")
            OUT("247", "248", "20", "21", "22", "23", "%[ra]", "128", "144")
            OUT("245", "246", "24", "25", "26", "27", "v253", "0", "16")
            OUT("247", "248", "28", "29", "30", "31", "v253", "128", "144")
            TS(10)
            "s_waitcnt lgkmcnt(0)\n\t"
            "s_barrier\n\t"
            RDC
            "v_lshrrev_b32 v247, 2, %[l16]\n\t"
            "s_waitcnt lgkmcnt(2)\n\t"
            ADD("224", "224", "226") ADD("225", "225", "227")
            "s_waitcnt lgkmcnt(0)\n\t"
            ADD("228", "228", "230") ADD("229", "229", "231") ADD("224", "224", "228") ADD("225", "225", "229")
            "v_cvt_pk_bf16_f32 v245, v224, v225\n\t"
            "buffer_store_dword v245, v247, %[rs], %[cs] offen sc1\n\t"
            "s_branch 9f\n\t"
            "10:\n\t"
            "v_max3_f32 v250, v236, v231, v230\n\t"
            "v_max3_f32 v250, v250, v229, v228\n\t"
            "s_nop 1\n\t"
            "v_max_f32_dpp v251, v250, v250 quad_perm:[1,0,3,2] row_mask:0xf bank_mask:0xf\n\t"
            "v_max_f32_dpp v244, v250, v250 quad_perm:[1,0,3,2] row_mask:0xf bank_mask:0xf\n\t"
            "s_nop 1\n\t"
            "v_permlane16_swap_b32 v251, v244\n\t"
            "v_max_f32 v250, v251, v244\n\t"
            "v_max_f32 v251, v251, v244\n\t"
            Z2("224:225") Z2("226:227")
            "v_permlane32_swap_b32 v250, v251\n\t"
            "v_max_f32 v250, v250, v251\n\t"
            Z2("16:17") Z2("18:19")
            SUBM("228") SUBM("229") SUBM("230") SUBM("231") SUBM("236")
            EXD("180", "236") EXD("179", "231") EXD("178", "230") EXD("177", "229") EXD("176", "228")
            PCVM PCVX
            "s_nop 1\n\t"
            SM("224:227", "240:243", "200:207", "v252") "s_nop 1\n\t"
            SM("16:19", "240:243", "128:135", "v252")
            "s_branch 13b\n\t"
            "5:\n\t" CN(1)
            LDX("192:195", "%[l16]", "%[bb]", "0") LDX("196:199", "%[l16]", "%[bb]", "1024")
            LDPA
            "s_sub_u32 %[lim], %[lim], 1\n\t"
            "s_nop 1\n\t"
            ZAM
            "s_cbranch_scc0 4b\n\t"
            "s_mov_b32 %[bad], 1500\n\t"
            "s_mov_b32 %[lim], 0\n\t"
            "s_branch 1b\n\t"
            "7:\n\t" CN(65536)
            LDPA
            "s_sub_u32 %[lim], %[lim], 1\n\t"
            "s_cbranch_scc0 1b\n\t"
            "s_mov_b32 %[bad], 1000\n\t"
            "s_branch 2b\n\t"
            "9:\n\t"
            : [lim] "+s"(lim), [bad] "+s"(bad), "+{v[128:159]}"(yb) TSOUT CNOUT
            : "{v[32:63]}"(wO[0]), "{v[64:95]}"(wO[1]), "{v[104:111]}"(sel), "{v[112:113]}"(sel3), [sx] "v"(sidx), [l16] "v"(l * 16),
              [ra] "v"(r3a), [rs] "s"(rsS), [bb] "s"(bb), [bq] "s"(bq), [bm] "s"(bm), [cs] "s"(o_cs), [rc] "v"(l0 + C2_R3 + wu * 512 + l * 8)
            : "memory", "vcc", "scc", CLOB_PX, CLOB16(16, 17, 18, 19, 20, 21, 22, 23, 24, 25, 26, 27, 28, 29, 30, 31),
              CLOB16(224, 225, 226, 227, 228, 229, 230, 231, 232, 233, 234, 235, 236, 237, 238, 239), CLOB16(240, 241, 242, 243, 244, 245, 246, 247, 248, 249, 250, 251, 252, 253, 254, 254));
        if (__builtin_expect(bad != 0, 0)) TMO_SET((unsigned)bad)
        __builtin_amdgcn_s_setprio(0);
        if (s == 0 && wu == 1) { PST(6) }

        if (__builtin_expect(s <= KVS + 1, 0)) {
            if (s == 0 || __builtin_amdgcn_readfirstlane(*(volatile unsigned*)(chsm + C2_MB + 32 + ((s - 1) & 1) * 16 + 4 + (wu >> 1) * 4)) == 0u) {
                wait_ge<6>(kv_flag(a.sync, s + 1, grp, l), 1u, a.tmo, 3001u);
                const char* kp = arena(a.slots) + VX_OFF + blk_off(s + 1, ha, tau);
#pragma unroll
                for (int e = 0; e < 8; ++e) glds16h((unsigned)l * 16u, kp + e * 1024, l0 + (s & 1 ? C2_V : C2_K) + wu * 8192 + e * 1024);
                asm volatile("s_waitcnt vmcnt(0)" ::: "memory");
            }
        }
        {
            int k0 = KX_OFF + blk_off(s + 1, hs, tau) + jq * 1024;
            asm volatile(
                DSV("160:163", "0") DSV("164:167", "1024") DSV("168:171", "2048") DSV("172:175", "3072") DSV("176:179", "128") DSV("180:183", "1152") DSV("184:187", "2176") DSV("188:191", "3200")
                "s_cmp_lg_u32 %[wu], 1\n\t"
                "s_cbranch_scc1 1f\n\t"
                KLD("%[k0]")
                "1:\n\t"
                "s_waitcnt lgkmcnt(4)\n\t"
                PXT("192:195", "196:199", "200:203", "204:207", "160:163", "164:167", "168:171", "172:175")
                "s_waitcnt lgkmcnt(0)\n\t"
                PXT("208:211", "212:215", "216:219", "220:223", "176:179", "180:183", "184:187", "188:191")
                CVP("128", "192", "193") CVP("129", "194", "195") CVP("136", "196", "197") CVP("137", "198", "199") CVP("144", "200", "201") CVP("145", "202", "203") CVP("152", "204", "205") CVP("153", "206", "207")
                CVP("132", "208", "209") CVP("133", "210", "211") CVP("140", "212", "213") CVP("141", "214", "215") CVP("148", "216", "217") CVP("149", "218", "219") CVP("156", "220", "221") CVP("157", "222", "223")
                : "+{v[128:159]}"(yb), "+{v[96:103]}"(kx), [k0] "+s"(k0)
                : "{v[32:63]}"(wO[0]), "{v[64:95]}"(wO[1]), [lv] "v"(l0 + (s & 1 ? C2_V : C2_K) + wu * 8192 + voffX), [vk] "v"(voffK), [rs] "s"(rsS), [wu] "s"(wu)
                : "memory", "scc", CLOB_PX);
        }
        __builtin_amdgcn_sched_barrier(0);
        o_1 = o1_of(s + 1); o_2 = km_off(s + 1, hs, tau) + jq * 512;
        bq = x_off(s + 1, ha, tau); bm = m_off(s + 1, ha, tau); bb = v_off(s + 1, ha, tau);
        o_f = f_off(s + 1, grp, tau);
        o_cs = rec_base(s + 1) + C_OFF + c * 16384 + near_half(s + 1, tau) + wu * 2048 + grp * 256;
        asm volatile("" : "+s"(o_cs), "+s"(o_f), "+s"(o_1), "+s"(o_2), "+s"(bq), "+s"(bm), "+s"(bb));
        __builtin_amdgcn_s_barrier();
        asm volatile(
            "s_cmp_lg_u32 %[wu], 3\n\t"
            "s_cbranch_scc1 1f\n\t"
            "v_mov_b32 v160, %[lf]\n\t"
            PAUSE(PF)
            "ds_read_b32 v160, v160\n\t"
            "s_waitcnt lgkmcnt(0)\n\t"
            "v_cmp_eq_u32 vcc, %[tg], v160\n\t"
            "s_cbranch_vccz 1f\n\t"
            "s_mov_b32 exec_hi, 0\n\t"
            "buffer_store_dword v160, %[l4], %[rs], %[fo] offen sc1\n\t"
            "s_mov_b64 exec, -1\n\t"
            "1:\n\t"
            :: [wu] "s"(wu), [lf] "s"(l0 + C2_MB + 16), [tg] "s"(s + 1), [l4] "v"(l * 4), [rs] "s"(rsS), [fo] "s"(o_f + c * 128)
            : "memory", "scc", "vcc", "v160");
        __builtin_amdgcn_s_setprio(1);
    }
    {
        constexpr int s = NSEG - 1;
        const int l = lane_now(), i = l & 15, g = l >> 4;
        v4u qf[8], kf[8], pv[2], px[4];
        asm volatile("s_waitcnt vmcnt(0)" ::: "memory");
        __builtin_amdgcn_s_sleep(4);
#pragma unroll
        for (int e = 0; e < 8; ++e) kf[e] = ld_sc1s(rsS, l * 16, KX_OFF + blk_off(s, ha, tau) + e * 1024);
        hand_over(o_1, o_2, o_f, s);
        const int bk = km_off(s, ha, tau);
        auto ask = [&]() {
            asm volatile("" ::: "memory");
            pv[0] = ld_sc1s(rsS, l * 16, bb); pv[1] = ld_sc1s(rsS, l * 16, bb + 1024);
#pragma unroll
            for (int ks = 0; ks < 4; ++ks) px[ks] = ld_sc1s(rsS, g * 128 + (i >> 1) * 16, bk + ks * 512);
            asm volatile("" ::: "memory");
        };
        asm volatile(PAUSE(PLL) ::: "memory");
        ask();
#pragma unroll
        for (int e = 0; e < 8; ++e) qf[e] = *(const v4u*)(chsm + C2_V + 32768 + wu * 8192 + e * 1024 + l * 16);
        f32x4 sx[3] = {ZERO4, ZERO4, ZERO4};
        float mxx[2] = {-3.0e38f, -3.0e38f};
#pragma unroll
        for (int ks = 0; ks < 4; ++ks) {
            sx[0] = MM(kf[ks], qf[ks], sx[0]);
            sx[1] = MM(kf[ks], qf[4 + ks], sx[1]);
            sx[2] = MM(kf[4 + ks], qf[4 + ks], sx[2]);
        }
#pragma unroll
        for (int r = 0; r < 4; ++r) {
            if (4 * g + r > i) { sx[0][r] = -3.0e38f; sx[2][r] = -3.0e38f; }
            mxx[0] = fmaxf(mxx[0], sx[0][r]);
            mxx[1] = fmaxf(mxx[1], fmaxf(sx[1][r], sx[2][r]));
        }
        asm volatile("" : "+v"(sx[0]), "+v"(sx[1]), "+v"(sx[2]), "+v"(mxx[0]), "+v"(mxx[1]));
#pragma nounroll
        for (int spin = 0;; ++spin) {
            if (__all(mx4(mx4(mx4(mx4(mx4(mx4(0u, pv[0]), pv[1]), px[0]), px[1]), px[2]), px[3]) != SENT)) break;
            if (dead) break;
            if (spin > SPIN_LIMIT) { TMO_SET(1700u) break; }
            ask();
        }
#define SUB8(v, o) v8u{(v)[(o)], (v)[(o) + 1], (v)[(o) + 2], (v)[(o) + 3], (v)[(o) + 4], (v)[(o) + 5], (v)[(o) + 6], (v)[(o) + 7]}
#pragma unroll
        for (int ct = 0; ct < 4; ++ct) {
            f32x4 am = smm(pv[0], SUB8(wO[ct >> 1], (ct & 1) * 16), ZERO4, sidx);
            am = smm(pv[1], SUB8(wO[ct >> 1], (ct & 1) * 16 + 8), am, sidx);
            yb[8 * ct + 2] = cvt_pk_bf16(am[0] + am[1], 0.f); yb[8 * ct + 6] = cvt_pk_bf16(am[2] + am[3], 0.f);
        }
        const v8u one = {0x3F803F80u, 0x3F803F80u, 0x3F80u, 0u, 0x3F803F80u, 0x3F803F80u, 0x3F80u, 0u};
        const float SCL2E = 0.08838834764831845f * 1.4426950408889634f;
        float* xo = (float*)(chsm + C2_K) + (wu * 32 + 4 * g) * XP + i;
#pragma unroll
        for (int qt = 0; qt < 2; ++qt) {
            f32x4 sc[3] = {sx[qt], sx[2], ZERO4};
#pragma unroll
            for (int ks = 0; ks < 4; ++ks) sc[2] = MM(px[ks], qf[4 * qt + ks], sc[2]);
            float mx = fmaxf(mxx[qt], fmaxf(sc[2][1], sc[2][3]));
            mx = xor32_max(xor16_max(mx));
            const float nmx = -mx * SCL2E;
#define PE(x) __builtin_amdgcn_exp2f(fmaf(x, SCL2E, nmx))
            const float m0 = PE(sc[2][1]), m1 = PE(sc[2][3]);
            const v4u ae = {cvt_pk_bf16(PE(sc[0][0]), PE(sc[0][1])), cvt_pk_bf16(m0, m0), cvt_pk_bf16(PE(sc[0][2]), PE(sc[0][3])), 0u};
            const v4u ao = {qt ? cvt_pk_bf16(PE(sc[1][0]), PE(sc[1][1])) : 0u, 0u, qt ? cvt_pk_bf16(PE(sc[1][2]), PE(sc[1][3])) : 0u, cvt_pk_bf16(m1, m1)};
            const f32x4 sum = smm(ao, one, smm(ae, one, ZERO4, 0xE444), 0x4EEE);
#pragma unroll
            for (int ct = 0; ct < 4; ++ct) {
                const f32x4 o = smm(ao, SUB8(yb, 8 * ct), smm(ae, SUB8(yb, 8 * ct), ZERO4, 0xE444), 0x4EEE);
#pragma unroll
                for (int r = 0; r < 4; ++r) xo[(16 * qt + r) * XP + 16 * ct] = o[r] * __builtin_amdgcn_rcpf(sum[r]);
            }
        }
        __builtin_amdgcn_s_waitcnt(0xC07F);
        last_rows(chsm, rsS, wu, grp, c, tau);
        if (wu < 2) {
            v4u sel, p0, p1;
#pragma unroll
            for (int d = 0; d < 4; ++d) sel[d] = (l & 6) == 2 * d ? (0x3F80u << ((l & 1) * 16)) : 0u;
            const int xb = XB_OFF + ((c >> 1) * 8 + grp) * 16384 + (int)(tau & 1u) * 8192 + (c & 1) * 4096 + wu * 256;
            __builtin_amdgcn_s_sleep(DLY_C);
#pragma nounroll
            for (int spin = 0;; ++spin) {
                asm volatile("" ::: "memory");
                p0 = ld_sc1s(rsS, g * 512 + i * 16, xb); p1 = ld_sc1s(rsS, g * 512 + i * 16, xb + 2048);
                if (__all(mx4(mx4(0u, p0), p1) != SENT)) break;
                if (dead) break;
                if (spin > SPIN_LIMIT) { TMO_SET(1800u) break; }
            }
            const f32x4 d = MM(sel, p1, MM(sel, p0, ZERO4));
            if (g < 2) out8(a.out + (size_t)(s * SEG + 4 * grp + 2 * wu + (i >> 3)) * OUTW + 64 * c + 8 * (i & 7) + 4 * g, v2u{cvt_pk_bf16(d[0], d[1]), cvt_pk_bf16(d[2], d[3])});
        }
    }
}

struct cpx { float x, y; };
__device__ __forceinline__ cpx cmul(cpx a, cpx b) { return {a.x * b.x - a.y * b.y, a.x * b.y + a.y * b.x}; }
__device__ __forceinline__ cpx cadd(cpx a, cpx b) { return {a.x + b.x, a.y + b.y}; }
__device__ __forceinline__ cpx csub(cpx a, cpx b) { return {a.x - b.x, a.y - b.y}; }
template <int DIR> __device__ __forceinline__ cpx mul_i(cpx a) {
    return (DIR < 0) ? cpx{a.y, -a.x} : cpx{-a.y, a.x};
}
template <int DIR> __device__ __forceinline__ void r4(cpx& a, cpx& b, cpx& c, cpx& d) {
    cpx t0 = cadd(a, c), t1 = csub(a, c), t2 = cadd(b, d), t3 = mul_i<DIR>(csub(b, d));
    a = cadd(t0, t2); b = cadd(t1, t3); c = csub(t0, t2); d = csub(t1, t3);
}
template <int DIR> __device__ __forceinline__ cpx twc(float c, float s) { return cpx{c, (float)DIR * s}; }
template <int DIR> __device__ __forceinline__ void dft16(cpx (&v)[16]) {
#pragma unroll
    for (int a = 0; a < 4; ++a) r4<DIR>(v[a], v[a + 4], v[a + 8], v[a + 12]);
    const float C1 = 0.92387953251128674f, S1 = 0.38268343236508977f, R2 = 0.70710678118654752f;
    v[1 + 4] = cmul(v[1 + 4], twc<DIR>(C1, S1));
    v[1 + 8] = cmul(v[1 + 8], twc<DIR>(R2, R2));
    v[1 + 12] = cmul(v[1 + 12], twc<DIR>(S1, C1));
    v[2 + 4] = cmul(v[2 + 4], twc<DIR>(R2, R2));
    v[2 + 8] = mul_i<DIR>(v[2 + 8]);
    v[2 + 12] = cmul(v[2 + 12], twc<DIR>(-R2, R2));
    v[3 + 4] = cmul(v[3 + 4], twc<DIR>(S1, C1));
    v[3 + 8] = cmul(v[3 + 8], twc<DIR>(-R2, R2));
    v[3 + 12] = cmul(v[3 + 12], twc<DIR>(-C1, -S1));
#pragma unroll
    for (int d = 0; d < 4; ++d) r4<DIR>(v[4 * d], v[4 * d + 1], v[4 * d + 2], v[4 * d + 3]);
}
__device__ __forceinline__ constexpr int x16(int q) { return 4 * (q & 3) + (q >> 2); }
template <int DIR> __device__ __forceinline__ void dft8(cpx (&v)[8]) {
    r4<DIR>(v[0], v[2], v[4], v[6]);
    r4<DIR>(v[1], v[3], v[5], v[7]);
    const float R2 = 0.70710678118654752f;
    v[3] = cmul(v[3], twc<DIR>(R2, R2));
    v[5] = mul_i<DIR>(v[5]);
    v[7] = cmul(v[7], twc<DIR>(-R2, R2));
#pragma unroll
    for (int d = 0; d < 4; ++d) { cpx p = v[2 * d], q = v[2 * d + 1]; v[2 * d] = cadd(p, q); v[2 * d + 1] = csub(p, q); }
}
__device__ __forceinline__ int pad(int i) { return i + (i >> 4); }
__device__ __forceinline__ cpx expi(float x) { float s, c; sincospif(x, &s, &c); return cpx{c, s}; }
template <int R, int DIR> __device__ __forceinline__ void twiddle(cpx* v, cpx w1) {
    cpx w[R];
    w[1] = w1;
#pragma unroll
    for (int r = 2; r < R; ++r) w[r] = (r & 1) ? cmul(w[r - 1], w1) : cmul(w[r / 2], w[r / 2]);
#pragma unroll
    for (int r = 1; r < R; ++r) v[r] = DIR < 0 ? cmul(v[r], w[r]) : cpx{v[r].x * w[r].x + v[r].y * w[r].y, v[r].y * w[r].x - v[r].x * w[r].y};
}
struct TwBase { cpx b; cpx c[4]; };
template <int N> __device__ __forceinline__ TwBase make_tw(int t) {
    TwBase tb;
    tb.b = expi(-(float)(t & 15) * (1.0f / 128.0f));
    constexpr int NQ = (N == 1024) ? 4 : 2, T = N / 16;
#pragma unroll
    for (int q = 0; q < NQ; ++q) tb.c[q] = expi(-(float)(t + T * q) * (2.0f / (float)N));
    return tb;
}
template <int N> __device__ __forceinline__ void fft_sync() {
    if constexpr (N == 1024) { __builtin_amdgcn_fence(__ATOMIC_ACQ_REL, "wavefront"); __builtin_amdgcn_wave_barrier(); } else __syncthreads();
}
template <int N, int DIR, bool RIN, bool ROUT>
__device__ __forceinline__ void fft_reg(cpx* buf, const TwBase& tb, int t, cpx (&v)[16]) {
    constexpr int T = N / 16, TP = T + T / 16;
    cpx* const bt = buf + pad(t);
    if constexpr (!RIN) {
#pragma unroll
        for (int r = 0; r < 16; ++r) v[r] = bt[TP * r];
        fft_sync<N>();
    }
    dft16<DIR>(v);
#pragma unroll
    for (int q = 0; q < 16; ++q) buf[17 * t + x16(q)] = v[q];
    fft_sync<N>();
    {
#pragma unroll
        for (int r = 0; r < 16; ++r) v[r] = bt[TP * r];
        fft_sync<N>();
        twiddle<16, DIR>(v, tb.b);
        dft16<DIR>(v);
        cpx* const bo = buf + (t >> 4) * 272 + (t & 15);
#pragma unroll
        for (int q = 0; q < 16; ++q) bo[17 * x16(q)] = v[q];
        fft_sync<N>();
    }
    if constexpr (N == 1024) {
#pragma unroll
        for (int q = 0; q < 4; ++q)
#pragma unroll
            for (int r = 0; r < 4; ++r) v[4 * q + r] = bt[68 * q + 272 * r];
#pragma unroll
        for (int q = 0; q < 4; ++q) {
            twiddle<4, DIR>(&v[4 * q], tb.c[q]);
            r4<DIR>(v[4 * q], v[4 * q + 1], v[4 * q + 2], v[4 * q + 3]);
#pragma unroll
            for (int r = 0; r < 4; ++r) bt[68 * q + 272 * r] = v[4 * q + r];
        }
        fft_sync<N>();
    } else {
        cpx u[2][8];
#pragma unroll
        for (int q = 0; q < 2; ++q)
#pragma unroll
            for (int r = 0; r < 8; ++r) u[q][r] = bt[136 * q + 272 * r];
        if constexpr (ROUT) fft_sync<N>();
#pragma unroll
        for (int q = 0; q < 2; ++q) {
            twiddle<8, DIR>(u[q], tb.c[q]);
            dft8<DIR>(u[q]);
#pragma unroll
            for (int d = 0; d < 4; ++d) {
                if constexpr (ROUT) { v[q + 2 * d] = u[q][2 * d]; v[q + 2 * d + 8] = u[q][2 * d + 1]; }
                else { bt[136 * q + 272 * d] = u[q][2 * d]; bt[136 * q + 272 * (d + 4)] = u[q][2 * d + 1]; }
            }
        }
        if constexpr (!ROUT) fft_sync<N>();
    }
}
__device__ __forceinline__ int f_hi(int rb, int j, unsigned tw) { j = (j + rb) & 7; return rb * 65536 + (j >> 1) * 16384 + (j & 1) * 4096 + (int)((tw ^ (unsigned)(rb >> 5)) & 1u) * 8192; }
__device__ __forceinline__ int f_lo(int rb, int x) { return (x + rb * 128) & 4095; }
__device__ __forceinline__ int w_hi(int k, int j, unsigned tw) { return k * 16384 + ((j + k) & 15) * 512 + (int)((tw ^ (unsigned)(k >> 7)) & 1u) * 8192; }
__device__ __forceinline__ int w_lo(int k, int x) { return (x + k * 128) & 511; }
constexpr int PB1 = 1024 + 64, PB2 = 2048 + 128;
typedef float f32x4v __attribute__((ext_vector_type(4)));
typedef unsigned u32x4v __attribute__((ext_vector_type(4)));

__device__ __forceinline__ void fft_rows_fwd(const u16* __restrict__ x, rsrc_t rsF, const unsigned tw, int rb, char* smem, int tid, const char* wr, const char* wi, const unsigned spare) {
    cpx (*buf)[PB1] = (cpx (*)[PB1])smem;
    int f = tid >> 6, t = tid & 63;
    const u16* p0 = x + (size_t)(rb * 8 + 2 * f) * 1024 + 16 * t;
    u32x4v a0 = *(const u32x4v*)p0, a1 = *(const u32x4v*)(p0 + 8), b0 = *(const u32x4v*)(p0 + 1024), b1 = *(const u32x4v*)(p0 + 1032);
    const TwBase tb = make_tw<1024>(t);
    for (int it = 0; it < 4; ++it, rb += 64) {
        asm volatile("" : "+v"(tid));
        f = tid >> 6; t = tid & 63;
        __syncthreads();
        cpx* d = &buf[f][17 * t];
#pragma unroll
        for (int e = 0; e < 4; ++e) {
            d[2 * e] = cpx{__uint_as_float(a0[e] << 16), __uint_as_float(b0[e] << 16)};
            d[2 * e + 1] = cpx{__uint_as_float(a0[e] & 0xFFFF0000u), __uint_as_float(b0[e] & 0xFFFF0000u)};
            d[8 + 2 * e] = cpx{__uint_as_float(a1[e] << 16), __uint_as_float(b1[e] << 16)};
            d[8 + 2 * e + 1] = cpx{__uint_as_float(a1[e] & 0xFFFF0000u), __uint_as_float(b1[e] & 0xFFFF0000u)};
        }
        if (it < 3) p0 += 512 * 1024;
        touch(((it & 1) ? wi : wr) + (size_t)((rb >> 1) & 31) * 32832 + (it >> 1) * 32 * 32832 + ((rb & 1) * 128 + (tid >> 1)) * 128, spare);
        a0 = *(const u32x4v*)p0; a1 = *(const u32x4v*)(p0 + 8); b0 = *(const u32x4v*)(p0 + 1024); b1 = *(const u32x4v*)(p0 + 1032);
        fft_sync<1024>();
        cpx z[16];
        fft_reg<1024, -1, false, false>(buf[f], tb, t, z);
        __syncthreads();
        const cpx* pk = &buf[tid & 3][pad(tid >> 2)];
        const cpx* pn = &buf[tid & 3][pad(1024 - (tid >> 2))];
        const int vo = f_lo(rb, tid * 16);
#pragma unroll
        for (int m = 0; m < 8; ++m) {
            const cpx zk = pk[68 * m], zn = m == 0 && tid < 4 ? pk[544] : pn[-68 * m];
            f32x4v o = {zk.x + zn.x, zk.y - zn.y, zk.y + zn.y, zn.x - zk.x};
            if (m == 0 && tid < 4) o = f32x4v{2.f * zk.x, 2.f * zn.x, 2.f * zk.y, 2.f * zn.y};
            st_sc1s(rsF, vo, f_hi(rb, m, tw), __builtin_bit_cast(v4u, o));
        }
    }
}
__device__ __forceinline__ void filt_transpose(const u16* __restrict__ wr, const u16* __restrict__ wi, rsrc_t rsW, const unsigned tw, const int tb, char* smem, const int tid) {
    const v4u* s0 = (const v4u*)(wr + (size_t)tb * 32 * 513);
    const v4u* s1 = (const v4u*)(wi + (size_t)tb * 32 * 513);
    v4u r0[5], r1[5];
#pragma unroll
    for (int e = 0; e < 5; ++e) {
        const int idx = tid + 512 * e, ic = idx < 2052 ? idx : 2051;
        r0[e] = s0[ic]; r1[e] = s1[ic];
    }
#pragma unroll
    for (int e = 0; e < 5; ++e) {
        const int idx = tid + 512 * e;
        if (idx < 2052) { *(v4u*)(smem + idx * 16) = r0[e]; *(v4u*)(smem + 32832 + idx * 16) = r1[e]; }
    }
    __syncthreads();
    const u16* a0 = (const u16*)smem;
    const u16* a1 = (const u16*)(smem + 32832);
    for (int idx = tid; idx < 513 * 8; idx += 512) {
        const int k = idx >> 3, n4 = (idx & 7) * 4;
        v4u v;
#pragma unroll
        for (int j = 0; j < 4; ++j) v[j] = (unsigned)a0[(n4 + j) * 513 + k] | ((unsigned)a1[(n4 + j) * 513 + k] << 16);
        st_sc1(rsW, w_hi(k, tb >> 2, tw) + w_lo(k, (tb & 3) * 128 + n4 * 4), v);
    }
}

__device__ __forceinline__ void fft_cols(rsrc_t rsF, rsrc_t rsW, const unsigned tw, int blk, const int n, char* smem, int tid) {
    cpx (*buf)[PB2] = (cpx (*)[PB2])smem;
    int c = tid >> 7, t = tid & 127;
    f32x4v v[8];
    unsigned wv[16], wn[16];
    auto wld = [&](const int k, const int n) { return __builtin_amdgcn_raw_buffer_load_b32(rsW, w_hi(k, n >> 7, tw) + w_lo(k, (n & 127) * 4), 0, 0); };
    auto f_at = [&](const int bk) { return f_hi(tid >> 3, bk >> 5, tw) + f_lo(tid >> 3, (bk & 31) * 128 + (tid & 7) * 16); };
    auto fetch = [&](const int bk) {
        const int kc = bk * 2 + __builtin_amdgcn_readfirstlane(c), vo = f_at(bk), vw = w_lo(kc, t * 4);
#pragma unroll
        for (int m = 0; m < 8; ++m) v[m] = __builtin_bit_cast(f32x4v, __builtin_amdgcn_raw_buffer_load_b128(rsF, m & 1 ? vo ^ 8192 : vo, m * 2097152, 0));
#pragma unroll
        for (int m = 0; m < 16; ++m) wn[m] = __builtin_amdgcn_raw_buffer_load_b32(rsW, vw, w_hi(kc, m, tw), 0);
    };
    fetch(blk);
    const TwBase tb = make_tw<2048>(t);
    for (int it = 0; it < n; ++it, blk += 64) {
        asm volatile("" : "+v"(tid));
        c = tid >> 7; t = tid & 127;
        __syncthreads();
        cpx* const d = &buf[(tid >> 2) & 1][pad((tid >> 3) * 8 + 2 * (tid & 3))];
#pragma unroll
        for (int m = 0; m < 8; ++m) { d[272 * m] = cpx{v[m][0], v[m][1]}; d[272 * m + 1] = cpx{v[m][2], v[m][3]}; }
#pragma unroll
        for (int m = 0; m < 16; ++m) wv[m] = wn[m];
        fetch(it + 1 < n ? blk + 64 : blk);
        __syncthreads();
        cpx z[16];
        fft_reg<2048, -1, false, true>(buf[c], tb, t, z);
        auto wcx = [](const unsigned w) { return cpx{__uint_as_float(w << 16), __uint_as_float(w & 0xFFFF0000u)}; };
        if (blk < 2) {
#pragma unroll
            for (int m = 0; m < 16; ++m) buf[c][pad(t + 128 * m)] = z[m];
            __syncthreads();
            if (blk == 0 && c == 0) {
#pragma unroll
                for (int m = 0; m < 16; ++m) {
                    const int n = t + 128 * m, nn = (2048 - n) & 2047;
                    const cpx zn = buf[0][pad(nn)], a = wcx(wv[m]), b = wcx(wld(0, nn)), e = wcx(wld(512, n)), f = wcx(wld(512, nn));
                    const cpx h0 = cmul(cpx{0.5f * (z[m].x + zn.x), 0.5f * (z[m].y - zn.y)}, cpx{0.5f * (a.x + b.x), 0.5f * (a.y - b.y)});
                    const cpx h5 = cmul(cpx{0.5f * (z[m].y + zn.y), -0.5f * (z[m].x - zn.x)}, cpx{0.5f * (e.x + f.x), 0.5f * (e.y - f.y)});
                    z[m] = cpx{h0.x - h5.y, h0.y + h5.x};
                    wv[m] = 0x3F80u;
                }
            }
            __syncthreads();
        }
#pragma unroll
        for (int m = 0; m < 16; ++m) z[m] = cmul(z[m], wcx(wv[m]));
        fft_reg<2048, 1, true, false>(buf[c], tb, t, z);
        const int vo = f_at(blk);
#pragma unroll
        for (int m = 0; m < 8; ++m)
            st_sc1s(rsF, m & 1 ? vo ^ 8192 : vo, m * 2097152, __builtin_bit_cast(v4u, f32x4v{d[272 * m].x, d[272 * m].y, d[272 * m + 1].x, d[272 * m + 1].y}));
    }
}

__device__ __forceinline__ void fft_rows_inv(rsrc_t rsF, const unsigned tw, u16* __restrict__ out, int rb, const int step, const int n, char* smem, int tid) {
    cpx (*buf)[PB1] = (cpx (*)[PB1])smem;
    int f = tid >> 6, t = tid & 63;
    f32x4v v[8];
    auto fetch = [&](const int r) {
        const int vo = f_lo(r, tid * 16);
#pragma unroll
        for (int m = 0; m < 8; ++m) v[m] = __builtin_bit_cast(f32x4v, __builtin_amdgcn_raw_buffer_load_b128(rsF, vo, f_hi(r, m, tw), 0));
    };
    fetch(rb);
    const TwBase tb = make_tw<1024>(t);
    for (int it = 0; it < n; ++it, rb += step) {
        asm volatile("" : "+v"(tid));
        f = tid >> 6; t = tid & 63;
        __syncthreads();
        cpx* pk = &buf[tid & 3][pad(tid >> 2)];
        cpx* pn = &buf[tid & 3][pad(1024 - (tid >> 2))];
#pragma unroll
        for (int m = 0; m < 8; ++m) {
            if (m == 0 && tid < 4) { pk[0] = cpx{v[0][0], v[0][2]}; pk[544] = cpx{v[0][1], v[0][3]}; }
            else { pk[68 * m] = cpx{v[m][0] - v[m][3], v[m][1] + v[m][2]}; pn[-68 * m] = cpx{v[m][0] + v[m][3], v[m][2] - v[m][1]}; }
        }
        fetch(it + 1 < n ? rb + step : rb);
        __syncthreads();
        cpx z[16];
        fft_reg<1024, 1, false, false>(buf[f], tb, t, z);
        const float sc = 0.5f / (1024.0f * 2048.0f);
        const cpx* d = &buf[f][17 * t];
        u32x4v a[2], b[2];
#pragma unroll
        for (int h = 0; h < 2; ++h)
#pragma unroll
            for (int e = 0; e < 4; ++e) {
                cpx p = d[8 * h + 2 * e], q = d[8 * h + 2 * e + 1];
                a[h][e] = cvt_pk_bf16(p.x * sc, q.x * sc);
                b[h][e] = cvt_pk_bf16(p.y * sc, q.y * sc);
            }
        u16* o0 = out + (size_t)(rb * 8 + 2 * f) * OUTW + 1024 + 16 * t;
        *(u32x4v*)o0 = a[0]; *(u32x4v*)(o0 + 8) = a[1];
        *(u32x4v*)(o0 + OUTW) = b[0]; *(u32x4v*)(o0 + OUTW + 8) = b[1];
    }
}

__device__ __forceinline__ void glds16s(unsigned voff, const void* sbase, unsigned lds_dst) {
    unsigned keep;
    asm volatile("s_mov_b32 %0, m0\n\ts_mov_b32 m0, %3\n\ts_nop 0\n\tglobal_load_lds_dwordx4 %1, %2\n\ts_mov_b32 m0, %0" : "=&s"(keep) : "v"(voff), "s"(sbase), "s"(lds_dst) : "memory");
}
#define K8_BAR do { asm volatile("" ::: "memory"); __builtin_amdgcn_s_barrier(); asm volatile("" ::: "memory"); } while (0)
#define K8_WV(n) asm volatile("s_waitcnt vmcnt(" #n ")" ::: "memory")
#define K8_WL(n) asm volatile("s_waitcnt lgkmcnt(" #n ")" ::: "memory")
#define K8_SCHED __builtin_amdgcn_sched_barrier(0)
template <int BN>
__device__ __forceinline__ int bswz2(int row) {
    if (BN >= 128) return (row & 3) | (((row >> 3) & 1) << 2);
    else return ((row >> 1) & 1) | (((row >> 3) & 1) << 1);
}
template <int BM>
__device__ __forceinline__ void gemm_tile(u16* sm, const char* A, const int s0, const unsigned tau, const u16* B, u16* C) {
    constexpr int K = HD, LDB = DM, LDC = OUTW;
    constexpr int BN = 128, BK = 64, ST = BM == 128 ? 4 : 3, WN = BM == 128 ? 4 : 2;
    constexpr int ATILE = BM * BK, BTILE = BK * BN;
    constexpr int NT = 8 / WN, GA = BM / 64, GB = 2, GS = GA + GB;
    const int tid = opaque_tid(), w = tid >> 6, l = tid & 63, i = l & 15, g = l >> 4;
    const int wr = w / WN, wc = w % WN;
    const unsigned wu = __builtin_amdgcn_readfirstlane(w);
    unsigned vb;
    { const int row = l >> 4, c = l & 15; vb = (unsigned)(row * LDB + 8 * (c ^ ((row | ((wu & 1) << 2)) << 1))) * 2u; }
    const int sg = s0 + (BM == 128 ? wu >> 1 : wu);
    const char* ab = A + (BM == 128 ? (wu & 1) * 4096 : 0);
    const char* bb = (const char*)(B + (size_t)wu * 8 * LDB);
    const unsigned lds0 = (unsigned)(uintptr_t)(__attribute__((address_space(3))) u16*)sm;
    const unsigned va = (unsigned)l * 16u, va2 = va + 2048u, vb2 = vb + 4u * LDB * 2u - 1024u;
    auto req = [&](const int buf, const int kt) {
        const unsigned d = lds0 + buf * (ATILE + BTILE) * 2;
        const int wh = kt >> 1;
        const char* pa = ab + ((blk_off(sg, (wh & 7) * 4 + (wh >> 3), tau) ^ 8192) + (kt & 1) * 2048);
        const char* pb = bb + (size_t)kt * BK * LDB * 2;
        unsigned keep;
        if (BM == 128) asm volatile("s_mov_b32 %0, m0\n\ts_mov_b32 m0, %2\n\ts_nop 0\n\tglobal_load_lds_dwordx4 %6, %8\n\tglobal_load_lds_dwordx4 %7, %8 offset:1024\n\t"
                     "s_mov_b32 m0, %1\n\ts_nop 0\n\tglobal_load_lds_dwordx4 %3, %5\n\tglobal_load_lds_dwordx4 %3, %5 offset:1024\n\ts_mov_b32 m0, %0"
                     : "=&s"(keep) : "s"(d + wu * GA * 1024), "s"(d + ATILE * 2 + wu * GB * 1024), "v"(va), "v"(va2), "s"(pa), "v"(vb), "v"(vb2), "s"(pb) : "memory");
        else asm volatile("s_mov_b32 %0, m0\n\ts_mov_b32 m0, %2\n\ts_nop 0\n\tglobal_load_lds_dwordx4 %6, %8\n\tglobal_load_lds_dwordx4 %7, %8 offset:1024\n\t"
                     "s_mov_b32 m0, %1\n\ts_nop 0\n\tglobal_load_lds_dwordx4 %3, %5\n\tglobal_load_lds_dwordx4 %3, %5 offset:1024\n\t"
                     "global_load_lds_dwordx4 %4, %5 offset:2048\n\tglobal_load_lds_dwordx4 %4, %5 offset:3072\n\ts_mov_b32 m0, %0"
                     : "=&s"(keep) : "s"(d + wu * GA * 1024), "s"(d + ATILE * 2 + wu * GB * 1024), "v"(va), "v"(va2), "s"(pa), "v"(vb), "v"(vb2), "s"(pb) : "memory");
    };
    f32x4 acc[4][NT] = {};
    constexpr int nt = K / BK;
#pragma unroll
    for (int s = 0; s < ST - 1; ++s) req(s, s);
    int boff[NT];
    const int aoff = wr * 4096 + l * 8;
#pragma unroll
    for (int n = 0; n < NT; ++n) { const int row = 8 * g + (i >> 2); boff[n] = row * BN + 16 * ((wc * NT + n) ^ bswz2<BN>(row)) + 4 * (i & 3); }
    asm volatile("s_waitcnt vmcnt(%0)" :: "i"((ST - 2) * GS) : "memory");
    K8_BAR;
    if (wu >= 4) K8_BAR;
    for (int t0 = 0; t0 < nt + ST - 1 - (nt + ST - 1) % ST; t0 += ST) {
#pragma unroll
        for (int rb = 0; rb < ST; ++rb) {
            const int t = t0 + rb;
            if (nt % ST != 0 && t >= nt) break;
            const int kk = min(t + ST - 1, nt - 1);
            const u16* as = sm + rb * (ATILE + BTILE);
            const u16* bs = as + ATILE;
            bf16x8 af[2][4], bf[2][NT];
#pragma unroll
            for (int ks = 0; ks < 2; ++ks) {
#pragma unroll
                for (int n = 0; n < NT; ++n) bf[ks][n] = tr_frag(bs + boff[n] + ks * 32 * BN, bs + boff[n] + ks * 32 * BN + 4 * BN);
#pragma unroll
                for (int m = 0; m < 4; ++m) af[ks][m] = *(const bf16x8*)(as + aoff + m * 1024 + ks * 512);
            }
            K8_SCHED;
            req((rb + ST - 1) % ST, kk);
            K8_WL(0);
            asm volatile("s_waitcnt vmcnt(%0)" :: "i"((ST - 2) * GS + GA) : "memory");
            K8_BAR;
            __builtin_amdgcn_s_setprio(1);
#pragma unroll
            for (int q = 0; q < 8; ++q)
#pragma unroll
                for (int n = 0; n < NT; ++n) acc[q & 3][n] = __builtin_amdgcn_mfma_f32_16x16x32_bf16(bf[q >> 2][n], af[q >> 2][q & 3], acc[q & 3][n], 0, 0, 0);
            __builtin_amdgcn_s_setprio(0);
            K8_SCHED;
            asm volatile("s_waitcnt vmcnt(%0)" :: "i"((ST - 2) * GS) : "memory");
            K8_BAR;
        }
    }
    if (wu < 4) K8_BAR;
    asm volatile("s_waitcnt vmcnt(0)" ::: "memory");
#pragma unroll
    for (int m = 0; m < 4; ++m)
#pragma unroll
        for (int n = 0; n < NT; n += 2) {
            auto r0 = __builtin_amdgcn_permlane16_swap(cvt_pk_bf16(acc[m][n][0], acc[m][n][1]), cvt_pk_bf16(acc[m][n + 1][0], acc[m][n + 1][1]), false, false);
            auto r1 = __builtin_amdgcn_permlane16_swap(cvt_pk_bf16(acc[m][n][2], acc[m][n][3]), cvt_pk_bf16(acc[m][n + 1][2], acc[m][n + 1][3]), false, false);
            *(v4u*)(C + (wr * 64 + m * 16 + i) * LDC + (wc * NT + n + (g & 1)) * 16 + 8 * (g >> 1)) = v4u{r0[0], r1[0], r0[1], r1[1]};
        }
}

__device__ __forceinline__ void xattn_unit(const ChainArgs& a, rsrc_t rsQ, rsrc_t rsK, rsrc_t rsV, rsrc_t rsS, const int s, const int sn, const int h, u16 (*Vs)[VP], unsigned* mbox, const unsigned tau, int& pend, bool& ready) {
    const int l = opaque_tid() & 63, i = l & 15, g = l >> 4;
    const unsigned known = __builtin_amdgcn_readfirstlane(*(volatile unsigned*)mbox);
    const bool early = known > (unsigned)(s + 1);
    const int bq = km_off(s, h, tau), bb = v_off(s, h, tau);
    auto count = [&](const int t) {
        if (t < SSEG) { arrive(a.sync, SY_OX + (t >> 2) * 8, 128u, 8); return; }
        unsigned n = 0;
        if (l == 0) n = atomicAdd(mbox + 8 + t - SSEG, 1u);
        if (__builtin_amdgcn_readfirstlane(n) == 3u && l < 8) __hip_atomic_store((gu32*)(a.sync + (SY_OXS + (t - SSEG) * 8 + l) * 32 + (h >> 2)), 16u, __ATOMIC_RELAXED, __HIP_MEMORY_SCOPE_AGENT);
    };
    const int xk = blk_off(s, h, tau), xq = xk ^ 8192;
    v4u vv[8], vm[2], k0f[4], k1f[4], kmf[4], q0f[4], q1f[4];
#pragma unroll
    for (int e = 0; e < 8; ++e) vv[e] = ld_sc1s(rsV, l * 16 + e * 1024, xk);
#pragma unroll
    for (int ks = 0; ks < 4; ++ks) {
        k0f[ks] = ld_sc1s(rsK, l * 16 + ks * 1024, xk);
        k1f[ks] = ld_sc1s(rsK, l * 16 + ks * 1024, xk + 4096);
        q0f[ks] = ld_sc1s(rsQ, l * 16 + ks * 1024, xq);
        q1f[ks] = ld_sc1s(rsQ, l * 16 + ks * 1024, xq + 4096);
    }
#pragma unroll
    for (int e = 0; e < 2; ++e) vm[e] = ld_sc1s(rsS, l * 16 + e * 1024, early ? bb : 1 << 30);
#pragma unroll
    for (int ks = 0; ks < 4; ++ks) kmf[ks] = ld_sc1s(rsS, ks * 512 + g * 128 + (i & 7) * 16, early ? bq : 1 << 30);
    const unsigned fl = ld_flag(a.sync + (l >= 8 ? SY_PROG * 32 : (((l & 4) ? SY_Q : SY_KV) + (min(sn, NSEG - 1) >> 3) * 8) * 32 + (l & 3)) + (h >> 2) * 32);
    if (pend >= 0) { asm volatile("s_waitcnt vmcnt(31)" ::: "memory"); count(pend); }
#pragma unroll
    for (int e = 0; e < 8; ++e) *(v4u*)&Vs[16 * (e >> 2) + i][32 * (e & 3) + 8 * g] = vv[e];
    const float scale = 0.08838834764831845f;
    const float L2E = 1.4426950408889634f;
    auto round2 = [&](const f32x4 c, float* o) {
#pragma unroll
        for (int p = 0; p < 2; ++p) {
            unsigned u = cvt_pk_bf16(c[2 * p], c[2 * p + 1]);
            float lo = __uint_as_float(u << 16) * scale, hi = __uint_as_float(u & 0xFFFF0000u) * scale;
            unsigned u2 = cvt_pk_bf16(lo, hi);
            o[2 * p] = __uint_as_float(u2 << 16);
            o[2 * p + 1] = __uint_as_float(u2 & 0xFFFF0000u);
        }
    };
    float sv[2][12], mxx[2];
    {
        f32x4 sx[3] = {};
#pragma unroll
        for (int ks = 0; ks < 4; ++ks) {
            sx[0] = __builtin_amdgcn_mfma_f32_16x16x32_bf16(as_frag(k0f[ks]), as_frag(q0f[ks]), sx[0], 0, 0, 0);
            sx[1] = __builtin_amdgcn_mfma_f32_16x16x32_bf16(as_frag(k0f[ks]), as_frag(q1f[ks]), sx[1], 0, 0, 0);
            sx[2] = __builtin_amdgcn_mfma_f32_16x16x32_bf16(as_frag(k1f[ks]), as_frag(q1f[ks]), sx[2], 0, 0, 0);
        }
        round2(sx[0], &sv[0][0]); round2(sx[1], &sv[1][0]); round2(sx[2], &sv[1][4]);
        mxx[0] = mxx[1] = -3.0e38f;
#pragma unroll
        for (int r = 0; r < 4; ++r) {
            if (4 * g + r > i) { sv[0][r] = -3.0e38f; sv[1][4 + r] = -3.0e38f; }
            mxx[0] = fmaxf(mxx[0], sv[0][r]);
            mxx[1] = fmaxf(mxx[1], fmaxf(sv[1][r], sv[1][4 + r]));
        }
    }
    const u16* vb = &Vs[4 * g + (i >> 2)][4 * (i & 3)];
    bf16x8 vfx[8];
#pragma unroll
    for (int t = 0; t < 8; ++t) vfx[t] = tr_frag(vb + 16 * t, vb + 16 * VP + 16 * t);
#pragma unroll
    for (int t = 0; t < 8; ++t) asm volatile("" : "+v"(vfx[t]));
#pragma unroll
    for (int r = 0; r < 4; ++r) asm volatile("" : "+v"(sv[0][r]), "+v"(sv[1][r]), "+v"(sv[1][4 + r]));
    asm volatile("" : "+v"(mxx[0]), "+v"(mxx[1]));
    bool have = false;
    unsigned seen = __builtin_amdgcn_readlane(fl, 8);
    ready = (sn >> 3) == (s >> 3) || (__ballot(fl != 0u) & 255ull) == 255ull;
    if (early) {
        if ((h & 3) == 0 && l == 0) atomicMax(mbox, seen);
        unsigned m = mx4(mx4(0u, vm[0]), vm[1]);
#pragma unroll
        for (int ks = 0; ks < 4; ++ks) m = mx4(m, kmf[ks]);
        have = __all(m != SENT);
    } else if ((h & 3) == 0) {
        const unsigned* pw = a.sync + (SY_PROG + (h >> 2)) * 32;
        unsigned v = seen;
        if (v <= (unsigned)(s + 1)) {
            if (s < NSEG - XA_FAST) { wait_ge<32>(pw, (unsigned)(s + 1), a.tmo, 4000u); __builtin_amdgcn_s_sleep(24); }
            else { wait_ge<6>(pw, (unsigned)(s + 1), a.tmo, 4000u); __builtin_amdgcn_s_sleep(XA_D); }
            v = (unsigned)(s + 1);
        }
        if (l == 0) atomicMax(mbox, v);
    } else {
#pragma nounroll
        for (int spin = 0; spin < 4000000 && *(volatile unsigned*)mbox < (unsigned)(s + 1); ++spin) __builtin_amdgcn_s_sleep(2);
        asm volatile("" ::: "memory");
    }
#pragma nounroll
    for (int spin = 0; !have; ++spin) {
        asm volatile("" ::: "memory");
#pragma unroll
        for (int e = 0; e < 2; ++e) vm[e] = ld_sc1s(rsS, l * 16 + e * 1024, bb);
#pragma unroll
        for (int ks = 0; ks < 4; ++ks) kmf[ks] = ld_sc1s(rsS, ks * 512 + g * 128 + (i & 7) * 16, bq);
        unsigned m = mx4(mx4(0u, vm[0]), vm[1]);
#pragma unroll
        for (int ks = 0; ks < 4; ++ks) m = mx4(m, kmf[ks]);
        if (__all(m != SENT) || spin > 20000) break;
        __builtin_amdgcn_s_sleep(2);
    }
#pragma unroll
    for (int e = 0; e < 8; ++e) *(unsigned*)&Vs[32 + (i >> 2) + 4 * ((i >> 1) & 1)][64 * (e >> 2) + 16 * g + 4 * (e & 3) + 2 * (i & 1)] = vm[e >> 2][e & 3];
    f32x4 sm_[2] = {};
#pragma unroll
    for (int ks = 0; ks < 4; ++ks) {
        sm_[0] = __builtin_amdgcn_mfma_f32_16x16x32_bf16(as_frag(kmf[ks]), as_frag(q0f[ks]), sm_[0], 0, 0, 0);
        sm_[1] = __builtin_amdgcn_mfma_f32_16x16x32_bf16(as_frag(kmf[ks]), as_frag(q1f[ks]), sm_[1], 0, 0, 0);
    }
    v4u pf[2][2];
#pragma unroll
    for (int qt = 0; qt < 2; ++qt) {
        round2(sm_[qt], &sv[qt][8]);
        float mx = fmaxf(fmaxf(mxx[qt], fmaxf(sv[qt][8], sv[qt][9])), fmaxf(sv[qt][10], sv[qt][11]));
        mx = xor16_max(mx);
        mx = xor32_max(mx);
        const float nmx = -mx * L2E;
        float sum = 0.f;
#pragma unroll
        for (int e = 0; e < 12; ++e) {
            if (qt == 0 && e >= 4 && e < 8) { sv[qt][e] = 0.f; continue; }
            float ev = __builtin_amdgcn_exp2f(fmaf(sv[qt][e], L2E, nmx));
            if (e >= 8) ev = g < 2 ? ev : 0.f;
            sv[qt][e] = ev; sum += ev;
        }
        sum = xor16_sum(sum);
        sum = xor32_sum(sum);
        const float inv = 1.0f / sum;
        pf[qt][0][0] = cvt_pk_bf16(sv[qt][0] * inv, sv[qt][1] * inv);
        pf[qt][0][1] = cvt_pk_bf16(sv[qt][2] * inv, sv[qt][3] * inv);
        pf[qt][0][2] = qt ? cvt_pk_bf16(sv[qt][4] * inv, sv[qt][5] * inv) : 0u;
        pf[qt][0][3] = qt ? cvt_pk_bf16(sv[qt][6] * inv, sv[qt][7] * inv) : 0u;
        pf[qt][1][0] = cvt_pk_bf16(sv[qt][8] * inv, sv[qt][9] * inv);
        pf[qt][1][1] = cvt_pk_bf16(sv[qt][10] * inv, sv[qt][11] * inv);
        pf[qt][1][2] = 0u; pf[qt][1][3] = 0u;
    }
    v2u vmf[8];
#pragma unroll
    for (int t = 0; t < 8; ++t) vmf[t] = tr4(&Vs[32 + 4 * (g & 1) + (i >> 2)][16 * t + 4 * (i & 3)]);
#pragma unroll
    for (int t = 0; t < 8; t += 2) {
        unsigned pk[2][2][2];
#pragma unroll
        for (int tt = 0; tt < 2; ++tt) {
            const bf16x8 vf0 = vfx[t + tt];
            const bf16x8 vf1 = as_frag(v4u{vmf[t + tt][0], vmf[t + tt][1], 0u, 0u});
#pragma unroll
            for (int qt = 0; qt < 2; ++qt) {
                f32x4 o = {};
                o = __builtin_amdgcn_mfma_f32_16x16x32_bf16(vf0, as_frag(pf[qt][0]), o, 0, 0, 0);
                o = __builtin_amdgcn_mfma_f32_16x16x32_bf16(vf1, as_frag(pf[qt][1]), o, 0, 0, 0);
                pk[qt][tt][0] = cvt_pk_bf16(o[0], o[1]);
                pk[qt][tt][1] = cvt_pk_bf16(o[2], o[3]);
            }
        }
#pragma unroll
        for (int qt = 0; qt < 2; ++qt) {
            auto r0 = __builtin_amdgcn_permlane16_swap(pk[qt][0][0], pk[qt][1][0], false, false);
            auto r1 = __builtin_amdgcn_permlane16_swap(pk[qt][0][1], pk[qt][1][1], false, false);
            v4u o = {r0[0], r1[0], r0[1], r1[1]};
            st_sc1s(rsV, (t >> 1) * 1024 + ((g & 1) * 2 + (g >> 1)) * 256 + i * 16, xq + qt * 4096, o);
        }
    }
    pend = seen > (unsigned)(sn + 1) && ready ? s : -1;
    if (pend < 0) { asm volatile("s_waitcnt vmcnt(0)" ::: "memory"); count(s); }
}

__device__ __forceinline__ void tile_stream(const ChainArgs& a, const u16* X, unsigned long long todo, const int xc, unsigned& tau, const bool early, const int nw, u16* sm8 WEVP) {
    constexpr int K = DM, LDB = HD;
    const int tid = opaque_tid(), w = tid >> 6, l = tid & 63, i = l & 15, g = l >> 4;
    const int wr = w >> 2, wc = w & 3;
    const unsigned lds0 = (unsigned)(uintptr_t)(__attribute__((address_space(3))) u16*)sm8;
    const unsigned wu = __builtin_amdgcn_readfirstlane(w);
    const unsigned ldsw = lds0 + wu * 2048;
    unsigned voffA, voffB;
    { int row = 16 * w + (l >> 3), c = l & 7; voffA = (unsigned)(row * K + 8 * (c ^ (row & 7))) * 2u; }
    { int row = 8 * w + g, c = i; voffB = (unsigned)(row * LDB + 8 * (c ^ (bswz2<128>(row) << 1))) * 2u; }
    const unsigned voffA2 = voffA + 8u * K * 2u - 1024u, voffB2 = voffB + 4u * LDB * 2u - 1024u;
    auto st2 = [&](const unsigned v0, const unsigned v1, const char* p, const unsigned d) {
        unsigned keep;
        asm volatile("s_mov_b32 %0, m0\n\ts_mov_b32 m0, %1\n\ts_nop 0\n\tglobal_load_lds_dwordx4 %2, %4\n\tglobal_load_lds_dwordx4 %3, %4 offset:1024\n\ts_mov_b32 m0, %0"
                     : "=&s"(keep) : "s"(d), "v"(v0), "v"(v1), "s"(p) : "memory");
    };
    auto stA = [&](int b, int h, const char* p) {
        st2(voffA, voffA2, p + (size_t)(h * 128) * K * 2, ldsw + (2 * b + h) * 16384);
    };
    auto stB = [&](int b, int h, const char* p) {
        st2(voffB, voffB2, p + h * 2048, ldsw + 65536 + (2 * b + h) * 16384);
    };
    constexpr int SA = 128, SB = 64 * LDB * 2;
    auto a_of = [&](const unsigned t) { return (const char*)(X + (size_t)((t >> 4) & 7) * 256 * DM); };
    auto b_of = [&](const unsigned t) { const int kind = t & 7; return (const char*)((kind < 2 ? a.Wk : (kind < 4 ? a.Wv : a.Wq)) + hcol(4 * xc + (kind & 1) * 2)); };
    auto c_of = [&](const unsigned t) { const int kind = t & 7; return ((kind & 6) == 2 ? VX_OFF - KX_OFF : 0) + (int)((t >> 4) & 7) * 4194304 + blk_off(0, 4 * xc + (kind & 1) * 2, kind < 4 ? tau : tau ^ 1u); };
    auto publish = [&](const unsigned t) {
        const int kind = t & 7, ln = lane_now();
        if (wu == 0 && ln < (kind < 4 ? 1 : 2))
            __hip_atomic_store((gu32*)(a.sync + ((kind < 4 ? SY_KV : SY_Q) + ((t >> 4) & 7) * 8 + xc) * 32 + (kind < 4 ? kind : 2 * kind - 8 + ln)), 16u, __ATOMIC_RELAXED, __HIP_MEMORY_SCOPE_AGENT);
    };
    const char* smc = (const char*)sm8;
    int aoff[2], boff[2];
#pragma unroll
    for (int ks = 0; ks < 2; ++ks) aoff[ks] = (wr * 64 + i) * 128 + (((ks * 4 + g) ^ (i & 7)) << 4);
#pragma unroll
    for (int n = 0; n < 2; ++n) { int row = 8 * g + (i >> 2); boff[n] = 65536 + row * 256 + (((wc * 2 + n) ^ bswz2<128>(row)) << 5) + 8 * (i & 3); }
    f32x4 acc[2][2][4][2] = {};
    bf16x8 At[4][2], B0[2][2], B1[2][2];
#define K8_LDA(b, h) _Pragma("unroll") for (int m = 0; m < 4; ++m) _Pragma("unroll") for (int ks = 0; ks < 2; ++ks) \
        At[m][ks] = *(const bf16x8*)(smc + aoff[ks] + (2 * (b) + (h)) * 16384 + m * 2048)
#define K8_LDB(dst, b, h) _Pragma("unroll") for (int n = 0; n < 2; ++n) _Pragma("unroll") for (int ks = 0; ks < 2; ++ks) { \
        const char* p_ = smc + boff[n] + (2 * (b) + (h)) * 16384 + ks * 8192; \
        v4i16 lo_ = __builtin_amdgcn_ds_read_tr16_b64_v4i16((__attribute__((address_space(3))) v4i16*)p_); \
        v4i16 hi_ = __builtin_amdgcn_ds_read_tr16_b64_v4i16((__attribute__((address_space(3))) v4i16*)(p_ + 1024)); \
        dst[n][ks] = bf16x8{lo_[0], lo_[1], lo_[2], lo_[3], hi_[0], hi_[1], hi_[2], hi_[3]}; }
#define K8_MMA(ai, bj, Bt) do { __builtin_amdgcn_s_setprio(1); \
        _Pragma("unroll") for (int m = 0; m < 4; ++m) _Pragma("unroll") for (int n = 0; n < 2; ++n) _Pragma("unroll") for (int ks = 0; ks < 2; ++ks) \
            acc[ai][bj][m][n] = __builtin_amdgcn_mfma_f32_16x16x32_bf16(Bt[n][ks], At[m][ks], acc[ai][bj][m][n], 0, 0, 0); \
        __builtin_amdgcn_s_setprio(0); } while (0)
    const rsrc_t rsC = __builtin_amdgcn_make_buffer_rsrc((void*)(arena(a.slots) + KX_OFF), 0, FX_OFF - KX_OFF, 0x00020000);
#define K8_EPI(ai, bj, cb) do { const int ln_ = lane_now(); \
        const int vo_ = (int)(wu & 3) * 1024 + (((ln_ >> 4) & 1) * 2 + (ln_ >> 5)) * 256 + (ln_ & 15) * 16; \
        _Pragma("unroll") for (int m = 0; m < 4; ++m) { \
            const f32x4 c0 = acc[ai][bj][m][0], c1 = acc[ai][bj][m][1]; \
            auto r0 = __builtin_amdgcn_permlane16_swap(cvt_pk_bf16(c0[0], c0[1]), cvt_pk_bf16(c1[0], c1[1]), false, false); \
            auto r1 = __builtin_amdgcn_permlane16_swap(cvt_pk_bf16(c0[2], c0[3]), cvt_pk_bf16(c1[2], c1[3]), false, false); \
            const int sg_ = 4 * ai + 2 * (int)(wu >> 2) + (m >> 1); \
            st_sc1s(rsC, vo_, ((cb) ^ (ai * 8192)) + sg_ * 524288 + bj * 16384 + (m & 1) * 4096, v4u{r0[0], r1[0], r0[1], r1[1]}); \
            acc[ai][bj][m][0] = ZERO4; acc[ai][bj][m][1] = ZERO4; } } while (0)
    constexpr int nt = K / 64;
    unsigned cur = (unsigned)todo & 255u, prev = 0u;
    const char* pA = a_of(cur); const char* pB = b_of(cur);
    if (early) touch((const char*)X + 524288 + (size_t)(nw * 128 + (tid >> 2)) * 128, lds0 + 163840 - 256);
    stB(0, 0, pB); stB(0, 1, pB); stA(0, 0, pA); stA(0, 1, pA);
    stB(1, 0, pB + SB); stB(1, 1, pB + SB); stA(1, 0, pA + SA);
    K8_WV(8); K8_BAR;
    if (wu >= 4) K8_BAR;
#define K8_GO(ai) __builtin_amdgcn_s_waitcnt(0xC07F); K8_BAR; K8_MMA(ai, 0, B0); K8_MMA(ai, 1, B1); K8_SCHED; K8_BAR
    int cbp = 0;
    for (;;) {
        todo >>= 8;
        const unsigned nxt = todo != 0ull ? (unsigned)todo & 255u : cur;
        int cb = c_of(cur);
#pragma nounroll
        for (int t = 0; t < nt; t += 2) {
            if (__builtin_expect(early && prev == 0u && t == 10, 0))
                touch((const char*)X + 3 * 524288 + (size_t)(nw * 320 + min(TIDX, 319)) * 128, lds0 + 163840 - 256);
            if (__builtin_expect(early && prev == 0u && t == nt - 4, 0)) {
                tau = __builtin_amdgcn_readfirstlane((unsigned)boot_wait(a.boot) & 255u);
                cb = c_of(cur);
            }
            const bool first = t == 0 && prev != 0u, last = t == nt - 2;
            if (t == 2 && prev != 0u) { publish(prev); WEV(3) }
            K8_LDB(B0, 0, 0); K8_LDA(0, 0); K8_LDB(B1, 0, 1); K8_SCHED; stA(1, 1, pA + SA);
            pA = last ? a_of(nxt) : pA + 2 * SA; pB = last ? b_of(nxt) : pB + 2 * SB;
            if (first) { K8_EPI(1, 0, cbp); K8_EPI(1, 1, cbp); K8_WV(24); } else K8_WV(8);
            K8_GO(0);
            K8_LDA(0, 1); K8_SCHED; stB(0, 0, pB); stB(0, 1, pB); stA(0, 0, pA);
            if (first) K8_WV(24); else K8_WV(8);
            K8_GO(1);
            K8_LDB(B0, 1, 0); K8_LDA(1, 0); K8_LDB(B1, 1, 1); K8_SCHED; stA(0, 1, pA);
            if (first) K8_WV(16); else K8_WV(8);
            K8_GO(0);
            K8_LDA(1, 1); K8_SCHED; stB(1, 0, pB + SB); stB(1, 1, pB + SB); stA(1, 0, pA + SA);
            if (last) { K8_EPI(0, 0, cb); K8_EPI(0, 1, cb); K8_WV(16); } else K8_WV(8);
            K8_GO(1);
        }
        WEV(2)
        prev = cur; cbp = cb; cur = nxt;
        if (todo == 0ull) break;
    }
    K8_EPI(1, 0, cbp); K8_EPI(1, 1, cbp);
    K8_WV(0);
    if (wu < 4) K8_BAR;
    K8_BAR;
    publish(prev);
    WEV(3)
}

__device__ __forceinline__ void oproj_squad(const ChainArgs& a, u16* out, const int ct, const int rh, const int xc, const unsigned tau, char* smem) {
    const int tid = opaque_tid(), w = tid >> 6, l = tid & 63, i = l & 15, g = l >> 4;
    const int wu = __builtin_amdgcn_readfirstlane(w);
    const int c0 = (ct / 3) * 128 + (ct % 3) * 48, nq = ct % 3 == 2 ? 8 : 12;
    bf16x8 wf[2][16];
    char* w3 = smem + wu * 16384 + l * 16;
    {
        u16* stg = (u16*)(smem + wu * 16384);
        bf16x8 wt[16];
#pragma unroll
        for (int ch = 0; ch < 4; ++ch) {
            const u16* src = a.Wo + (size_t)hcol(4 * wu + ch) * DM;
            v4u raw[12];
#pragma unroll
            for (int e = 0; e < 12; ++e) { const int idx = l + 64 * e, r = idx / 6, q = idx % 6; raw[e] = *(const v4u*)(src + (size_t)r * DM + min(c0 + 8 * q, DM - 8)); }
#pragma unroll
            for (int e = 0; e < 12; ++e) { const int idx = l + 64 * e, r = idx / 6, q = idx % 6; *(v4u*)(stg + r * 56 + 8 * q) = raw[e]; }
            __builtin_amdgcn_s_waitcnt(0xC07F);
#pragma unroll
            for (int jj = 0; jj < 3; ++jj)
#pragma unroll
                for (int ks = 0; ks < 4; ++ks) {
                    const u16* p = stg + (32 * ks + 8 * g + (i >> 2)) * 56 + 16 * jj + 4 * (i & 3);
                    (jj < 2 ? wf[jj & 1] : wt)[4 * ch + ks] = tr_frag(p, p + 4 * 56);
                }
            __builtin_amdgcn_s_waitcnt(0xC07F);
        }
#pragma unroll
        for (int ks = 0; ks < 16; ++ks) *(bf16x8*)(w3 + ks * 1024) = wt[ks];
    }
    const rsrc_t rsO = __builtin_amdgcn_make_buffer_rsrc((void*)(arena(a.slots) + VX_OFF), 0, 32 << 20, 0x00020000);
    float* rp = (float*)(smem + 131072);
    const int voff = rh * 4096 + l * 16;
    bool pre = false;
    v4u xf[16];
    const unsigned* fq = a.sync + (SY_OXS + xc) * 32 + wu;
    for (int s = SSEG; s < NSEG - 1; ++s, fq += 256) {
        if (!pre) {
#pragma nounroll
            for (int spin = 0;; ++spin) {
                asm volatile("" ::: "memory");
                if (ld_flag(fq) != 0u) break;
                if (spin > 20000) { __hip_atomic_store((gu32*)a.tmo, 7000u, __ATOMIC_RELAXED, __HIP_MEMORY_SCOPE_AGENT); break; }
                if (s == SSEG) __builtin_amdgcn_s_sleep(64); else if (s < NSEG - 3) __builtin_amdgcn_s_sleep(SQ_SL); else __builtin_amdgcn_s_sleep(SQ_SF);
            }
            asm volatile("" ::: "memory");
        }
        const unsigned nf = ld_flag(fq + 256);
        if (!pre) {
#pragma unroll
            for (int ks = 0; ks < 16; ++ks) xf[ks] = ld_sc1s(rsO, voff + (ks & 3) * 1024, blk_off(s, 4 * wu + (ks >> 2), tau) ^ 8192);
        }
        pre = __builtin_amdgcn_readfirstlane(nf) != 0u;
        f32x4 acc[3] = {};
        if (pre) {
            bf16x8 wn = *(const bf16x8*)w3;
#pragma unroll
            for (int ks = 0; ks < 16; ++ks) {
#pragma unroll
                for (int jj = 0; jj < 2; ++jj) acc[jj] = __builtin_amdgcn_mfma_f32_16x16x32_bf16(wf[jj][ks], as_frag(xf[ks]), acc[jj], 0, 0, 0);
                acc[2] = __builtin_amdgcn_mfma_f32_16x16x32_bf16(wn, as_frag(xf[ks]), acc[2], 0, 0, 0);
                wn = *(const bf16x8*)(w3 + ((ks + 1) & 15) * 1024);
                K8_SCHED;
                xf[ks] = ld_sc1s(rsO, voff + (ks & 3) * 1024, blk_off(s + 1, 4 * wu + (ks >> 2), tau) ^ 8192);
                K8_SCHED;
            }
        } else {
#pragma unroll
            for (int ks = 0; ks < 16; ++ks) {
#pragma unroll
                for (int jj = 0; jj < 2; ++jj) acc[jj] = __builtin_amdgcn_mfma_f32_16x16x32_bf16(wf[jj][ks], as_frag(xf[ks]), acc[jj], 0, 0, 0);
                acc[2] = __builtin_amdgcn_mfma_f32_16x16x32_bf16(*(const bf16x8*)(w3 + ks * 1024), as_frag(xf[ks]), acc[2], 0, 0, 0);
            }
        }
        K8_BAR;
#pragma unroll
        for (int jj = 0; jj < 3; ++jj) *(f32x4*)&rp[(wu * 16 + i) * 52 + 16 * jj + 4 * g] = acc[jj];
        K8_WL(0); K8_BAR;
        if (tid >= 256 && (tid & 15) < nq) {
            const int row = (tid >> 4) & 15, q4 = tid & 15;
            f32x4 sv_[8];
#pragma unroll
            for (int e = 0; e < 8; ++e) sv_[e] = *(const f32x4*)&rp[(e * 16 + row) * 52 + 4 * q4];
            const f32x4 s0 = ((sv_[0] + sv_[1]) + (sv_[2] + sv_[3])) + ((sv_[4] + sv_[5]) + (sv_[6] + sv_[7]));
            v2u o; o[0] = cvt_pk_bf16(s0[0], s0[1]); o[1] = cvt_pk_bf16(s0[2], s0[3]);
            out8(out + (size_t)(s * SEG + 16 * rh + row) * OUTW + c0 + 4 * q4, o);
        }
    }
}

__device__ __forceinline__ void kv_stream(const ChainArgs& a, const u16* __restrict__ X, const u16* __restrict__ B, char* C, const int head, const int xc, const int wk, unsigned& tau, char* smem) {
    const int tid = opaque_tid(), w = tid >> 6, l = tid & 63, i = l & 15, g = l >> 4;
    const int wu = __builtin_amdgcn_readfirstlane(w), kh = wu >> 2, cq = wu & 3;
    const unsigned lds0 = (unsigned)(uintptr_t)(__attribute__((address_space(3))) char*)smem;
    unsigned voA, voB;
    { const int row = (wu & 1) * 8 + (l >> 3), c = l & 7; voA = (unsigned)(row * DM + 8 * (c ^ (row & 7))) * 2u + (wu >> 1) * 512; }
    { const int row = tid >> 4, c = tid & 15; voB = (unsigned)(row * HD + 8 * (c ^ (bswz2<128>(row) << 1))) * 2u; }
    auto stB = [&](int ch, int slot) {
#pragma unroll
        for (int e = 0; e < 4; ++e) glds16s(voB, (const char*)B + (size_t)(4 * ch + e) * 32 * HD * 2, lds0 + 65536 + slot * 32768 + e * 8192 + wu * 1024);
    };
    const unsigned ldA = lds0 + (wu >> 1) * 8192 + (wu & 1) * 1024;
    auto stP = [&](const char* p, const unsigned d) {
        unsigned keep;
        asm volatile("s_mov_b32 %0, m0\n\ts_mov_b32 m0, %1\n\ts_nop 0\n\tglobal_load_lds_dwordx4 %3, %2 offset:-4096\n\tglobal_load_lds_dwordx4 %4, %2 offset:-2048\n\t"
                     "global_load_lds_dwordx4 %5, %2\n\tglobal_load_lds_dwordx4 %6, %2 offset:2048\n\ts_mov_b32 m0, %0"
                     : "=&s"(keep) : "s"(d), "s"(p), "v"(voA + 8192u), "v"(voA + 6272u), "v"(voA + 4352u), "v"(voA + 2432u) : "memory");
    };
    auto srcA = [&](int h) { return (const char*)X + (size_t)min(h, 2 * KVS - 1) * 32768 - 4096; };
    auto stA = [&](int h) { stP(srcA(h), ldA + (h & 3) * 32768 + 4096); };
    stB(0, 0); stA(0); stA(1); stB(1, 1);
    touch((const char*)X + 65536 + min(wk * 512 + tid, (KVS - 1) * 512 - 1) * 128, lds0 + 131072 + wu * 1024);
    stB(2, 2);
    bf16x8 bf[16][2], af[8];
    f32x4 acc[2][2] = {}, keep[2];
    const int rb = 64 * kh + 8 * g + (i >> 2), sw = bswz2<128>(rb);
    const char* bp = smem + 65536 + rb * 256 + 8 * (i & 3);
    const char* sa0 = smem + kh * 2048 + i * 128 + 16 * (g ^ (i & 7));
    const char* sa1 = smem + kh * 2048 + i * 128 + 16 * ((4 + g) ^ (i & 7));
#define KV_AF(slot, q) *(const bf16x8*)((((q) & 1) ? sa1 : sa0) + (slot) * 32768 + ((q) >> 1) * 4096)
#pragma unroll
    for (int ch = 0; ch < 8; ++ch) {
        if (ch == 0) K8_WV(9); else if (ch < 6) K8_WV(8); else if (ch == 6) K8_WV(4); else K8_WV(0);
        K8_BAR;
#pragma unroll
        for (int e = 0; e < 4; ++e) { const char* p_ = bp + (ch % 3) * 32768 + (e >> 1) * 8192 + 32 * ((2 * cq + (e & 1)) ^ sw); bf[2 * ch + (e >> 1)][e & 1] = tr_frag(p_, p_ + 1024); }
#pragma unroll
        for (int e = 0; e < 4; ++e) af[e] = KV_AF(e >> 1, 2 * ch + (e & 1));
        K8_WL(0); K8_SCHED; K8_BAR;
        if (ch < 5) stB(ch + 3, ch % 3);
        if (ch == 7) { stA(2); stA(3); }
#pragma unroll
        for (int e = 0; e < 8; ++e) acc[e >> 2][e & 1] = __builtin_amdgcn_mfma_f32_16x16x32_bf16(bf[2 * ch + ((e >> 1) & 1)][e & 1], af[e >> 1], acc[e >> 2][e & 1], 0, 0, 0);
    }
    float* red = (float*)(smem + 131072) + l * 4;
#pragma unroll
    for (int p = 0; p < 2; ++p) { *(f32x4*)(red + (p * 8 + wu) * 256) = kh ? acc[p][0] : acc[p][1]; keep[p] = kh ? acc[p][1] : acc[p][0]; }
    unsigned long long tag = 0;
    if (wu == 4) { tag = boot_wait(a.boot); *(volatile unsigned*)(smem + 163840 - 512) = (unsigned)tag & 255u; }
    const rsrc_t rc = __builtin_amdgcn_make_buffer_rsrc((void*)C, 0, KVS * 524288, 0x00020000);
    const int vo = cq * 1024 + (2 * kh + (g >> 1)) * 256 + i * 16 + (g & 1) * 8;
    auto put = [&](const f32x4 o, const int so) {
        const v2u pk = {cvt_pk_bf16(o[0], o[1]), cvt_pk_bf16(o[2], o[3])};
        asm volatile("buffer_store_dwordx2 %0, %1, %2, %3 offen sc1" :: "v"(pk), "v"(vo), "s"(rc), "s"(so) : "memory");
    };
    K8_WL(0); K8_WV(0); K8_BAR;
    tau = __builtin_amdgcn_readfirstlane(*(volatile unsigned*)(smem + 163840 - 512));
#pragma unroll
    for (int p = 0; p < 2; ++p) put(keep[p] + *(const f32x4*)(red + (p * 8 + (wu ^ 4)) * 256), blk_off(0, head, tau) + p * 4096);
    stA(4);
    K8_WV(4); K8_BAR;
    if (tid == 256) __hip_atomic_store((gu64*)(a.boot + 256 + 16 * xc + wk), tag, __ATOMIC_RELAXED, __HIP_MEMORY_SCOPE_AGENT);
    auto stages = [&](auto kc) {
        constexpr int KH = decltype(kc)::value;
        if (KH) K8_BAR;
        f32x4 ac[2] = {ZERO4, ZERO4}, kp = ZERO4;
        int h_first = 2, so = OOB;
        asm volatile("" : "+s"(h_first));
        const char* pn = srcA(5);
        unsigned dn = ldA + 32768 + 4096;
        int r0 = (int)(sa0 - smem) + 2 * 32768, r1 = (int)(sa1 - smem) + 2 * 32768;
        for (int h = h_first; h < 2 * KVS + 4; ++h) {
            const f32x4 oth = *(const f32x4*)(red + (wu ^ 4) * 256);
            *(f32x4*)(red + wu * 256) = ac[KH ^ 1];
            bf16x8 ax[16];
#pragma unroll
            for (int q = 0; q < 16; ++q) ax[q] = *(const bf16x8*)(smem + ((q & 1) ? r1 : r0) + (q >> 1) * 4096);
            K8_SCHED;
            stP(pn, dn);
            put((KH ? ac[1] : kp) + oth, so);
            kp = ac[0];
            __builtin_amdgcn_s_waitcnt(0xC07F);
            K8_WV(10);
            if (__builtin_expect(h == 5, 0)) K8_WV(0);
            K8_BAR;
            if (KH && (0xAAA20u >> h) & 1u && tid == 256)
                __hip_atomic_store((gu32*)(a.sync + (SY_KV0 + (h / 2 - (h == 5 ? 1 : 2)) * 8 + xc) * 32 + wk), 16u, __ATOMIC_RELAXED, __HIP_MEMORY_SCOPE_AGENT);
            if (__builtin_expect(h < 2 * KVS, 1)) {
                __builtin_amdgcn_s_setprio(1);
#pragma unroll
                for (int q = 0; q < 16; ++q)
#pragma unroll
                    for (int n = 0; n < 2; ++n) ac[n] = __builtin_amdgcn_mfma_f32_16x16x32_bf16(bf[q][n], ax[q], q ? ac[n] : ZERO4, 0, 0, 0);
                __builtin_amdgcn_s_setprio(0);
            }
            {
                const int hs = h - 1 + KH;
                so = blk_off(hs >> 1, head, tau) + (hs & 1) * 4096;
                asm volatile("" : "+s"(so));
                so = hs > 1 && hs < 2 * KVS ? so : OOB;
                pn = srcA(h + 4); dn = ldA + (h & 3) * 32768 + 4096;
                r0 = (r0 + 32768) & 131071; r1 = (r1 + 32768) & 131071;
                asm volatile("" : "+s"(so), "+s"(pn), "+s"(dn), "+v"(r0), "+v"(r1));
            }
            K8_BAR;
        }
        if (!KH) K8_BAR;
    };
    if (kh) stages(std::integral_constant<int, 1>{}); else stages(std::integral_constant<int, 0>{});
    K8_WV(0);
}

struct MegaArgs {
    ChainArgs ch;
    const u16* x; u16* out;
    const u16* bx; const u16* wr; const u16* wi;
};
constexpr int MEGA_LDS = 163840;
#define WSTAMP(n)
__global__ __launch_bounds__(512, 2) void mega_kernel(MegaArgs m) {
    extern __shared__ __attribute__((aligned(256))) char chsm[];
    const int b = BIDX, xc = (b & 3) + 4 * (b >> 7), sl = (b >> 3) & 15;
    if ((b & 4) == 0) { chain_body(m.ch, xc, sl, chsm); return; }
    const ChainArgs& a = m.ch;
    const int wk = sl, tid = TIDX;
    char* ar = arena(a.slots);
    unsigned tauw = 0;
    const int hf = __builtin_amdgcn_readfirstlane(TIDX >> 8);
    WSTAMP(0)
    if (wk < 8) kv_stream(a, m.x, (wk < 4 ? a.Wk : a.Wv) + hcol(4 * xc + (wk & 3)), ar + (wk < 4 ? KX_OFF : VX_OFF), 4 * xc + (wk & 3), xc, wk, tauw, chsm);
    WSTAMP(8) WEV(1)
    {
        const int p = wk & 1, q = wk & 3;
        const unsigned long long todo = wk < 2 ? 0x84u + p : wk < 4 ? 0x74704240u + 0x01010101u * p : wk < 8 ? 0ull : wk < 10 ? 0x60503010ull + 0x01010101ull * q
                                      : wk < 12 ? 0x7060503010ull + 0x0101010101ull * q : (wk < 14 ? 0x54341420u : 0x64442422u) + 0x01010101u * p;
        __syncthreads();
        if (todo != 0ull) tile_stream(a, m.x, todo, xc, tauw, wk >= 8, xc * 8 + (wk & 7), (u16*)chsm WEVA);
    }
    WSTAMP(2)
    if (wk < 2) {
        unsigned* mbox = (unsigned*)(chsm + 131072);
        if (tid < 40) mbox[tid] = tid >= 1 && tid < 5 ? 2u : 0u;
        __syncthreads();
        const int w = __builtin_amdgcn_readfirstlane(tid >> 6), h = 4 * xc + (w & 3);
        const rsrc_t rsQ = __builtin_amdgcn_make_buffer_rsrc((void*)(ar + KX_OFF), 0, S_LEN * HD * 4, 0x00020000);
        const rsrc_t rsK = __builtin_amdgcn_make_buffer_rsrc((void*)(ar + KX_OFF), 0, S_LEN * HD * 4, 0x00020000);
        const rsrc_t rsV = __builtin_amdgcn_make_buffer_rsrc((void*)(ar + VX_OFF), 0, S_LEN * HD * 4, 0x00020000);
        const rsrc_t rsS = slot_rsrc(a.slots);
        u16 (*Vs)[VP] = (u16 (*)[VP])(chsm + w * (40 * VP * 2));
        const unsigned tau = tauw;
        int pend = -1;
        bool ready = false;
        for (int s = 2 * wk + (w >> 2), sn; s < NSEG - 1; s = sn) {
            const int band = s >> 3;
            sn = s + 4;
            if (s < XST) {
                unsigned n = 0;
                if (lane_now() == 0) n = atomicAdd(mbox + 1 + (w & 3), 1u);
                n = __builtin_amdgcn_readfirstlane(n);
                sn = 2 * wk + (n < XST / 2 ? (int)(n & 1u) + 4 * (int)(n >> 1) : XST + (w >> 2));
            }
            if (sn - s > 4) __builtin_amdgcn_s_setprio(2); else __builtin_amdgcn_s_setprio(0);
            if (s < 4) wait_ge<32>(a.sync + (SY_KV0 + (KVS - 1) * 8 + xc) * 32 + (tid & 7), 16u, a.tmo, 4100u);
            if (!ready) {
                if (band > 0) wait_ge<32>(a.sync + (SY_KV + band * 8 + xc) * 32 + (tid & 3), 1u, a.tmo, 4100u);
                wait_ge<32>(a.sync + (SY_Q + band * 8 + xc) * 32 + (tid & 3), 1u, a.tmo, 4200u);
            }
            xattn_unit(a, rsQ, rsK, rsV, rsS, s, sn, h, Vs, mbox, tau, pend, ready);
        }
        return;
    }
    if (wk >= 4 && wk < 8) {
        const int fw = (wk - 4) * 8 + xc;
        const rsrc_t rsF = __builtin_amdgcn_make_buffer_rsrc((void*)(ar + FX_OFF), 0, 16 << 20, 0x00020000);
        const rsrc_t rsW = __builtin_amdgcn_make_buffer_rsrc((void*)(ar + WT_OFF), 0, 10 << 20, 0x00020000);
        const unsigned tw = tauw ^ 1u;
        {
            const int tq = opaque_tid();
            fft_rows_fwd(m.bx, rsF, tw, 2 * fw + hf, chsm + hf * 36864, tq & 255, (const char*)m.wr, (const char*)m.wi, (unsigned)(uintptr_t)(__attribute__((address_space(3))) char*)chsm + 163840 - 256);
        }
        for (int t = fw; t < 64; t += 32) {
            __syncthreads();
            filt_transpose(m.wr, m.wi, rsW, tw, t, chsm, opaque_tid());
        }
        wg_publish(a.sync, SY_DONE, 32u, 8);
        WSTAMP(3) WEV(4)
        wg_wait_acquire<16>(a.sync + (SY_DONE + xc) * 32, a.tmo, 6000u);
        {
            const int tq = opaque_tid();
            fft_cols(rsF, rsW, tw, 2 * fw + hf, 4, chsm + hf * 36864, tq & 255);
        }
        wg_publish(a.sync, SY_DONE + 8, 32u, 8);
        WSTAMP(4) WEV(5)
    }
    if (wk >= 4 && wk < 10) {
        const rsrc_t rsF = __builtin_amdgcn_make_buffer_rsrc((void*)(ar + FX_OFF), 0, 16 << 20, 0x00020000);
        wg_wait_acquire<16>(a.sync + (SY_DONE + 8 + xc) * 32, a.tmo, 6001u);
        const int tq = opaque_tid();
        fft_rows_inv(rsF, tauw ^ 1u, m.out, 2 * ((wk & 3) * 8 + xc) + hf + (wk < 8 ? 0 : 192), wk < 8 ? 64 : 32, wk < 8 ? 3 : 2, chsm + hf * 36864, tq & 255);
        WSTAMP(5) WEV(6)
    }
    if (wk >= 12) {
        const int r = 2 * (wk - 12);
        wg_wait_acquire<16>(a.sync + (SY_OX + r * 8 + xc) * 32, a.tmo, 5000u);
        wg_wait_acquire<16>(a.sync + (SY_OX + (r + 1) * 8 + xc) * 32, a.tmo, 5000u);
        WEV(7)
        gemm_tile<256>((u16*)chsm, ar + VX_OFF, 4 * r, tauw, a.Wo + xc * 128, m.out + (size_t)r * 128 * OUTW + xc * 128);
        WEV(8)
    }
    if (wk < 4 || wk == 10) {
        const int r = wk < 4 ? wk + 6 : 10;
        wg_wait_acquire<16>(a.sync + (SY_OX + r * 8 + xc) * 32, a.tmo, 5000u);
        WEV(7)
        gemm_tile<128>((u16*)chsm, ar + VX_OFF, 4 * r, tauw, a.Wo + xc * 128, m.out + (size_t)r * 128 * OUTW + xc * 128);
        WEV(8)
        WSTAMP(9)
    }
    if (wk >= 4 && wk < 10) {
        __syncthreads();
        oproj_squad(a, m.out, (wk - 4) * 4 + (xc & 3), xc >> 2, xc, tauw, chsm);
        WSTAMP(7)
    }
}

#if defined(__HIP_DEVICE_COMPILE__)
#pragma clang attribute pop
#endif
extern "C" void kernel_launch(void* const* d_in, const int* in_sizes, int n_in,
                              void* d_out, int out_size, void* d_ws, size_t ws_size,
                              hipStream_t stream) {
    char* ws = (char*)d_ws;
    size_t off = 0;
    auto alloc = [&](size_t bytes) { void* p = ws + off; off += (bytes + 255) & ~(size_t)255; return p; };
    char* slots = (char*)alloc((size_t)ARENA_BYTES + (2u << 20));
    static_assert((SY_WORDS + 2) * 128 <= 131072, "sync block");
    unsigned* sync = (unsigned*)alloc(131072);
    unsigned long long* boot = (unsigned long long*)alloc(4096);

    MegaArgs m;
    m.ch.mem0 = (const u16*)d_in[1]; m.ch.Wq = (const u16*)d_in[2]; m.ch.Wk = (const u16*)d_in[3]; m.ch.Wv = (const u16*)d_in[4]; m.ch.Wo = (const u16*)d_in[5];
    m.ch.out = (u16*)d_out; m.ch.slots = slots; m.ch.tmo = sync + SY_WORDS * 32;
    m.ch.stamps = (unsigned long long*)(ws + (240ull << 20));
    m.ch.sync = sync; m.ch.boot = boot;
    m.x = (const u16*)d_in[0]; m.out = (u16*)d_out;
    m.bx = (const u16*)d_in[7]; m.wr = (const u16*)d_in[8]; m.wi = (const u16*)d_in[9];
    (void)hipFuncSetAttribute((const void*)mega_kernel, hipFuncAttributeMaxDynamicSharedMemorySize, MEGA_LDS);
    mega_kernel<<<256, 512, MEGA_LDS, stream>>>(m);
}
```

```cpp
#if defined(__HIP_DEVICE_COMPILE__)
#pragma clang attribute push (__attribute__((target("no-packed-fp32-ops,no-load-store-opt"))), apply_to = function)
#endif
#include <hip/hip_runtime.h>
#include <hip/hip_bf16.h>
#include <stdint.h>
#include <type_traits>

typedef unsigned short u16;
#define TIDX ((int)__builtin_amdgcn_workitem_id_x())
#define BIDX ((int)__builtin_amdgcn_workgroup_id_x())
__device__ __forceinline__ int opaque_tid() { int t = TIDX; asm volatile("" : "+v"(t)); return t; }
__device__ __forceinline__ int lane_now() { int t; asm volatile("v_mbcnt_lo_u32_b32 %0, -1, 0\n\tv_mbcnt_hi_u32_b32 %0, -1, %0" : "=v"(t)); return t; }
typedef __attribute__((ext_vector_type(8))) short bf16x8;
typedef __attribute__((ext_vector_type(4))) float f32x4;

constexpr int S_LEN = 2048, DM = 1024, NH = 32, DH = 128, MM = 8, SEG = 32;
constexpr int NSEG = S_LEN / SEG;
constexpr int HD = NH * DH;
constexpr int OUTW = 2048;

typedef unsigned v4u __attribute__((ext_vector_type(4)));
typedef unsigned v2u __attribute__((ext_vector_type(2)));
typedef short v4i16 __attribute__((ext_vector_type(4)));
typedef __amdgpu_buffer_rsrc_t rsrc_t;
#define AUX_SC1 16
__device__ __forceinline__ v4u ld_sc1s(rsrc_t rs, int voff, int soff) { return __builtin_amdgcn_raw_buffer_load_b128(rs, voff, soff, AUX_SC1); }
__device__ __forceinline__ void st_sc1s(rsrc_t rs, int voff, int soff, v4u v) { __builtin_amdgcn_raw_buffer_store_b128(v, rs, voff, soff, AUX_SC1); asm volatile("s_nop 1" :: "v"(v) : "memory"); }
__device__ __forceinline__ void st8_sc1s(rsrc_t rs, int voff, int soff, v2u v) { __builtin_amdgcn_raw_buffer_store_b64(v, rs, voff, soff, AUX_SC1); }
__device__ __forceinline__ void st_sc1(rsrc_t rs, int off, v4u v) { __builtin_amdgcn_raw_buffer_store_b128(v, rs, off, 0, AUX_SC1); }
__device__ __forceinline__ void out8(void* p, v2u v) { asm volatile("global_store_dwordx2 %0, %1, off sc1" :: "v"(p), "v"(v) : "memory"); }
__device__ __forceinline__ void out16(void* p, v4u v) { asm volatile("global_store_dwordx4 %0, %1, off sc1\n\ts_nop 1" :: "v"(p), "v"(v) : "memory"); }
typedef __bf16 bf16x2_t __attribute__((ext_vector_type(2)));
typedef float f32x2_t __attribute__((ext_vector_type(2)));
__device__ __forceinline__ unsigned cvt_pk_bf16(float lo, float hi) {
    f32x2_t f = {lo, hi};
    return __builtin_bit_cast(unsigned, __builtin_convertvector(f, bf16x2_t));
}
#define SENT 0xFFBFFFBF
__device__ __forceinline__ unsigned umax3(unsigned a, unsigned b, unsigned c) { return max(max(a, b), c); }
__device__ __forceinline__ unsigned mx4(unsigned acc, v4u v) { return umax3(umax3(acc, v[0], v[1]), v[2], v[3]); }
__device__ __forceinline__ bf16x8 as_frag(v4u v) { return __builtin_bit_cast(bf16x8, v); }
__device__ __forceinline__ float xor16_sum(float x) {
    auto r = __builtin_amdgcn_permlane16_swap(__float_as_uint(x), __float_as_uint(x), false, false);
    return __uint_as_float(r[0]) + __uint_as_float(r[1]);
}
__device__ __forceinline__ float xor32_sum(float x) {
    auto r = __builtin_amdgcn_permlane32_swap(__float_as_uint(x), __float_as_uint(x), false, false);
    return __uint_as_float(r[0]) + __uint_as_float(r[1]);
}
__device__ __forceinline__ float xor16_max(float x) {
    auto r = __builtin_amdgcn_permlane16_swap(__float_as_uint(x), __float_as_uint(x), false, false);
    return fmaxf(__uint_as_float(r[0]), __uint_as_float(r[1]));
}
__device__ __forceinline__ float xor32_max(float x) {
    auto r = __builtin_amdgcn_permlane32_swap(__float_as_uint(x), __float_as_uint(x), false, false);
    return fmaxf(__uint_as_float(r[0]), __uint_as_float(r[1]));
}
constexpr int SPIN_LIMIT = 400000;
constexpr int OOB = 0x7FFFFF00;
#define XST 32
#define SQ_SL 16
#define PLL 0
#define SE 6
#define TDE 72
#define WT 1
#define PB 14
#define TD 60
#define PS 46
#define TE0 10
#define PVA 8
#define PV 1
#define PV1 2
#define PV2 11
#define PL 20
#define PF 48
#define PC 31
#define PP 15
#define PKA 28
#define PG0 2
#define PG1 32
#define DLY_U 4
#define DLY_C 4
#define XA_FAST 6
#define XA_D 12
#define SQ_SF 4
#define LW 0
constexpr int NPROBE = 2;
constexpr int M_OFF = 32 * 16384, F_OFF = M_OFF + 2 * 16384, C_OFF = F_OFF + 2 * 16384, REC = C_OFF + 16 * 16384, PERWG = REC / 128, SLOT_BYTES = 34 * 2097152;
static_assert(PERWG == 6656, "arming shares");
__device__ __forceinline__ int rec_base(int s) { return (s >> 1) * 2097152 + (s & 1) * REC; }
__device__ __forceinline__ char* arena(char* slots) { return (char*)(((uintptr_t)slots + 2097151u) & ~(uintptr_t)2097151u); }
constexpr int KX_OFF = SLOT_BYTES, VX_OFF = KX_OFF + (32 << 20), FX_OFF = VX_OFF + (32 << 20), WT_OFF = FX_OFF + (16 << 20), ARENA_BYTES = WT_OFF + (10 << 20);
__device__ __forceinline__ rsrc_t slot_rsrc(char* slots) { return __builtin_amdgcn_make_buffer_rsrc((void*)arena(slots), 0, FX_OFF, 0x00020000); }
__device__ __forceinline__ int blk_off(int seg, int h, unsigned tau) { return seg * 524288 + (((h & 3) >> 1) * 16 + (h >> 2) * 2 + (h & 1)) * 16384 + (int)((tau ^ (unsigned)(seg >> 2)) & 1u) * 8192; }
constexpr int TM_OFF = 124 * 16384;
constexpr int CAL_OFF = 32 * 2097152;
__device__ __forceinline__ int near_half(int s, unsigned tau) { return (int)((tau ^ (unsigned)(s >> 1)) & 1u) * 8192; }
__device__ __forceinline__ int chunk_off(int s, int g, int j, unsigned tau) { return rec_base(s) + (j < 2 ? (g & 3) * 2 + j : 6 + 3 * g + j) * 16384 + near_half(s, tau); }
__device__ __forceinline__ int x_off(int s, int h, unsigned tau) { return chunk_off(s, h >> 2, 2, tau) + (h & 3) * 2048; }
__device__ __forceinline__ int km_off(int s, int h, unsigned tau) { return chunk_off(s, h >> 2, 3, tau) + (h & 3) * 2048; }
__device__ __forceinline__ int m_off(int s, int h, unsigned tau) { return rec_base(s) + M_OFF + (h >> 4) * 16384 + near_half(s, tau) + (h & 15) * 512; }
__device__ __forceinline__ int v_off(int s, int h, unsigned tau) { return chunk_off(s, h >> 2, 4, tau) + (h & 3) * 2048; }
__device__ __forceinline__ int f_off(int s, int g, unsigned tau) { return rec_base(s) + F_OFF + (g >> 2) * 16384 + near_half(s, tau) + (g & 3) * 2048; }
__device__ __forceinline__ int hcol(int h) { return ((h & 3) * 8 + (h >> 2)) * 128; }
constexpr int VP = 144;

struct ChainArgs {
    u16* out;
    const u16* mem0; const u16* Wq; const u16* Wk; const u16* Wv; const u16* Wo;
    char* slots;
    unsigned* tmo;
    unsigned long long* stamps;
    unsigned* sync;
    unsigned long long* boot;
};
constexpr int SY_KV = 0;
constexpr int SY_Q = 64;
constexpr int SY_PROG = 128;
constexpr int SY_OX = 136;
constexpr int SY_DONE = 264;
constexpr int SY_OXS = 472;
constexpr int SY_KV0 = 408;
constexpr int KVS = 8;
constexpr int SY_FLAGS = 680;
constexpr int SY_WORDS = SY_FLAGS + SY_FLAGS / 8;
constexpr int SSEG = 44;
typedef __attribute__((address_space(1))) unsigned gu32;
typedef __attribute__((address_space(1))) unsigned long long gu64;
__device__ __forceinline__ unsigned ld_flag(const unsigned* p) { return __hip_atomic_load((gu32*)p, __ATOMIC_RELAXED, __HIP_MEMORY_SCOPE_AGENT); }
__device__ __forceinline__ unsigned long long boot_tag(const unsigned long long* boot) {
    return __hip_atomic_load((gu64*)(boot + 144), __ATOMIC_RELAXED, __HIP_MEMORY_SCOPE_AGENT) + 1ull;
}
__device__ __forceinline__ unsigned long long boot_wait(const unsigned long long* boot) {
    const int l = TIDX & 63;
    unsigned long long tag = 0, x = 0, y = 0;
#pragma nounroll
    for (int spin = 0; spin < 100000; ++spin) {
        asm volatile("" ::: "memory");
        x = __hip_atomic_load((gu64*)(boot + l), __ATOMIC_RELAXED, __HIP_MEMORY_SCOPE_AGENT);
        y = __hip_atomic_load((gu64*)(boot + 64 + l), __ATOMIC_RELAXED, __HIP_MEMORY_SCOPE_AGENT);
        tag = boot_tag(boot);
        if (__all((x | 3u) == ((tag << 2) | 3u) && (y | 3u) == ((tag << 2) | 3u))) break;
        __builtin_amdgcn_s_sleep(16);
    }
    asm volatile("" ::: "memory");
    const unsigned m = ((unsigned)__ballot(x & 1u) ^ (__popcll(__ballot(x & 2u)) + __popcll(__ballot(y & 2u)) > 64 ? 1u : 0u)) & 1u;
    return (tag << 8) | m;
}
template <int SL>
__device__ __forceinline__ void wait_ge(const unsigned* p, unsigned need, unsigned* tmo, unsigned code) {
#pragma nounroll
    for (int spin = 0;; ++spin) {
        asm volatile("" ::: "memory");
        if (__all(ld_flag(p) >= need)) break;
        if (spin > 400000 / SL) { __hip_atomic_store((gu32*)tmo, code, __ATOMIC_RELAXED, __HIP_MEMORY_SCOPE_AGENT); break; }
        __builtin_amdgcn_s_sleep(SL);
    }
    asm volatile("" ::: "memory");
}
template <int SL>
__device__ __forceinline__ void wg_wait_acquire(const unsigned* p, unsigned* tmo, unsigned code) {
    if (TIDX < 64) {
        wait_ge<SL>(p, 1u, tmo, code);
        __builtin_amdgcn_fence(__ATOMIC_ACQUIRE, "agent");
        asm volatile("s_waitcnt vmcnt(0)" ::: "memory");
    }
    __syncthreads();
}
__device__ __forceinline__ void arrive(unsigned* sync, const int f, const unsigned need, const int n) {
    const int l = lane_now();
    unsigned old = 0;
    if (l == 0) old = __hip_atomic_fetch_add((gu32*)(sync + (SY_FLAGS + (f >> 3)) * 32), 1u, __ATOMIC_RELAXED, __HIP_MEMORY_SCOPE_AGENT);
    if (__builtin_amdgcn_readfirstlane(old) == need - 1u && l < n) __hip_atomic_store((gu32*)(sync + (f + l) * 32), 16u, __ATOMIC_RELAXED, __HIP_MEMORY_SCOPE_AGENT);
}
__device__ __forceinline__ void wg_publish(unsigned* sync, const int f, const unsigned need, const int n) {
    asm volatile("s_waitcnt vmcnt(0)" ::: "memory");
    __syncthreads();
    if (TIDX < 64) arrive(sync, f, need, n);
}
#define TMO_SET(code) { dead = true; if (l == 0) __hip_atomic_store(a.tmo, (code) + s, __ATOMIC_RELAXED, __HIP_MEMORY_SCOPE_AGENT); }

__device__ __forceinline__ void glds16h(unsigned voff, const void* sbase, unsigned lds_dst) {
    unsigned keep;
    asm volatile("s_mov_b32 %0, m0\n\ts_mov_b32 m0, %3\n\ts_nop 0\n\tglobal_load_lds_dwordx4 %1, %2 sc1\n\ts_mov_b32 m0, %0" : "=&s"(keep) : "v"(voff), "s"(sbase), "s"(lds_dst) : "memory");
}
__device__ __forceinline__ void touch(const void* p, unsigned lds_dst) {
    unsigned keep;
    asm volatile("s_mov_b32 %0, m0\n\ts_mov_b32 m0, %2\n\ts_nop 0\n\tglobal_load_lds_dword %1, off\n\ts_mov_b32 m0, %0" : "=&s"(keep) : "v"(p), "s"(lds_dst) : "memory");
}
__device__ __forceinline__ void touch_sc1(const void* p, unsigned lds_dst) {
    unsigned keep;
    asm volatile("s_mov_b32 %0, m0\n\ts_mov_b32 m0, %2\n\ts_nop 0\n\tglobal_load_lds_dword %1, off sc1\n\ts_mov_b32 m0, %0" : "=&s"(keep) : "v"(p), "s"(lds_dst) : "memory");
}
#define WEV(code)
#define WEVP
#define WEVA
constexpr int C2_K = 32768, C2_V = 65536;
constexpr int C2_R1 = 0, R1S = 3200;
constexpr int C2_R3 = C2_R1 + 4 * R1S;
constexpr int C2_MB = 24576;
#define PST(n)
#define LT(n)
#define LTA(n)
#define LTOUT0
#define LTOUT
#define CN(n)
#define CNS
#define CNOUT
#define CLOB16(a, b, c, d, e, f, g, h, i, j, k, l, m, n, o, p) "v" #a, "v" #b, "v" #c, "v" #d, "v" #e, "v" #f, "v" #g, "v" #h, "v" #i, "v" #j, "v" #k, "v" #l, "v" #m, "v" #n, "v" #o, "v" #p
#define LDS3(p) ((__attribute__((address_space(3))) v4i16*)(p))
__device__ __forceinline__ bf16x8 tr_frag(const void* p0, const void* p1) {
    v4i16 lo = __builtin_amdgcn_ds_read_tr16_b64_v4i16(LDS3(p0));
    v4i16 hi = __builtin_amdgcn_ds_read_tr16_b64_v4i16(LDS3(p1));
    return bf16x8{lo[0], lo[1], lo[2], lo[3], hi[0], hi[1], hi[2], hi[3]};
}
typedef __bf16 bf16x8v __attribute__((ext_vector_type(8)));
typedef __bf16 bf16x16v __attribute__((ext_vector_type(16)));
typedef unsigned v8u __attribute__((ext_vector_type(8)));
typedef unsigned v32u __attribute__((ext_vector_type(32)));
#define PUT8(v, o, x) { const v8u x_ = (x); _Pragma("unroll") for (int e_ = 0; e_ < 8; ++e_) (v)[(o) + e_] = x_[e_]; }
#define SUB4(v, o) v4u{(v)[(o)], (v)[(o) + 1], (v)[(o) + 2], (v)[(o) + 3]}
#define PUT4(v, o, x) { const v4u x_ = (x); (v)[(o)] = x_[0]; (v)[(o) + 1] = x_[1]; (v)[(o) + 2] = x_[2]; (v)[(o) + 3] = x_[3]; }
__device__ __forceinline__ v2u tr4(const u16* p) { return __builtin_bit_cast(v2u, __builtin_amdgcn_ds_read_tr16_b64_v4i16(LDS3(p))); }
__device__ __forceinline__ f32x4 smm(v4u a, v8u b, f32x4 c, int idx) {
    return __builtin_amdgcn_smfmac_f32_16x16x64_bf16(__builtin_bit_cast(bf16x8v, a), __builtin_bit_cast(bf16x16v, b), c, idx, 0, 0);
}
__device__ __forceinline__ v8u tr_frag64(const u16* p, const int pitch) {
    const v2u r0 = tr4(p), r1 = tr4(p + 8 * pitch), r2 = tr4(p + 32 * pitch), r3 = tr4(p + 40 * pitch);
    return v8u{r0[0], r1[0], r0[1], r1[1], r2[0], r3[0], r2[1], r3[1]};
}
#define MM(a_, b_, c_) __builtin_amdgcn_mfma_f32_16x16x32_bf16(as_frag(a_), as_frag(b_), c_, 0, 0, 0)
#define STR2(x) #x
#define STR(x) STR2(x)
#define PAUSE(n) ".rept (" STR(n) ") / 4\n\ts_nop 15\n\t.endr\n\t.rept (" STR(n) ") %% 4\n\ts_nop 3\n\t.endr\n\t"
#define ZERO4 f32x4{0.f, 0.f, 0.f, 0.f}
constexpr int XB_OFF = CAL_OFF + 262144, XP = 68;
__device__ __forceinline__ void last_rows(const char* chsm, rsrc_t rsS, const int wu, const int grp, const int c, const unsigned tau) {
    const int l = lane_now();
    __builtin_amdgcn_s_barrier();
    asm volatile("" ::: "memory");
    const float* rp = (const float*)(chsm + C2_K) + (4 * wu + (l >> 4)) * XP + 4 * (l & 15);
    const f32x4 s0 = (*(const f32x4*)rp + *(const f32x4*)(rp + 32 * XP)) + (*(const f32x4*)(rp + 64 * XP) + *(const f32x4*)(rp + 96 * XP));
    v2u o;
    o[0] = cvt_pk_bf16(s0[0], s0[1]); o[1] = cvt_pk_bf16(s0[2], s0[3]);
    st8_sc1s(rsS, l * 8, XB_OFF + ((c >> 1) * 8 + wu) * 16384 + (int)(tau & 1u) * 8192 + (c & 1) * 4096 + grp * 512, o);
}
__device__ __forceinline__ const unsigned* kv_flag(const unsigned* sync, const int sq, const int grp, const int l) { return sync + (sq < KVS ? (SY_KV0 + sq * 8 + grp) * 32 + (l & 7) : (SY_KV + 8 + grp) * 32 + (l & 3)); }
__device__ __forceinline__ void chain_body(const ChainArgs& a, const int grp, const int c, char* chsm) {
    const int wu = __builtin_amdgcn_readfirstlane(TIDX >> 6);
    const int widx = grp * 16 + c;
    const int hs = 4 * grp + (c >> 2), jq = c & 3;
    const rsrc_t rsS = slot_rsrc(a.slots);
    const unsigned l0 = (unsigned)(uintptr_t)(__attribute__((address_space(3))) char*)chsm;
    bool dead = false;

    const int cal_off = CAL_OFF + grp * 32768 + (c >> 3) * 16384 + (c & 7) * 1024;
        if (wu == 0) {
            const int l = lane_now();
            const v4u ff = {SENT, SENT, SENT, SENT};
#pragma unroll
            for (int e = 0; e < 13; ++e) st_sc1s(rsS, l * 16, widx * 2 * PERWG + min(e * 1024, 2 * PERWG - 1024), ff);
            const rsrc_t rsY = __builtin_amdgcn_make_buffer_rsrc((void*)a.sync, 0, 131072, 0x00020000);
            st_sc1s(rsY, l * 16, widx * 1024, v4u{0u, 0u, 0u, 0u});
            __builtin_amdgcn_raw_buffer_store_b32(0u, rsS, (l & 3) * 256 + ((l >> 2) & 1) * 8192, cal_off, AUX_SC1);
            if (c < 2) __builtin_amdgcn_raw_buffer_store_b32(0u, rsS, (l >> 5) * 8192 + (l & 31) * 4, TM_OFF + c * 16384 + grp * 256, AUX_SC1);
        }
    asm volatile("" ::: "memory");
    if (wu >= 4) {
        __builtin_amdgcn_s_sleep(DLY_U);
        const int l = opaque_tid() & 63, i = l & 15, g = l >> 4;
        const int sidx = (i & 1) ? 0xEEEE : 0x4444;
        const int u = wu - 4;
        const int k0 = 256 * u;
        u16* stg = (u16*)(chsm + 40960 + u * 20480);
        v4u af[4];
        v32u wS[6];
        {
            v4u raw[3][16];
            {
                const size_t wo = (size_t)k0 * HD + hcol(hs) + 32 * jq;
                const rsrc_t rq = __builtin_amdgcn_make_buffer_rsrc((void*)(a.Wq + wo), 0, 256 * HD * 2, 0x00020000);
                const rsrc_t rk = __builtin_amdgcn_make_buffer_rsrc((void*)(a.Wk + wo), 0, 256 * HD * 2, 0x00020000);
                const rsrc_t rv = __builtin_amdgcn_make_buffer_rsrc((void*)(a.Wv + wo), 0, 256 * HD * 2, 0x00020000);
                const int vw = (l >> 2) * HD * 2 + (l & 3) * 16;
#pragma unroll
                for (int p = 0; p < 4; ++p) af[p] = *(const v4u*)(a.mem0 + (size_t)(i >> 1) * DM + k0 + 64 * p + 16 * g + 8 * (i & 1));
#pragma unroll
                for (int e = 0; e < 16; ++e) raw[0][e] = __builtin_amdgcn_raw_buffer_load_b128(rq, vw, e * 16 * HD * 2, 0);
#pragma unroll
                for (int e = 0; e < 16; ++e) raw[1][e] = __builtin_amdgcn_raw_buffer_load_b128(rk, vw, e * 16 * HD * 2, 0);
#pragma unroll
                for (int e = 0; e < 12; ++e) raw[2][e] = __builtin_amdgcn_raw_buffer_load_b128(rv, vw, e * 16 * HD * 2, 0);
                __builtin_amdgcn_sched_barrier(0);
#pragma unroll
                for (int e = 0; e < 16; ++e) *(v4u*)(stg + (16 * e + (l >> 2)) * 40 + 8 * (l & 3)) = raw[0][e];
                __builtin_amdgcn_sched_barrier(0);
#pragma unroll
                for (int e = 12; e < 16; ++e) raw[2][e] = __builtin_amdgcn_raw_buffer_load_b128(rv, vw, e * 16 * HD * 2, 0);
            }
            const int fr = 16 * (g >> 1) + 4 * (g & 1) + (i >> 2);
            __builtin_amdgcn_sched_barrier(0);
#pragma unroll
            for (int hx = 0; hx < 3; ++hx) {
                if (hx) {
#pragma unroll
                    for (int e = 0; e < 16; ++e) *(v4u*)(stg + (16 * e + (l >> 2)) * 40 + 8 * (l & 3)) = raw[hx][e];
                }
                __builtin_amdgcn_s_waitcnt(0xC07F);
#pragma unroll
                for (int jj = 0; jj < 2; ++jj)
#pragma unroll
                    for (int p = 0; p < 4; ++p) PUT8(wS[2 * hx + jj], 8 * p, tr_frag64(stg + (64 * p + fr) * 40 + 16 * jj + 4 * (i & 3), 40))
                __builtin_amdgcn_s_waitcnt(0xC07F);
                __builtin_amdgcn_sched_barrier(0);
            }
        }
        PST(u)
        if (u == 0) { PST(4) }
        const unsigned r1w = l0 + C2_R1 + u * R1S + (2 * g * 16 + i) * 4, r1v = l0 + C2_R1 + u * R1S + 2176 + (2 * g * 16 + ((i & 9) | ((i & 2) << 1) | ((i & 4) >> 1))) * 4;
#define LD4(d, va, so, o) "buffer_load_dwordx4 v[" d "], " va ", %[rs], " so " offen offset:" o " sc1\n\t"
#define CHK(a, b, c, d) "v_max3_u32 v244, v244, v" a ", v" b "\n\tv_max3_u32 v244, v244, v" c ", v" d "\n\t"
#define CHK0(a, b, c, d) "v_max_u32 v244, v" a ", v" b "\n\tv_max3_u32 v244, v244, v" c ", v" d "\n\t"
#define SMF(d, a, w) "v_smfmac_f32_16x16x64_bf16 v[" d "], v[" a "], v[" w "], %[sx]\n\t"
#define ADD(d, a, b) "v_add_f32 v" d ", v" a ", v" b "\n\t"
#define Z2(r) "v_mov_b64 v[" r "], 0\n\t"
#define DW2(ad, x, y, o0, o1) "ds_write2_b32 " ad ", v" x ", v" y " offset0:" o0 " offset1:" o1 "\n\t"
#define A0 "224:227"
#define A1 "228:231"
#define A2 "232:235"
#define A3 "236:239"
#define Q0 "200:203"
#define Q1 "204:207"
#define Q2 "208:211"
#define Q3 "212:215"
#define V0 "216:219"
#define V1 "220:223"
#define S1_HEAD \
            Z2("216:217") Z2("218:219") Z2("220:221") Z2("222:223") \
            "s_waitcnt vmcnt(4)\n\t" ZQ \
            "s_waitcnt vmcnt(3)\n\t" SMF(V0, A0, "136:143") CHK0("224", "225", "226", "227") SMF(V1, A0, "168:175") \
            "s_waitcnt vmcnt(2)\n\t" SMF(V0, A1, "144:151") CHK("228", "229", "230", "231") SMF(V1, A1, "176:183") \
            "s_waitcnt vmcnt(1)\n\t" SMF(V0, A2, "152:159") CHK("232", "233", "234", "235") SMF(V1, A2, "184:191") \
            "s_waitcnt vmcnt(0)\n\t" SMF(V0, A3, "160:167") CHK("236", "237", "238", "239") SMF(V1, A3, "192:199") \
            "v_cmp_eq_u32 vcc, " STR(SENT) ", v244\n\t"
#define S1_TAIL(B1, B2) \
            SMF(Q0, A0, "8:15") SMF(Q0, A1, "16:23") SMF(Q0, A2, "24:31") \
            ADD("240", "216", "217") ADD("241", "218", "219") SMF(Q0, A3, "32:39") DW2("%[rv]", "240", "241", "0", "16") \
            ADD("242", "220", "221") ADD("243", "222", "223") SMF(Q1, A0, "40:47") DW2("%[rv]", "242", "243", "128", "144") \
            SMF(Q1, A1, "48:55") SMF(Q1, A2, "56:63") SMF(Q1, A3, "64:71") \
            ADD("240", "200", "201") ADD("241", "202", "203") SMF(Q2, A0, "72:79") DW2("%[rw]", "240", "241", "0", "16") \
            SMF(Q2, A1, "80:87") SMF(Q2, A2, "88:95") "s_nop 1\n\t" \
            ADD("242", "204", "205") ADD("243", "206", "207") SMF(Q2, A3, "96:103") DW2("%[rw]", "242", "243", "136", "152") \
            SMF(Q3, A0, "104:111") SMF(Q3, A1, "112:119") \
            B1        \
            LTA(0) \
            SMF(Q3, A2, "120:127") SMF(Q3, A3, "128:135") "s_nop 0\n\t" \
            ADD("240", "208", "209") ADD("241", "210", "211") DW2("v246", "240", "241", "0", "16") \
            "s_nop 3\n\t" \
            ADD("242", "212", "213") ADD("243", "214", "215") DW2("v246", "242", "243", "136", "152") \
            "s_waitcnt lgkmcnt(0)\n\t" B2 "s_barrier\n\t"
#define ZQ Z2("200:201") Z2("202:203") Z2("204:205") Z2("206:207") Z2("208:209") Z2("210:211") Z2("212:213") Z2("214:215")
#define U_W "{v[8:39]}"(wS[0]), "{v[40:71]}"(wS[1]), "{v[72:103]}"(wS[2]), "{v[104:135]}"(wS[3]), "{v[136:167]}"(wS[4]), "{v[168:199]}"(wS[5])
#define U_CLOB CLOB16(200, 201, 202, 203, 204, 205, 206, 207, 208, 209, 210, 211, 212, 213, 214, 215), CLOB16(216, 217, 218, 219, 220, 221, 222, 223, 224, 225, 226, 227, 228, 229, 230, 231), \
            CLOB16(232, 233, 234, 235, 236, 237, 238, 239, 240, 241, 242, 243, 244, 245, 246, 247)
        __builtin_amdgcn_s_setprio(2);
        {
            asm volatile(
                "v_add_u32 v246, 1088, %[rw]\n\t"
                S1_HEAD "s_nop 0\n\t" S1_TAIL("s_nop 1\n\t", "s_barrier\n\t")
                : LTOUT0
                : U_W, "{v[224:227]}"(af[0]), "{v[228:231]}"(af[1]), "{v[232:235]}"(af[2]), "{v[236:239]}"(af[3]), [sx] "v"(sidx), [rv] "v"(r1v), [rw] "v"(r1w)
                : "memory", "vcc", CLOB16(200, 201, 202, 203, 204, 205, 206, 207, 208, 209, 210, 211, 212, 213, 214, 215), CLOB16(216, 217, 218, 219, 220, 221, 222, 223, 240, 241, 242, 243, 244, 245, 246, 247));
        }
        const unsigned tau = __builtin_amdgcn_readfirstlane(*(volatile unsigned*)(chsm + C2_MB + 8));
        const unsigned long long wmask = u == 3 ? (c == 0 ? ~0ull : 0ull) : u == 1 ? 0x0020202020202000ull : u == 2 ? 1ull << (NSEG - 3) : 0ull;
        const int pvv = u == 3 ? 2 : -1, pvm = u == 3 ? -1 : 0;
        const unsigned* pfl = a.sync + (u == 3 ? SY_PROG + grp : u == 1 ? SY_KV + 16 + grp : SY_Q + 7 * 8 + grp) * 32;
        int s_first = 0;
        asm volatile("s_branch 1f\n\t.p2align 8\n1:" : "+s"(s_first));
        for (int s = s_first; s < NSEG - 1; ++s) {
            __builtin_amdgcn_s_setprio(0);
            LT(1)
            const int pv = (wmask >> s) & 1ull ? pvv + (s & pvm) : 0;
            const unsigned* pa = pfl;
            if (pv < 0) pfl += 256;
            const int cb = grp & 3;
            int soffc = rec_base(s) + C_OFF + c * 16384 + near_half(s, tau) + cb * 2048;
            int o_as = rec_base(s + 1) + ((c >> 3) + (grp >> 2) * 4) * 16384 + near_half(s + 1, tau) + ((4 * c + cb) & 31) * 256;
            int soff = chunk_off(s + 1, grp, u >> 1, tau) + (u & 1) * 4096;
            const int o_tm = (s >> 1) * 2097152 + TM_OFF + (s & 1) * 16384;
            int lim = dead ? 0 : SPIN_LIMIT, bad = 0;
            const bool rcd = u > 0 || widx < 32;
            const int pc = u > 0 ? 3 * widx + u - 1 : rcd ? 384 + widx : s * 96 + widx - 32;
            const int o_arm = (rcd ? s + 2 < NSEG : pc < 512) ? (rcd ? rec_base(s + 2) : XB_OFF) + (pc >> 3) * 16384 + near_half(rcd ? s + 2 : 0, tau) + (pc & 7) * 1024 : OOB;
            const unsigned* ck = s <= KVS ? kv_flag(a.sync, s + 2, grp, 0) : nullptr;
            const char* vb = arena(a.slots) + (s + 2 >= NSEG ? KX_OFF : VX_OFF) + (blk_off(min(s + 2, NSEG - 1), 4 * grp + 2 * u - 2, tau) ^ (s + 2 >= NSEG ? 8192 : 0));
            asm volatile("" : "+s"(soffc), "+s"(o_as), "+s"(soff), "+s"(lim), "+s"(vb), "+s"(ck));
#define GL(o) "global_load_lds_dwordx4 %[l16], s[84:85] offset:" o " sc1\n\t"
#define GL4 GL("0") GL("1024") GL("2048") GL("3072")
#define GLN(d) "s_add_u32 m0, m0, 4096\n\ts_add_u32 s84, s84, " d "\n\ts_addc_u32 s85, s85, 0\n\t"
#define PKE PAUSE(PKA)
#define C_TRY(x, x0, x1, x2, x3, y, y0, y1, y2, y3) \
                "s_waitcnt vmcnt(3)\n\t" \
                "v_mfma_f32_16x16x32_bf16 v[240:243], v[216:219], v[" x "], 0\n\t" CHK0(x0, x1, x2, x3) \
                "s_waitcnt vmcnt(2)\n\t" \
                "v_mfma_f32_16x16x32_bf16 v[240:243], v[216:219], v[" y "], v[240:243]\n\t" CHK(y0, y1, y2, y3) \
                "v_cmp_eq_u32 vcc, " STR(SENT) ", v244\n\t" \
                "s_cbranch_vccz 3b\n\t" CN(1) \
                LD4(x, "%[l16]", "%[sc]", "0") LD4(y, "%[l16]", "%[sc]", "1024")
            asm volatile(
                "v_and_b32 v221, 16, %[l16]\n\t"
                "v_mov_b32 v222, 0x3f80\n\t"
                "v_bfe_u32 v223, %[l16], 5, 2\n\t"
                "v_lshlrev_b32 v221, v221, v222\n\t"
                "v_cmp_eq_u32 vcc, 0, v223\n\tv_and_b32 v220, 0xf0, %[l16]\n\tv_bfe_u32 v222, %[l16], 8, 1\n\tv_cndmask_b32 v216, 0, v221, vcc\n\t"
                "v_cmp_eq_u32 vcc, 1, v223\n\tv_lshl_add_u32 v220, v222, 3, v220\n\tv_lshrrev_b32 v222, 9, %[l16]\n\tv_cndmask_b32 v217, 0, v221, vcc\n\t"
                "v_cmp_eq_u32 vcc, 2, v223\n\tv_lshl_add_u32 v220, v222, 30, v220\n\ts_nop 0\n\tv_cndmask_b32 v218, 0, v221, vcc\n\t"
                "v_cmp_eq_u32 vcc, 3, v223\n\ts_nop 1\n\tv_cndmask_b32 v219, 0, v221, vcc\n\t"
                "v_mul_u32_u24 v222, 0x8000, v222\n\t"
                "v_and_b32 v221, 0xff, v220\n\t"
                "v_add_u32 v221, v221, v222\n\t"
                "v_lshrrev_b32 v247, 2, %[l16]\n\t"
                PKE
                "v_mov_b32 v240, " STR(SENT) "\n\tv_mov_b32 v241, v240\n\tv_mov_b32 v242, v240\n\tv_mov_b32 v243, v240\n\t"
                "buffer_store_dwordx4 v[240:243], %[l16], %[rs], %[arm] offen sc1\n\t"
                LTA(2)
                "s_setprio 2\n\t"
                "s_barrier\n\t"
                LTA(3)
                "v_add_u32 v246, 1088, %[rw]\n\t"
                "s_cmp_lt_u32 %[uu], 1\n\t"
                "s_cbranch_scc1 7f\n\t"
                "s_cmp_eq_u32 %[uu], 3\n\t"
                "s_cbranch_scc1 20f\n\t"
                "s_memrealtime s[84:85]\n\t"
                "v_lshrrev_b32 v241, 9, %[l16]\n\t"
                "v_and_b32 v242, 0x7c, v247\n\t"
                "v_lshl_add_u32 v241, v241, 13, v242\n\t"
                "s_waitcnt lgkmcnt(0)\n\t"
                "v_mov_b32 v240, s84\n\t"
                "buffer_store_dword v240, v241, %[rs], %[ts] offen sc1\n\t"
                "s_cmp_eq_u64 %[ck], 0\n\t"
                "s_cbranch_scc1 14f\n\t"
                "v_and_b32 v241, %[cm], v247\n\t"
                "global_load_dword v240, v241, %[ck] sc1\n\t"
                "v_mov_b32 v241, %[cw]\n\t"
                "s_waitcnt vmcnt(0)\n\t"
                "v_cmp_gt_u32 vcc, 16, v240\n\t"
                "s_cmp_eq_u64 vcc, 0\n\t"
                "s_cselect_b32 s84, 1, 0\n\t"
                "v_mov_b32 v240, s84\n\t"
                "ds_write_b32 v241, v240\n\t"
                "s_cbranch_scc0 11f\n\t"
                "14:\n\t"
                "s_mov_b32 s86, m0\n\t"
                "s_mov_b64 s[84:85], %[vb]\n\t"
                "s_mov_b32 m0, %[lv]\n\t"
                PAUSE(PG0)
                "s_cmp_eq_u32 %[uu], 1\n\t"
                "s_cbranch_scc1 12f\n\t"
                PAUSE(PG1)
                "12:\n\t"
                GL4 GLN("4096") GL4 GLN("12288") GL4 GLN("4096") GL4
                "s_mov_b32 m0, s86\n\t"
                "11:\n\t"
                "s_cmp_lt_i32 %[pv], 0\n\t"
                "s_cbranch_scc0 8f\n\t"
                "v_and_b32 v241, 12, v247\n\t"
                "10:\n\t"
                "global_load_dword v240, v241, %[pa] sc1\n\t"
                "s_waitcnt vmcnt(0)\n\t"
                "v_cmp_gt_u32 vcc, 16, v240\n\t"
                "s_cbranch_vccz 8f\n\t"
                "s_sleep 6\n\t"
                "s_sub_u32 %[lim], %[lim], 1\n\t"
                "s_cbranch_scc0 10b\n\t"
                "s_mov_b32 %[bad], 3000\n\t"
                "s_mov_b32 %[lim], 0\n\t"
                "s_branch 8f\n\t"
                "20:\n\t"
                PAUSE(PS)
                "s_memrealtime s[84:85]\n\t"
                "s_load_dword s86, %[tb], 0\n\ts_load_dword s87, %[tb], 0x100\n\ts_load_dword s88, %[tb], 0x200\n\ts_load_dword s89, %[tb], 0x300\n\t"
                "s_load_dword s90, %[tb], 0x400\n\ts_load_dword s91, %[tb], 0x500\n\ts_load_dword s92, %[tb], 0x600\n\ts_load_dword s93, %[tb], 0x700\n\t"
                "s_waitcnt lgkmcnt(0)\n\t"
                "s_sub_u32 s86, s84, s86\n\ts_sub_u32 s87, s84, s87\n\ts_sub_u32 s88, s84, s88\n\ts_sub_u32 s89, s84, s89\n\t"
                "s_sub_u32 s90, s84, s90\n\ts_sub_u32 s91, s84, s91\n\ts_sub_u32 s92, s84, s92\n\ts_sub_u32 s93, s84, s93\n\t"
                "s_or_b32 s85, s86, s87\n\ts_min_u32 s86, s86, s87\n\ts_or_b32 s85, s85, s88\n\ts_min_u32 s86, s86, s88\n\ts_or_b32 s85, s85, s89\n\ts_min_u32 s86, s86, s89\n\t"
                "s_memrealtime s[88:89]\n\t"
                "s_or_b32 s85, s85, s90\n\ts_min_u32 s86, s86, s90\n\ts_or_b32 s85, s85, s91\n\ts_min_u32 s86, s86, s91\n\ts_or_b32 s85, s85, s92\n\ts_min_u32 s86, s86, s92\n\t"
                "s_or_b32 s85, s85, s93\n\ts_min_u32 s86, s86, s93\n\t"
                "s_cmp_gt_u32 s85, 127\n\t"
                CNS
                "s_cselect_b32 s85, " STR(TE0) ", 127\n\t"
                "s_min_u32 s86, s86, s85\n\t"
                "s_sub_u32 s84, s84, s86\n\t"
                "s_add_u32 s84, s84, %[td]\n\t"
                "s_cmp_lt_i32 %[pv], 1\n\t"
                "s_cbranch_scc1 4f\n\t"
                "v_mov_b32 v247, %[pv]\n\t"
                "s_mov_b64 exec, 1\n\t"
                "global_store_dword %[l16], v247, %[pa] sc1\n\t"
                "s_mov_b64 exec, -1\n\t"
                "4:\n\t"
                "s_waitcnt lgkmcnt(0)\n\t"
                "s_sub_i32 s84, s84, s88\n\t"
                "s_max_i32 s84, s84, 0\n\t"
                "s_min_i32 s84, s84, 40\n\t"
                "21:\n\t"
                "s_sub_u32 s84, s84, 1\n\t"
                "s_cbranch_scc0 21b\n\t"
                "s_branch 8f\n\t"
                "7:\n\t"
                PAUSE(PC)
                LD4(A0, "%[l16]", "%[sc]", "0") LD4(A1, "%[l16]", "%[sc]", "1024")
                PAUSE(PP)
                LD4(A2, "%[l16]", "%[sc]", "0") LD4(A3, "%[l16]", "%[sc]", "1024")
                "s_waitcnt vmcnt(3)\n\t"
                "v_mfma_f32_16x16x32_bf16 v[240:243], v[216:219], v[224:227], 0\n\t" CHK0("224", "225", "226", "227")
                "s_waitcnt vmcnt(2)\n\t"
                "v_mfma_f32_16x16x32_bf16 v[240:243], v[216:219], v[228:231], v[240:243]\n\t" CHK("228", "229", "230", "231")
                "v_cmp_eq_u32 vcc, " STR(SENT) ", v244\n\t"
                "s_cbranch_vccnz 30f\n\t"
                "3:\n\t"
                "s_nop 3\n\t"
                "v_cvt_pk_bf16_f32 v244, v240, v241\n\t"
                "v_cvt_pk_bf16_f32 v245, v242, v243\n\t"
                "buffer_store_dwordx2 v[244:245], v221, %[rs], %[oas] offen sc1\n\t"
                PAUSE(PB)
                "8:\n\t"
                LTA(4)
                "s_barrier\n\t"
                PAUSE(1)
                "2:\n\t"
                LD4(A0, "%[l16]", "%[sa]", "0") LD4(A1, "%[l16]", "%[sa]", "1024") LD4(A2, "%[l16]", "%[sa]", "2048") LD4(A3, "%[l16]", "%[sa]", "3072")
                S1_HEAD
                "s_cbranch_vccnz 6f\n\t"
                "5:\n\t"
                LTA(5)
                S1_TAIL("s_waitcnt lgkmcnt(0)\n\ts_barrier\n\t", "")
                "s_setprio 0\n\t"
                "s_branch 9f\n\t"
                "30:\n\t" CN(1)
                LD4(A0, "%[l16]", "%[sc]", "0") LD4(A1, "%[l16]", "%[sc]", "1024")
                "1:\n\t"
                C_TRY(A2, "232", "233", "234", "235", A3, "236", "237", "238", "239")
                C_TRY(A0, "224", "225", "226", "227", A1, "228", "229", "230", "231")
                "s_sub_u32 %[lim], %[lim], 1\n\t"
                "s_cbranch_scc0 1b\n\t"
                "s_mov_b32 %[bad], 2000\n\t"
                "s_mov_b32 %[lim], 0\n\t"
                "s_branch 3b\n\t"
                "6:\n\t" CN(65536)
                "v_mov_b32 v240, %[tg]\n\t"
                "v_mov_b32 v241, %[lf]\n\t"
                "ds_write_b32 v241, v240\n\t"
                "s_nop 0\n\t"
                "s_sub_u32 %[lim], %[lim], 1\n\t"
                "s_cbranch_scc0 2b\n\t"
                "s_mov_b32 %[bad], 1\n\t"
                "s_branch 5b\n\t"
                "9:\n\t"
                : [lim] "+s"(lim), [bad] "+s"(bad), [oas] "+s"(o_as) LTOUT CNOUT
                : U_W, [l16] "v"(l * 16), [rs] "s"(rsS), [sx] "v"(sidx), [rv] "v"(r1v), [rw] "v"(r1w),
                  [sc] "s"(soffc), [sa] "s"(soff), [uu] "s"(u), [pv] "s"(pv), [pa] "s"(pa), [vb] "s"(vb), [ck] "s"(ck), [cm] "s"(s + 2 < KVS ? 28 : 12), [cw] "s"(l0 + C2_MB + 32 + (s & 1) * 16 + u * 4), [lv] "s"(l0 + (s + 2 >= NSEG ? C2_V + 32768 : s & 1 ? C2_K : C2_V) + (u - 1) * 16384), [td] "s"(s < SE ? TDE : TD), [ts] "s"(c == 0 && (u == 2 || s < NSEG - 3) ? o_tm + grp * 256 + (2 - u) * 2097152 : OOB), [tb] "s"(arena(a.slots) + o_tm + near_half(s, tau)),

                  [arm] "s"(o_arm),
                  [tg] "s"(s + 1), [lf] "s"(l0 + C2_MB + 16)
                : "memory", "vcc", "scc", "s84", "s85", "s86", "s87", "s88", "s89", "s90", "s91", "s92", "s93", U_CLOB);
            if (__builtin_expect(bad != 0, 0)) TMO_SET((unsigned)bad)
        }
        last_rows(chsm, rsS, wu, grp, c, tau);
        if (u == 3 && widx == 0 && l == 0) __hip_atomic_store((gu64*)(a.boot + 144), *(volatile unsigned long long*)(chsm + C2_MB), __ATOMIC_RELAXED, __HIP_MEMORY_SCOPE_AGENT);
        return;
    }
    const int l = opaque_tid() & 63, i = l & 15, g = l >> 4;
    const int sidx = (i & 1) ? 0xEEEE : 0x4444;
    const int ha = 4 * grp + wu;
    unsigned long long tag;
    v32u wO[2];
    {
        u16* stg = (u16*)(chsm + 122880 + wu * 10240);
        tag = boot_tag(a.boot);
        asm volatile("" ::: "memory");
        v4u rawo[16];
        const u16* src = a.Wo + (size_t)(hcol(ha) + (l >> 3)) * DM + 64 * c + 8 * (l & 7);
#pragma unroll
        for (int e = 0; e < 16; ++e) rawo[e] = *(const v4u*)(src + (size_t)8 * e * DM);
        __builtin_amdgcn_sched_barrier(0);
        if (wu == 0) {
            asm volatile("s_waitcnt vmcnt(16)" ::: "memory");
            unsigned tm[2] = {~0u, ~0u};
            const char* pb = arena(a.slots) + cal_off;
#pragma unroll
            for (int k = 0; k < 2 * NPROBE; ++k) {
                unsigned long long c0, c1; unsigned d;
                asm volatile("s_memtime %0\n\ts_waitcnt lgkmcnt(0)\n\ts_load_dword %2, %3, %4\n\ts_waitcnt lgkmcnt(0)\n\ts_memtime %1\n\ts_waitcnt lgkmcnt(0)"
                             : "=&s"(c0), "=&s"(c1), "=&s"(d) : "s"(pb), "i"((k >> 1) * 256 + (k & 1) * 8192) : "memory");
                tm[k & 1] = min(tm[k & 1], (unsigned)(c1 - c0));
            }
            const unsigned side = (__builtin_amdgcn_s_getreg(20 | (3 << 11)) >> 2) & 1u;
            if (l == 0) __hip_atomic_store((gu64*)(a.boot + widx), (tag << 2) | (((tm[0] > tm[1] ? 1u : 0u) ^ side) << 1) | side, __ATOMIC_RELAXED, __HIP_MEMORY_SCOPE_AGENT);
            PST(7)
        }
#pragma unroll
        for (int m = 0; m < 2; ++m) {
#pragma unroll
            for (int e = 0; e < 8; ++e) *(v4u*)(stg + (8 * e + (l >> 3)) * 72 + 8 * (l & 7)) = rawo[8 * m + e];
            __builtin_amdgcn_s_waitcnt(0xC07F);
#pragma unroll
            for (int ct = 0; ct < 4; ++ct) {
                const u16* p = stg + (8 * g + (i >> 2)) * 72 + 16 * ct + 4 * (i & 3);
                const v2u r0 = tr4(p), r1 = tr4(p + 4 * 72), r2 = tr4(p + 32 * 72), r3 = tr4(p + 36 * 72);
                PUT4(wO[ct >> 1], (ct & 1) * 16 + m * 8, (v4u{r0[0], r0[1], r1[0], r1[1]})) PUT4(wO[ct >> 1], (ct & 1) * 16 + m * 8 + 4, (v4u{r2[0], r2[1], r3[0], r3[1]}))
            }
            __builtin_amdgcn_s_waitcnt(0xC07F);
        }
        if (wu == 0) { PST(5) }
        if (widx == 0 && wu == 0) *(volatile unsigned long long*)(chsm + C2_MB) = tag;
    }
    if (wu >= 2) touch_sc1(arena(a.slots) + (size_t)min(l + (wu - 2) * 64, FX_OFF / 2097152 - 1) * 2097152 + 2097024 - 128, l0 + C2_MB + 256);
    unsigned long long fw = 0;
#pragma nounroll
    for (int spin = 0; spin < 100000; ++spin) {
        asm volatile("" ::: "memory");
        fw = __hip_atomic_load((gu64*)(a.boot + 256 + 16 * grp + (l & 7)), __ATOMIC_RELAXED, __HIP_MEMORY_SCOPE_AGENT);
        if (__all((fw | 255u) == ((tag << 8) | 255u))) break;
        __builtin_amdgcn_s_sleep(4);
    }
    asm volatile("" ::: "memory");
    const unsigned tau = __builtin_amdgcn_readfirstlane((unsigned)fw & 255u);
    if (wu == 3) { *(volatile unsigned*)(chsm + C2_MB + 8) = tau; *(volatile unsigned*)(chsm + C2_MB + 16) = ~0u; __builtin_amdgcn_s_waitcnt(0xC07F); }
    if (wu == 1) { PST(9) }

    const unsigned r3a = l0 + C2_R3 + wu * 2048 + g * 128 + i * 4;
    v32u yb;
    v8u kx;
    v8u sel; v2u sel3;
#pragma unroll
    for (int d = 0; d < 8; ++d) sel[d] = (i >> 1) == d ? (0x3C00u << ((i & 1) * 16)) : 0u;
#pragma unroll
    for (int d = 0; d < 2; ++d) sel3[d] = (i & 3) == 0 && (i >> 3) == d ? (0x3C00u << (((i >> 2) & 1) * 16)) : 0u;
    asm volatile("s_setreg_imm32_b32 hwreg(HW_REG_MODE, 23, 1), 1\n\ts_nop 3");
#define MF(d, a, b, c) "v_mfma_f32_16x16x32_bf16 v[" d "], v[" a "], v[" b "], " c "\n\t"
#define PX4(d0, d1, d2, d3, a, w0, w1, w2, w3) MF(d0, a, w0, "v[" d0 "]") MF(d1, a, w1, "v[" d1 "]") MF(d2, a, w2, "v[" d2 "]") MF(d3, a, w3, "v[" d3 "]")
#define PXT(d0, d1, d2, d3, a0, a1, a2, a3) MF(d0, a0, "32:35", "0") MF(d1, a0, "48:51", "0") MF(d2, a0, "64:67", "0") MF(d3, a0, "80:83", "0") \
        PX4(d0, d1, d2, d3, a1, "36:39", "52:55", "68:71", "84:87") PX4(d0, d1, d2, d3, a2, "40:43", "56:59", "72:75", "88:91") PX4(d0, d1, d2, d3, a3, "44:47", "60:63", "76:79", "92:95")
#define CVP(d, a, b) "v_cvt_pk_bf16_f32 v" d ", v" a ", v" b "\n\t"
#define PROJ_X(cnt) \
        "s_waitcnt " cnt "(8)\n\t" PXT("192:195", "196:199", "200:203", "204:207", "160:163", "164:167", "168:171", "172:175") \
        "s_waitcnt " cnt "(4)\n\t" PXT("208:211", "212:215", "216:219", "220:223", "176:179", "180:183", "184:187", "188:191") \
        CVP("128", "192", "193") CVP("129", "194", "195") CVP("136", "196", "197") CVP("137", "198", "199") CVP("144", "200", "201") CVP("145", "202", "203") CVP("152", "204", "205") CVP("153", "206", "207") \
        CVP("132", "208", "209") CVP("133", "210", "211") CVP("140", "212", "213") CVP("141", "214", "215") CVP("148", "216", "217") CVP("149", "218", "219") CVP("156", "220", "221") CVP("157", "222", "223") \
        "s_waitcnt " cnt "(0)\n\t"
#define CLOB_PX CLOB16(160, 161, 162, 163, 164, 165, 166, 167, 168, 169, 170, 171, 172, 173, 174, 175), CLOB16(176, 177, 178, 179, 180, 181, 182, 183, 184, 185, 186, 187, 188, 189, 190, 191), \
        CLOB16(192, 193, 194, 195, 196, 197, 198, 199, 200, 201, 202, 203, 204, 205, 206, 207), CLOB16(208, 209, 210, 211, 212, 213, 214, 215, 216, 217, 218, 219, 220, 221, 222, 223)
#define LDX(d, va, so, o) "buffer_load_dwordx4 v[" d "], " va ", %[rs], " so " offen offset:" o " sc1\n\t"
#define DSV(d, o) "ds_read_b128 v[" d "], %[lv] offset:" o "\n\t"
    const int voffX = ((i >> 1) & 1) * 4096 + g * 256 + (2 * (i >> 2) + (i & 1)) * 16;
    const int pk = (((i >> 1) & 3) << 1) | (i >> 3);
    const unsigned hv = wu >= 2 ? l0 + C2_R1 + 2176 + ((wu & 1) * 128 + ((l >> 4) + 4 * ((l >> 3) & 1)) * 16 + 8 * ((l >> 1) & 1) + 4 * ((l >> 2) & 1) + 2 * (l & 1)) * 4
                                : l0 + C2_R1 + (i & 1) * 544 + (i >> 1) * 64 + g * 16;
    const unsigned hk = l0 + C2_R1 + 1088 + (i & 1) * 544 + pk * 64 + g * 16;
    const int vo1 = wu >= 2 ? l * 4 : wu == 1 ? (g >> 1) * 1024 + i * 16 + (g & 1) * 8 : (i & 1) ? 1 << 30 : (i >> 1) * 16 + 4 * g;
    const int vo2 = (2 * (i & 1) + (g >> 1)) * 128 + pk * 16 + (g & 1) * 8;
    const int voffK = (g >> 1) * 256 + i * 16 + (g & 1) * 8;
#define RD4(w, ad, a, b, c, d) "ds_read_" w " v[" a "], " ad "\n\tds_read_" w " v[" b "], " ad " offset:3200\n\tds_read_" w " v[" c "], " ad " offset:6400\n\tds_read_" w " v[" d "], " ad " offset:9600\n\t"
#define AD4(d0, d1, d2, d3, a0, a1, a2, a3) ADD(d0, d0, a0) ADD(d1, d1, a1) ADD(d2, d2, a2) ADD(d3, d3, a3)
#define QSC(r) "v_mul_f32 v" r ", 0x3e0293ee, v" r "\n\t"
#define FLD "buffer_load_dword v114, %[vf], %[rs], %[fl] offen sc1\n\t"
#define DUM "buffer_store_dword v168, off, %[rs], %[ob] sc1\n\t"
#define QHEAD "s_barrier\n\t" RD4("b128", "%[hv]", "160:163", "164:167", "168:171", "172:175") FLD \
        "s_waitcnt lgkmcnt(2)\n\t" AD4("160", "161", "162", "163", "164", "165", "166", "167") \
        "s_waitcnt lgkmcnt(0)\n\t" AD4("168", "169", "170", "171", "172", "173", "174", "175") \
        "s_barrier\n\t"
#define QTAIL AD4("160", "161", "162", "163", "168", "169", "170", "171") QSC("160") QSC("161") QSC("162") QSC("163")
#define CVH(d, a, b) "v_cvt_pk_f16_f32 v" d ", v" a ", v" b "\n\t"
#define SWA(d, a, b) "v_add_f32_dpp v" d ", v" a ", v" b " quad_perm:[1,0,3,2] row_mask:0xf bank_mask:0xf\n\t"
#define KLD(so) "buffer_load_dwordx2 v[96:97], %[vk], %[rs], " so " offen sc1\n\tbuffer_load_dwordx2 v[102:103], %[vk], %[rs], " so " offen offset:512 sc1\n\t" \
        "s_add_u32 " so ", " so ", 4096\n\t" \
        "buffer_load_dwordx2 v[100:101], %[vk], %[rs], " so " offen sc1\n\tbuffer_load_dwordx2 v[98:99], %[vk], %[rs], " so " offen offset:512 sc1\n\t"
    auto hand_over = [&](const int o_1, const int o_2, const int o_f, const int tg) {
        int dmy = 0;
        asm volatile(
            "s_cmp_eq_u32 %[wu], 0\n\t"
            "s_cbranch_scc1 2f\n\t"
            "s_cmp_eq_u32 %[wu], 1\n\t"
            "s_cbranch_scc1 1f\n\t"
            "s_barrier\n\t"
            RD4("b64", "%[hv]", "160:161", "162:163", "164:165", "166:167") FLD
            "s_waitcnt lgkmcnt(2)\n\t" ADD("160", "160", "162") ADD("161", "161", "163")
            "s_waitcnt lgkmcnt(0)\n\t" ADD("164", "164", "166") ADD("165", "165", "167") ADD("160", "160", "164") ADD("161", "161", "165")
            CVP("168", "160", "161")
            "buffer_store_dword v168, %[v1], %[rs], %[o1] offen sc1\n\t"
            "s_barrier\n\t"
            DUM PAUSE(PV2)
            "s_branch 3f\n\t"
            "5:\n\t"
            "v_mov_b32 v115, %[lf]\n\t"
            "ds_read_b32 v115, v115\n\t"
            "s_waitcnt lgkmcnt(0)\n\t"
            "v_cmp_eq_u32 vcc, %[tg], v115\n\t"
            "s_cbranch_vccnz 4f\n\t" CN(256)
            PAUSE(PL)
            "s_branch 4f\n\t"
            "1:\n\t"
            Z2("176:177") Z2("178:179")
            "s_waitcnt vmcnt(0)\n\t"
            QHEAD QTAIL
            "s_mov_b64 exec, %[em]\n\t"
            CVP("176", "160", "161") CVP("177", "162", "163")
            "s_not_b64 exec, exec\n\t"
            CVP("178", "160", "161") CVP("179", "162", "163")
            "s_mov_b64 exec, -1\n\t"
            "s_nop 0\n\t"
            MF("180:183", "96:99", "176:179", "0") MF("184:187", "100:103", "176:179", "0")
            "s_nop 7\n\t"
            SWA("180", "184", "180") SWA("181", "185", "181") SWA("182", "186", "182") SWA("183", "187", "183")
            CVH("188", "180", "181") CVH("189", "182", "183")
            "buffer_store_dwordx2 v[188:189], %[v1], %[rs], %[o1] offen sc1\n\t"
            DUM PAUSE(PV1)
            "s_branch 3f\n\t"
            "2:\n\t"
            QHEAD
            RD4("b128", "%[hk]", "176:179", "180:183", "184:187", "188:191")
            QTAIL CVP("164", "160", "161") CVP("165", "162", "163")
            "s_waitcnt lgkmcnt(2)\n\t" AD4("176", "177", "178", "179", "180", "181", "182", "183")
            "s_waitcnt lgkmcnt(0)\n\t" AD4("184", "185", "186", "187", "188", "189", "190", "191") AD4("176", "177", "178", "179", "184", "185", "186", "187")
            CVP("166", "176", "177") CVP("167", "178", "179")
            "s_nop 1\n\t"
            "v_mfma_f32_16x16x16_bf16 v[168:171], v[166:167], v[164:165], 0\n\t"
            "buffer_store_dwordx2 v[166:167], %[v2], %[rs], %[o2] offen sc1\n\t"
            "s_nop 6\n\t"
            SWA("172", "169", "168") SWA("173", "171", "170")
            CVH("174", "172", "173")
            "buffer_store_dword v174, %[v1], %[rs], %[o1] offen sc1\n\t"
            "3:\n\t"
            PAUSE(PVA)
            "s_waitcnt vmcnt(2)\n\t"
            "v_cmp_ne_u32 vcc, " STR(SENT) ", v114\n\t"
            "s_cbranch_vccnz 5b\n\t"
            "4:\n\t"
            : [dm] "+s"(dmy) CNOUT : "{v[96:103]}"(kx), [fl] "s"(o_f), [vf] "v"(i * 128), [ob] "s"(1 << 30), [tg] "s"(tg), [lf] "s"(l0 + C2_MB + 16), [wu] "s"(wu), [hv] "v"(hv), [hk] "v"(hk), [v1] "v"(vo1), [v2] "v"(vo2), [rs] "s"(rsS), [o1] "s"(o_1), [o2] "s"(o_2), [em] "s"(0x5555555555555555ull)
            : "memory", "scc", "vcc", "v114", "v115", CLOB16(160, 161, 162, 163, 164, 165, 166, 167, 168, 169, 170, 171, 172, 173, 174, 175), CLOB16(176, 177, 178, 179, 180, 181, 182, 183, 184, 185, 186, 187, 188, 189, 190, 191));
    };
    auto o1_of = [&](const int s) { return wu >= 2 ? v_off(s, hs, tau) + jq * 512 + (wu & 1) * 256 : wu == 1 ? x_off(s, hs, tau) + jq * 256 : m_off(s, hs, tau) + jq * 128; };

    __builtin_amdgcn_s_setprio(1);
    int o_f = f_off(0, grp, tau);
    int o_cs = rec_base(0) + C_OFF + c * 16384 + near_half(0, tau) + wu * 2048 + grp * 256;
    int o_1 = o1_of(0), o_2 = km_off(0, hs, tau) + jq * 512, bq = x_off(0, ha, tau), bm = m_off(0, ha, tau), bb = v_off(0, ha, tau);
    {
            const int b0 = blk_off(0, ha, tau);
            int k0 = KX_OFF + blk_off(0, hs, tau) + jq * 1024;
            asm volatile(
                LDX("160:163", "%[vx]", "%[vb]", "0") LDX("164:167", "%[vx]", "%[vb]", "1024") LDX("168:171", "%[vx]", "%[vb]", "2048") LDX("172:175", "%[vx]", "%[vb]", "3072")
                LDX("176:179", "%[vx]", "%[vb]", "128") LDX("180:183", "%[vx]", "%[vb]", "1152") LDX("184:187", "%[vx]", "%[vb]", "2176") LDX("188:191", "%[vx]", "%[vb]", "3200")
                KLD("%[k0]")
                "v_mov_b32 v131, 0\n\tv_mov_b32 v135, 0\n\tv_mov_b32 v139, 0\n\tv_mov_b32 v143, 0\n\tv_mov_b32 v147, 0\n\tv_mov_b32 v151, 0\n\tv_mov_b32 v155, 0\n\tv_mov_b32 v159, 0\n\t"
                PROJ_X("vmcnt")
                : "=&{v[128:159]}"(yb), "=&{v[96:103]}"(kx), [k0] "+s"(k0)
                : "{v[32:63]}"(wO[0]), "{v[64:95]}"(wO[1]), [vx] "v"(voffX), [vk] "v"(voffK), [rs] "s"(rsS), [vb] "s"(VX_OFF + b0)
                : "memory", "scc", CLOB_PX);
        if (wu == 1) { PST(10) }
    }
    int s_first = 0;
    asm volatile("s_branch 1f\n\t.p2align 8\n1:" : "+s"(s_first));
    for (int s = s_first; s < NSEG - 1; ++s) {
        hand_over(o_1, o_2, o_f, s);
        if (s == 0 && wu == 1) { PST(8) }
        LT(0)
#define LDPA "buffer_load_dwordx2 v[160:161], v254, %[rs], %[bm] offen sc1\n\t" LDX("164:167", "%[l16]", "%[bq]", "0") LDX("168:171", "%[l16]", "%[bq]", "1024")
#define SM(d, a, b, x) "v_smfmac_f32_16x16x64_bf16 v[" d "], v[" a "], v[" b "], " x "\n\t"
#define MX2(a, b) "v_max_u32 v244, v" a ", v" b "\n\t"
#define MX3(a, b) "v_max3_u32 v244, v244, v" a ", v" b "\n\t"
#define ZAM Z2("208:209") Z2("210:211") Z2("212:213") Z2("214:215") Z2("216:217") Z2("218:219") Z2("220:221") Z2("222:223")
#define MUL(d, a, b) "v_mul_f32 v" d ", v" a ", v" b "\n\t"
#define CV0(d, a) "v_cvt_pk_bf16_f32 v" d ", v" a ", 0\n\t"
#define RDC "ds_read_b64 v[224:225], %[rc]\n\tds_read_b64 v[226:227], %[rc] offset:2048\n\tds_read_b64 v[228:229], %[rc] offset:4096\n\tds_read_b64 v[230:231], %[rc] offset:6144\n\t"
#define EXD(d, r) "v_exp_f32 v" d ", v" r "\n\t"
#define SUBM(r) "v_sub_f32 v" r ", v" r ", v250\n\t"
#define PCVM CVP("241", "180", "180") CVP("243", "180", "180")
#define PCVX CVP("242", "178", "179") CVP("240", "176", "177")
#define OUT(t0, t1, a0, a1, a2, a3, ad, o0, o1) ADD(t0, a0, a1) ADD(t1, a2, a3) MUL(t0, t0, "250") MUL(t1, t1, "251") DW2(ad, t0, t1, o0, o1)
#define TS(n)
#define TSOUT
        int lim = dead ? 0 : SPIN_LIMIT, bad = 0;
        asm volatile(
            LDX("192:195", "%[l16]", "%[bb]", "0") LDX("196:199", "%[l16]", "%[bb]", "1024")
            ZAM Z2("224:225") Z2("226:227")
            Z2("16:17") Z2("18:19") Z2("20:21") Z2("22:23") Z2("24:25") Z2("26:27") Z2("28:29") Z2("30:31")
            "v_mov_b32 v200, 0x3f803f80\n\tv_mov_b32 v201, 0x3f803f80\n\tv_mov_b32 v202, 0x3f80\n\tv_mov_b32 v203, 0\n\t"
            "v_mov_b64 v[204:205], v[200:201]\n\tv_mov_b64 v[206:207], v[202:203]\n\t"
            "v_xor_b32 v252, 0xa000, %[sx]\n\tv_add_u32 v253, 1024, %[ra]\n\tv_lshrrev_b32 v254, 1, %[l16]\n\t"
            PAUSE(PV)
            TS(8)
            LDPA
            "4:\n\t"
            "s_waitcnt vmcnt(4)\n\t"
            SM("208:211", "192:195", "32:39", "%[sx]") MX2("192", "193") SM("212:215", "192:195", "48:55", "%[sx]") MX3("194", "195")
            SM("216:219", "192:195", "64:71", "%[sx]") SM("220:223", "192:195", "80:87", "%[sx]")
            "s_waitcnt vmcnt(3)\n\t"
            SM("208:211", "196:199", "40:47", "%[sx]") MX3("196", "197") SM("212:215", "196:199", "56:63", "%[sx]") MX3("198", "199")
            SM("216:219", "196:199", "72:79", "%[sx]") "v_cmp_eq_u32 vcc, " STR(SENT) ", v244\n\t" SM("220:223", "196:199", "88:95", "%[sx]")
            "s_cbranch_vccnz 5f\n\t"
            "1:\n\t" TS(11)
            "s_waitcnt vmcnt(2)\n\t"
            "v_mfma_f32_16x16x16_f16 v[236:239], v[112:113], v[160:161], 0\n\t"
            MX2("160", "161") ADD("245", "208", "209") ADD("246", "210", "211") CV0("130", "245") CV0("134", "246")
            ADD("245", "212", "213") ADD("246", "214", "215") CV0("138", "245") CV0("142", "246")
            EXD("180", "236")
            "s_waitcnt vmcnt(1)\n\t"
            "v_mfma_f32_16x16x32_f16 v[228:231], v[104:107], v[164:167], 0\n\t"
            MX3("164", "165") MX3("166", "167") ADD("245", "216", "217") ADD("246", "218", "219") CV0("146", "245") CV0("150", "246")
            ADD("245", "220", "221") ADD("246", "222", "223") CV0("154", "245") CV0("158", "246") PCVM
            "s_waitcnt vmcnt(0)\n\t"
            "v_mfma_f32_16x16x32_f16 v[228:231], v[108:111], v[168:171], v[228:231]\n\t"
            MX3("168", "169") MX3("170", "171")
            "v_cmp_eq_u32 vcc, " STR(SENT) ", v244\n\t"
            "s_cbranch_vccnz 7f\n\t"
            "2:\n\t" TS(9)
            "s_nop 3\n\t"
            EXD("179", "231") EXD("178", "230") EXD("177", "229") EXD("176", "228")
            PCVX
            "v_max3_f32 v250, |v236|, |v231|, |v230|\n\t"
            "v_max3_f32 v250, v250, |v229|, |v228|\n\t"
            SM("224:227", "240:243", "200:207", "v252")
            "v_cmp_nge_f32 vcc, 0x41c00000, v250\n\t"
            "s_nop 0\n\t"
            SM("16:19", "240:243", "128:135", "v252")
            "s_cbranch_vccnz 10f\n\t"
            "13:\n\t"
            "s_nop 0\n\t"
            SM("20:23", "240:243", "136:143", "v252") "s_nop 1\n\t"
            ADD("250", "224", "225") ADD("251", "226", "227")
            SM("24:27", "240:243", "144:151", "v252")
            "v_rcp_f32 v250, v250\n\t"
            SM("28:31", "240:243", "152:159", "v252")
            "v_rcp_f32 v251, v251\n\t"
            OUT("245", "246", "16", "17", "18", "19", "%[ra]", "0", "16")
            OUT("247", "248", "20", "21", "22", "23", "%[ra]", "128", "144")
            OUT("245", "246", "24", "25", "26", "27", "v253", "0", "16")
            OUT("247", "248", "28", "29", "30", "31", "v253", "128", "144")
            TS(10)
            "s_waitcnt lgkmcnt(0)\n\t"
            "s_barrier\n\t"
            RDC
            "v_lshrrev_b32 v247, 2, %[l16]\n\t"
            "s_waitcnt lgkmcnt(2)\n\t"
            ADD("224", "224", "226") ADD("225", "225", "227")
            "s_waitcnt lgkmcnt(0)\n\t"
            ADD("228", "228", "230") ADD("229", "229", "231") ADD("224", "224", "228") ADD("225", "225", "229")
            "v_cvt_pk_bf16_f32 v245, v224, v225\n\t"
            "buffer_store_dword v245, v247, %[rs], %[cs] offen sc1\n\t"
            "s_branch 9f\n\t"
            "10:\n\t"
            "v_max3_f32 v250, v236, v231, v230\n\t"
            "v_max3_f32 v250, v250, v229, v228\n\t"
            "s_nop 1\n\t"
            "v_max_f32_dpp v251, v250, v250 quad_perm:[1,0,3,2] row_mask:0xf bank_mask:0xf\n\t"
            "v_max_f32_dpp v244, v250, v250 quad_perm:[1,0,3,2] row_mask:0xf bank_mask:0xf\n\t"
            "s_nop 1\n\t"
            "v_permlane16_swap_b32 v251, v244\n\t"
            "v_max_f32 v250, v251, v244\n\t"
            "v_max_f32 v251, v251, v244\n\t"
            Z2("224:225") Z2("226:227")
            "v_permlane32_swap_b32 v250, v251\n\t"
            "v_max_f32 v250, v250, v251\n\t"
            Z2("16:17") Z2("18:19")
            SUBM("228") SUBM("229") SUBM("230") SUBM("231") SUBM("236")
            EXD("180", "236") EXD("179", "231") EXD("178", "230") EXD("177", "229") EXD("176", "228")
            PCVM PCVX
            "s_nop 1\n\t"
            SM("224:227", "240:243", "200:207", "v252") "s_nop 1\n\t"
            SM("16:19", "240:243", "128:135", "v252")
            "s_branch 13b\n\t"
            "5:\n\t" CN(1)
            LDX("192:195", "%[l16]", "%[bb]", "0") LDX("196:199", "%[l16]", "%[bb]", "1024")
            LDPA
            "s_sub_u32 %[lim], %[lim], 1\n\t"
            "s_nop 1\n\t"
            ZAM
            "s_cbranch_scc0 4b\n\t"
            "s_mov_b32 %[bad], 1500\n\t"
            "s_mov_b32 %[lim], 0\n\t"
            "s_branch 1b\n\t"
            "7:\n\t" CN(65536)
            LDPA
            "s_sub_u32 %[lim], %[lim], 1\n\t"
            "s_cbranch_scc0 1b\n\t"
            "s_mov_b32 %[bad], 1000\n\t"
            "s_branch 2b\n\t"
            "9:\n\t"
            : [lim] "+s"(lim), [bad] "+s"(bad), "+{v[128:159]}"(yb) TSOUT CNOUT
            : "{v[32:63]}"(wO[0]), "{v[64:95]}"(wO[1]), "{v[104:111]}"(sel), "{v[112:113]}"(sel3), [sx] "v"(sidx), [l16] "v"(l * 16),
              [ra] "v"(r3a), [rs] "s"(rsS), [bb] "s"(bb), [bq] "s"(bq), [bm] "s"(bm), [cs] "s"(o_cs), [rc] "v"(l0 + C2_R3 + wu * 512 + l * 8)
            : "memory", "vcc", "scc", CLOB_PX, CLOB16(16, 17, 18, 19, 20, 21, 22, 23, 24, 25, 26, 27, 28, 29, 30, 31),
              CLOB16(224, 225, 226, 227, 228, 229, 230, 231, 232, 233, 234, 235, 236, 237, 238, 239), CLOB16(240, 241, 242, 243, 244, 245, 246, 247, 248, 249, 250, 251, 252, 253, 254, 254));
        if (__builtin_expect(bad != 0, 0)) TMO_SET((unsigned)bad)
        __builtin_amdgcn_s_setprio(0);
        if (s == 0 && wu == 1) { PST(6) }

        if (__builtin_expect(s <= KVS + 1, 0)) {
            if (s == 0 || __builtin_amdgcn_readfirstlane(*(volatile unsigned*)(chsm + C2_MB + 32 + ((s - 1) & 1) * 16 + 4 + (wu >> 1) * 4)) == 0u) {
                wait_ge<6>(kv_flag(a.sync, s + 1, grp, l), 1u, a.tmo, 3001u);
                const char* kp = arena(a.slots) + VX_OFF + blk_off(s + 1, ha, tau);
#pragma unroll
                for (int e = 0; e < 8; ++e) glds16h((unsigned)l * 16u, kp + e * 1024, l0 + (s & 1 ? C2_V : C2_K) + wu * 8192 + e * 1024);
                asm volatile("s_waitcnt vmcnt(0)" ::: "memory");
            }
        }
        {
            int k0 = KX_OFF + blk_off(s + 1, hs, tau) + jq * 1024;
            asm volatile(
                DSV("160:163", "0") DSV("164:167", "1024") DSV("168:171", "2048") DSV("172:175", "3072") DSV("176:179", "128") DSV("180:183", "1152") DSV("184:187", "2176") DSV("188:191", "3200")
                "s_cmp_lg_u32 %[wu], 1\n\t"
                "s_cbranch_scc1 1f\n\t"
                KLD("%[k0]")
                "1:\n\t"
                "s_waitcnt lgkmcnt(4)\n\t"
                PXT("192:195", "196:199", "200:203", "204:207", "160:163", "164:167", "168:171", "172:175")
                "s_waitcnt lgkmcnt(0)\n\t"
                PXT("208:211", "212:215", "216:219", "220:223", "176:179", "180:183", "184:187", "188:191")
                CVP("128", "192", "193") CVP("129", "194", "195") CVP("136", "196", "197") CVP("137", "198", "199") CVP("144", "200", "201") CVP("145", "202", "203") CVP("152", "204", "205") CVP("153", "206", "207")
                CVP("132", "208", "209") CVP("133", "210", "211") CVP("140", "212", "213") CVP("141", "214", "215") CVP("148", "216", "217") CVP("149", "218", "219") CVP("156", "220", "221") CVP("157", "222", "223")
                : "+{v[128:159]}"(yb), "+{v[96:103]}"(kx), [k0] "+s"(k0)
                : "{v[32:63]}"(wO[0]), "{v[64:95]}"(wO[1]), [lv] "v"(l0 + (s & 1 ? C2_V : C2_K) + wu * 8192 + voffX), [vk] "v"(voffK), [rs] "s"(rsS), [wu] "s"(wu)
                : "memory", "scc", CLOB_PX);
        }
        __builtin_amdgcn_sched_barrier(0);
        o_1 = o1_of(s + 1); o_2 = km_off(s + 1, hs, tau) + jq * 512;
        bq = x_off(s + 1, ha, tau); bm = m_off(s + 1, ha, tau); bb = v_off(s + 1, ha, tau);
        o_f = f_off(s + 1, grp, tau);
        o_cs = rec_base(s + 1) + C_OFF + c * 16384 + near_half(s + 1, tau) + wu * 2048 + grp * 256;
        asm volatile("" : "+s"(o_cs), "+s"(o_f), "+s"(o_1), "+s"(o_2), "+s"(bq), "+s"(bm), "+s"(bb));
        __builtin_amdgcn_s_barrier();
        asm volatile(
            "s_cmp_lg_u32 %[wu], 3\n\t"
            "s_cbranch_scc1 1f\n\t"
            "v_mov_b32 v160, %[lf]\n\t"
            PAUSE(PF)
            "ds_read_b32 v160, v160\n\t"
            "s_waitcnt lgkmcnt(0)\n\t"
            "v_cmp_eq_u32 vcc, %[tg], v160\n\t"
            "s_cbranch_vccz 1f\n\t"
            "s_mov_b32 exec_hi, 0\n\t"
            "buffer_store_dword v160, %[l4], %[rs], %[fo] offen sc1\n\t"
            "s_mov_b64 exec, -1\n\t"
            "1:\n\t"
            :: [wu] "s"(wu), [lf] "s"(l0 + C2_MB + 16), [tg] "s"(s + 1), [l4] "v"(l * 4), [rs] "s"(rsS), [fo] "s"(o_f + c * 128)
            : "memory", "scc", "vcc", "v160");
        __builtin_amdgcn_s_setprio(1);
    }
    {
        constexpr int s = NSEG - 1;
        const int l = lane_now(), i = l & 15, g = l >> 4;
        v4u qf[8], kf[8], pv[2], px[4];
        asm volatile("s_waitcnt vmcnt(0)" ::: "memory");
        __builtin_amdgcn_s_sleep(4);
#pragma unroll
        for (int e = 0; e < 8; ++e) kf[e] = ld_sc1s(rsS, l * 16, KX_OFF + blk_off(s, ha, tau) + e * 1024);
        hand_over(o_1, o_2, o_f, s);
        const int bk = km_off(s, ha, tau);
        auto ask = [&]() {
            asm volatile("" ::: "memory");
            pv[0] = ld_sc1s(rsS, l * 16, bb); pv[1] = ld_sc1s(rsS, l * 16, bb + 1024);
#pragma unroll
            for (int ks = 0; ks < 4; ++ks) px[ks] = ld_sc1s(rsS, g * 128 + (i >> 1) * 16, bk + ks * 512);
            asm volatile("" ::: "memory");
        };
        asm volatile(PAUSE(PLL) ::: "memory");
        ask();
#pragma unroll
        for (int e = 0; e < 8; ++e) qf[e] = *(const v4u*)(chsm + C2_V + 32768 + wu * 8192 + e * 1024 + l * 16);
        f32x4 sx[3] = {ZERO4, ZERO4, ZERO4};
        float mxx[2] = {-3.0e38f, -3.0e38f};
#pragma unroll
        for (int ks = 0; ks < 4; ++ks) {
            sx[0] = MM(kf[ks], qf[ks], sx[0]);
            sx[1] = MM(kf[ks], qf[4 + ks], sx[1]);
            sx[2] = MM(kf[4 + ks], qf[4 + ks], sx[2]);
        }
#pragma unroll
        for (int r = 0; r < 4; ++r) {
            if (4 * g + r > i) { sx[0][r] = -3.0e38f; sx[2][r] = -3.0e38f; }
            mxx[0] = fmaxf(mxx[0], sx[0][r]);
            mxx[1] = fmaxf(mxx[1], fmaxf(sx[1][r], sx[2][r]));
        }
        asm volatile("" : "+v"(sx[0]), "+v"(sx[1]), "+v"(sx[2]), "+v"(mxx[0]), "+v"(mxx[1]));
#pragma nounroll
        for (int spin = 0;; ++spin) {
            if (__all(mx4(mx4(mx4(mx4(mx4(mx4(0u, pv[0]), pv[1]), px[0]), px[1]), px[2]), px[3]) != SENT)) break;
            if (dead) break;
            if (spin > SPIN_LIMIT) { TMO_SET(1700u) break; }
            ask();
        }
#define SUB8(v, o) v8u{(v)[(o)], (v)[(o) + 1], (v)[(o) + 2], (v)[(o) + 3], (v)[(o) + 4], (v)[(o) + 5], (v)[(o) + 6], (v)[(o) + 7]}
#pragma unroll
        for (int ct = 0; ct < 4; ++ct) {
            f32x4 am = smm(pv[0], SUB8(wO[ct >> 1], (ct & 1) * 16), ZERO4, sidx);
            am = smm(pv[1], SUB8(wO[ct >> 1], (ct & 1) * 16 + 8), am, sidx);
            yb[8 * ct + 2] = cvt_pk_bf16(am[0] + am[1], 0.f); yb[8 * ct + 6] = cvt_pk_bf16(am[2] + am[3], 0.f);
        }
        const v8u one = {0x3F803F80u, 0x3F803F80u, 0x3F80u, 0u, 0x3F803F80u, 0x3F803F80u, 0x3F80u, 0u};
        const float SCL2E = 0.08838834764831845f * 1.4426950408889634f;
        float* xo = (float*)(chsm + C2_K) + (wu * 32 + 4 * g) * XP + i;
#pragma unroll
        for (int qt = 0; qt < 2; ++qt) {
            f32x4 sc[3] = {sx[qt], sx[2], ZERO4};
#pragma unroll
            for (int ks = 0; ks < 4; ++ks) sc[2] = MM(px[ks], qf[4 * qt + ks], sc[2]);
            float mx = fmaxf(mxx[qt], fmaxf(sc[2][1], sc[2][3]));
            mx = xor32_max(xor16_max(mx));
            const float nmx = -mx * SCL2E;
#define PE(x) __builtin_amdgcn_exp2f(fmaf(x, SCL2E, nmx))
            const float m0 = PE(sc[2][1]), m1 = PE(sc[2][3]);
            const v4u ae = {cvt_pk_bf16(PE(sc[0][0]), PE(sc[0][1])), cvt_pk_bf16(m0, m0), cvt_pk_bf16(PE(sc[0][2]), PE(sc[0][3])), 0u};
            const v4u ao = {qt ? cvt_pk_bf16(PE(sc[1][0]), PE(sc[1][1])) : 0u, 0u, qt ? cvt_pk_bf16(PE(sc[1][2]), PE(sc[1][3])) : 0u, cvt_pk_bf16(m1, m1)};
            const f32x4 sum = smm(ao, one, smm(ae, one, ZERO4, 0xE444), 0x4EEE);
#pragma unroll
            for (int ct = 0; ct < 4; ++ct) {
                const f32x4 o = smm(ao, SUB8(yb, 8 * ct), smm(ae, SUB8(yb, 8 * ct), ZERO4, 0xE444), 0x4EEE);
#pragma unroll
                for (int r = 0; r < 4; ++r) xo[(16 * qt + r) * XP + 16 * ct] = o[r] * __builtin_amdgcn_rcpf(sum[r]);
            }
        }
        __builtin_amdgcn_s_waitcnt(0xC07F);
        last_rows(chsm, rsS, wu, grp, c, tau);
        if (wu < 2) {
            v4u sel, p0, p1;
#pragma unroll
            for (int d = 0; d < 4; ++d) sel[d] = (l & 6) == 2 * d ? (0x3F80u << ((l & 1) * 16)) : 0u;
            const int xb = XB_OFF + ((c >> 1) * 8 + grp) * 16384 + (int)(tau & 1u) * 8192 + (c & 1) * 4096 + wu * 256;
            __builtin_amdgcn_s_sleep(DLY_C);
#pragma nounroll
            for (int spin = 0;; ++spin) {
                asm volatile("" ::: "memory");
                p0 = ld_sc1s(rsS, g * 512 + i * 16, xb); p1 = ld_sc1s(rsS, g * 512 + i * 16, xb + 2048);
                if (__all(mx4(mx4(0u, p0), p1) != SENT)) break;
                if (dead) break;
                if (spin > SPIN_LIMIT) { TMO_SET(1800u) break; }
            }
            const f32x4 d = MM(sel, p1, MM(sel, p0, ZERO4));
            if (g < 2) out8(a.out + (size_t)(s * SEG + 4 * grp + 2 * wu + (i >> 3)) * OUTW + 64 * c + 8 * (i & 7) + 4 * g, v2u{cvt_pk_bf16(d[0], d[1]), cvt_pk_bf16(d[2], d[3])});
        }
    }
}

struct cpx { float x, y; };
__device__ __forceinline__ cpx cmul(cpx a, cpx b) { return {a.x * b.x - a.y * b.y, a.x * b.y + a.y * b.x}; }
__device__ __forceinline__ cpx cadd(cpx a, cpx b) { return {a.x + b.x, a.y + b.y}; }
__device__ __forceinline__ cpx csub(cpx a, cpx b) { return {a.x - b.x, a.y - b.y}; }
template <int DIR> __device__ __forceinline__ cpx mul_i(cpx a) {
    return (DIR < 0) ? cpx{a.y, -a.x} : cpx{-a.y, a.x};
}
template <int DIR> __device__ __forceinline__ void r4(cpx& a, cpx& b, cpx& c, cpx& d) {
    cpx t0 = cadd(a, c), t1 = csub(a, c), t2 = cadd(b, d), t3 = mul_i<DIR>(csub(b, d));
    a = cadd(t0, t2); b = cadd(t1, t3); c = csub(t0, t2); d = csub(t1, t3);
}
template <int DIR> __device__ __forceinline__ cpx twc(float c, float s) { return cpx{c, (float)DIR * s}; }
template <int DIR> __device__ __forceinline__ void dft16(cpx (&v)[16]) {
#pragma unroll
    for (int a = 0; a < 4; ++a) r4<DIR>(v[a], v[a + 4], v[a + 8], v[a + 12]);
    const float C1 = 0.92387953251128674f, S1 = 0.38268343236508977f, R2 = 0.70710678118654752f;
    v[1 + 4] = cmul(v[1 + 4], twc<DIR>(C1, S1));
    v[1 + 8] = cmul(v[1 + 8], twc<DIR>(R2, R2));
    v[1 + 12] = cmul(v[1 + 12], twc<DIR>(S1, C1));
    v[2 + 4] = cmul(v[2 + 4], twc<DIR>(R2, R2));
    v[2 + 8] = mul_i<DIR>(v[2 + 8]);
    v[2 + 12] = cmul(v[2 + 12], twc<DIR>(-R2, R2));
    v[3 + 4] = cmul(v[3 + 4], twc<DIR>(S1, C1));
    v[3 + 8] = cmul(v[3 + 8], twc<DIR>(-R2, R2));
    v[3 + 12] = cmul(v[3 + 12], twc<DIR>(-C1, -S1));
#pragma unroll
    for (int d = 0; d < 4; ++d) r4<DIR>(v[4 * d], v[4 * d + 1], v[4 * d + 2], v[4 * d + 3]);
}
__device__ __forceinline__ constexpr int x16(int q) { return 4 * (q & 3) + (q >> 2); }
template <int DIR> __device__ __forceinline__ void dft8(cpx (&v)[8]) {
    r4<DIR>(v[0], v[2], v[4], v[6]);
    r4<DIR>(v[1], v[3], v[5], v[7]);
    const float R2 = 0.70710678118654752f;
    v[3] = cmul(v[3], twc<DIR>(R2, R2));
    v[5] = mul_i<DIR>(v[5]);
    v[7] = cmul(v[7], twc<DIR>(-R2, R2));
#pragma unroll
    for (int d = 0; d < 4; ++d) { cpx p = v[2 * d], q = v[2 * d + 1]; v[2 * d] = cadd(p, q); v[2 * d + 1] = csub(p, q); }
}
__device__ __forceinline__ int pad(int i) { return i + (i >> 4); }
__device__ __forceinline__ cpx expi(float x) { float s, c; sincospif(x, &s, &c); return cpx{c, s}; }
template <int R, int DIR> __device__ __forceinline__ void twiddle(cpx* v, cpx w1) {
    cpx w[R];
    w[1] = w1;
#pragma unroll
    for (int r = 2; r < R; ++r) w[r] = (r & 1) ? cmul(w[r - 1], w1) : cmul(w[r / 2], w[r / 2]);
#pragma unroll
    for (int r = 1; r < R; ++r) v[r] = DIR < 0 ? cmul(v[r], w[r]) : cpx{v[r].x * w[r].x + v[r].y * w[r].y, v[r].y * w[r].x - v[r].x * w[r].y};
}
struct TwBase { cpx b; cpx c[4]; };
template <int N> __device__ __forceinline__ TwBase make_tw(int t) {
    TwBase tb;
    tb.b = expi(-(float)(t & 15) * (1.0f / 128.0f));
    constexpr int NQ = (N == 1024) ? 4 : 2, T = N / 16;
#pragma unroll
    for (int q = 0; q < NQ; ++q) tb.c[q] = expi(-(float)(t + T * q) * (2.0f / (float)N));
    return tb;
}
template <int N> __device__ __forceinline__ void fft_sync() {
    if constexpr (N == 1024) { __builtin_amdgcn_fence(__ATOMIC_ACQ_REL, "wavefront"); __builtin_amdgcn_wave_barrier(); } else __syncthreads();
}
template <int N, int DIR, bool RIN, bool ROUT>
__device__ __forceinline__ void fft_reg(cpx* buf, const TwBase& tb, int t, cpx (&v)[16]) {
    constexpr int T = N / 16, TP = T + T / 16;
    cpx* const bt = buf + pad(t);
    if constexpr (!RIN) {
#pragma unroll
        for (int r = 0; r < 16; ++r) v[r] = bt[TP * r];
        fft_sync<N>();
    }
    dft16<DIR>(v);
#pragma unroll
    for (int q = 0; q < 16; ++q) buf[17 * t + x16(q)] = v[q];
    fft_sync<N>();
    {
#pragma unroll
        for (int r = 0; r < 16; ++r) v[r] = bt[TP * r];
        fft_sync<N>();
        twiddle<16, DIR>(v, tb.b);
        dft16<DIR>(v);
        cpx* const bo = buf + (t >> 4) * 272 + (t & 15);
#pragma unroll
        for (int q = 0; q < 16; ++q) bo[17 * x16(q)] = v[q];
        fft_sync<N>();
    }
    if constexpr (N == 1024) {
#pragma unroll
        for (int q = 0; q < 4; ++q)
#pragma unroll
            for (int r = 0; r < 4; ++r) v[4 * q + r] = bt[68 * q + 272 * r];
#pragma unroll
        for (int q = 0; q < 4; ++q) {
            twiddle<4, DIR>(&v[4 * q], tb.c[q]);
            r4<DIR>(v[4 * q], v[4 * q + 1], v[4 * q + 2], v[4 * q + 3]);
#pragma unroll
            for (int r = 0; r < 4; ++r) bt[68 * q + 272 * r] = v[4 * q + r];
        }
        fft_sync<N>();
    } else {
        cpx u[2][8];
#pragma unroll
        for (int q = 0; q < 2; ++q)
#pragma unroll
            for (int r = 0; r < 8; ++r) u[q][r] = bt[136 * q + 272 * r];
        if constexpr (ROUT) fft_sync<N>();
#pragma unroll
        for (int q = 0; q < 2; ++q) {
            twiddle<8, DIR>(u[q], tb.c[q]);
            dft8<DIR>(u[q]);
#pragma unroll
            for (int d = 0; d < 4; ++d) {
                if constexpr (ROUT) { v[q + 2 * d] = u[q][2 * d]; v[q + 2 * d + 8] = u[q][2 * d + 1]; }
                else { bt[136 * q + 272 * d] = u[q][2 * d]; bt[136 * q + 272 * (d + 4)] = u[q][2 * d + 1]; }
            }
        }
        if constexpr (!ROUT) fft_sync<N>();
    }
}
__device__ __forceinline__ int f_hi(int rb, int j, unsigned tw) { j = (j + rb) & 7; return rb * 65536 + (j >> 1) * 16384 + (j & 1) * 4096 + (int)((tw ^ (unsigned)(rb >> 5)) & 1u) * 8192; }
__device__ __forceinline__ int f_lo(int rb, int x) { return (x + rb * 128) & 4095; }
__device__ __forceinline__ int w_hi(int k, int j, unsigned tw) { return k * 16384 + ((j + k) & 15) * 512 + (int)((tw ^ (unsigned)(k >> 7)) & 1u) * 8192; }
__device__ __forceinline__ int w_lo(int k, int x) { return (x + k * 128) & 511; }
constexpr int PB1 = 1024 + 64, PB2 = 2048 + 128;
typedef float f32x4v __attribute__((ext_vector_type(4)));
typedef unsigned u32x4v __attribute__((ext_vector_type(4)));

__device__ __forceinline__ void fft_rows_fwd(const u16* __restrict__ x, rsrc_t rsF, const unsigned tw, int rb, char* smem, int tid, const char* wr, const char* wi, const unsigned spare) {
    cpx (*buf)[PB1] = (cpx (*)[PB1])smem;
    int f = tid >> 6, t = tid & 63;
    const u16* p0 = x + (size_t)(rb * 8 + 2 * f) * 1024 + 16 * t;
    u32x4v a0 = *(const u32x4v*)p0, a1 = *(const u32x4v*)(p0 + 8), b0 = *(const u32x4v*)(p0 + 1024), b1 = *(const u32x4v*)(p0 + 1032);
    const TwBase tb = make_tw<1024>(t);
    for (int it = 0; it < 4; ++it, rb += 64) {
        asm volatile("" : "+v"(tid));
        f = tid >> 6; t = tid & 63;
        __syncthreads();
        cpx* d = &buf[f][17 * t];
#pragma unroll
        for (int e = 0; e < 4; ++e) {
            d[2 * e] = cpx{__uint_as_float(a0[e] << 16), __uint_as_float(b0[e] << 16)};
            d[2 * e + 1] = cpx{__uint_as_float(a0[e] & 0xFFFF0000u), __uint_as_float(b0[e] & 0xFFFF0000u)};
            d[8 + 2 * e] = cpx{__uint_as_float(a1[e] << 16), __uint_as_float(b1[e] << 16)};
            d[8 + 2 * e + 1] = cpx{__uint_as_float(a1[e] & 0xFFFF0000u), __uint_as_float(b1[e] & 0xFFFF0000u)};
        }
        if (it < 3) p0 += 512 * 1024;
        touch(((it & 1) ? wi : wr) + (size_t)((rb >> 1) & 31) * 32832 + (it >> 1) * 32 * 32832 + ((rb & 1) * 128 + (tid >> 1)) * 128, spare);
        a0 = *(const u32x4v*)p0; a1 = *(const u32x4v*)(p0 + 8); b0 = *(const u32x4v*)(p0 + 1024); b1 = *(const u32x4v*)(p0 + 1032);
        fft_sync<1024>();
        cpx z[16];
        fft_reg<1024, -1, false, false>(buf[f], tb, t, z);
        __syncthreads();
        const cpx* pk = &buf[tid & 3][pad(tid >> 2)];
        const cpx* pn = &buf[tid & 3][pad(1024 - (tid >> 2))];
        const int vo = f_lo(rb, tid * 16);
#pragma unroll
        for (int m = 0; m < 8; ++m) {
            const cpx zk = pk[68 * m], zn = m == 0 && tid < 4 ? pk[544] : pn[-68 * m];
            f32x4v o = {zk.x + zn.x, zk.y - zn.y, zk.y + zn.y, zn.x - zk.x};
            if (m == 0 && tid < 4) o = f32x4v{2.f * zk.x, 2.f * zn.x, 2.f * zk.y, 2.f * zn.y};
            st_sc1s(rsF, vo, f_hi(rb, m, tw), __builtin_bit_cast(v4u, o));
        }
    }
}
__device__ __forceinline__ void filt_transpose(const u16* __restrict__ wr, const u16* __restrict__ wi, rsrc_t rsW, const unsigned tw, const int tb, char* smem, const int tid) {
    const v4u* s0 = (const v4u*)(wr + (size_t)tb * 32 * 513);
    const v4u* s1 = (const v4u*)(wi + (size_t)tb * 32 * 513);
    v4u r0[5], r1[5];
#pragma unroll
    for (int e = 0; e < 5; ++e) {
        const int idx = tid + 512 * e, ic = idx < 2052 ? idx : 2051;
        r0[e] = s0[ic]; r1[e] = s1[ic];
    }
#pragma unroll
    for (int e = 0; e < 5; ++e) {
        const int idx = tid + 512 * e;
        if (idx < 2052) { *(v4u*)(smem + idx * 16) = r0[e]; *(v4u*)(smem + 32832 + idx * 16) = r1[e]; }
    }
    __syncthreads();
    const u16* a0 = (const u16*)smem;
    const u16* a1 = (const u16*)(smem + 32832);
    for (int idx = tid; idx < 513 * 8; idx += 512) {
        const int k = idx >> 3, n4 = (idx & 7) * 4;
        v4u v;
#pragma unroll
        for (int j = 0; j < 4; ++j) v[j] = (unsigned)a0[(n4 + j) * 513 + k] | ((unsigned)a1[(n4 + j) * 513 + k] << 16);
        st_sc1(rsW, w_hi(k, tb >> 2, tw) + w_lo(k, (tb & 3) * 128 + n4 * 4), v);
    }
}

__device__ __forceinline__ void fft_cols(rsrc_t rsF, rsrc_t rsW, const unsigned tw, int blk, const int n, char* smem, int tid) {
    cpx (*buf)[PB2] = (cpx (*)[PB2])smem;
    int c = tid >> 7, t = tid & 127;
    f32x4v v[8];
    unsigned wv[16], wn[16];
    auto wld = [&](const int k, const int n) { return __builtin_amdgcn_raw_buffer_load_b32(rsW, w_hi(k, n >> 7, tw) + w_lo(k, (n & 127) * 4), 0, 0); };
    auto f_at = [&](const int bk) { return f_hi(tid >> 3, bk >> 5, tw) + f_lo(tid >> 3, (bk & 31) * 128 + (tid & 7) * 16); };
    auto fetch = [&](const int bk) {
        const int kc = bk * 2 + __builtin_amdgcn_readfirstlane(c), vo = f_at(bk), vw = w_lo(kc, t * 4);
#pragma unroll
        for (int m = 0; m < 8; ++m) v[m] = __builtin_bit_cast(f32x4v, __builtin_amdgcn_raw_buffer_load_b128(rsF, m & 1 ? vo ^ 8192 : vo, m * 2097152, 0));
#pragma unroll
        for (int m = 0; m < 16; ++m) wn[m] = __builtin_amdgcn_raw_buffer_load_b32(rsW, vw, w_hi(kc, m, tw), 0);
    };
    fetch(blk);
    const TwBase tb = make_tw<2048>(t);
    for (int it = 0; it < n; ++it, blk += 64) {
        asm volatile("" : "+v"(tid));
        c = tid >> 7; t = tid & 127;
        __syncthreads();
        cpx* const d = &buf[(tid >> 2) & 1][pad((tid >> 3) * 8 + 2 * (tid & 3))];
#pragma unroll
        for (int m = 0; m < 8; ++m) { d[272 * m] = cpx{v[m][0], v[m][1]}; d[272 * m + 1] = cpx{v[m][2], v[m][3]}; }
#pragma unroll
        for (int m = 0; m < 16; ++m) wv[m] = wn[m];
        fetch(it + 1 < n ? blk + 64 : blk);
        __syncthreads();
        cpx z[16];
        fft_reg<2048, -1, false, true>(buf[c], tb, t, z);
        auto wcx = [](const unsigned w) { return cpx{__uint_as_float(w << 16), __uint_as_float(w & 0xFFFF0000u)}; };
        if (blk < 2) {
#pragma unroll
            for (int m = 0; m < 16; ++m) buf[c][pad(t + 128 * m)] = z[m];
            __syncthreads();
            if (blk == 0 && c == 0) {
#pragma unroll
                for (int m = 0; m < 16; ++m) {
                    const int n = t + 128 * m, nn = (2048 - n) & 2047;
                    const cpx zn = buf[0][pad(nn)], a = wcx(wv[m]), b = wcx(wld(0, nn)), e = wcx(wld(512, n)), f = wcx(wld(512, nn));
                    const cpx h0 = cmul(cpx{0.5f * (z[m].x + zn.x), 0.5f * (z[m].y - zn.y)}, cpx{0.5f * (a.x + b.x), 0.5f * (a.y - b.y)});
                    const cpx h5 = cmul(cpx{0.5f * (z[m].y + zn.y), -0.5f * (z[m].x - zn.x)}, cpx{0.5f * (e.x + f.x), 0.5f * (e.y - f.y)});
                    z[m] = cpx{h0.x - h5.y, h0.y + h5.x};
                    wv[m] = 0x3F80u;
                }
            }
            __syncthreads();
        }
#pragma unroll
        for (int m = 0; m < 16; ++m) z[m] = cmul(z[m], wcx(wv[m]));
        fft_reg<2048, 1, true, false>(buf[c], tb, t, z);
        const int vo = f_at(blk);
#pragma unroll
        for (int m = 0; m < 8; ++m)
            st_sc1s(rsF, m & 1 ? vo ^ 8192 : vo, m * 2097152, __builtin_bit_cast(v4u, f32x4v{d[272 * m].x, d[272 * m].y, d[272 * m + 1].x, d[272 * m + 1].y}));
    }
}

__device__ __forceinline__ void fft_rows_inv(rsrc_t rsF, const unsigned tw, u16* __restrict__ out, int rb, const int step, const int n, char* smem, int tid) {
    cpx (*buf)[PB1] = (cpx (*)[PB1])smem;
    int f = tid >> 6, t = tid & 63;
    f32x4v v[8];
    auto fetch = [&](const int r) {
        const int vo = f_lo(r, tid * 16);
#pragma unroll
        for (int m = 0; m < 8; ++m) v[m] = __builtin_bit_cast(f32x4v, __builtin_amdgcn_raw_buffer_load_b128(rsF, vo, f_hi(r, m, tw), 0));
    };
    fetch(rb);
    const TwBase tb = make_tw<1024>(t);
    for (int it = 0; it < n; ++it, rb += step) {
        asm volatile("" : "+v"(tid));
        f = tid >> 6; t = tid & 63;
        __syncthreads();
        cpx* pk = &buf[tid & 3][pad(tid >> 2)];
        cpx* pn = &buf[tid & 3][pad(1024 - (tid >> 2))];
#pragma unroll
        for (int m = 0; m < 8; ++m) {
            if (m == 0 && tid < 4) { pk[0] = cpx{v[0][0], v[0][2]}; pk[544] = cpx{v[0][1], v[0][3]}; }
            else { pk[68 * m] = cpx{v[m][0] - v[m][3], v[m][1] + v[m][2]}; pn[-68 * m] = cpx{v[m][0] + v[m][3], v[m][2] - v[m][1]}; }
        }
        fetch(it + 1 < n ? rb + step : rb);
        __syncthreads();
        cpx z[16];
        fft_reg<1024, 1, false, false>(buf[f], tb, t, z);
        const float sc = 0.5f / (1024.0f * 2048.0f);
        const cpx* d = &buf[f][17 * t];
        u32x4v a[2], b[2];
#pragma unroll
        for (int h = 0; h < 2; ++h)
#pragma unroll
            for (int e = 0; e < 4; ++e) {
                cpx p = d[8 * h + 2 * e], q = d[8 * h + 2 * e + 1];
                a[h][e] = cvt_pk_bf16(p.x * sc, q.x * sc);
                b[h][e] = cvt_pk_bf16(p.y * sc, q.y * sc);
            }
        u16* o0 = out + (size_t)(rb * 8 + 2 * f) * OUTW + 1024 + 16 * t;
        if (WT & 2) { out16(o0, a[0]); out16(o0 + 8, a[1]); out16(o0 + OUTW, b[0]); out16(o0 + OUTW + 8, b[1]); }
        else {
        *(u32x4v*)o0 = a[0]; *(u32x4v*)(o0 + 8) = a[1];
        *(u32x4v*)(o0 + OUTW) = b[0]; *(u32x4v*)(o0 + OUTW + 8) = b[1]; }
    }
}

__device__ __forceinline__ void glds16s(unsigned voff, const void* sbase, unsigned lds_dst) {
    unsigned keep;
    asm volatile("s_mov_b32 %0, m0\n\ts_mov_b32 m0, %3\n\ts_nop 0\n\tglobal_load_lds_dwordx4 %1, %2\n\ts_mov_b32 m0, %0" : "=&s"(keep) : "v"(voff), "s"(sbase), "s"(lds_dst) : "memory");
}
#define K8_BAR do { asm volatile("" ::: "memory"); __builtin_amdgcn_s_barrier(); asm volatile("" ::: "memory"); } while (0)
#define K8_WV(n) asm volatile("s_waitcnt vmcnt(" #n ")" ::: "memory")
#define K8_WL(n) asm volatile("s_waitcnt lgkmcnt(" #n ")" ::: "memory")
#define K8_SCHED __builtin_amdgcn_sched_barrier(0)
template <int BN>
__device__ __forceinline__ int bswz2(int row) {
    if (BN >= 128) return (row & 3) | (((row >> 3) & 1) << 2);
    else return ((row >> 1) & 1) | (((row >> 3) & 1) << 1);
}
template <int BM>
__device__ __forceinline__ void gemm_tile(u16* sm, const char* A, const int s0, const unsigned tau, const u16* B, u16* C) {
    constexpr int K = HD, LDB = DM, LDC = OUTW;
    constexpr int BN = 128, BK = 64, ST = BM == 128 ? 4 : 3, WN = BM == 128 ? 4 : 2;
    constexpr int ATILE = BM * BK, BTILE = BK * BN;
    constexpr int NT = 8 / WN, GA = BM / 64, GB = 2, GS = GA + GB;
    const int tid = opaque_tid(), w = tid >> 6, l = tid & 63, i = l & 15, g = l >> 4;
    const int wr = w / WN, wc = w % WN;
    const unsigned wu = __builtin_amdgcn_readfirstlane(w);
    unsigned vb;
    { const int row = l >> 4, c = l & 15; vb = (unsigned)(row * LDB + 8 * (c ^ ((row | ((wu & 1) << 2)) << 1))) * 2u; }
    const int sg = s0 + (BM == 128 ? wu >> 1 : wu);
    const char* ab = A + (BM == 128 ? (wu & 1) * 4096 : 0);
    const char* bb = (const char*)(B + (size_t)wu * 8 * LDB);
    const unsigned lds0 = (unsigned)(uintptr_t)(__attribute__((address_space(3))) u16*)sm;
    const unsigned va = (unsigned)l * 16u, va2 = va + 2048u, vb2 = vb + 4u * LDB * 2u - 1024u;
    auto req = [&](const int buf, const int kt) {
        const unsigned d = lds0 + buf * (ATILE + BTILE) * 2;
        const int wh = kt >> 1;
        const char* pa = ab + ((blk_off(sg, (wh & 7) * 4 + (wh >> 3), tau) ^ 8192) + (kt & 1) * 2048);
        const char* pb = bb + (size_t)kt * BK * LDB * 2;
        unsigned keep;
        if (BM == 128) asm volatile("s_mov_b32 %0, m0\n\ts_mov_b32 m0, %2\n\ts_nop 0\n\tglobal_load_lds_dwordx4 %6, %8\n\tglobal_load_lds_dwordx4 %7, %8 offset:1024\n\t"
                     "s_mov_b32 m0, %1\n\ts_nop 0\n\tglobal_load_lds_dwordx4 %3, %5\n\tglobal_load_lds_dwordx4 %3, %5 offset:1024\n\ts_mov_b32 m0, %0"
                     : "=&s"(keep) : "s"(d + wu * GA * 1024), "s"(d + ATILE * 2 + wu * GB * 1024), "v"(va), "v"(va2), "s"(pa), "v"(vb), "v"(vb2), "s"(pb) : "memory");
        else asm volatile("s_mov_b32 %0, m0\n\ts_mov_b32 m0, %2\n\ts_nop 0\n\tglobal_load_lds_dwordx4 %6, %8\n\tglobal_load_lds_dwordx4 %7, %8 offset:1024\n\t"
                     "s_mov_b32 m0, %1\n\ts_nop 0\n\tglobal_load_lds_dwordx4 %3, %5\n\tglobal_load_lds_dwordx4 %3, %5 offset:1024\n\t"
                     "global_load_lds_dwordx4 %4, %5 offset:2048\n\tglobal_load_lds_dwordx4 %4, %5 offset:3072\n\ts_mov_b32 m0, %0"
                     : "=&s"(keep) : "s"(d + wu * GA * 1024), "s"(d + ATILE * 2 + wu * GB * 1024), "v"(va), "v"(va2), "s"(pa), "v"(vb), "v"(vb2), "s"(pb) : "memory");
    };
    f32x4 acc[4][NT] = {};
    constexpr int nt = K / BK;
#pragma unroll
    for (int s = 0; s < ST - 1; ++s) req(s, s);
    int boff[NT];
    const int aoff = wr * 4096 + l * 8;
#pragma unroll
    for (int n = 0; n < NT; ++n) { const int row = 8 * g + (i >> 2); boff[n] = row * BN + 16 * ((wc * NT + n) ^ bswz2<BN>(row)) + 4 * (i & 3); }
    asm volatile("s_waitcnt vmcnt(%0)" :: "i"((ST - 2) * GS) : "memory");
    K8_BAR;
    if (wu >= 4) K8_BAR;
    for (int t0 = 0; t0 < nt + ST - 1 - (nt + ST - 1) % ST; t0 += ST) {
#pragma unroll
        for (int rb = 0; rb < ST; ++rb) {
            const int t = t0 + rb;
            if (nt % ST != 0 && t >= nt) break;
            const int kk = min(t + ST - 1, nt - 1);
            const u16* as = sm + rb * (ATILE + BTILE);
            const u16* bs = as + ATILE;
            bf16x8 af[2][4], bf[2][NT];
#pragma unroll
            for (int ks = 0; ks < 2; ++ks) {
#pragma unroll
                for (int n = 0; n < NT; ++n) bf[ks][n] = tr_frag(bs + boff[n] + ks * 32 * BN, bs + boff[n] + ks * 32 * BN + 4 * BN);
#pragma unroll
                for (int m = 0; m < 4; ++m) af[ks][m] = *(const bf16x8*)(as + aoff + m * 1024 + ks * 512);
            }
            K8_SCHED;
            req((rb + ST - 1) % ST, kk);
            K8_WL(0);
            asm volatile("s_waitcnt vmcnt(%0)" :: "i"((ST - 2) * GS + GA) : "memory");
            K8_BAR;
            __builtin_amdgcn_s_setprio(1);
#pragma unroll
            for (int q = 0; q < 8; ++q)
#pragma unroll
                for (int n = 0; n < NT; ++n) acc[q & 3][n] = __builtin_amdgcn_mfma_f32_16x16x32_bf16(bf[q >> 2][n], af[q >> 2][q & 3], acc[q & 3][n], 0, 0, 0);
            __builtin_amdgcn_s_setprio(0);
            K8_SCHED;
            asm volatile("s_waitcnt vmcnt(%0)" :: "i"((ST - 2) * GS) : "memory");
            K8_BAR;
        }
    }
    if (wu < 4) K8_BAR;
    asm volatile("s_waitcnt vmcnt(0)" ::: "memory");
#pragma unroll
    for (int m = 0; m < 4; ++m)
#pragma unroll
        for (int n = 0; n < NT; n += 2) {
            auto r0 = __builtin_amdgcn_permlane16_swap(cvt_pk_bf16(acc[m][n][0], acc[m][n][1]), cvt_pk_bf16(acc[m][n + 1][0], acc[m][n + 1][1]), false, false);
            auto r1 = __builtin_amdgcn_permlane16_swap(cvt_pk_bf16(acc[m][n][2], acc[m][n][3]), cvt_pk_bf16(acc[m][n + 1][2], acc[m][n + 1][3]), false, false);
            if (WT & 1) out16(C + (wr * 64 + m * 16 + i) * LDC + (wc * NT + n + (g & 1)) * 16 + 8 * (g >> 1), v4u{r0[0], r1[0], r0[1], r1[1]});
            else *(v4u*)(C + (wr * 64 + m * 16 + i) * LDC + (wc * NT + n + (g & 1)) * 16 + 8 * (g >> 1)) = v4u{r0[0], r1[0], r0[1], r1[1]};
        }
}

__device__ __forceinline__ void xattn_unit(const ChainArgs& a, rsrc_t rsQ, rsrc_t rsK, rsrc_t rsV, rsrc_t rsS, const int s, const int sn, const int h, u16 (*Vs)[VP], unsigned* mbox, const unsigned tau, int& pend, bool& ready) {
    const int l = opaque_tid() & 63, i = l & 15, g = l >> 4;
    const unsigned known = __builtin_amdgcn_readfirstlane(*(volatile unsigned*)mbox);
    const bool early = known > (unsigned)(s + 1);
    const int bq = km_off(s, h, tau), bb = v_off(s, h, tau);
    auto count = [&](const int t) {
        if (t < SSEG) { arrive(a.sync, SY_OX + (t >> 2) * 8, 128u, 8); return; }
        unsigned n = 0;
        if (l == 0) n = atomicAdd(mbox + 8 + t - SSEG, 1u);
        if (__builtin_amdgcn_readfirstlane(n) == 3u && l < 8) __hip_atomic_store((gu32*)(a.sync + (SY_OXS + (t - SSEG) * 8 + l) * 32 + (h >> 2)), 16u, __ATOMIC_RELAXED, __HIP_MEMORY_SCOPE_AGENT);
    };
    const int xk = blk_off(s, h, tau), xq = xk ^ 8192;
    v4u vv[8], vm[2], k0f[4], k1f[4], kmf[4], q0f[4], q1f[4];
#pragma unroll
    for (int e = 0; e < 8; ++e) vv[e] = ld_sc1s(rsV, l * 16 + e * 1024, xk);
#pragma unroll
    for (int ks = 0; ks < 4; ++ks) {
        k0f[ks] = ld_sc1s(rsK, l * 16 + ks * 1024, xk);
        k1f[ks] = ld_sc1s(rsK, l * 16 + ks * 1024, xk + 4096);
        q0f[ks] = ld_sc1s(rsQ, l * 16 + ks * 1024, xq);
        q1f[ks] = ld_sc1s(rsQ, l * 16 + ks * 1024, xq + 4096);
    }
#pragma unroll
    for (int e = 0; e < 2; ++e) vm[e] = ld_sc1s(rsS, l * 16 + e * 1024, early ? bb : 1 << 30);
#pragma unroll
    for (int ks = 0; ks < 4; ++ks) kmf[ks] = ld_sc1s(rsS, ks * 512 + g * 128 + (i & 7) * 16, early ? bq : 1 << 30);
    const unsigned fl = ld_flag(a.sync + (l >= 8 ? SY_PROG * 32 : (((l & 4) ? SY_Q : SY_KV) + (min(sn, NSEG - 1) >> 3) * 8) * 32 + (l & 3)) + (h >> 2) * 32);
    if (pend >= 0) { asm volatile("s_waitcnt vmcnt(31)" ::: "memory"); count(pend); }
#pragma unroll
    for (int e = 0; e < 8; ++e) *(v4u*)&Vs[16 * (e >> 2) + i][32 * (e & 3) + 8 * g] = vv[e];
    const float scale = 0.08838834764831845f;
    const float L2E = 1.4426950408889634f;
    auto round2 = [&](const f32x4 c, float* o) {
#pragma unroll
        for (int p = 0; p < 2; ++p) {
            unsigned u = cvt_pk_bf16(c[2 * p], c[2 * p + 1]);
            float lo = __uint_as_float(u << 16) * scale, hi = __uint_as_float(u & 0xFFFF0000u) * scale;
            unsigned u2 = cvt_pk_bf16(lo, hi);
            o[2 * p] = __uint_as_float(u2 << 16);
            o[2 * p + 1] = __uint_as_float(u2 & 0xFFFF0000u);
        }
    };
    float sv[2][12], mxx[2];
    {
        f32x4 sx[3] = {};
#pragma unroll
        for (int ks = 0; ks < 4; ++ks) {
            sx[0] = __builtin_amdgcn_mfma_f32_16x16x32_bf16(as_frag(k0f[ks]), as_frag(q0f[ks]), sx[0], 0, 0, 0);
            sx[1] = __builtin_amdgcn_mfma_f32_16x16x32_bf16(as_frag(k0f[ks]), as_frag(q1f[ks]), sx[1], 0, 0, 0);
            sx[2] = __builtin_amdgcn_mfma_f32_16x16x32_bf16(as_frag(k1f[ks]), as_frag(q1f[ks]), sx[2], 0, 0, 0);
        }
        round2(sx[0], &sv[0][0]); round2(sx[1], &sv[1][0]); round2(sx[2], &sv[1][4]);
        mxx[0] = mxx[1] = -3.0e38f;
#pragma unroll
        for (int r = 0; r < 4; ++r) {
            if (4 * g + r > i) { sv[0][r] = -3.0e38f; sv[1][4 + r] = -3.0e38f; }
            mxx[0] = fmaxf(mxx[0], sv[0][r]);
            mxx[1] = fmaxf(mxx[1], fmaxf(sv[1][r], sv[1][4 + r]));
        }
    }
    const u16* vb = &Vs[4 * g + (i >> 2)][4 * (i & 3)];
    bf16x8 vfx[8];
#pragma unroll
    for (int t = 0; t < 8; ++t) vfx[t] = tr_frag(vb + 16 * t, vb + 16 * VP + 16 * t);
#pragma unroll
    for (int t = 0; t < 8; ++t) asm volatile("" : "+v"(vfx[t]));
#pragma unroll
    for (int r = 0; r < 4; ++r) asm volatile("" : "+v"(sv[0][r]), "+v"(sv[1][r]), "+v"(sv[1][4 + r]));
    asm volatile("" : "+v"(mxx[0]), "+v"(mxx[1]));
    bool have = false;
    unsigned seen = __builtin_amdgcn_readlane(fl, 8);
    ready = (sn >> 3) == (s >> 3) || (__ballot(fl != 0u) & 255ull) == 255ull;
    if (early) {
        if ((h & 3) == 0 && l == 0) atomicMax(mbox, seen);
        unsigned m = mx4(mx4(0u, vm[0]), vm[1]);
#pragma unroll
        for (int ks = 0; ks < 4; ++ks) m = mx4(m, kmf[ks]);
        have = __all(m != SENT);
    } else if ((h & 3) == 0) {
        const unsigned* pw = a.sync + (SY_PROG + (h >> 2)) * 32;
        unsigned v = seen;
        if (v <= (unsigned)(s + 1)) {
            if (s < NSEG - XA_FAST) { wait_ge<32>(pw, (unsigned)(s + 1), a.tmo, 4000u); __builtin_amdgcn_s_sleep(24); }
            else { wait_ge<6>(pw, (unsigned)(s + 1), a.tmo, 4000u); __builtin_amdgcn_s_sleep(XA_D); }
            v = (unsigned)(s + 1);
        }
        if (l == 0) atomicMax(mbox, v);
    } else {
#pragma nounroll
        for (int spin = 0; spin < 4000000 && *(volatile unsigned*)mbox < (unsigned)(s + 1); ++spin) __builtin_amdgcn_s_sleep(2);
        asm volatile("" ::: "memory");
    }
#pragma nounroll
    for (int spin = 0; !have; ++spin) {
        asm volatile("" ::: "memory");
#pragma unroll
        for (int e = 0; e < 2; ++e) vm[e] = ld_sc1s(rsS, l * 16 + e * 1024, bb);
#pragma unroll
        for (int ks = 0; ks < 4; ++ks) kmf[ks] = ld_sc1s(rsS, ks * 512 + g * 128 + (i & 7) * 16, bq);
        unsigned m = mx4(mx4(0u, vm[0]), vm[1]);
#pragma unroll
        for (int ks = 0; ks < 4; ++ks) m = mx4(m, kmf[ks]);
        if (__all(m != SENT) || spin > 20000) break;
        __builtin_amdgcn_s_sleep(2);
    }
#pragma unroll
    for (int e = 0; e < 8; ++e) *(unsigned*)&Vs[32 + (i >> 2) + 4 * ((i >> 1) & 1)][64 * (e >> 2) + 16 * g + 4 * (e & 3) + 2 * (i & 1)] = vm[e >> 2][e & 3];
    f32x4 sm_[2] = {};
#pragma unroll
    for (int ks = 0; ks < 4; ++ks) {
        sm_[0] = __builtin_amdgcn_mfma_f32_16x16x32_bf16(as_frag(kmf[ks]), as_frag(q0f[ks]), sm_[0], 0, 0, 0);
        sm_[1] = __builtin_amdgcn_mfma_f32_16x16x32_bf16(as_frag(kmf[ks]), as_frag(q1f[ks]), sm_[1], 0, 0, 0);
    }
    v4u pf[2][2];
#pragma unroll
    for (int qt = 0; qt < 2; ++qt) {
        round2(sm_[qt], &sv[qt][8]);
        float mx = fmaxf(fmaxf(mxx[qt], fmaxf(sv[qt][8], sv[qt][9])), fmaxf(sv[qt][10], sv[qt][11]));
        mx = xor16_max(mx);
        mx = xor32_max(mx);
        const float nmx = -mx * L2E;
        float sum = 0.f;
#pragma unroll
        for (int e = 0; e < 12; ++e) {
            if (qt == 0 && e >= 4 && e < 8) { sv[qt][e] = 0.f; continue; }
            float ev = __builtin_amdgcn_exp2f(fmaf(sv[qt][e], L2E, nmx));
            if (e >= 8) ev = g < 2 ? ev : 0.f;
            sv[qt][e] = ev; sum += ev;
        }
        sum = xor16_sum(sum);
        sum = xor32_sum(sum);
        const float inv = 1.0f / sum;
        pf[qt][0][0] = cvt_pk_bf16(sv[qt][0] * inv, sv[qt][1] * inv);
        pf[qt][0][1] = cvt_pk_bf16(sv[qt][2] * inv, sv[qt][3] * inv);
        pf[qt][0][2] = qt ? cvt_pk_bf16(sv[qt][4] * inv, sv[qt][5] * inv) : 0u;
        pf[qt][0][3] = qt ? cvt_pk_bf16(sv[qt][6] * inv, sv[qt][7] * inv) : 0u;
        pf[qt][1][0] = cvt_pk_bf16(sv[qt][8] * inv, sv[qt][9] * inv);
        pf[qt][1][1] = cvt_pk_bf16(sv[qt][10] * inv, sv[qt][11] * inv);
        pf[qt][1][2] = 0u; pf[qt][1][3] = 0u;
    }
    v2u vmf[8];
#pragma unroll
    for (int t = 0; t < 8; ++t) vmf[t] = tr4(&Vs[32 + 4 * (g & 1) + (i >> 2)][16 * t + 4 * (i & 3)]);
#pragma unroll
    for (int t = 0; t < 8; t += 2) {
        unsigned pk[2][2][2];
#pragma unroll
        for (int tt = 0; tt < 2; ++tt) {
            const bf16x8 vf0 = vfx[t + tt];
            const bf16x8 vf1 = as_frag(v4u{vmf[t + tt][0], vmf[t + tt][1], 0u, 0u});
#pragma unroll
            for (int qt = 0; qt < 2; ++qt) {
                f32x4 o = {};
                o = __builtin_amdgcn_mfma_f32_16x16x32_bf16(vf0, as_frag(pf[qt][0]), o, 0, 0, 0);
                o = __builtin_amdgcn_mfma_f32_16x16x32_bf16(vf1, as_frag(pf[qt][1]), o, 0, 0, 0);
                pk[qt][tt][0] = cvt_pk_bf16(o[0], o[1]);
                pk[qt][tt][1] = cvt_pk_bf16(o[2], o[3]);
            }
        }
#pragma unroll
        for (int qt = 0; qt < 2; ++qt) {
            auto r0 = __builtin_amdgcn_permlane16_swap(pk[qt][0][0], pk[qt][1][0], false, false);
            auto r1 = __builtin_amdgcn_permlane16_swap(pk[qt][0][1], pk[qt][1][1], false, false);
            v4u o = {r0[0], r1[0], r0[1], r1[1]};
            st_sc1s(rsV, (t >> 1) * 1024 + ((g & 1) * 2 + (g >> 1)) * 256 + i * 16, xq + qt * 4096, o);
        }
    }
    pend = seen > (unsigned)(sn + 1) && ready ? s : -1;
    if (pend < 0) { asm volatile("s_waitcnt vmcnt(0)" ::: "memory"); count(s); }
}

__device__ __forceinline__ void tile_stream(const ChainArgs& a, const u16* X, unsigned long long todo, const int xc, unsigned& tau, const bool early, const int nw, u16* sm8 WEVP) {
    constexpr int K = DM, LDB = HD;
    const int tid = opaque_tid(), w = tid >> 6, l = tid & 63, i = l & 15, g = l >> 4;
    const int wr = w >> 2, wc = w & 3;
    const unsigned lds0 = (unsigned)(uintptr_t)(__attribute__((address_space(3))) u16*)sm8;
    const unsigned wu = __builtin_amdgcn_readfirstlane(w);
    const unsigned ldsw = lds0 + wu * 2048;
    unsigned voffA, voffB;
    { int row = 16 * w + (l >> 3), c = l & 7; voffA = (unsigned)(row * K + 8 * (c ^ (row & 7))) * 2u; }
    { int row = 8 * w + g, c = i; voffB = (unsigned)(row * LDB + 8 * (c ^ (bswz2<128>(row) << 1))) * 2u; }
    const unsigned voffA2 = voffA + 8u * K * 2u - 1024u, voffB2 = voffB + 4u * LDB * 2u - 1024u;
    auto st2 = [&](const unsigned v0, const unsigned v1, const char* p, const unsigned d) {
        unsigned keep;
        asm volatile("s_mov_b32 %0, m0\n\ts_mov_b32 m0, %1\n\ts_nop 0\n\tglobal_load_lds_dwordx4 %2, %4\n\tglobal_load_lds_dwordx4 %3, %4 offset:1024\n\ts_mov_b32 m0, %0"
                     : "=&s"(keep) : "s"(d), "v"(v0), "v"(v1), "s"(p) : "memory");
    };
    auto stA = [&](int b, int h, const char* p) {
        st2(voffA, voffA2, p + (size_t)(h * 128) * K * 2, ldsw + (2 * b + h) * 16384);
    };
    auto stB = [&](int b, int h, const char* p) {
        st2(voffB, voffB2, p + h * 2048, ldsw + 65536 + (2 * b + h) * 16384);
    };
    constexpr int SA = 128, SB = 64 * LDB * 2;
    auto a_of = [&](const unsigned t) { return (const char*)(X + (size_t)((t >> 4) & 7) * 256 * DM); };
    auto b_of = [&](const unsigned t) { const int kind = t & 7; return (const char*)((kind < 2 ? a.Wk : (kind < 4 ? a.Wv : a.Wq)) + hcol(4 * xc + (kind & 1) * 2)); };
    auto c_of = [&](const unsigned t) { const int kind = t & 7; return ((kind & 6) == 2 ? VX_OFF - KX_OFF : 0) + (int)((t >> 4) & 7) * 4194304 + blk_off(0, 4 * xc + (kind & 1) * 2, kind < 4 ? tau : tau ^ 1u); };
    auto publish = [&](const unsigned t) {
        const int kind = t & 7, ln = lane_now();
        if (wu == 0 && ln < (kind < 4 ? 1 : 2))
            __hip_atomic_store((gu32*)(a.sync + ((kind < 4 ? SY_KV : SY_Q) + ((t >> 4) & 7) * 8 + xc) * 32 + (kind < 4 ? kind : 2 * kind - 8 + ln)), 16u, __ATOMIC_RELAXED, __HIP_MEMORY_SCOPE_AGENT);
    };
    const char* smc = (const char*)sm8;
    int aoff[2], boff[2];
#pragma unroll
    for (int ks = 0; ks < 2; ++ks) aoff[ks] = (wr * 64 + i) * 128 + (((ks * 4 + g) ^ (i & 7)) << 4);
#pragma unroll
    for (int n = 0; n < 2; ++n) { int row = 8 * g + (i >> 2); boff[n] = 65536 + row * 256 + (((wc * 2 + n) ^ bswz2<128>(row)) << 5) + 8 * (i & 3); }
    f32x4 acc[2][2][4][2] = {};
    bf16x8 At[4][2], B0[2][2], B1[2][2];
#define K8_LDA(b, h) _Pragma("unroll") for (int m = 0; m < 4; ++m) _Pragma("unroll") for (int ks = 0; ks < 2; ++ks) \
        At[m][ks] = *(const bf16x8*)(smc + aoff[ks] + (2 * (b) + (h)) * 16384 + m * 2048)
#define K8_LDB(dst, b, h) _Pragma("unroll") for (int n = 0; n < 2; ++n) _Pragma("unroll") for (int ks = 0; ks < 2; ++ks) { \
        const char* p_ = smc + boff[n] + (2 * (b) + (h)) * 16384 + ks * 8192; \
        v4i16 lo_ = __builtin_amdgcn_ds_read_tr16_b64_v4i16((__attribute__((address_space(3))) v4i16*)p_); \
        v4i16 hi_ = __builtin_amdgcn_ds_read_tr16_b64_v4i16((__attribute__((address_space(3))) v4i16*)(p_ + 1024)); \
        dst[n][ks] = bf16x8{lo_[0], lo_[1], lo_[2], lo_[3], hi_[0], hi_[1], hi_[2], hi_[3]}; }
#define K8_MMA(ai, bj, Bt) do { __builtin_amdgcn_s_setprio(1); \
        _Pragma("unroll") for (int m = 0; m < 4; ++m) _Pragma("unroll") for (int n = 0; n < 2; ++n) _Pragma("unroll") for (int ks = 0; ks < 2; ++ks) \
            acc[ai][bj][m][n] = __builtin_amdgcn_mfma_f32_16x16x32_bf16(Bt[n][ks], At[m][ks], acc[ai][bj][m][n], 0, 0, 0); \
        __builtin_amdgcn_s_setprio(0); } while (0)
    const rsrc_t rsC = __builtin_amdgcn_make_buffer_rsrc((void*)(arena(a.slots) + KX_OFF), 0, FX_OFF - KX_OFF, 0x00020000);
#define K8_EPI(ai, bj, cb) do { const int ln_ = lane_now(); \
        const int vo_ = (int)(wu & 3) * 1024 + (((ln_ >> 4) & 1) * 2 + (ln_ >> 5)) * 256 + (ln_ & 15) * 16; \
        _Pragma("unroll") for (int m = 0; m < 4; ++m) { \
            const f32x4 c0 = acc[ai][bj][m][0], c1 = acc[ai][bj][m][1]; \
            auto r0 = __builtin_amdgcn_permlane16_swap(cvt_pk_bf16(c0[0], c0[1]), cvt_pk_bf16(c1[0], c1[1]), false, false); \
            auto r1 = __builtin_amdgcn_permlane16_swap(cvt_pk_bf16(c0[2], c0[3]), cvt_pk_bf16(c1[2], c1[3]), false, false); \
            const int sg_ = 4 * ai + 2 * (int)(wu >> 2) + (m >> 1); \
            st_sc1s(rsC, vo_, ((cb) ^ (ai * 8192)) + sg_ * 524288 + bj * 16384 + (m & 1) * 4096, v4u{r0[0], r1[0], r0[1], r1[1]}); \
            acc[ai][bj][m][0] = ZERO4; acc[ai][bj][m][1] = ZERO4; } } while (0)
    constexpr int nt = K / 64;
    unsigned cur = (unsigned)todo & 255u, prev = 0u;
    const char* pA = a_of(cur); const char* pB = b_of(cur);
    if (early) touch((const char*)X + 524288 + (size_t)(nw * 128 + (tid >> 2)) * 128, lds0 + 163840 - 256);
    stB(0, 0, pB); stB(0, 1, pB); stA(0, 0, pA); stA(0, 1, pA);
    stB(1, 0, pB + SB); stB(1, 1, pB + SB); stA(1, 0, pA + SA);
    K8_WV(8); K8_BAR;
    if (wu >= 4) K8_BAR;
#define K8_GO(ai) __builtin_amdgcn_s_waitcnt(0xC07F); K8_BAR; K8_MMA(ai, 0, B0); K8_MMA(ai, 1, B1); K8_SCHED; K8_BAR
    int cbp = 0;
    for (;;) {
        todo >>= 8;
        const unsigned nxt = todo != 0ull ? (unsigned)todo & 255u : cur;
        int cb = c_of(cur);
#pragma nounroll
        for (int t = 0; t < nt; t += 2) {
            if (__builtin_expect(early && prev == 0u && t == 10, 0))
                touch((const char*)X + 3 * 524288 + (size_t)(nw * 320 + min(TIDX, 319)) * 128, lds0 + 163840 - 256);
            if (__builtin_expect(early && prev == 0u && t == nt - 4, 0)) {
                tau = __builtin_amdgcn_readfirstlane((unsigned)boot_wait(a.boot) & 255u);
                cb = c_of(cur);
            }
            const bool first = t == 0 && prev != 0u, last = t == nt - 2;
            if (t == 2 && prev != 0u) { publish(prev); WEV(3) }
            K8_LDB(B0, 0, 0); K8_LDA(0, 0); K8_LDB(B1, 0, 1); K8_SCHED; stA(1, 1, pA + SA);
            pA = last ? a_of(nxt) : pA + 2 * SA; pB = last ? b_of(nxt) : pB + 2 * SB;
            if (first) { K8_EPI(1, 0, cbp); K8_EPI(1, 1, cbp); K8_WV(24); } else K8_WV(8);
            K8_GO(0);
            K8_LDA(0, 1); K8_SCHED; stB(0, 0, pB); stB(0, 1, pB); stA(0, 0, pA);
            if (first) K8_WV(24); else K8_WV(8);
            K8_GO(1);
            K8_LDB(B0, 1, 0); K8_LDA(1, 0); K8_LDB(B1, 1, 1); K8_SCHED; stA(0, 1, pA);
            if (first) K8_WV(16); else K8_WV(8);
            K8_GO(0);
            K8_LDA(1, 1); K8_SCHED; stB(1, 0, pB + SB); stB(1, 1, pB + SB); stA(1, 0, pA + SA);
            if (last) { K8_EPI(0, 0, cb); K8_EPI(0, 1, cb); K8_WV(16); } else K8_WV(8);
            K8_GO(1);
        }
        WEV(2)
        prev = cur; cbp = cb; cur = nxt;
        if (todo == 0ull) break;
    }
    K8_EPI(1, 0, cbp); K8_EPI(1, 1, cbp);
    K8_WV(0);
    if (wu < 4) K8_BAR;
    K8_BAR;
    publish(prev);
    WEV(3)
}

__device__ __forceinline__ void oproj_squad(const ChainArgs& a, u16* out, const int ct, const int rh, const int xc, const unsigned tau, char* smem) {
    const int tid = opaque_tid(), w = tid >> 6, l = tid & 63, i = l & 15, g = l >> 4;
    const int wu = __builtin_amdgcn_readfirstlane(w);
    const int c0 = (ct / 3) * 128 + (ct % 3) * 48, nq = ct % 3 == 2 ? 8 : 12;
    bf16x8 wf[2][16];
    char* w3 = smem + wu * 16384 + l * 16;
    {
        u16* stg = (u16*)(smem + wu * 16384);
        bf16x8 wt[16];
#pragma unroll
        for (int ch = 0; ch < 4; ++ch) {
            const u16* src = a.Wo + (size_t)hcol(4 * wu + ch) * DM;
            v4u raw[12];
#pragma unroll
            for (int e = 0; e < 12; ++e) { const int idx = l + 64 * e, r = idx / 6, q = idx % 6; raw[e] = *(const v4u*)(src + (size_t)r * DM + min(c0 + 8 * q, DM - 8)); }
#pragma unroll
            for (int e = 0; e < 12; ++e) { const int idx = l + 64 * e, r = idx / 6, q = idx % 6; *(v4u*)(stg + r * 56 + 8 * q) = raw[e]; }
            __builtin_amdgcn_s_waitcnt(0xC07F);
#pragma unroll
            for (int jj = 0; jj < 3; ++jj)
#pragma unroll
                for (int ks = 0; ks < 4; ++ks) {
                    const u16* p = stg + (32 * ks + 8 * g + (i >> 2)) * 56 + 16 * jj + 4 * (i & 3);
                    (jj < 2 ? wf[jj & 1] : wt)[4 * ch + ks] = tr_frag(p, p + 4 * 56);
                }
            __builtin_amdgcn_s_waitcnt(0xC07F);
        }
#pragma unroll
        for (int ks = 0; ks < 16; ++ks) *(bf16x8*)(w3 + ks * 1024) = wt[ks];
    }
    const rsrc_t rsO = __builtin_amdgcn_make_buffer_rsrc((void*)(arena(a.slots) + VX_OFF), 0, 32 << 20, 0x00020000);
    float* rp = (float*)(smem + 131072);
    const int voff = rh * 4096 + l * 16;
    bool pre = false;
    v4u xf[16];
    const unsigned* fq = a.sync + (SY_OXS + xc) * 32 + wu;
    for (int s = SSEG; s < NSEG - 1; ++s, fq += 256) {
        if (!pre) {
#pragma nounroll
            for (int spin = 0;; ++spin) {
                asm volatile("" ::: "memory");
                if (ld_flag(fq) != 0u) break;
                if (spin > 20000) { __hip_atomic_store((gu32*)a.tmo, 7000u, __ATOMIC_RELAXED, __HIP_MEMORY_SCOPE_AGENT); break; }
                if (s == SSEG) __builtin_amdgcn_s_sleep(64); else if (s < NSEG - 3) __builtin_amdgcn_s_sleep(SQ_SL); else __builtin_amdgcn_s_sleep(SQ_SF);
            }
            asm volatile("" ::: "memory");
        }
        const unsigned nf = ld_flag(fq + 256);
        if (!pre) {
#pragma unroll
            for (int ks = 0; ks < 16; ++ks) xf[ks] = ld_sc1s(rsO, voff + (ks & 3) * 1024, blk_off(s, 4 * wu + (ks >> 2), tau) ^ 8192);
        }
        pre = __builtin_amdgcn_readfirstlane(nf) != 0u;
        f32x4 acc[3] = {};
        if (pre) {
            bf16x8 wn = *(const bf16x8*)w3;
#pragma unroll
            for (int ks = 0; ks < 16; ++ks) {
#pragma unroll
                for (int jj = 0; jj < 2; ++jj) acc[jj] = __builtin_amdgcn_mfma_f32_16x16x32_bf16(wf[jj][ks], as_frag(xf[ks]), acc[jj], 0, 0, 0);
                acc[2] = __builtin_amdgcn_mfma_f32_16x16x32_bf16(wn, as_frag(xf[ks]), acc[2], 0, 0, 0);
                wn = *(const bf16x8*)(w3 + ((ks + 1) & 15) * 1024);
                K8_SCHED;
                xf[ks] = ld_sc1s(rsO, voff + (ks & 3) * 1024, blk_off(s + 1, 4 * wu + (ks >> 2), tau) ^ 8192);
                K8_SCHED;
            }
        } else {
#pragma unroll
            for (int ks = 0; ks < 16; ++ks) {
#pragma unroll
                for (int jj = 0; jj < 2; ++jj) acc[jj] = __builtin_amdgcn_mfma_f32_16x16x32_bf16(wf[jj][ks], as_frag(xf[ks]), acc[jj], 0, 0, 0);
                acc[2] = __builtin_amdgcn_mfma_f32_16x16x32_bf16(*(const bf16x8*)(w3 + ks * 1024), as_frag(xf[ks]), acc[2], 0, 0, 0);
            }
        }
        K8_BAR;
#pragma unroll
        for (int jj = 0; jj < 3; ++jj) *(f32x4*)&rp[(wu * 16 + i) * 52 + 16 * jj + 4 * g] = acc[jj];
        K8_WL(0); K8_BAR;
        if (tid >= 256 && (tid & 15) < nq) {
            const int row = (tid >> 4) & 15, q4 = tid & 15;
            f32x4 sv_[8];
#pragma unroll
            for (int e = 0; e < 8; ++e) sv_[e] = *(const f32x4*)&rp[(e * 16 + row) * 52 + 4 * q4];
            const f32x4 s0 = ((sv_[0] + sv_[1]) + (sv_[2] + sv_[3])) + ((sv_[4] + sv_[5]) + (sv_[6] + sv_[7]));
            v2u o; o[0] = cvt_pk_bf16(s0[0], s0[1]); o[1] = cvt_pk_bf16(s0[2], s0[3]);
            out8(out + (size_t)(s * SEG + 16 * rh + row) * OUTW + c0 + 4 * q4, o);
        }
    }
}

__device__ __forceinline__ void kv_stream(const ChainArgs& a, const u16* __restrict__ X, const u16* __restrict__ B, char* C, const int head, const int xc, const int wk, unsigned& tau, char* smem) {
    const int tid = opaque_tid(), w = tid >> 6, l = tid & 63, i = l & 15, g = l >> 4;
    const int wu = __builtin_amdgcn_readfirstlane(w), kh = wu >> 2, cq = wu & 3;
    const unsigned lds0 = (unsigned)(uintptr_t)(__attribute__((address_space(3))) char*)smem;
    unsigned voA, voB;
    { const int row = (wu & 1) * 8 + (l >> 3), c = l & 7; voA = (unsigned)(row * DM + 8 * (c ^ (row & 7))) * 2u + (wu >> 1) * 512; }
    { const int row = tid >> 4, c = tid & 15; voB = (unsigned)(row * HD + 8 * (c ^ (bswz2<128>(row) << 1))) * 2u; }
    auto stB = [&](int ch, int slot) {
#pragma unroll
        for (int e = 0; e < 4; ++e) glds16s(voB, (const char*)B + (size_t)(4 * ch + e) * 32 * HD * 2, lds0 + 65536 + slot * 32768 + e * 8192 + wu * 1024);
    };
    const unsigned ldA = lds0 + (wu >> 1) * 8192 + (wu & 1) * 1024;
    auto stP = [&](const char* p, const unsigned d) {
        unsigned keep;
        asm volatile("s_mov_b32 %0, m0\n\ts_mov_b32 m0, %1\n\ts_nop 0\n\tglobal_load_lds_dwordx4 %3, %2 offset:-4096\n\tglobal_load_lds_dwordx4 %4, %2 offset:-2048\n\t"
                     "global_load_lds_dwordx4 %5, %2\n\tglobal_load_lds_dwordx4 %6, %2 offset:2048\n\ts_mov_b32 m0, %0"
                     : "=&s"(keep) : "s"(d), "s"(p), "v"(voA + 8192u), "v"(voA + 6272u), "v"(voA + 4352u), "v"(voA + 2432u) : "memory");
    };
    auto srcA = [&](int h) { return (const char*)X + (size_t)min(h, 2 * KVS - 1) * 32768 - 4096; };
    auto stA = [&](int h) { stP(srcA(h), ldA + (h & 3) * 32768 + 4096); };
    stB(0, 0); stA(0); stA(1); stB(1, 1);
    touch((const char*)X + 65536 + min(wk * 512 + tid, (KVS - 1) * 512 - 1) * 128, lds0 + 131072 + wu * 1024);
    stB(2, 2);
    bf16x8 bf[16][2], af[8];
    f32x4 acc[2][2] = {}, keep[2];
    const int rb = 64 * kh + 8 * g + (i >> 2), sw = bswz2<128>(rb);
    const char* bp = smem + 65536 + rb * 256 + 8 * (i & 3);
    const char* sa0 = smem + kh * 2048 + i * 128 + 16 * (g ^ (i & 7));
    const char* sa1 = smem + kh * 2048 + i * 128 + 16 * ((4 + g) ^ (i & 7));
#define KV_AF(slot, q) *(const bf16x8*)((((q) & 1) ? sa1 : sa0) + (slot) * 32768 + ((q) >> 1) * 4096)
#pragma unroll
    for (int ch = 0; ch < 8; ++ch) {
        if (ch == 0) K8_WV(9); else if (ch < 6) K8_WV(8); else if (ch == 6) K8_WV(4); else K8_WV(0);
        K8_BAR;
#pragma unroll
        for (int e = 0; e < 4; ++e) { const char* p_ = bp + (ch % 3) * 32768 + (e >> 1) * 8192 + 32 * ((2 * cq + (e & 1)) ^ sw); bf[2 * ch + (e >> 1)][e & 1] = tr_frag(p_, p_ + 1024); }
#pragma unroll
        for (int e = 0; e < 4; ++e) af[e] = KV_AF(e >> 1, 2 * ch + (e & 1));
        K8_WL(0); K8_SCHED; K8_BAR;
        if (ch < 5) stB(ch + 3, ch % 3);
        if (ch == 7) { stA(2); stA(3); }
#pragma unroll
        for (int e = 0; e < 8; ++e) acc[e >> 2][e & 1] = __builtin_amdgcn_mfma_f32_16x16x32_bf16(bf[2 * ch + ((e >> 1) & 1)][e & 1], af[e >> 1], acc[e >> 2][e & 1], 0, 0, 0);
    }
    float* red = (float*)(smem + 131072) + l * 4;
#pragma unroll
    for (int p = 0; p < 2; ++p) { *(f32x4*)(red + (p * 8 + wu) * 256) = kh ? acc[p][0] : acc[p][1]; keep[p] = kh ? acc[p][1] : acc[p][0]; }
    unsigned long long tag = 0;
    if (wu == 4) { tag = boot_wait(a.boot); *(volatile unsigned*)(smem + 163840 - 512) = (unsigned)tag & 255u; }
    const rsrc_t rc = __builtin_amdgcn_make_buffer_rsrc((void*)C, 0, KVS * 524288, 0x00020000);
    const int vo = cq * 1024 + (2 * kh + (g >> 1)) * 256 + i * 16 + (g & 1) * 8;
    auto put = [&](const f32x4 o, const int so) {
        const v2u pk = {cvt_pk_bf16(o[0], o[1]), cvt_pk_bf16(o[2], o[3])};
        asm volatile("buffer_store_dwordx2 %0, %1, %2, %3 offen sc1" :: "v"(pk), "v"(vo), "s"(rc), "s"(so) : "memory");
    };
    K8_WL(0); K8_WV(0); K8_BAR;
    tau = __builtin_amdgcn_readfirstlane(*(volatile unsigned*)(smem + 163840 - 512));
#pragma unroll
    for (int p = 0; p < 2; ++p) put(keep[p] + *(const f32x4*)(red + (p * 8 + (wu ^ 4)) * 256), blk_off(0, head, tau) + p * 4096);
    stA(4);
    K8_WV(4); K8_BAR;
    if (tid == 256) __hip_atomic_store((gu64*)(a.boot + 256 + 16 * xc + wk), tag, __ATOMIC_RELAXED, __HIP_MEMORY_SCOPE_AGENT);
    auto stages = [&](auto kc) {
        constexpr int KH = decltype(kc)::value;
        if (KH) K8_BAR;
        f32x4 ac[2] = {ZERO4, ZERO4}, kp = ZERO4;
        int h_first = 2, so = OOB;
        asm volatile("" : "+s"(h_first));
        const char* pn = srcA(5);
        unsigned dn = ldA + 32768 + 4096;
        int r0 = (int)(sa0 - smem) + 2 * 32768, r1 = (int)(sa1 - smem) + 2 * 32768;
        for (int h = h_first; h < 2 * KVS + 4; ++h) {
            const f32x4 oth = *(const f32x4*)(red + (wu ^ 4) * 256);
            *(f32x4*)(red + wu * 256) = ac[KH ^ 1];
            bf16x8 ax[16];
#pragma unroll
            for (int q = 0; q < 16; ++q) ax[q] = *(const bf16x8*)(smem + ((q & 1) ? r1 : r0) + (q >> 1) * 4096);
            K8_SCHED;
            stP(pn, dn);
            put((KH ? ac[1] : kp) + oth, so);
            kp = ac[0];
            __builtin_amdgcn_s_waitcnt(0xC07F);
            K8_WV(10);
            if (__builtin_expect(h == 5, 0)) K8_WV(0);
            K8_BAR;
            if (KH && (0xAAA20u >> h) & 1u && tid == 256)
                __hip_atomic_store((gu32*)(a.sync + (SY_KV0 + (h / 2 - (h == 5 ? 1 : 2)) * 8 + xc) * 32 + wk), 16u, __ATOMIC_RELAXED, __HIP_MEMORY_SCOPE_AGENT);
            if (__builtin_expect(h < 2 * KVS, 1)) {
                __builtin_amdgcn_s_setprio(1);
#pragma unroll
                for (int q = 0; q < 16; ++q)
#pragma unroll
                    for (int n = 0; n < 2; ++n) ac[n] = __builtin_amdgcn_mfma_f32_16x16x32_bf16(bf[q][n], ax[q], q ? ac[n] : ZERO4, 0, 0, 0);
                __builtin_amdgcn_s_setprio(0);
            }
            {
                const int hs = h - 1 + KH;
                so = blk_off(hs >> 1, head, tau) + (hs & 1) * 4096;
                asm volatile("" : "+s"(so));
                so = hs > 1 && hs < 2 * KVS ? so : OOB;
                pn = srcA(h + 4); dn = ldA + (h & 3) * 32768 + 4096;
                r0 = (r0 + 32768) & 131071; r1 = (r1 + 32768) & 131071;
                asm volatile("" : "+s"(so), "+s"(pn), "+s"(dn), "+v"(r0), "+v"(r1));
            }
            K8_BAR;
        }
        if (!KH) K8_BAR;
    };
    if (kh) stages(std::integral_constant<int, 1>{}); else stages(std::integral_constant<int, 0>{});
    K8_WV(0);
}

struct MegaArgs {
    ChainArgs ch;
    const u16* x; u16* out;
    const u16* bx; const u16* wr; const u16* wi;
};
constexpr int MEGA_LDS = 163840;
#define WSTAMP(n)
__global__ __launch_bounds__(512, 2) void mega_kernel(MegaArgs m) {
    extern __shared__ __attribute__((aligned(256))) char chsm[];
    const int b = BIDX, xc = (b & 3) + 4 * (b >> 7), sl = (b >> 3) & 15;
    if ((b & 4) == 0) { chain_body(m.ch, xc, sl, chsm); return; }
    const ChainArgs& a = m.ch;
    const int wk = sl, tid = TIDX;
    char* ar = arena(a.slots);
    unsigned tauw = 0;
    const int hf = __builtin_amdgcn_readfirstlane(TIDX >> 8);
    WSTAMP(0)
    if (wk < 8) kv_stream(a, m.x, (wk < 4 ? a.Wk : a.Wv) + hcol(4 * xc + (wk & 3)), ar + (wk < 4 ? KX_OFF : VX_OFF), 4 * xc + (wk & 3), xc, wk, tauw, chsm);
    WSTAMP(8) WEV(1)
    {
        const int p = wk & 1, q = wk & 3;
        const unsigned long long todo = wk < 2 ? 0x84u + p : wk < 4 ? 0x74704240u + 0x01010101u * p : wk < 8 ? 0ull : wk < 10 ? 0x60503010ull + 0x01010101ull * q
                                      : wk < 12 ? 0x7060503010ull + 0x0101010101ull * q : (wk < 14 ? 0x54341420u : 0x64442422u) + 0x01010101u * p;
        __syncthreads();
        if (todo != 0ull) tile_stream(a, m.x, todo, xc, tauw, wk >= 8, xc * 8 + (wk & 7), (u16*)chsm WEVA);
    }
    WSTAMP(2)
    if (wk < 2) {
        unsigned* mbox = (unsigned*)(chsm + 131072);
        if (tid < 40) mbox[tid] = tid >= 1 && tid < 5 ? 2u : 0u;
        __syncthreads();
        const int w = __builtin_amdgcn_readfirstlane(tid >> 6), h = 4 * xc + (w & 3);
        const rsrc_t rsQ = __builtin_amdgcn_make_buffer_rsrc((void*)(ar + KX_OFF), 0, S_LEN * HD * 4, 0x00020000);
        const rsrc_t rsK = __builtin_amdgcn_make_buffer_rsrc((void*)(ar + KX_OFF), 0, S_LEN * HD * 4, 0x00020000);
        const rsrc_t rsV = __builtin_amdgcn_make_buffer_rsrc((void*)(ar + VX_OFF), 0, S_LEN * HD * 4, 0x00020000);
        const rsrc_t rsS = slot_rsrc(a.slots);
        u16 (*Vs)[VP] = (u16 (*)[VP])(chsm + w * (40 * VP * 2));
        const unsigned tau = tauw;
        int pend = -1;
        bool ready = false;
        for (int s = 2 * wk + (w >> 2), sn; s < NSEG - 1; s = sn) {
            const int band = s >> 3;
            sn = s + 4;
            if (s < XST) {
                unsigned n = 0;
                if (lane_now() == 0) n = atomicAdd(mbox + 1 + (w & 3), 1u);
                n = __builtin_amdgcn_readfirstlane(n);
                sn = 2 * wk + (n < XST / 2 ? (int)(n & 1u) + 4 * (int)(n >> 1) : XST + (w >> 2));
            }
            if (sn - s > 4) __builtin_amdgcn_s_setprio(2); else __builtin_amdgcn_s_setprio(0);
            if (s < 4) wait_ge<32>(a.sync + (SY_KV0 + (KVS - 1) * 8 + xc) * 32 + (tid & 7), 16u, a.tmo, 4100u);
            if (!ready) {
                if (band > 0) wait_ge<32>(a.sync + (SY_KV + band * 8 + xc) * 32 + (tid & 3), 1u, a.tmo, 4100u);
                wait_ge<32>(a.sync + (SY_Q + band * 8 + xc) * 32 + (tid & 3), 1u, a.tmo, 4200u);
            }
            xattn_unit(a, rsQ, rsK, rsV, rsS, s, sn, h, Vs, mbox, tau, pend, ready);
        }
        return;
    }
    if (wk >= 4 && wk < 8) {
        const int fw = (wk - 4) * 8 + xc;
        const rsrc_t rsF = __builtin_amdgcn_make_buffer_rsrc((void*)(ar + FX_OFF), 0, 16 << 20, 0x00020000);
        const rsrc_t rsW = __builtin_amdgcn_make_buffer_rsrc((void*)(ar + WT_OFF), 0, 10 << 20, 0x00020000);
        const unsigned tw = tauw ^ 1u;
        {
            const int tq = opaque_tid();
            fft_rows_fwd(m.bx, rsF, tw, 2 * fw + hf, chsm + hf * 36864, tq & 255, (const char*)m.wr, (const char*)m.wi, (unsigned)(uintptr_t)(__attribute__((address_space(3))) char*)chsm + 163840 - 256);
        }
        for (int t = fw; t < 64; t += 32) {
            __syncthreads();
            filt_transpose(m.wr, m.wi, rsW, tw, t, chsm, opaque_tid());
        }
        wg_publish(a.sync, SY_DONE, 32u, 8);
        WSTAMP(3) WEV(4)
        wg_wait_acquire<16>(a.sync + (SY_DONE + xc) * 32, a.tmo, 6000u);
        {
            const int tq = opaque_tid();
            fft_cols(rsF, rsW, tw, 2 * fw + hf, 4, chsm + hf * 36864, tq & 255);
        }
        wg_publish(a.sync, SY_DONE + 8, 32u, 8);
        WSTAMP(4) WEV(5)
    }
    if (wk >= 4 && wk < 10) {
        const rsrc_t rsF = __builtin_amdgcn_make_buffer_rsrc((void*)(ar + FX_OFF), 0, 16 << 20, 0x00020000);
        wg_wait_acquire<16>(a.sync + (SY_DONE + 8 + xc) * 32, a.tmo, 6001u);
        const int tq = opaque_tid();
        fft_rows_inv(rsF, tauw ^ 1u, m.out, 2 * ((wk & 3) * 8 + xc) + hf + (wk < 8 ? 0 : 192), wk < 8 ? 64 : 32, wk < 8 ? 3 : 2, chsm + hf * 36864, tq & 255);
        WSTAMP(5) WEV(6)
    }
    if (wk >= 12) {
        const int r = 2 * (wk - 12);
        wg_wait_acquire<16>(a.sync + (SY_OX + r * 8 + xc) * 32, a.tmo, 5000u);
        wg_wait_acquire<16>(a.sync + (SY_OX + (r + 1) * 8 + xc) * 32, a.tmo, 5000u);
        WEV(7)
        gemm_tile<256>((u16*)chsm, ar + VX_OFF, 4 * r, tauw, a.Wo + xc * 128, m.out + (size_t)r * 128 * OUTW + xc * 128);
        WEV(8)
    }
    if (wk < 4 || wk == 10) {
        const int r = wk < 4 ? wk + 6 : 10;
        wg_wait_acquire<16>(a.sync + (SY_OX + r * 8 + xc) * 32, a.tmo, 5000u);
        WEV(7)
        gemm_tile<128>((u16*)chsm, ar + VX_OFF, 4 * r, tauw, a.Wo + xc * 128, m.out + (size_t)r * 128 * OUTW + xc * 128);
        WEV(8)
        WSTAMP(9)
    }
    if (wk >= 4 && wk < 10) {
        __syncthreads();
        oproj_squad(a, m.out, (wk - 4) * 4 + (xc & 3), xc >> 2, xc, tauw, chsm);
        WSTAMP(7)
    }
}

#if defined(__HIP_DEVICE_COMPILE__)
#pragma clang attribute pop
#endif
extern "C" void kernel_launch(void* const* d_in, const int* in_sizes, int n_in,
                              void* d_out, int out_size, void* d_ws, size_t ws_size,
                              hipStream_t stream) {
    char* ws = (char*)d_ws;
    size_t off = 0;
    auto alloc = [&](size_t bytes) { void* p = ws + off; off += (bytes + 255) & ~(size_t)255; return p; };
    char* slots = (char*)alloc((size_t)ARENA_BYTES + (2u << 20));
    static_assert((SY_WORDS + 2) * 128 <= 131072, "sync block");
    unsigned* sync = (unsigned*)alloc(131072);
    unsigned long long* boot = (unsigned long long*)alloc(4096);

    MegaArgs m;
    m.ch.mem0 = (const u16*)d_in[1]; m.ch.Wq = (const u16*)d_in[2]; m.ch.Wk = (const u16*)d_in[3]; m.ch.Wv = (const u16*)d_in[4]; m.ch.Wo = (const u16*)d_in[5];
    m.ch.out = (u16*)d_out; m.ch.slots = slots; m.ch.tmo = sync + SY_WORDS * 32;
    m.ch.stamps = (unsigned long long*)(ws + (240ull << 20));
    m.ch.sync = sync; m.ch.boot = boot;
    m.x = (const u16*)d_in[0]; m.out = (u16*)d_out;
    m.bx = (const u16*)d_in[7]; m.wr = (const u16*)d_in[8]; m.wi = (const u16*)d_in[9];
    (void)hipFuncSetAttribute((const void*)mega_kernel, hipFuncAttributeMaxDynamicSharedMemorySize, MEGA_LDS);
    mega_kernel<<<256, 512, MEGA_LDS, stream>>>(m);
}
```

```cpp
#if defined(__HIP_DEVICE_COMPILE__)
#pragma clang attribute push (__attribute__((target("no-packed-fp32-ops"))), apply_to = function)
#endif
#include <hip/hip_runtime.h>
#include <hip/hip_bf16.h>
#include <stdint.h>
#include <type_traits>

typedef unsigned short u16;
#define TIDX ((int)__builtin_amdgcn_workitem_id_x())
#define BIDX ((int)__builtin_amdgcn_workgroup_id_x())
__device__ __forceinline__ int opaque_tid() { int t = TIDX; asm volatile("" : "+v"(t)); return t; }
__device__ __forceinline__ int lane_now() { int t; asm volatile("v_mbcnt_lo_u32_b32 %0, -1, 0\n\tv_mbcnt_hi_u32_b32 %0, -1, %0" : "=v"(t)); return t; }
typedef __attribute__((ext_vector_type(8))) short bf16x8;
typedef __attribute__((ext_vector_type(4))) float f32x4;

constexpr int S_LEN = 2048, DM = 1024, NH = 32, DH = 128, MM = 8, SEG = 32;
constexpr int NSEG = S_LEN / SEG;
constexpr int HD = NH * DH;
constexpr int OUTW = 2048;

typedef unsigned v4u __attribute__((ext_vector_type(4)));
typedef unsigned v2u __attribute__((ext_vector_type(2)));
typedef short v4i16 __attribute__((ext_vector_type(4)));
typedef __amdgpu_buffer_rsrc_t rsrc_t;
#define AUX_SC1 16
__device__ __forceinline__ v4u ld_sc1s(rsrc_t rs, int voff, int soff) { return __builtin_amdgcn_raw_buffer_load_b128(rs, voff, soff, AUX_SC1); }
__device__ __forceinline__ void st_sc1s(rsrc_t rs, int voff, int soff, v4u v) { __builtin_amdgcn_raw_buffer_store_b128(v, rs, voff, soff, AUX_SC1); asm volatile("s_nop 1" :: "v"(v) : "memory"); }
__device__ __forceinline__ void st8_sc1s(rsrc_t rs, int voff, int soff, v2u v) { __builtin_amdgcn_raw_buffer_store_b64(v, rs, voff, soff, AUX_SC1); }
__device__ __forceinline__ void st_sc1(rsrc_t rs, int off, v4u v) { __builtin_amdgcn_raw_buffer_store_b128(v, rs, off, 0, AUX_SC1); }
__device__ __forceinline__ void out8(void* p, v2u v) { asm volatile("global_store_dwordx2 %0, %1, off sc1" :: "v"(p), "v"(v) : "memory"); }
typedef __bf16 bf16x2_t __attribute__((ext_vector_type(2)));
typedef float f32x2_t __attribute__((ext_vector_type(2)));
__device__ __forceinline__ unsigned cvt_pk_bf16(float lo, float hi) {
    f32x2_t f = {lo, hi};
    return __builtin_bit_cast(unsigned, __builtin_convertvector(f, bf16x2_t));
}
#define SENT 0xFFBFFFBF
__device__ __forceinline__ unsigned umax3(unsigned a, unsigned b, unsigned c) { return max(max(a, b), c); }
__device__ __forceinline__ unsigned mx4(unsigned acc, v4u v) { return umax3(umax3(acc, v[0], v[1]), v[2], v[3]); }
__device__ __forceinline__ bf16x8 as_frag(v4u v) { return __builtin_bit_cast(bf16x8, v); }
__device__ __forceinline__ float xor16_sum(float x) {
    auto r = __builtin_amdgcn_permlane16_swap(__float_as_uint(x), __float_as_uint(x), false, false);
    return __uint_as_float(r[0]) + __uint_as_float(r[1]);
}
__device__ __forceinline__ float xor32_sum(float x) {
    auto r = __builtin_amdgcn_permlane32_swap(__float_as_uint(x), __float_as_uint(x), false, false);
    return __uint_as_float(r[0]) + __uint_as_float(r[1]);
}
__device__ __forceinline__ float xor16_max(float x) {
    auto r = __builtin_amdgcn_permlane16_swap(__float_as_uint(x), __float_as_uint(x), false, false);
    return fmaxf(__uint_as_float(r[0]), __uint_as_float(r[1]));
}
__device__ __forceinline__ float xor32_max(float x) {
    auto r = __builtin_amdgcn_permlane32_swap(__float_as_uint(x), __float_as_uint(x), false, false);
    return fmaxf(__uint_as_float(r[0]), __uint_as_float(r[1]));
}
constexpr int SPIN_LIMIT = 400000;
constexpr int OOB = 0x7FFFFF00;
#define PV1 2
#define PV2 11
#define LW 0
#define PVA 8
#define PL 20
#define PF 48
#define CHS 0
#define DLY_U 4
#define SWZ 0
#define FROT 4224
#define WROT 384
#define FNEAR 1u
#define FST 0
#define PG0 2
#define PG1 32
#define PGP 0
#define FTR 1
#define FSG 0
#define FSR 0
#define PB 14
#define TD2 60
#define SL0 44
#define PS 46
#define TE0 10
#define TD 60
#define PV 1
#define PC 31
#define PP 15
#define PK0 34
#define PKP 1
#define PKM 1
#define PKA 28
#define PXD 1
constexpr int NPROBE = 2, DLY_L = 7, DLY_C = 5;
constexpr int M_OFF = 32 * 16384, F_OFF = M_OFF + 2 * 16384, C_OFF = F_OFF + 2 * 16384, REC = C_OFF + 16 * 16384, PERWG = REC / 128, SLOT_BYTES = 34 * 2097152;
static_assert(PERWG == 6656, "arming shares");
__device__ __forceinline__ int rec_base(int s) { return (s >> 1) * 2097152 + (s & 1) * REC; }
__device__ __forceinline__ char* arena(char* slots) { return (char*)(((uintptr_t)slots + 2097151u) & ~(uintptr_t)2097151u); }
constexpr int KX_OFF = SLOT_BYTES, VX_OFF = KX_OFF + (32 << 20), FX_OFF = VX_OFF + (32 << 20), WT_OFF = FX_OFF + (16 << 20), ARENA_BYTES = WT_OFF + (10 << 20);
__device__ __forceinline__ rsrc_t slot_rsrc(char* slots) { return __builtin_amdgcn_make_buffer_rsrc((void*)arena(slots), 0, FX_OFF, 0x00020000); }
__device__ __forceinline__ int blk_off(int seg, int h, unsigned tau) { return seg * 524288 + (((h & 3) >> 1) * 16 + ((h >> 2) ^ (seg & SWZ)) * 2 + (h & 1)) * 16384 + (int)((tau ^ (unsigned)(seg >> 2)) & 1u) * 8192; }
constexpr int TM_OFF = 124 * 16384;
constexpr int CAL_OFF = 32 * 2097152;
__device__ __forceinline__ unsigned half_bits(int s, unsigned tau) { return ((tau ^ (unsigned)(s >> 1)) & 1u) ? 0xFFu : 0u; }
__device__ __forceinline__ int chunk_off(int s, int g, int j, unsigned tau) { return rec_base(s) + (j < 2 ? (g & 3) * 2 + j : 6 + 3 * g + j) * 16384 + (int)(half_bits(s, tau) & 1u) * 8192; }
__device__ __forceinline__ int x_off(int s, int h, unsigned tau) { return chunk_off(s, h >> 2, 2, tau) + (h & 3) * 2048; }
__device__ __forceinline__ int km_off(int s, int h, unsigned tau) { return chunk_off(s, h >> 2, 3, tau) + (h & 3) * 2048; }
__device__ __forceinline__ int m_off(int s, int h, unsigned tau) { return rec_base(s) + M_OFF + (h >> 4) * 16384 + (int)((half_bits(s, tau) >> (h >> 2)) & 1u) * 8192 + (h & 15) * 512; }
__device__ __forceinline__ int v_off(int s, int h, unsigned tau) { return chunk_off(s, h >> 2, 4, tau) + (h & 3) * 2048; }
__device__ __forceinline__ int f_off(int s, int g, unsigned tau) { return rec_base(s) + F_OFF + (g >> 2) * 16384 + (int)((half_bits(s, tau) >> g) & 1u) * 8192 + (g & 3) * 2048; }
__device__ __forceinline__ int hcol(int h) { return ((h & 3) * 8 + (h >> 2)) * 128; }
constexpr int VP = 144;

struct ChainArgs {
    u16* out;
    const u16* mem0; const u16* Wq; const u16* Wk; const u16* Wv; const u16* Wo;
    char* slots;
    unsigned* tmo;
    unsigned long long* stamps;
    unsigned* sync;
    unsigned long long* boot;
};
constexpr int SY_KV = 0;
constexpr int SY_Q = 64;
constexpr int SY_PROG = 128;
constexpr int SY_OX = 136;
constexpr int SY_DONE = 264;
constexpr int SY_OXS = 472;
constexpr int SY_KV0 = 408;
#define TCH 0
#define TXT 10
#define XPR 2
#define F0E 1
#define STRICT 0x20u
#define DLY_T 0
#define TC0 0
#define PLAN 1
#define TD23 0x74704240u
#define TD89 0x60503010ull
#define TM89 0x01010101ull
#define TX0 1
#define KVS 8
constexpr int SY_FLAGS = 680;
constexpr int SY_WORDS = SY_FLAGS + SY_FLAGS / 8;
#define XA_FAST 6
#define XA_D 12
#define SQ_FAST 49
#define SQ_SF 4
#define SSEGV 40
constexpr int SSEG = SSEGV;
typedef __attribute__((address_space(1))) unsigned gu32;
typedef __attribute__((address_space(1))) unsigned long long gu64;
__device__ __forceinline__ unsigned ld_flag(const unsigned* p) { return __hip_atomic_load((gu32*)p, __ATOMIC_RELAXED, __HIP_MEMORY_SCOPE_AGENT); }
__device__ __forceinline__ unsigned long long boot_tag(const unsigned long long* boot) {
    return __hip_atomic_load((gu64*)(boot + 144), __ATOMIC_RELAXED, __HIP_MEMORY_SCOPE_AGENT) + 1ull;
}
__device__ __forceinline__ unsigned long long boot_wait(const unsigned long long* boot) {
    const int l = TIDX & 63;
    unsigned long long tag = 0, x = 0, y = 0;
#pragma nounroll
    for (int spin = 0; spin < 100000; ++spin) {
        asm volatile("" ::: "memory");
        x = __hip_atomic_load((gu64*)(boot + l), __ATOMIC_RELAXED, __HIP_MEMORY_SCOPE_AGENT);
        y = __hip_atomic_load((gu64*)(boot + 64 + l), __ATOMIC_RELAXED, __HIP_MEMORY_SCOPE_AGENT);
        tag = boot_tag(boot);
        if (__all((x | 3u) == ((tag << 2) | 3u) && (y | 3u) == ((tag << 2) | 3u))) break;
        __builtin_amdgcn_s_sleep(16);
    }
    asm volatile("" ::: "memory");
    const unsigned long long side = __ballot(x & 1u);
    const unsigned sd = (unsigned)(side & 1u) | (unsigned)((side >> 15) & 2u) | (unsigned)((side >> 30) & 4u) | (unsigned)((side >> 45) & 8u);
    const unsigned m = (sd ^ (__popcll(__ballot(x & 2u)) + __popcll(__ballot(y & 2u)) > 64 ? 15u : 0u)) & 15u;
    return (tag << 8) | m | ((~m & 15u) << 4);
}
template <int SL>
__device__ __forceinline__ void wait_ge(const unsigned* p, unsigned need, unsigned* tmo, unsigned code) {
#pragma nounroll
    for (int spin = 0;; ++spin) {
        asm volatile("" ::: "memory");
        if (__all(ld_flag(p) >= need)) break;
        if (spin > 400000 / SL) { __hip_atomic_store((gu32*)tmo, code, __ATOMIC_RELAXED, __HIP_MEMORY_SCOPE_AGENT); break; }
        __builtin_amdgcn_s_sleep(SL);
    }
    asm volatile("" ::: "memory");
}
template <int SL>
__device__ __forceinline__ void wg_wait_acquire(const unsigned* p, unsigned* tmo, unsigned code) {
    if (TIDX < 64) {
        wait_ge<SL>(p, 1u, tmo, code);
        __builtin_amdgcn_fence(__ATOMIC_ACQUIRE, "agent");
        asm volatile("s_waitcnt vmcnt(0)" ::: "memory");
    }
    __syncthreads();
}
__device__ __forceinline__ void arrive(unsigned* sync, const int f, const unsigned need, const int n) {
    const int l = lane_now();
    unsigned old = 0;
    if (l == 0) old = __hip_atomic_fetch_add((gu32*)(sync + (SY_FLAGS + (f >> 3)) * 32), 1u, __ATOMIC_RELAXED, __HIP_MEMORY_SCOPE_AGENT);
    if (__builtin_amdgcn_readfirstlane(old) == need - 1u && l < n) __hip_atomic_store((gu32*)(sync + (f + l) * 32), 16u, __ATOMIC_RELAXED, __HIP_MEMORY_SCOPE_AGENT);
}
__device__ __forceinline__ void wg_publish(unsigned* sync, const int f, const unsigned need, const int n) {
    asm volatile("s_waitcnt vmcnt(0)" ::: "memory");
    __syncthreads();
    if (TIDX < 64) arrive(sync, f, need, n);
}
#define TMO_SET(code) { dead = true; if (l == 0) __hip_atomic_store(a.tmo, (code) + s, __ATOMIC_RELAXED, __HIP_MEMORY_SCOPE_AGENT); }

__device__ __forceinline__ void glds16h(unsigned voff, const void* sbase, unsigned lds_dst) {
    unsigned keep;
    asm volatile("s_mov_b32 %0, m0\n\ts_mov_b32 m0, %3\n\ts_nop 0\n\tglobal_load_lds_dwordx4 %1, %2 sc1\n\ts_mov_b32 m0, %0" : "=&s"(keep) : "v"(voff), "s"(sbase), "s"(lds_dst) : "memory");
}
__device__ __forceinline__ void touch(const void* p, unsigned lds_dst) {
    unsigned keep;
    asm volatile("s_mov_b32 %0, m0\n\ts_mov_b32 m0, %2\n\ts_nop 0\n\tglobal_load_lds_dword %1, off\n\ts_mov_b32 m0, %0" : "=&s"(keep) : "v"(p), "s"(lds_dst) : "memory");
}
__device__ __forceinline__ void touch_sc1(const void* p, unsigned lds_dst) {
    unsigned keep;
    asm volatile("s_mov_b32 %0, m0\n\ts_mov_b32 m0, %2\n\ts_nop 0\n\tglobal_load_lds_dword %1, off sc1\n\ts_mov_b32 m0, %0" : "=&s"(keep) : "v"(p), "s"(lds_dst) : "memory");
}
#define WEV(code)
#define WEVP
#define WEVA
constexpr int C2_K = 32768;
constexpr int C2_V = 65536;
constexpr int C2_R1 = 0, R1S = 3200;
constexpr int C2_R3 = C2_R1 + 4 * R1S;
constexpr int C2_MB = 24576;
#define PST(n)
#define LT(n)
#define LTA(n)
#define LTOUT0
#define LTOUT
#define CN(n)
#define CNS
#define CNOUT
#define CLOB16(a, b, c, d, e, f, g, h, i, j, k, l, m, n, o, p) "v" #a, "v" #b, "v" #c, "v" #d, "v" #e, "v" #f, "v" #g, "v" #h, "v" #i, "v" #j, "v" #k, "v" #l, "v" #m, "v" #n, "v" #o, "v" #p
#define LDS3(p) ((__attribute__((address_space(3))) v4i16*)(p))
__device__ __forceinline__ bf16x8 tr_frag(const void* p0, const void* p1) {
    v4i16 lo = __builtin_amdgcn_ds_read_tr16_b64_v4i16(LDS3(p0));
    v4i16 hi = __builtin_amdgcn_ds_read_tr16_b64_v4i16(LDS3(p1));
    return bf16x8{lo[0], lo[1], lo[2], lo[3], hi[0], hi[1], hi[2], hi[3]};
}
typedef __bf16 bf16x8v __attribute__((ext_vector_type(8)));
typedef __bf16 bf16x16v __attribute__((ext_vector_type(16)));
typedef unsigned v8u __attribute__((ext_vector_type(8)));
typedef unsigned v32u __attribute__((ext_vector_type(32)));
#define PUT8(v, o, x) { const v8u x_ = (x); _Pragma("unroll") for (int e_ = 0; e_ < 8; ++e_) (v)[(o) + e_] = x_[e_]; }
#define SUB4(v, o) v4u{(v)[(o)], (v)[(o) + 1], (v)[(o) + 2], (v)[(o) + 3]}
#define PUT4(v, o, x) { const v4u x_ = (x); (v)[(o)] = x_[0]; (v)[(o) + 1] = x_[1]; (v)[(o) + 2] = x_[2]; (v)[(o) + 3] = x_[3]; }
__device__ __forceinline__ v2u tr4(const u16* p) { return __builtin_bit_cast(v2u, __builtin_amdgcn_ds_read_tr16_b64_v4i16(LDS3(p))); }
__device__ __forceinline__ f32x4 smm(v4u a, v8u b, f32x4 c, int idx) {
    return __builtin_amdgcn_smfmac_f32_16x16x64_bf16(__builtin_bit_cast(bf16x8v, a), __builtin_bit_cast(bf16x16v, b), c, idx, 0, 0);
}
__device__ __forceinline__ v8u tr_frag64(const u16* p, const int pitch) {
    const v2u r0 = tr4(p), r1 = tr4(p + 8 * pitch), r2 = tr4(p + 32 * pitch), r3 = tr4(p + 40 * pitch);
    return v8u{r0[0], r1[0], r0[1], r1[1], r2[0], r3[0], r2[1], r3[1]};
}
#define MM(a_, b_, c_) __builtin_amdgcn_mfma_f32_16x16x32_bf16(as_frag(a_), as_frag(b_), c_, 0, 0, 0)
#define STR2(x) #x
#define STR(x) STR2(x)
#define PAUSE(n) ".rept (" STR(n) ") / 4\n\ts_nop 15\n\t.endr\n\t.rept (" STR(n) ") %% 4\n\ts_nop 3\n\t.endr\n\t"
#define ZERO4 f32x4{0.f, 0.f, 0.f, 0.f}
constexpr int XB_OFF = CAL_OFF + 262144, XP = 68;
__device__ __forceinline__ void last_rows(const char* chsm, rsrc_t rsS, const int wu, const int grp, const int c, const unsigned tau) {
    const int l = lane_now();
    __builtin_amdgcn_s_barrier();
    asm volatile("" ::: "memory");
    const float* rp = (const float*)(chsm + C2_K) + (4 * wu + (l >> 4)) * XP + 4 * (l & 15);
    const f32x4 s0 = (*(const f32x4*)rp + *(const f32x4*)(rp + 32 * XP)) + (*(const f32x4*)(rp + 64 * XP) + *(const f32x4*)(rp + 96 * XP));
    v2u o;
    o[0] = cvt_pk_bf16(s0[0], s0[1]); o[1] = cvt_pk_bf16(s0[2], s0[3]);
    st8_sc1s(rsS, l * 8, XB_OFF + ((c >> 1) * 8 + wu) * 16384 + (int)(tau & 1u) * 8192 + (c & 1) * 4096 + grp * 512, o);
}
__device__ __forceinline__ const unsigned* kv_flag(const unsigned* sync, const int sq, const int grp, const int l) { return sync + (sq < KVS ? (SY_KV0 + sq * 8 + grp) * 32 + (l & 7) : (SY_KV + 8 + grp) * 32 + (l & 3)); }
__device__ __forceinline__ void chain_body(const ChainArgs& a, const int grp, const int c, char* chsm) {
    const int wu = __builtin_amdgcn_readfirstlane(TIDX >> 6);
    const int widx = grp * 16 + c;
    const int hs = 4 * grp + (c >> 2), jq = c & 3;
    const rsrc_t rsS = slot_rsrc(a.slots);
    const unsigned l0 = (unsigned)(uintptr_t)(__attribute__((address_space(3))) char*)chsm;
    bool dead = false;

    const int cal_off = CAL_OFF + grp * 32768 + (c >> 3) * 16384 + (c & 7) * 1024;
        if (wu == 0) {
            const int l = lane_now();
            const v4u ff = {SENT, SENT, SENT, SENT};
#pragma unroll
            for (int e = 0; e < 13; ++e) st_sc1s(rsS, l * 16, widx * 2 * PERWG + min(e * 1024, 2 * PERWG - 1024), ff);
            const rsrc_t rsY = __builtin_amdgcn_make_buffer_rsrc((void*)a.sync, 0, 131072, 0x00020000);
            st_sc1s(rsY, l * 16, widx * 1024, v4u{0u, 0u, 0u, 0u});
            __builtin_amdgcn_raw_buffer_store_b32(0u, rsS, (l & 3) * 256 + ((l >> 2) & 1) * 8192, cal_off, AUX_SC1);
            if (c < 2) __builtin_amdgcn_raw_buffer_store_b32(0u, rsS, (l >> 5) * 8192 + (l & 31) * 4, TM_OFF + c * 16384 + grp * 256, AUX_SC1);
        }
    asm volatile("" ::: "memory");
    unsigned long long dg0 = __builtin_amdgcn_s_memrealtime(), dg1 = 0;
    if (wu >= 4) {
        __builtin_amdgcn_s_sleep(DLY_U);
        const int l = opaque_tid() & 63, i = l & 15, g = l >> 4;
        const int sidx = (i & 1) ? 0xEEEE : 0x4444;
        const int u = wu - 4;
        const int k0 = 256 * u;
        u16* stg = (u16*)(chsm + 40960 + u * 20480);
        v4u af[4];
        v32u wS[6];
        {
            v4u raw[3][16];
            {
                const size_t wo = (size_t)k0 * HD + hcol(hs) + 32 * jq;
                const rsrc_t rq = __builtin_amdgcn_make_buffer_rsrc((void*)(a.Wq + wo), 0, 256 * HD * 2, 0x00020000);
                const rsrc_t rk = __builtin_amdgcn_make_buffer_rsrc((void*)(a.Wk + wo), 0, 256 * HD * 2, 0x00020000);
                const rsrc_t rv = __builtin_amdgcn_make_buffer_rsrc((void*)(a.Wv + wo), 0, 256 * HD * 2, 0x00020000);
                const int vw = (l >> 2) * HD * 2 + (l & 3) * 16;
#pragma unroll
                for (int p = 0; p < 4; ++p) af[p] = *(const v4u*)(a.mem0 + (size_t)(i >> 1) * DM + k0 + 64 * p + 16 * g + 8 * (i & 1));
#pragma unroll
                for (int e = 0; e < 16; ++e) raw[0][e] = __builtin_amdgcn_raw_buffer_load_b128(rq, vw, e * 16 * HD * 2, 0);
#pragma unroll
                for (int e = 0; e < 16; ++e) raw[1][e] = __builtin_amdgcn_raw_buffer_load_b128(rk, vw, e * 16 * HD * 2, 0);
#pragma unroll
                for (int e = 0; e < 12; ++e) raw[2][e] = __builtin_amdgcn_raw_buffer_load_b128(rv, vw, e * 16 * HD * 2, 0);
                __builtin_amdgcn_sched_barrier(0);
#pragma unroll
                for (int e = 0; e < 16; ++e) *(v4u*)(stg + (16 * e + (l >> 2)) * 40 + 8 * (l & 3)) = raw[0][e];
                __builtin_amdgcn_sched_barrier(0);
#pragma unroll
                for (int e = 12; e < 16; ++e) raw[2][e] = __builtin_amdgcn_raw_buffer_load_b128(rv, vw, e * 16 * HD * 2, 0);
            }
            const int fr = 16 * (g >> 1) + 4 * (g & 1) + (i >> 2);
            __builtin_amdgcn_sched_barrier(0);
#pragma unroll
            for (int hx = 0; hx < 3; ++hx) {
                if (hx) {
#pragma unroll
                    for (int e = 0; e < 16; ++e) *(v4u*)(stg + (16 * e + (l >> 2)) * 40 + 8 * (l & 3)) = raw[hx][e];
                }
                __builtin_amdgcn_s_waitcnt(0xC07F);
#pragma unroll
                for (int jj = 0; jj < 2; ++jj)
#pragma unroll
                    for (int p = 0; p < 4; ++p) PUT8(wS[2 * hx + jj], 8 * p, tr_frag64(stg + (64 * p + fr) * 40 + 16 * jj + 4 * (i & 3), 40))
                __builtin_amdgcn_s_waitcnt(0xC07F);
                __builtin_amdgcn_sched_barrier(0);
            }
        }
        PST(u)
        if (u == 0) { PST(4) }
        const unsigned r1w = l0 + C2_R1 + u * R1S + (2 * g * 16 + i) * 4, r1v = l0 + C2_R1 + u * R1S + 2176 + (2 * g * 16 + ((i & 9) | ((i & 2) << 1) | ((i & 4) >> 1))) * 4;
#define LD4(d, va, so, o) "buffer_load_dwordx4 v[" d "], " va ", %[rs], " so " offen offset:" o " sc1\n\t"
#define CHK(a, b, c, d) "v_max3_u32 v244, v244, v" a ", v" b "\n\tv_max3_u32 v244, v244, v" c ", v" d "\n\t"
#define CHK0(a, b, c, d) "v_max_u32 v244, v" a ", v" b "\n\tv_max3_u32 v244, v244, v" c ", v" d "\n\t"
#define SMF(d, a, w) "v_smfmac_f32_16x16x64_bf16 v[" d "], v[" a "], v[" w "], %[sx]\n\t"
#define ADD(d, a, b) "v_add_f32 v" d ", v" a ", v" b "\n\t"
#define Z2(r) "v_mov_b64 v[" r "], 0\n\t"
#define DW2(ad, x, y, o0, o1) "ds_write2_b32 " ad ", v" x ", v" y " offset0:" o0 " offset1:" o1 "\n\t"
#define A0 "224:227"
#define A1 "228:231"
#define A2 "232:235"
#define A3 "236:239"
#define Q0 "200:203"
#define Q1 "204:207"
#define Q2 "208:211"
#define Q3 "212:215"
#define V0 "216:219"
#define V1 "220:223"
#define S1_HEAD \
            Z2("216:217") Z2("218:219") Z2("220:221") Z2("222:223") \
            "s_waitcnt vmcnt(4)\n\t" ZQ \
            "s_waitcnt vmcnt(3)\n\t" SMF(V0, A0, "136:143") CHK0("224", "225", "226", "227") SMF(V1, A0, "168:175") \
            "s_waitcnt vmcnt(2)\n\t" SMF(V0, A1, "144:151") CHK("228", "229", "230", "231") SMF(V1, A1, "176:183") \
            "s_waitcnt vmcnt(1)\n\t" SMF(V0, A2, "152:159") CHK("232", "233", "234", "235") SMF(V1, A2, "184:191") \
            "s_waitcnt vmcnt(0)\n\t" SMF(V0, A3, "160:167") CHK("236", "237", "238", "239") SMF(V1, A3, "192:199") \
            "v_cmp_eq_u32 vcc, " STR(SENT) ", v244\n\t"
#define S1_TAIL(B1, B2) \
            SMF(Q0, A0, "8:15") SMF(Q0, A1, "16:23") SMF(Q0, A2, "24:31") \
            ADD("240", "216", "217") ADD("241", "218", "219") SMF(Q0, A3, "32:39") DW2("%[rv]", "240", "241", "0", "16") \
            ADD("242", "220", "221") ADD("243", "222", "223") SMF(Q1, A0, "40:47") DW2("%[rv]", "242", "243", "128", "144") \
            SMF(Q1, A1, "48:55") SMF(Q1, A2, "56:63") SMF(Q1, A3, "64:71") \
            ADD("240", "200", "201") ADD("241", "202", "203") SMF(Q2, A0, "72:79") DW2("%[rw]", "240", "241", "0", "16") \
            SMF(Q2, A1, "80:87") SMF(Q2, A2, "88:95") "s_nop 1\n\t" \
            ADD("242", "204", "205") ADD("243", "206", "207") SMF(Q2, A3, "96:103") DW2("%[rw]", "242", "243", "136", "152") \
            SMF(Q3, A0, "104:111") SMF(Q3, A1, "112:119") \
            B1        \
            LTA(0) \
            SMF(Q3, A2, "120:127") SMF(Q3, A3, "128:135") "s_nop 0\n\t" \
            ADD("240", "208", "209") ADD("241", "210", "211") DW2("v246", "240", "241", "0", "16") \
            "s_nop 3\n\t" \
            ADD("242", "212", "213") ADD("243", "214", "215") DW2("v246", "242", "243", "136", "152") \
            "s_waitcnt lgkmcnt(0)\n\t" B2 "s_barrier\n\t"
#define ZQ Z2("200:201") Z2("202:203") Z2("204:205") Z2("206:207") Z2("208:209") Z2("210:211") Z2("212:213") Z2("214:215")
#define U_W "{v[8:39]}"(wS[0]), "{v[40:71]}"(wS[1]), "{v[72:103]}"(wS[2]), "{v[104:135]}"(wS[3]), "{v[136:167]}"(wS[4]), "{v[168:199]}"(wS[5])
#define U_CLOB CLOB16(200, 201, 202, 203, 204, 205, 206, 207, 208, 209, 210, 211, 212, 213, 214, 215), CLOB16(216, 217, 218, 219, 220, 221, 222, 223, 224, 225, 226, 227, 228, 229, 230, 231), \
            CLOB16(232, 233, 234, 235, 236, 237, 238, 239, 240, 241, 242, 243, 244, 245, 246, 247)
        __builtin_amdgcn_s_setprio(2);
        {
            asm volatile(
                "v_add_u32 v246, 1088, %[rw]\n\t"
                S1_HEAD "s_nop 0\n\t" S1_TAIL("s_nop 1\n\t", "s_barrier\n\t")
                : LTOUT0
                : U_W, "{v[224:227]}"(af[0]), "{v[228:231]}"(af[1]), "{v[232:235]}"(af[2]), "{v[236:239]}"(af[3]), [sx] "v"(sidx), [rv] "v"(r1v), [rw] "v"(r1w)
                : "memory", "vcc", CLOB16(200, 201, 202, 203, 204, 205, 206, 207, 208, 209, 210, 211, 212, 213, 214, 215), CLOB16(216, 217, 218, 219, 220, 221, 222, 223, 240, 241, 242, 243, 244, 245, 246, 247));
        }
        const unsigned tau = __builtin_amdgcn_readfirstlane(*(volatile unsigned*)(chsm + C2_MB + 8));
        const unsigned long long wmask = u == 3 ? (c == 0 ? ~0ull : 0ull) : u == 1 ? 0x0020202020202000ull : u == 2 ? 1ull << (NSEG - 3) : 0ull;
        const int pvv = u == 3 ? 2 : -1, pvm = u == 3 ? -1 : 0;
        const unsigned* pfl = a.sync + (u == 3 ? SY_PROG + grp : u == 1 ? SY_KV + 16 + grp : SY_Q + 7 * 8 + grp) * 32;
        int s_first = 0;
        asm volatile("s_branch 1f\n\t.p2align 8\n1:" : "+s"(s_first));
        for (int s = s_first; s < NSEG - 1; ++s) {
            __builtin_amdgcn_s_setprio(0);
            if (s == -1) dg0 = __builtin_amdgcn_s_memrealtime();
            if (s == 2) dg1 = __builtin_amdgcn_s_memrealtime();
            LT(1)
            const int pv = (wmask >> s) & 1ull ? pvv + (s & pvm) : 0;
            const unsigned* pa = pfl;
            if (pv < 0) pfl += 256;
            const int cb = grp & 3;
            int soffc = rec_base(s) + C_OFF + c * 16384 + (int)((half_bits(s, tau) >> grp) & 1u) * 8192 + cb * 2048;
            int o_as = rec_base(s + 1) + ((c >> 3) + (grp >> 2) * 4) * 16384 + (int)((half_bits(s + 1, tau) >> grp) & 1u) * 8192 + ((4 * c + cb) & 31) * 256;
            int soff = chunk_off(s + 1, grp, u >> 1, tau) + (u & 1) * 4096;
            const int o_tm = (s >> 1) * 2097152 + TM_OFF + (s & 1) * 16384;
            int lim = dead ? 0 : SPIN_LIMIT, bad = 0;
            const bool rcd = u > 0 || widx < 32;
            const int pc = u > 0 ? 3 * widx + u - 1 : rcd ? 384 + widx : s * 96 + widx - 32;
            const int o_arm = (rcd ? s + 2 < NSEG : pc < 512) ? (rcd ? rec_base(s + 2) : XB_OFF) + (pc >> 3) * 16384 + (int)(half_bits(rcd ? s + 2 : 0, tau) & 1u) * 8192 + (pc & 7) * 1024 : OOB;
            bool there = true;
            if (__builtin_expect(s <= KVS, 0)) {
                there = __all(ld_flag(kv_flag(a.sync, s + 2, grp, l)) != 0u);
                *(volatile unsigned*)(chsm + C2_MB + 32 + (s & 1) * 16 + u * 4) = there ? 1u : 0u;
                __builtin_amdgcn_s_waitcnt(0xC07F);
            }
            const char* vb = !there ? nullptr : arena(a.slots) + (s + 2 >= NSEG ? KX_OFF : VX_OFF) + (blk_off(min(s + 2, NSEG - 1), 4 * grp + 2 * u - 2, tau) ^ (s + 2 >= NSEG ? 8192 : 0));
            asm volatile("" : "+s"(soffc), "+s"(o_as), "+s"(soff), "+s"(lim), "+s"(vb));
#define GL(o) "global_load_lds_dwordx4 %[l16], s[84:85] offset:" o " sc1\n\t" PAUSE(PGP)
#define GL4 GL("0") GL("1024") GL("2048") GL("3072")
#define GLN(d) "s_add_u32 m0, m0, 4096\n\ts_add_u32 s84, s84, " d "\n\ts_addc_u32 s85, s85, 0\n\t"
#define PKE PAUSE(PKA)
#define C_TRY(x, x0, x1, x2, x3, y, y0, y1, y2, y3) \
                "s_waitcnt vmcnt(3)\n\t" \
                "v_mfma_f32_16x16x32_bf16 v[240:243], v[216:219], v[" x "], 0\n\t" CHK0(x0, x1, x2, x3) \
                "s_waitcnt vmcnt(2)\n\t" \
                "v_mfma_f32_16x16x32_bf16 v[240:243], v[216:219], v[" y "], v[240:243]\n\t" CHK(y0, y1, y2, y3) \
                "v_cmp_eq_u32 vcc, " STR(SENT) ", v244\n\t" \
                "s_cbranch_vccz 3b\n\t" CN(1) \
                LD4(x, "%[l16]", "%[sc]", "0") LD4(y, "%[l16]", "%[sc]", "1024")
            asm volatile(
                "v_and_b32 v221, 16, %[l16]\n\t"
                "v_mov_b32 v222, 0x3f80\n\t"
                "v_bfe_u32 v223, %[l16], 5, 2\n\t"
                "v_lshlrev_b32 v221, v221, v222\n\t"
                "v_cmp_eq_u32 vcc, 0, v223\n\tv_and_b32 v220, 0xf0, %[l16]\n\tv_bfe_u32 v222, %[l16], 8, 1\n\tv_cndmask_b32 v216, 0, v221, vcc\n\t"
                "v_cmp_eq_u32 vcc, 1, v223\n\tv_lshl_add_u32 v220, v222, 3, v220\n\tv_lshrrev_b32 v222, 9, %[l16]\n\tv_cndmask_b32 v217, 0, v221, vcc\n\t"
                "v_cmp_eq_u32 vcc, 2, v223\n\tv_lshl_add_u32 v220, v222, 30, v220\n\ts_nop 0\n\tv_cndmask_b32 v218, 0, v221, vcc\n\t"
                "v_cmp_eq_u32 vcc, 3, v223\n\ts_nop 1\n\tv_cndmask_b32 v219, 0, v221, vcc\n\t"
                "v_mul_u32_u24 v222, 0x8000, v222\n\t"
                "v_and_b32 v221, 0xff, v220\n\t"
                "v_add_u32 v221, v221, v222\n\t"
                "v_lshrrev_b32 v247, 2, %[l16]\n\t"
                PKE
                "v_mov_b32 v240, " STR(SENT) "\n\tv_mov_b32 v241, v240\n\tv_mov_b32 v242, v240\n\tv_mov_b32 v243, v240\n\t"
                "buffer_store_dwordx4 v[240:243], %[l16], %[rs], %[arm] offen sc1\n\t"
                LTA(2)
                "s_setprio 2\n\t"
                "s_barrier\n\t"
                LTA(3)
                "v_add_u32 v246, 1088, %[rw]\n\t"
                "s_cmp_lt_u32 %[uu], 1\n\t"
                "s_cbranch_scc1 7f\n\t"
                "s_cmp_eq_u32 %[uu], 3\n\t"
                "s_cbranch_scc1 20f\n\t"
                "s_memrealtime s[84:85]\n\t"
                "v_lshrrev_b32 v241, 9, %[l16]\n\t"
                "v_and_b32 v242, 0x7c, v247\n\t"
                "v_lshl_add_u32 v241, v241, 13, v242\n\t"
                "s_waitcnt lgkmcnt(0)\n\t"
                "v_mov_b32 v240, s84\n\t"
                "buffer_store_dword v240, v241, %[rs], %[ts] offen sc1\n\t"
                "s_cmp_eq_u64 %[vb], 0\n\t"
                "s_cbranch_scc1 11f\n\t"
                "s_mov_b32 s86, m0\n\t"
                "s_mov_b64 s[84:85], %[vb]\n\t"
                "s_mov_b32 m0, %[lv]\n\t"
                PAUSE(PG0)
                "s_cmp_eq_u32 %[uu], 1\n\t"
                "s_cbranch_scc1 12f\n\t"
                PAUSE(PG1)
                "12:\n\t"
                GL4 GLN("4096") GL4 GLN("12288") GL4 GLN("4096") GL4
                "s_mov_b32 m0, s86\n\t"
                "11:\n\t"
                "s_cmp_lt_i32 %[pv], 0\n\t"
                "s_cbranch_scc0 8f\n\t"
                "v_and_b32 v241, 12, v247\n\t"
                "10:\n\t"
                "global_load_dword v240, v241, %[pa] sc1\n\t"
                "s_waitcnt vmcnt(0)\n\t"
                "v_cmp_gt_u32 vcc, 16, v240\n\t"
                "s_cbranch_vccz 8f\n\t"
                "s_sleep 6\n\t"
                "s_sub_u32 %[lim], %[lim], 1\n\t"
                "s_cbranch_scc0 10b\n\t"
                "s_mov_b32 %[bad], 3000\n\t"
                "s_mov_b32 %[lim], 0\n\t"
                "s_branch 8f\n\t"
                "20:\n\t"
                PAUSE(PS)
                "s_memrealtime s[84:85]\n\t"
                "s_load_dword s86, %[tb], 0\n\ts_load_dword s87, %[tb], 0x100\n\ts_load_dword s88, %[tb], 0x200\n\ts_load_dword s89, %[tb], 0x300\n\t"
                "s_load_dword s90, %[tb], 0x400\n\ts_load_dword s91, %[tb], 0x500\n\ts_load_dword s92, %[tb], 0x600\n\ts_load_dword s93, %[tb], 0x700\n\t"
                "s_waitcnt lgkmcnt(0)\n\t"
                "s_sub_u32 s86, s84, s86\n\ts_sub_u32 s87, s84, s87\n\ts_sub_u32 s88, s84, s88\n\ts_sub_u32 s89, s84, s89\n\t"
                "s_sub_u32 s90, s84, s90\n\ts_sub_u32 s91, s84, s91\n\ts_sub_u32 s92, s84, s92\n\ts_sub_u32 s93, s84, s93\n\t"
                "s_or_b32 s85, s86, s87\n\ts_min_u32 s86, s86, s87\n\ts_or_b32 s85, s85, s88\n\ts_min_u32 s86, s86, s88\n\ts_or_b32 s85, s85, s89\n\ts_min_u32 s86, s86, s89\n\t"
                "s_memrealtime s[88:89]\n\t"
                "s_or_b32 s85, s85, s90\n\ts_min_u32 s86, s86, s90\n\ts_or_b32 s85, s85, s91\n\ts_min_u32 s86, s86, s91\n\ts_or_b32 s85, s85, s92\n\ts_min_u32 s86, s86, s92\n\t"
                "s_or_b32 s85, s85, s93\n\ts_min_u32 s86, s86, s93\n\t"
                "s_cmp_gt_u32 s85, 127\n\t"
                CNS
                "s_cselect_b32 s85, " STR(TE0) ", 127\n\t"
                "s_min_u32 s86, s86, s85\n\t"
                "s_sub_u32 s84, s84, s86\n\t"
                "s_add_u32 s84, s84, %[td]\n\t"
                "s_cmp_lt_i32 %[pv], 1\n\t"
                "s_cbranch_scc1 4f\n\t"
                "v_mov_b32 v247, %[pv]\n\t"
                "s_mov_b64 exec, 1\n\t"
                "global_store_dword %[l16], v247, %[pa] sc1\n\t"
                "s_mov_b64 exec, -1\n\t"
                "4:\n\t"
                "s_waitcnt lgkmcnt(0)\n\t"
                "s_sub_i32 s84, s84, s88\n\t"
                "s_max_i32 s84, s84, 0\n\t"
                "s_min_i32 s84, s84, 40\n\t"
                "21:\n\t"
                "s_sub_u32 s84, s84, 1\n\t"
                "s_cbranch_scc0 21b\n\t"
                "s_branch 8f\n\t"
                "7:\n\t"
                PAUSE(PC)
                LD4(A0, "%[l16]", "%[sc]", "0") LD4(A1, "%[l16]", "%[sc]", "1024")
                PAUSE(PP)
                LD4(A2, "%[l16]", "%[sc]", "0") LD4(A3, "%[l16]", "%[sc]", "1024")
                "s_waitcnt vmcnt(3)\n\t"
                "v_mfma_f32_16x16x32_bf16 v[240:243], v[216:219], v[224:227], 0\n\t" CHK0("224", "225", "226", "227")
                "s_waitcnt vmcnt(2)\n\t"
                "v_mfma_f32_16x16x32_bf16 v[240:243], v[216:219], v[228:231], v[240:243]\n\t" CHK("228", "229", "230", "231")
                "v_cmp_eq_u32 vcc, " STR(SENT) ", v244\n\t"
                "s_cbranch_vccnz 30f\n\t"
                "3:\n\t"
                "s_nop 3\n\t"
                "v_cvt_pk_bf16_f32 v244, v240, v241\n\t"
                "v_cvt_pk_bf16_f32 v245, v242, v243\n\t"
                "buffer_store_dwordx2 v[244:245], v221, %[rs], %[oas] offen sc1\n\t"
                PAUSE(PB)
                "8:\n\t"
                LTA(4)
                "s_barrier\n\t"
                PAUSE(1)
                "2:\n\t"
                LD4(A0, "%[l16]", "%[sa]", "0") LD4(A1, "%[l16]", "%[sa]", "1024") LD4(A2, "%[l16]", "%[sa]", "2048") LD4(A3, "%[l16]", "%[sa]", "3072")
                S1_HEAD
                "s_cbranch_vccnz 6f\n\t"
                "5:\n\t"
                LTA(5)
                S1_TAIL("s_waitcnt lgkmcnt(0)\n\ts_barrier\n\t", "")
                "s_setprio 0\n\t"
                "s_branch 9f\n\t"
                "30:\n\t" CN(1)
                LD4(A0, "%[l16]", "%[sc]", "0") LD4(A1, "%[l16]", "%[sc]", "1024")
                "1:\n\t"
                C_TRY(A2, "232", "233", "234", "235", A3, "236", "237", "238", "239")
                C_TRY(A0, "224", "225", "226", "227", A1, "228", "229", "230", "231")
                "s_sub_u32 %[lim], %[lim], 1\n\t"
                "s_cbranch_scc0 1b\n\t"
                "s_mov_b32 %[bad], 2000\n\t"
                "s_mov_b32 %[lim], 0\n\t"
                "s_branch 3b\n\t"
                "6:\n\t" CN(65536)
                "v_mov_b32 v240, %[tg]\n\t"
                "v_mov_b32 v241, %[lf]\n\t"
                "ds_write_b32 v241, v240\n\t"
                "s_nop 0\n\t"
                "s_sub_u32 %[lim], %[lim], 1\n\t"
                "s_cbranch_scc0 2b\n\t"
                "s_mov_b32 %[bad], 1\n\t"
                "s_branch 5b\n\t"
                "9:\n\t"
                : [lim] "+s"(lim), [bad] "+s"(bad), [oas] "+s"(o_as) LTOUT CNOUT
                : U_W, [l16] "v"(l * 16), [rs] "s"(rsS), [sx] "v"(sidx), [rv] "v"(r1v), [rw] "v"(r1w),
                  [sc] "s"(soffc), [sa] "s"(soff), [uu] "s"(u), [pv] "s"(pv), [pa] "s"(pa), [vb] "s"(vb), [lv] "s"(l0 + (s + 2 >= NSEG ? C2_V + 32768 : s & 1 ? C2_K : C2_V) + (u - 1) * 16384), [td] "s"(s >= SL0 ? TD2 : TD), [ts] "s"(c == 0 && (u == 2 || s < NSEG - 3) ? o_tm + grp * 256 + (2 - u) * 2097152 : OOB), [tb] "s"(arena(a.slots) + o_tm + ((half_bits(s, tau) >> grp) & 1u) * 8192),

                  [arm] "s"(o_arm)
,
                  [tg] "s"(s + 1), [lf] "s"(l0 + C2_MB + 16)
                : "memory", "vcc", "scc", "s84", "s85", "s86", "s87", "s88", "s89", "s90", "s91", "s92", "s93", U_CLOB);
            if (__builtin_expect(bad != 0, 0)) TMO_SET((unsigned)bad)
        }
        last_rows(chsm, rsS, wu, grp, c, tau);
        if (u == 2 && widx == 5) { const unsigned long long te = __builtin_amdgcn_s_memrealtime() + 2000 + 0 * (dg1 - dg0); while (__builtin_amdgcn_s_memrealtime() < te) __builtin_amdgcn_s_sleep(4); }
        if (u == 3 && widx == 0 && l == 0) __hip_atomic_store((gu64*)(a.boot + 144), *(volatile unsigned long long*)(chsm + C2_MB), __ATOMIC_RELAXED, __HIP_MEMORY_SCOPE_AGENT);
        return;
    }
    const int l = opaque_tid() & 63, i = l & 15, g = l >> 4;
    const int sidx = (i & 1) ? 0xEEEE : 0x4444;
    const int ha = 4 * grp + wu;
    unsigned long long tag;
    v32u wO[2];
    {
        u16* stg = (u16*)(chsm + 122880 + wu * 10240);
        tag = boot_tag(a.boot);
        asm volatile("" ::: "memory");
        v4u rawo[16];
        const u16* src = a.Wo + (size_t)(hcol(ha) + (l >> 3)) * DM + 64 * c + 8 * (l & 7);
#pragma unroll
        for (int e = 0; e < 16; ++e) rawo[e] = *(const v4u*)(src + (size_t)8 * e * DM);
        __builtin_amdgcn_sched_barrier(0);
        if (wu == 0) {
            asm volatile("s_waitcnt vmcnt(16)" ::: "memory");
            unsigned tm[2] = {~0u, ~0u};
            const char* pb = arena(a.slots) + cal_off;
#pragma unroll
            for (int k = 0; k < 2 * NPROBE; ++k) {
                unsigned long long c0, c1; unsigned d;
                asm volatile("s_memtime %0\n\ts_waitcnt lgkmcnt(0)\n\ts_load_dword %2, %3, %4\n\ts_waitcnt lgkmcnt(0)\n\ts_memtime %1\n\ts_waitcnt lgkmcnt(0)"
                             : "=&s"(c0), "=&s"(c1), "=&s"(d) : "s"(pb), "i"((k >> 1) * 256 + (k & 1) * 8192) : "memory");
                tm[k & 1] = min(tm[k & 1], (unsigned)(c1 - c0));
            }
            const unsigned side = (__builtin_amdgcn_s_getreg(20 | (3 << 11)) >> 2) & 1u;
            if (l == 0) __hip_atomic_store((gu64*)(a.boot + widx), (tag << 2) | (((tm[0] > tm[1] ? 1u : 0u) ^ side) << 1) | side, __ATOMIC_RELAXED, __HIP_MEMORY_SCOPE_AGENT);
            PST(7)
        }
#pragma unroll
        for (int m = 0; m < 2; ++m) {
#pragma unroll
            for (int e = 0; e < 8; ++e) *(v4u*)(stg + (8 * e + (l >> 3)) * 72 + 8 * (l & 7)) = rawo[8 * m + e];
            __builtin_amdgcn_s_waitcnt(0xC07F);
#pragma unroll
            for (int ct = 0; ct < 4; ++ct) {
                const u16* p = stg + (8 * g + (i >> 2)) * 72 + 16 * ct + 4 * (i & 3);
                const v2u r0 = tr4(p), r1 = tr4(p + 4 * 72), r2 = tr4(p + 32 * 72), r3 = tr4(p + 36 * 72);
                PUT4(wO[ct >> 1], (ct & 1) * 16 + m * 8, (v4u{r0[0], r0[1], r1[0], r1[1]})) PUT4(wO[ct >> 1], (ct & 1) * 16 + m * 8 + 4, (v4u{r2[0], r2[1], r3[0], r3[1]}))
            }
            __builtin_amdgcn_s_waitcnt(0xC07F);
        }
        if (wu == 0) { PST(5) }
        if (widx == 0 && wu == 0) *(volatile unsigned long long*)(chsm + C2_MB) = tag;
    }
    if (wu >= 2) touch_sc1(arena(a.slots) + (size_t)min(l + (wu - 2) * 64, FX_OFF / 2097152 - 1) * 2097152 + 2097024 - 128, l0 + C2_MB + 256);
    unsigned long long fw = 0;
#pragma nounroll
    for (int spin = 0; spin < 100000; ++spin) {
        asm volatile("" ::: "memory");
        fw = __hip_atomic_load((gu64*)(a.boot + 256 + 16 * grp + (l & 7)), __ATOMIC_RELAXED, __HIP_MEMORY_SCOPE_AGENT);
        if (__all((fw | 255u) == ((tag << 8) | 255u))) break;
        __builtin_amdgcn_s_sleep(4);
    }
    asm volatile("" ::: "memory");
    const unsigned tau = __builtin_amdgcn_readfirstlane((unsigned)fw & 255u);
    if (wu == 3) { *(volatile unsigned*)(chsm + C2_MB + 8) = tau; *(volatile unsigned*)(chsm + C2_MB + 16) = ~0u; __builtin_amdgcn_s_waitcnt(0xC07F); }
    if (wu == 1) { PST(9) }

    const unsigned r3a = l0 + C2_R3 + wu * 2048 + g * 128 + i * 4;
    v32u yb;
    v8u kx;
    v8u sel; v2u sel3;
#pragma unroll
    for (int d = 0; d < 8; ++d) sel[d] = (i >> 1) == d ? (0x3F80u << ((i & 1) * 16)) : 0u;
#pragma unroll
    for (int d = 0; d < 2; ++d) sel3[d] = (i & 3) == 0 && (i >> 3) == d ? (0x3F80u << (((i >> 2) & 1) * 16)) : 0u;
#define MF(d, a, b, c) "v_mfma_f32_16x16x32_bf16 v[" d "], v[" a "], v[" b "], " c "\n\t"
#define PX4(d0, d1, d2, d3, a, w0, w1, w2, w3) MF(d0, a, w0, "v[" d0 "]") MF(d1, a, w1, "v[" d1 "]") MF(d2, a, w2, "v[" d2 "]") MF(d3, a, w3, "v[" d3 "]")
#define PXT(d0, d1, d2, d3, a0, a1, a2, a3) MF(d0, a0, "32:35", "0") MF(d1, a0, "48:51", "0") MF(d2, a0, "64:67", "0") MF(d3, a0, "80:83", "0") \
        PX4(d0, d1, d2, d3, a1, "36:39", "52:55", "68:71", "84:87") PX4(d0, d1, d2, d3, a2, "40:43", "56:59", "72:75", "88:91") PX4(d0, d1, d2, d3, a3, "44:47", "60:63", "76:79", "92:95")
#define CVP(d, a, b) "v_cvt_pk_bf16_f32 v" d ", v" a ", v" b "\n\t"
#define PROJ_X(cnt) \
        "s_waitcnt " cnt "(8)\n\t" PXT("192:195", "196:199", "200:203", "204:207", "160:163", "164:167", "168:171", "172:175") \
        "s_waitcnt " cnt "(4)\n\t" PXT("208:211", "212:215", "216:219", "220:223", "176:179", "180:183", "184:187", "188:191") \
        CVP("128", "192", "193") CVP("129", "194", "195") CVP("136", "196", "197") CVP("137", "198", "199") CVP("144", "200", "201") CVP("145", "202", "203") CVP("152", "204", "205") CVP("153", "206", "207") \
        CVP("132", "208", "209") CVP("133", "210", "211") CVP("140", "212", "213") CVP("141", "214", "215") CVP("148", "216", "217") CVP("149", "218", "219") CVP("156", "220", "221") CVP("157", "222", "223") \
        "s_waitcnt " cnt "(0)\n\t"
#define CLOB_PX CLOB16(160, 161, 162, 163, 164, 165, 166, 167, 168, 169, 170, 171, 172, 173, 174, 175), CLOB16(176, 177, 178, 179, 180, 181, 182, 183, 184, 185, 186, 187, 188, 189, 190, 191), \
        CLOB16(192, 193, 194, 195, 196, 197, 198, 199, 200, 201, 202, 203, 204, 205, 206, 207), CLOB16(208, 209, 210, 211, 212, 213, 214, 215, 216, 217, 218, 219, 220, 221, 222, 223)
#define LDX(d, va, so, o) "buffer_load_dwordx4 v[" d "], " va ", %[rs], " so " offen offset:" o " sc1\n\t"
#define DSV(d, o) "ds_read_b128 v[" d "], %[lv] offset:" o "\n\t"
    const int voffX = ((i >> 1) & 1) * 4096 + g * 256 + (2 * (i >> 2) + (i & 1)) * 16;
    const int pk = (((i >> 1) & 3) << 1) | (i >> 3);
    const unsigned hv = wu >= 2 ? l0 + C2_R1 + 2176 + ((wu & 1) * 128 + ((l >> 4) + 4 * ((l >> 3) & 1)) * 16 + 8 * ((l >> 1) & 1) + 4 * ((l >> 2) & 1) + 2 * (l & 1)) * 4
                                : l0 + C2_R1 + (i & 1) * 544 + (i >> 1) * 64 + g * 16;
    const unsigned hk = l0 + C2_R1 + 1088 + (i & 1) * 544 + pk * 64 + g * 16;
    const int vo1 = wu >= 2 ? l * 4 : wu == 1 ? (g >> 1) * 1024 + i * 16 + (g & 1) * 8 : (i & 1) ? 1 << 30 : (i >> 1) * 16 + 4 * g;
    const int vo2 = (2 * (i & 1) + (g >> 1)) * 128 + pk * 16 + (g & 1) * 8;
    const int voffK = (g >> 1) * 256 + i * 16 + (g & 1) * 8;
#define RD4(w, ad, a, b, c, d) "ds_read_" w " v[" a "], " ad "\n\tds_read_" w " v[" b "], " ad " offset:3200\n\tds_read_" w " v[" c "], " ad " offset:6400\n\tds_read_" w " v[" d "], " ad " offset:9600\n\t"
#define AD4(d0, d1, d2, d3, a0, a1, a2, a3) ADD(d0, d0, a0) ADD(d1, d1, a1) ADD(d2, d2, a2) ADD(d3, d3, a3)
#define QSC(r) "v_mul_f32 v" r ", 0x3e0293ee, v" r "\n\t"
#define FLD "buffer_load_dword v114, %[vf], %[rs], %[fl] offen sc1\n\t"
#define DUM "buffer_store_dword v168, off, %[rs], %[ob] sc1\n\t"
#define QHEAD "s_barrier\n\t" RD4("b128", "%[hv]", "160:163", "164:167", "168:171", "172:175") FLD \
        "s_waitcnt lgkmcnt(2)\n\t" AD4("160", "161", "162", "163", "164", "165", "166", "167") \
        "s_waitcnt lgkmcnt(0)\n\t" AD4("168", "169", "170", "171", "172", "173", "174", "175") \
        "s_barrier\n\t"
#define QTAIL AD4("160", "161", "162", "163", "168", "169", "170", "171") QSC("160") QSC("161") QSC("162") QSC("163")
#define SWA(d, a, b) "v_add_f32_dpp v" d ", v" a ", v" b " quad_perm:[1,0,3,2] row_mask:0xf bank_mask:0xf\n\t"
#define KLD(so) "buffer_load_dwordx2 v[96:97], %[vk], %[rs], " so " offen sc1\n\tbuffer_load_dwordx2 v[102:103], %[vk], %[rs], " so " offen offset:512 sc1\n\t" \
        "s_add_u32 " so ", " so ", 4096\n\t" \
        "buffer_load_dwordx2 v[100:101], %[vk], %[rs], " so " offen sc1\n\tbuffer_load_dwordx2 v[98:99], %[vk], %[rs], " so " offen offset:512 sc1\n\t"
    auto hand_over = [&](const int o_1, const int o_2, const int o_f, const int tg) {
        int dmy = 0;
        asm volatile(
            "s_cmp_eq_u32 %[wu], 0\n\t"
            "s_cbranch_scc1 2f\n\t"
            "s_cmp_eq_u32 %[wu], 1\n\t"
            "s_cbranch_scc1 1f\n\t"
            "s_barrier\n\t"
            RD4("b64", "%[hv]", "160:161", "162:163", "164:165", "166:167") FLD
            "s_waitcnt lgkmcnt(2)\n\t" ADD("160", "160", "162") ADD("161", "161", "163")
            "s_waitcnt lgkmcnt(0)\n\t" ADD("164", "164", "166") ADD("165", "165", "167") ADD("160", "160", "164") ADD("161", "161", "165")
            CVP("168", "160", "161")
            "buffer_store_dword v168, %[v1], %[rs], %[o1] offen sc1\n\t"
            "s_barrier\n\t"
            DUM PAUSE(PV2)
            "s_branch 3f\n\t"
            "5:\n\t"
            "v_mov_b32 v115, %[lf]\n\t"
            "ds_read_b32 v115, v115\n\t"
            "s_waitcnt lgkmcnt(0)\n\t"
            "v_cmp_eq_u32 vcc, %[tg], v115\n\t"
            "s_cbranch_vccnz 4f\n\t" CN(256)
            PAUSE(PL)
            "s_branch 4f\n\t"
            "1:\n\t"
            Z2("176:177") Z2("178:179")
            "s_waitcnt vmcnt(0)\n\t"
            QHEAD QTAIL
            "s_mov_b64 exec, %[em]\n\t"
            CVP("176", "160", "161") CVP("177", "162", "163")
            "s_not_b64 exec, exec\n\t"
            CVP("178", "160", "161") CVP("179", "162", "163")
            "s_mov_b64 exec, -1\n\t"
            "s_nop 0\n\t"
            MF("180:183", "96:99", "176:179", "0") MF("184:187", "100:103", "176:179", "0")
            "s_nop 7\n\t"
            SWA("180", "184", "180") SWA("181", "185", "181") SWA("182", "186", "182") SWA("183", "187", "183")
            CVP("188", "180", "181") CVP("189", "182", "183")
            "buffer_store_dwordx2 v[188:189], %[v1], %[rs], %[o1] offen sc1\n\t"
            DUM PAUSE(PV1)
            "s_branch 3f\n\t"
            "2:\n\t"
            QHEAD
            RD4("b128", "%[hk]", "176:179", "180:183", "184:187", "188:191")
            QTAIL CVP("164", "160", "161") CVP("165", "162", "163")
            "s_waitcnt lgkmcnt(2)\n\t" AD4("176", "177", "178", "179", "180", "181", "182", "183")
            "s_waitcnt lgkmcnt(0)\n\t" AD4("184", "185", "186", "187", "188", "189", "190", "191") AD4("176", "177", "178", "179", "184", "185", "186", "187")
            CVP("166", "176", "177") CVP("167", "178", "179")
            "s_nop 1\n\t"
            "v_mfma_f32_16x16x16_bf16 v[168:171], v[166:167], v[164:165], 0\n\t"
            "buffer_store_dwordx2 v[166:167], %[v2], %[rs], %[o2] offen sc1\n\t"
            "s_nop 6\n\t"
            SWA("172", "169", "168") SWA("173", "171", "170")
            CVP("174", "172", "173")
            "buffer_store_dword v174, %[v1], %[rs], %[o1] offen sc1\n\t"
            "3:\n\t"
            PAUSE(PVA)
            "s_waitcnt vmcnt(2)\n\t"
            "v_cmp_ne_u32 vcc, " STR(SENT) ", v114\n\t"
            "s_cbranch_vccnz 5b\n\t"
            "4:\n\t"
            : [dm] "+s"(dmy) CNOUT : "{v[96:103]}"(kx), [fl] "s"(o_f), [vf] "v"(i * 128), [ob] "s"(1 << 30), [tg] "s"(tg), [lf] "s"(l0 + C2_MB + 16), [wu] "s"(wu), [hv] "v"(hv), [hk] "v"(hk), [v1] "v"(vo1), [v2] "v"(vo2), [rs] "s"(rsS), [o1] "s"(o_1), [o2] "s"(o_2), [em] "s"(0x5555555555555555ull)
            : "memory", "scc", "vcc", "v114", "v115", CLOB16(160, 161, 162, 163, 164, 165, 166, 167, 168, 169, 170, 171, 172, 173, 174, 175), CLOB16(176, 177, 178, 179, 180, 181, 182, 183, 184, 185, 186, 187, 188, 189, 190, 191));
    };
    auto o1_of = [&](const int s) { return wu >= 2 ? v_off(s, hs, tau) + jq * 512 + (wu & 1) * 256 : wu == 1 ? x_off(s, hs, tau) + jq * 256 : m_off(s, hs, tau) + jq * 128; };

    __builtin_amdgcn_s_setprio(1);
    int o_f = f_off(0, grp, tau);
    int o_cs = rec_base(0) + C_OFF + c * 16384 + (int)(half_bits(0, tau) & 1u) * 8192 + wu * 2048 + grp * 256;
    int o_1 = o1_of(0), o_2 = km_off(0, hs, tau) + jq * 512, bq = x_off(0, ha, tau), bm = m_off(0, ha, tau), bb = v_off(0, ha, tau);
    {
            const int b0 = blk_off(0, ha, tau);
            int k0 = KX_OFF + blk_off(0, hs, tau) + jq * 1024;
            asm volatile(
                LDX("160:163", "%[vx]", "%[vb]", "0") LDX("164:167", "%[vx]", "%[vb]", "1024") LDX("168:171", "%[vx]", "%[vb]", "2048") LDX("172:175", "%[vx]", "%[vb]", "3072")
                LDX("176:179", "%[vx]", "%[vb]", "128") LDX("180:183", "%[vx]", "%[vb]", "1152") LDX("184:187", "%[vx]", "%[vb]", "2176") LDX("188:191", "%[vx]", "%[vb]", "3200")
                KLD("%[k0]")
                "v_mov_b32 v131, 0\n\tv_mov_b32 v135, 0\n\tv_mov_b32 v139, 0\n\tv_mov_b32 v143, 0\n\tv_mov_b32 v147, 0\n\tv_mov_b32 v151, 0\n\tv_mov_b32 v155, 0\n\tv_mov_b32 v159, 0\n\t"
                PROJ_X("vmcnt")
                : "=&{v[128:159]}"(yb), "=&{v[96:103]}"(kx), [k0] "+s"(k0)
                : "{v[32:63]}"(wO[0]), "{v[64:95]}"(wO[1]), [vx] "v"(voffX), [vk] "v"(voffK), [rs] "s"(rsS), [vb] "s"(VX_OFF + b0)
                : "memory", "scc", CLOB_PX);
        if (wu == 1) { PST(10) }
    }
    int s_first = 0;
    asm volatile("s_branch 1f\n\t.p2align 8\n1:" : "+s"(s_first));
    for (int s = s_first; s < NSEG - 1; ++s) {
        hand_over(o_1, o_2, o_f, s);
        if (s == 0 && wu == 1) { PST(8) }
        LT(0)
#define LDPA "buffer_load_dwordx2 v[160:161], v254, %[rs], %[bm] offen sc1\n\t" LDX("164:167", "%[l16]", "%[bq]", "0") LDX("168:171", "%[l16]", "%[bq]", "1024")
#define SM(d, a, b, x) "v_smfmac_f32_16x16x64_bf16 v[" d "], v[" a "], v[" b "], " x "\n\t"
#define MX2(a, b) "v_max_u32 v244, v" a ", v" b "\n\t"
#define MX3(a, b) "v_max3_u32 v244, v244, v" a ", v" b "\n\t"
#define ZAM Z2("208:209") Z2("210:211") Z2("212:213") Z2("214:215") Z2("216:217") Z2("218:219") Z2("220:221") Z2("222:223")
#define MUL(d, a, b) "v_mul_f32 v" d ", v" a ", v" b "\n\t"
#define CV0(d, a) "v_cvt_pk_bf16_f32 v" d ", v" a ", 0\n\t"
#define RDC "ds_read_b64 v[224:225], %[rc]\n\tds_read_b64 v[226:227], %[rc] offset:2048\n\tds_read_b64 v[228:229], %[rc] offset:4096\n\tds_read_b64 v[230:231], %[rc] offset:6144\n\t"
#define EXD(d, r) "v_exp_f32 v" d ", v" r "\n\t"
#define SUBM(r) "v_sub_f32 v" r ", v" r ", v250\n\t"
#define PCVM CVP("241", "180", "180") CVP("243", "180", "180")
#define PCVX CVP("242", "178", "179") CVP("240", "176", "177")
#define OUT(t0, t1, a0, a1, a2, a3, ad, o0, o1) ADD(t0, a0, a1) ADD(t1, a2, a3) MUL(t0, t0, "250") MUL(t1, t1, "251") DW2(ad, t0, t1, o0, o1)
#define TS(n)
#define TSOUT
        int lim = dead ? 0 : SPIN_LIMIT, bad = 0;
        asm volatile(
            LDX("192:195", "%[l16]", "%[bb]", "0") LDX("196:199", "%[l16]", "%[bb]", "1024")
            ZAM Z2("224:225") Z2("226:227")
            Z2("16:17") Z2("18:19") Z2("20:21") Z2("22:23") Z2("24:25") Z2("26:27") Z2("28:29") Z2("30:31")
            "v_mov_b32 v200, 0x3f803f80\n\tv_mov_b32 v201, 0x3f803f80\n\tv_mov_b32 v202, 0x3f80\n\tv_mov_b32 v203, 0\n\t"
            "v_mov_b64 v[204:205], v[200:201]\n\tv_mov_b64 v[206:207], v[202:203]\n\t"
            "v_xor_b32 v252, 0xa000, %[sx]\n\tv_add_u32 v253, 1024, %[ra]\n\tv_lshrrev_b32 v254, 1, %[l16]\n\t"
            PAUSE(PV)
            TS(8)
            LDPA
            "4:\n\t"
            "s_waitcnt vmcnt(4)\n\t"
            SM("208:211", "192:195", "32:39", "%[sx]") MX2("192", "193") SM("212:215", "192:195", "48:55", "%[sx]") MX3("194", "195")
            SM("216:219", "192:195", "64:71", "%[sx]") SM("220:223", "192:195", "80:87", "%[sx]")
            "s_waitcnt vmcnt(3)\n\t"
            SM("208:211", "196:199", "40:47", "%[sx]") MX3("196", "197") SM("212:215", "196:199", "56:63", "%[sx]") MX3("198", "199")
            SM("216:219", "196:199", "72:79", "%[sx]") "v_cmp_eq_u32 vcc, " STR(SENT) ", v244\n\t" SM("220:223", "196:199", "88:95", "%[sx]")
            "s_cbranch_vccnz 5f\n\t"
            "1:\n\t" TS(11)
            "s_waitcnt vmcnt(2)\n\t"
            "v_mfma_f32_16x16x16_bf16 v[236:239], v[112:113], v[160:161], 0\n\t"
            MX2("160", "161") ADD("245", "208", "209") ADD("246", "210", "211") CV0("130", "245") CV0("134", "246")
            ADD("245", "212", "213") ADD("246", "214", "215") CV0("138", "245") CV0("142", "246")
            EXD("180", "236")
            "s_waitcnt vmcnt(1)\n\t"
            MF("228:231", "104:107", "164:167", "0")
            MX3("164", "165") MX3("166", "167") ADD("245", "216", "217") ADD("246", "218", "219") CV0("146", "245") CV0("150", "246")
            ADD("245", "220", "221") ADD("246", "222", "223") CV0("154", "245") CV0("158", "246") PCVM
            "s_waitcnt vmcnt(0)\n\t"
            MF("228:231", "108:111", "168:171", "v[228:231]")
            MX3("168", "169") MX3("170", "171")
            "v_cmp_eq_u32 vcc, " STR(SENT) ", v244\n\t"
            "s_cbranch_vccnz 7f\n\t"
            "2:\n\t" TS(9)
            "s_nop 3\n\t"
            EXD("179", "231") EXD("178", "230") EXD("177", "229") EXD("176", "228")
            PCVX
            "v_max3_f32 v250, |v236|, |v231|, |v230|\n\t"
            "v_max3_f32 v250, v250, |v229|, |v228|\n\t"
            SM("224:227", "240:243", "200:207", "v252")
            "v_cmp_nge_f32 vcc, 0x41c00000, v250\n\t"
            "s_nop 0\n\t"
            SM("16:19", "240:243", "128:135", "v252")
            "s_cbranch_vccnz 10f\n\t"
            "13:\n\t"
            "s_nop 0\n\t"
            SM("20:23", "240:243", "136:143", "v252") "s_nop 1\n\t"
            ADD("250", "224", "225") ADD("251", "226", "227")
            SM("24:27", "240:243", "144:151", "v252")
            "v_rcp_f32 v250, v250\n\t"
            SM("28:31", "240:243", "152:159", "v252")
            "v_rcp_f32 v251, v251\n\t"
            OUT("245", "246", "16", "17", "18", "19", "%[ra]", "0", "16")
            OUT("247", "248", "20", "21", "22", "23", "%[ra]", "128", "144")
            OUT("245", "246", "24", "25", "26", "27", "v253", "0", "16")
            OUT("247", "248", "28", "29", "30", "31", "v253", "128", "144")
            TS(10)
            "s_waitcnt lgkmcnt(0)\n\t"
            "s_barrier\n\t"
            RDC
            "v_lshrrev_b32 v247, 2, %[l16]\n\t"
            "s_waitcnt lgkmcnt(2)\n\t"
            ADD("224", "224", "226") ADD("225", "225", "227")
            "s_waitcnt lgkmcnt(0)\n\t"
            ADD("228", "228", "230") ADD("229", "229", "231") ADD("224", "224", "228") ADD("225", "225", "229")
            "v_cvt_pk_bf16_f32 v245, v224, v225\n\t"
            "buffer_store_dword v245, v247, %[rs], %[cs] offen sc1\n\t"
            "s_branch 9f\n\t"
            "10:\n\t"
            "v_max3_f32 v250, v236, v231, v230\n\t"
            "v_max3_f32 v250, v250, v229, v228\n\t"
            "s_nop 1\n\t"
            "v_max_f32_dpp v251, v250, v250 quad_perm:[1,0,3,2] row_mask:0xf bank_mask:0xf\n\t"
            "v_max_f32_dpp v244, v250, v250 quad_perm:[1,0,3,2] row_mask:0xf bank_mask:0xf\n\t"
            "s_nop 1\n\t"
            "v_permlane16_swap_b32 v251, v244\n\t"
            "v_max_f32 v250, v251, v244\n\t"
            "v_max_f32 v251, v251, v244\n\t"
            Z2("224:225") Z2("226:227")
            "v_permlane32_swap_b32 v250, v251\n\t"
            "v_max_f32 v250, v250, v251\n\t"
            Z2("16:17") Z2("18:19")
            SUBM("228") SUBM("229") SUBM("230") SUBM("231") SUBM("236")
            EXD("180", "236") EXD("179", "231") EXD("178", "230") EXD("177", "229") EXD("176", "228")
            PCVM PCVX
            "s_nop 1\n\t"
            SM("224:227", "240:243", "200:207", "v252") "s_nop 1\n\t"
            SM("16:19", "240:243", "128:135", "v252")
            "s_branch 13b\n\t"
            "5:\n\t" CN(1)
            LDX("192:195", "%[l16]", "%[bb]", "0") LDX("196:199", "%[l16]", "%[bb]", "1024")
            LDPA
            "s_sub_u32 %[lim], %[lim], 1\n\t"
            "s_nop 1\n\t"
            ZAM
            "s_cbranch_scc0 4b\n\t"
            "s_mov_b32 %[bad], 1500\n\t"
            "s_mov_b32 %[lim], 0\n\t"
            "s_branch 1b\n\t"
            "7:\n\t" CN(65536)
            LDPA
            "s_sub_u32 %[lim], %[lim], 1\n\t"
            "s_cbranch_scc0 1b\n\t"
            "s_mov_b32 %[bad], 1000\n\t"
            "s_branch 2b\n\t"
            "9:\n\t"
            : [lim] "+s"(lim), [bad] "+s"(bad), "+{v[128:159]}"(yb) TSOUT CNOUT
            : "{v[32:63]}"(wO[0]), "{v[64:95]}"(wO[1]), "{v[104:111]}"(sel), "{v[112:113]}"(sel3), [sx] "v"(sidx), [l16] "v"(l * 16),
              [ra] "v"(r3a), [rs] "s"(rsS), [bb] "s"(bb), [bq] "s"(bq), [bm] "s"(bm), [cs] "s"(o_cs), [rc] "v"(l0 + C2_R3 + wu * 512 + l * 8)
            : "memory", "vcc", "scc", CLOB_PX, CLOB16(16, 17, 18, 19, 20, 21, 22, 23, 24, 25, 26, 27, 28, 29, 30, 31),
              CLOB16(224, 225, 226, 227, 228, 229, 230, 231, 232, 233, 234, 235, 236, 237, 238, 239), CLOB16(240, 241, 242, 243, 244, 245, 246, 247, 248, 249, 250, 251, 252, 253, 254, 254));
        if (__builtin_expect(bad != 0, 0)) TMO_SET((unsigned)bad)
        __builtin_amdgcn_s_setprio(0);
        if (s == 0 && wu == 1) { PST(6) }

        if (__builtin_expect(s <= KVS + 1, 0)) {
            if (s == 0 || __builtin_amdgcn_readfirstlane(*(volatile unsigned*)(chsm + C2_MB + 32 + ((s - 1) & 1) * 16 + 4 + (wu >> 1) * 4)) == 0u) {
                wait_ge<6>(kv_flag(a.sync, s + 1, grp, l), 1u, a.tmo, 3001u);
                const char* kp = arena(a.slots) + VX_OFF + blk_off(s + 1, ha, tau);
#pragma unroll
                for (int e = 0; e < 8; ++e) glds16h((unsigned)l * 16u, kp + e * 1024, l0 + (s & 1 ? C2_V : C2_K) + wu * 8192 + e * 1024);
                asm volatile("s_waitcnt vmcnt(0)" ::: "memory");
            }
        }
        {
            int k0 = KX_OFF + blk_off(s + 1, hs, tau) + jq * 1024;
            asm volatile(
                DSV("160:163", "0") DSV("164:167", "1024") DSV("168:171", "2048") DSV("172:175", "3072") DSV("176:179", "128") DSV("180:183", "1152") DSV("184:187", "2176") DSV("188:191", "3200")
                "s_cmp_lg_u32 %[wu], 1\n\t"
                "s_cbranch_scc1 1f\n\t"
                KLD("%[k0]")
                "1:\n\t"
                "s_waitcnt lgkmcnt(4)\n\t"
                PXT("192:195", "196:199", "200:203", "204:207", "160:163", "164:167", "168:171", "172:175")
                "s_waitcnt lgkmcnt(0)\n\t"
                PXT("208:211", "212:215", "216:219", "220:223", "176:179", "180:183", "184:187", "188:191")
                CVP("128", "192", "193") CVP("129", "194", "195") CVP("136", "196", "197") CVP("137", "198", "199") CVP("144", "200", "201") CVP("145", "202", "203") CVP("152", "204", "205") CVP("153", "206", "207")
                CVP("132", "208", "209") CVP("133", "210", "211") CVP("140", "212", "213") CVP("141", "214", "215") CVP("148", "216", "217") CVP("149", "218", "219") CVP("156", "220", "221") CVP("157", "222", "223")
                : "+{v[128:159]}"(yb), "+{v[96:103]}"(kx), [k0] "+s"(k0)
                : "{v[32:63]}"(wO[0]), "{v[64:95]}"(wO[1]), [lv] "v"(l0 + (s & 1 ? C2_V : C2_K) + wu * 8192 + voffX), [vk] "v"(voffK), [rs] "s"(rsS), [wu] "s"(wu)
                : "memory", "scc", CLOB_PX);
        }
        __builtin_amdgcn_sched_barrier(0);
        o_1 = o1_of(s + 1); o_2 = km_off(s + 1, hs, tau) + jq * 512;
        bq = x_off(s + 1, ha, tau); bm = m_off(s + 1, ha, tau); bb = v_off(s + 1, ha, tau);
        o_f = f_off(s + 1, grp, tau);
        o_cs = rec_base(s + 1) + C_OFF + c * 16384 + (int)(half_bits(s + 1, tau) & 1u) * 8192 + wu * 2048 + grp * 256;
        asm volatile("" : "+s"(o_cs), "+s"(o_f), "+s"(o_1), "+s"(o_2), "+s"(bq), "+s"(bm), "+s"(bb));
        __builtin_amdgcn_s_barrier();
        asm volatile(
            "s_cmp_lg_u32 %[wu], 3\n\t"
            "s_cbranch_scc1 1f\n\t"
            "v_mov_b32 v160, %[lf]\n\t"
            PAUSE(PF)
            "ds_read_b32 v160, v160\n\t"
            "s_waitcnt lgkmcnt(0)\n\t"
            "v_cmp_eq_u32 vcc, %[tg], v160\n\t"
            "s_cbranch_vccz 1f\n\t"
            "s_mov_b32 exec_hi, 0\n\t"
            "buffer_store_dword v160, %[l4], %[rs], %[fo] offen sc1\n\t"
            "s_mov_b64 exec, -1\n\t"
            "1:\n\t"
            :: [wu] "s"(wu), [lf] "s"(l0 + C2_MB + 16), [tg] "s"(s + 1), [l4] "v"(l * 4), [rs] "s"(rsS), [fo] "s"(o_f + c * 128)
            : "memory", "scc", "vcc", "v160");
        __builtin_amdgcn_s_setprio(1);
    }
    {
        constexpr int s = NSEG - 1;
        const int l = lane_now(), i = l & 15, g = l >> 4;
        v4u qf[8], kf[8], pv[2], px[4];
        asm volatile("s_waitcnt vmcnt(0)" ::: "memory");
        __builtin_amdgcn_s_sleep(4);
#pragma unroll
        for (int e = 0; e < 8; ++e) kf[e] = ld_sc1s(rsS, l * 16, KX_OFF + blk_off(s, ha, tau) + e * 1024);
#pragma unroll
        for (int e = 0; e < 8; ++e) qf[e] = *(const v4u*)(chsm + C2_V + 32768 + wu * 8192 + e * 1024 + l * 16);
        f32x4 sx[3] = {ZERO4, ZERO4, ZERO4};
        float mxx[2] = {-3.0e38f, -3.0e38f};
#pragma unroll
        for (int ks = 0; ks < 4; ++ks) {
            sx[0] = MM(kf[ks], qf[ks], sx[0]);
            sx[1] = MM(kf[ks], qf[4 + ks], sx[1]);
            sx[2] = MM(kf[4 + ks], qf[4 + ks], sx[2]);
        }
#pragma unroll
        for (int r = 0; r < 4; ++r) {
            if (4 * g + r > i) { sx[0][r] = -3.0e38f; sx[2][r] = -3.0e38f; }
            mxx[0] = fmaxf(mxx[0], sx[0][r]);
            mxx[1] = fmaxf(mxx[1], fmaxf(sx[1][r], sx[2][r]));
        }
        asm volatile("" : "+v"(sx[0]), "+v"(sx[1]), "+v"(sx[2]), "+v"(mxx[0]), "+v"(mxx[1]));
        hand_over(o_1, o_2, o_f, s);
        const int bk = km_off(s, ha, tau);
        __builtin_amdgcn_s_sleep(DLY_L);
#pragma nounroll
        for (int spin = 0;; ++spin) {
            asm volatile("" ::: "memory");
            pv[0] = ld_sc1s(rsS, l * 16, bb); pv[1] = ld_sc1s(rsS, l * 16, bb + 1024);
#pragma unroll
            for (int ks = 0; ks < 4; ++ks) px[ks] = ld_sc1s(rsS, g * 128 + (i >> 1) * 16, bk + ks * 512);
            if (__all(mx4(mx4(mx4(mx4(mx4(mx4(0u, pv[0]), pv[1]), px[0]), px[1]), px[2]), px[3]) != SENT)) break;
            if (dead) break;
            if (spin > SPIN_LIMIT) { TMO_SET(1700u) break; }
        }
#define SUB8(v, o) v8u{(v)[(o)], (v)[(o) + 1], (v)[(o) + 2], (v)[(o) + 3], (v)[(o) + 4], (v)[(o) + 5], (v)[(o) + 6], (v)[(o) + 7]}
#pragma unroll
        for (int ct = 0; ct < 4; ++ct) {
            f32x4 am = smm(pv[0], SUB8(wO[ct >> 1], (ct & 1) * 16), ZERO4, sidx);
            am = smm(pv[1], SUB8(wO[ct >> 1], (ct & 1) * 16 + 8), am, sidx);
            yb[8 * ct + 2] = cvt_pk_bf16(am[0] + am[1], 0.f); yb[8 * ct + 6] = cvt_pk_bf16(am[2] + am[3], 0.f);
        }
        const v8u one = {0x3F803F80u, 0x3F803F80u, 0x3F80u, 0u, 0x3F803F80u, 0x3F803F80u, 0x3F80u, 0u};
        const float SCL2E = 0.08838834764831845f * 1.4426950408889634f;
        float* xo = (float*)(chsm + C2_K) + (wu * 32 + 4 * g) * XP + i;
#pragma unroll
        for (int qt = 0; qt < 2; ++qt) {
            f32x4 sc[3] = {sx[qt], sx[2], ZERO4};
#pragma unroll
            for (int ks = 0; ks < 4; ++ks) sc[2] = MM(px[ks], qf[4 * qt + ks], sc[2]);
            float mx = fmaxf(mxx[qt], fmaxf(sc[2][1], sc[2][3]));
            mx = xor32_max(xor16_max(mx));
            const float nmx = -mx * SCL2E;
#define PE(x) __builtin_amdgcn_exp2f(fmaf(x, SCL2E, nmx))
            const float m0 = PE(sc[2][1]), m1 = PE(sc[2][3]);
            const v4u ae = {cvt_pk_bf16(PE(sc[0][0]), PE(sc[0][1])), cvt_pk_bf16(m0, m0), cvt_pk_bf16(PE(sc[0][2]), PE(sc[0][3])), 0u};
            const v4u ao = {qt ? cvt_pk_bf16(PE(sc[1][0]), PE(sc[1][1])) : 0u, 0u, qt ? cvt_pk_bf16(PE(sc[1][2]), PE(sc[1][3])) : 0u, cvt_pk_bf16(m1, m1)};
            const f32x4 sum = smm(ao, one, smm(ae, one, ZERO4, 0xE444), 0x4EEE);
#pragma unroll
            for (int ct = 0; ct < 4; ++ct) {
                const f32x4 o = smm(ao, SUB8(yb, 8 * ct), smm(ae, SUB8(yb, 8 * ct), ZERO4, 0xE444), 0x4EEE);
#pragma unroll
                for (int r = 0; r < 4; ++r) xo[(16 * qt + r) * XP + 16 * ct] = o[r] * __builtin_amdgcn_rcpf(sum[r]);
            }
        }
        __builtin_amdgcn_s_waitcnt(0xC07F);
        last_rows(chsm, rsS, wu, grp, c, tau);
        if (wu < 2) {
            v4u sel, p0, p1;
#pragma unroll
            for (int d = 0; d < 4; ++d) sel[d] = (l & 6) == 2 * d ? (0x3F80u << ((l & 1) * 16)) : 0u;
            const int xb = XB_OFF + ((c >> 1) * 8 + grp) * 16384 + (int)(tau & 1u) * 8192 + (c & 1) * 4096 + wu * 256;
            __builtin_amdgcn_s_sleep(DLY_C);
#pragma nounroll
            for (int spin = 0;; ++spin) {
                asm volatile("" ::: "memory");
                p0 = ld_sc1s(rsS, g * 512 + i * 16, xb); p1 = ld_sc1s(rsS, g * 512 + i * 16, xb + 2048);
                if (__all(mx4(mx4(0u, p0), p1) != SENT)) break;
                if (dead) break;
                if (spin > SPIN_LIMIT) { TMO_SET(1800u) break; }
            }
            const f32x4 d = MM(sel, p1, MM(sel, p0, ZERO4));
            if (g < 2) out8(a.out + (size_t)(s * SEG + 4 * grp + 2 * wu + (i >> 3)) * OUTW + 64 * c + 8 * (i & 7) + 4 * g, v2u{cvt_pk_bf16(d[0], d[1]), cvt_pk_bf16(d[2], d[3])});
        }
    }
}

struct cpx { float x, y; };
__device__ __forceinline__ cpx cmul(cpx a, cpx b) { return {a.x * b.x - a.y * b.y, a.x * b.y + a.y * b.x}; }
__device__ __forceinline__ cpx cadd(cpx a, cpx b) { return {a.x + b.x, a.y + b.y}; }
__device__ __forceinline__ cpx csub(cpx a, cpx b) { return {a.x - b.x, a.y - b.y}; }
template <int DIR> __device__ __forceinline__ cpx mul_i(cpx a) {
    return (DIR < 0) ? cpx{a.y, -a.x} : cpx{-a.y, a.x};
}
template <int DIR> __device__ __forceinline__ void r4(cpx& a, cpx& b, cpx& c, cpx& d) {
    cpx t0 = cadd(a, c), t1 = csub(a, c), t2 = cadd(b, d), t3 = mul_i<DIR>(csub(b, d));
    a = cadd(t0, t2); b = cadd(t1, t3); c = csub(t0, t2); d = csub(t1, t3);
}
template <int DIR> __device__ __forceinline__ cpx twc(float c, float s) { return cpx{c, (float)DIR * s}; }
template <int DIR> __device__ __forceinline__ void dft16(cpx (&v)[16]) {
#pragma unroll
    for (int a = 0; a < 4; ++a) r4<DIR>(v[a], v[a + 4], v[a + 8], v[a + 12]);
    const float C1 = 0.92387953251128674f, S1 = 0.38268343236508977f, R2 = 0.70710678118654752f;
    v[1 + 4] = cmul(v[1 + 4], twc<DIR>(C1, S1));
    v[1 + 8] = cmul(v[1 + 8], twc<DIR>(R2, R2));
    v[1 + 12] = cmul(v[1 + 12], twc<DIR>(S1, C1));
    v[2 + 4] = cmul(v[2 + 4], twc<DIR>(R2, R2));
    v[2 + 8] = mul_i<DIR>(v[2 + 8]);
    v[2 + 12] = cmul(v[2 + 12], twc<DIR>(-R2, R2));
    v[3 + 4] = cmul(v[3 + 4], twc<DIR>(S1, C1));
    v[3 + 8] = cmul(v[3 + 8], twc<DIR>(-R2, R2));
    v[3 + 12] = cmul(v[3 + 12], twc<DIR>(-C1, -S1));
#pragma unroll
    for (int d = 0; d < 4; ++d) r4<DIR>(v[4 * d], v[4 * d + 1], v[4 * d + 2], v[4 * d + 3]);
}
__device__ __forceinline__ constexpr int x16(int q) { return 4 * (q & 3) + (q >> 2); }
template <int DIR> __device__ __forceinline__ void dft8(cpx (&v)[8]) {
    r4<DIR>(v[0], v[2], v[4], v[6]);
    r4<DIR>(v[1], v[3], v[5], v[7]);
    const float R2 = 0.70710678118654752f;
    v[3] = cmul(v[3], twc<DIR>(R2, R2));
    v[5] = mul_i<DIR>(v[5]);
    v[7] = cmul(v[7], twc<DIR>(-R2, R2));
#pragma unroll
    for (int d = 0; d < 4; ++d) { cpx p = v[2 * d], q = v[2 * d + 1]; v[2 * d] = cadd(p, q); v[2 * d + 1] = csub(p, q); }
}
__device__ __forceinline__ int pad(int i) { return i + (i >> 4); }
__device__ __forceinline__ cpx expi(float x) { float s, c; sincospif(x, &s, &c); return cpx{c, s}; }
template <int R, int DIR> __device__ __forceinline__ void twiddle(cpx* v, cpx w1) {
    cpx w[R];
    w[1] = w1;
#pragma unroll
    for (int r = 2; r < R; ++r) w[r] = (r & 1) ? cmul(w[r - 1], w1) : cmul(w[r / 2], w[r / 2]);
#pragma unroll
    for (int r = 1; r < R; ++r) v[r] = DIR < 0 ? cmul(v[r], w[r]) : cpx{v[r].x * w[r].x + v[r].y * w[r].y, v[r].y * w[r].x - v[r].x * w[r].y};
}
struct TwBase { cpx b; cpx c[4]; };
template <int N> __device__ __forceinline__ TwBase make_tw(int t) {
    TwBase tb;
    tb.b = expi(-(float)(t & 15) * (1.0f / 128.0f));
    constexpr int NQ = (N == 1024) ? 4 : 2, T = N / 16;
#pragma unroll
    for (int q = 0; q < NQ; ++q) tb.c[q] = expi(-(float)(t + T * q) * (2.0f / (float)N));
    return tb;
}
template <int N> __device__ __forceinline__ void fft_sync() {
    if constexpr (N == 1024) { __builtin_amdgcn_fence(__ATOMIC_ACQ_REL, "wavefront"); __builtin_amdgcn_wave_barrier(); } else __syncthreads();
}
template <int N, int DIR, bool RIN, bool ROUT>
__device__ __forceinline__ void fft_reg(cpx* buf, const TwBase& tb, int t, cpx (&v)[16]) {
    constexpr int T = N / 16;
    if constexpr (!RIN) {
#pragma unroll
        for (int r = 0; r < 16; ++r) v[r] = buf[pad(t + T * r)];
        fft_sync<N>();
    }
    dft16<DIR>(v);
#pragma unroll
    for (int q = 0; q < 16; ++q) buf[pad(16 * t + x16(q))] = v[q];
    fft_sync<N>();
    {
        const int k = t & 15;
#pragma unroll
        for (int r = 0; r < 16; ++r) v[r] = buf[pad(t + T * r)];
        fft_sync<N>();
        twiddle<16, DIR>(v, tb.b);
        dft16<DIR>(v);
        const int o = (t >> 4) * 256 + k;
#pragma unroll
        for (int q = 0; q < 16; ++q) buf[pad(o + 16 * x16(q))] = v[q];
        fft_sync<N>();
    }
    if constexpr (N == 1024) {
#pragma unroll
        for (int q = 0; q < 4; ++q)
#pragma unroll
            for (int r = 0; r < 4; ++r) v[4 * q + r] = buf[pad(t + 64 * q + 256 * r)];
#pragma unroll
        for (int q = 0; q < 4; ++q) {
            twiddle<4, DIR>(&v[4 * q], tb.c[q]);
            r4<DIR>(v[4 * q], v[4 * q + 1], v[4 * q + 2], v[4 * q + 3]);
#pragma unroll
            for (int r = 0; r < 4; ++r) buf[pad(t + 64 * q + 256 * r)] = v[4 * q + r];
        }
        fft_sync<N>();
    } else {
        cpx u[2][8];
#pragma unroll
        for (int q = 0; q < 2; ++q)
#pragma unroll
            for (int r = 0; r < 8; ++r) u[q][r] = buf[pad(t + 128 * q + 256 * r)];
        if constexpr (ROUT) fft_sync<N>();
#pragma unroll
        for (int q = 0; q < 2; ++q) {
            twiddle<8, DIR>(u[q], tb.c[q]);
            dft8<DIR>(u[q]);
#pragma unroll
            for (int d = 0; d < 4; ++d) {
                if constexpr (ROUT) { v[q + 2 * d] = u[q][2 * d]; v[q + 2 * d + 8] = u[q][2 * d + 1]; }
                else { buf[pad(t + 128 * q + 256 * d)] = u[q][2 * d]; buf[pad(t + 128 * q + 256 * (d + 4))] = u[q][2 * d + 1]; }
            }
        }
        if constexpr (!ROUT) fft_sync<N>();
    }
}
__device__ __forceinline__ int f_pos(int rb, int x, unsigned tw) { x = (x + rb * FROT) & 32767; return rb * 65536 + x + (x & ~8191) + (int)((tw ^ (unsigned)(rb >> 5)) & 1u) * 8192; }
__device__ __forceinline__ int w_pos(int k, int n, unsigned tw) { return k * 16384 + ((n * 4 + k * WROT) & 8191) + (int)((tw ^ (unsigned)(k >> 7)) & 1u) * 8192; }
constexpr int PB1 = 1024 + 64, PB2 = 2048 + 128;
typedef float f32x4v __attribute__((ext_vector_type(4)));
typedef unsigned u32x4v __attribute__((ext_vector_type(4)));

__device__ __forceinline__ void fft_rows_fwd(const u16* __restrict__ x, rsrc_t rsF, const unsigned tw, int rb, char* smem, int tid, const char* wr, const char* wi, const unsigned spare) {
    cpx (*buf)[PB1] = (cpx (*)[PB1])smem;
    int f = tid >> 6, t = tid & 63;
    const u16* p0 = x + (size_t)(rb * 8 + 2 * f) * 1024 + 16 * t;
    u32x4v a0 = *(const u32x4v*)p0, a1 = *(const u32x4v*)(p0 + 8), b0 = *(const u32x4v*)(p0 + 1024), b1 = *(const u32x4v*)(p0 + 1032);
    const TwBase tb = make_tw<1024>(t);
    for (int it = 0; it < 4; ++it, rb += 64) {
        asm volatile("" : "+v"(tid));
        f = tid >> 6; t = tid & 63;
        __syncthreads();
        cpx* d = &buf[f][pad(16 * t)];
#pragma unroll
        for (int e = 0; e < 4; ++e) {
            d[2 * e] = cpx{__uint_as_float(a0[e] << 16), __uint_as_float(b0[e] << 16)};
            d[2 * e + 1] = cpx{__uint_as_float(a0[e] & 0xFFFF0000u), __uint_as_float(b0[e] & 0xFFFF0000u)};
            d[8 + 2 * e] = cpx{__uint_as_float(a1[e] << 16), __uint_as_float(b1[e] << 16)};
            d[8 + 2 * e + 1] = cpx{__uint_as_float(a1[e] & 0xFFFF0000u), __uint_as_float(b1[e] & 0xFFFF0000u)};
        }
        if (it < 3) p0 += 512 * 1024;
        if (FTR) touch(((it & 1) ? wi : wr) + (size_t)((rb >> 1) & 31) * 32832 + (it >> 1) * 32 * 32832 + ((rb & 1) * 128 + (tid >> 1)) * 128, spare);
        a0 = *(const u32x4v*)p0; a1 = *(const u32x4v*)(p0 + 8); b0 = *(const u32x4v*)(p0 + 1024); b1 = *(const u32x4v*)(p0 + 1032);
        fft_sync<1024>();
        cpx z[16];
        fft_reg<1024, -1, false, false>(buf[f], tb, t, z);
        __syncthreads();
#pragma unroll
        for (int m = 0; m < 8; ++m) {
            const int idx = tid + 256 * m, k = idx >> 2, ff = idx & 3;
            const cpx zk = buf[ff][pad(k)], zn = buf[ff][pad(k ? 1024 - k : 512)];
            f32x4v o = {0.5f * (zk.x + zn.x), 0.5f * (zk.y - zn.y), 0.5f * (zk.y + zn.y), -0.5f * (zk.x - zn.x)};
            if (m == 0 && k == 0) o = f32x4v{zk.x, zn.x, zk.y, zn.y};
            st_sc1(rsF, f_pos(rb, idx * 16, tw), __builtin_bit_cast(v4u, o));
        }
    }
}
__device__ __forceinline__ void filt_transpose(const u16* __restrict__ wr, const u16* __restrict__ wi, rsrc_t rsW, const unsigned tw, const int tb, char* smem, const int tid) {
    const v4u* s0 = (const v4u*)(wr + (size_t)tb * 32 * 513);
    const v4u* s1 = (const v4u*)(wi + (size_t)tb * 32 * 513);
    v4u r0[5], r1[5];
#pragma unroll
    for (int e = 0; e < 5; ++e) {
        const int idx = tid + 512 * e, ic = idx < 2052 ? idx : 2051;
        r0[e] = s0[ic]; r1[e] = s1[ic];
    }
#pragma unroll
    for (int e = 0; e < 5; ++e) {
        const int idx = tid + 512 * e;
        if (idx < 2052) { *(v4u*)(smem + idx * 16) = r0[e]; *(v4u*)(smem + 32832 + idx * 16) = r1[e]; }
    }
    __syncthreads();
    const u16* a0 = (const u16*)smem;
    const u16* a1 = (const u16*)(smem + 32832);
    for (int idx = tid; idx < 513 * 8; idx += 512) {
        const int k = idx >> 3, n4 = (idx & 7) * 4;
        v4u v;
#pragma unroll
        for (int j = 0; j < 4; ++j) v[j] = (unsigned)a0[(n4 + j) * 513 + k] | ((unsigned)a1[(n4 + j) * 513 + k] << 16);
        st_sc1(rsW, w_pos(k, tb * 32 + n4, tw), v);
    }
}

__device__ __forceinline__ void fft_cols(rsrc_t rsF, rsrc_t rsW, const unsigned tw, int blk, const int n, char* smem, int tid) {
    cpx (*buf)[PB2] = (cpx (*)[PB2])smem;
    int c = tid >> 7, t = tid & 127;
    f32x4v v[8];
    unsigned wv[16], wn[16];
    auto wld = [&](const int k, const int n) { return __builtin_amdgcn_raw_buffer_load_b32(rsW, w_pos(k, n, tw), 0, 0); };
    auto fetch = [&](const int bk) {
        const int k0 = bk * 2, kc = k0 + c;
#pragma unroll
        for (int m = 0; m < 8; ++m) { const int idx = tid + 256 * m; v[m] = __builtin_bit_cast(f32x4v, __builtin_amdgcn_raw_buffer_load_b128(rsF, f_pos(idx >> 3, k0 * 64 + (idx & 7) * 16, tw), 0, 0)); }
#pragma unroll
        for (int m = 0; m < 16; ++m) wn[m] = wld(kc, t + 128 * m);
    };
    fetch(blk);
    const TwBase tb = make_tw<2048>(t);
    for (int it = 0; it < n; ++it, blk += 64) {
        asm volatile("" : "+v"(tid));
        c = tid >> 7; t = tid & 127;
        __syncthreads();
#pragma unroll
        for (int m = 0; m < 8; ++m) {
            const int idx = tid + 256 * m, rb = idx >> 3, pc = idx & 7;
            cpx* d = &buf[pc >> 2][pad(rb * 8 + 2 * (pc & 3))];
            d[0] = cpx{v[m][0], v[m][1]}; d[1] = cpx{v[m][2], v[m][3]};
        }
#pragma unroll
        for (int m = 0; m < 16; ++m) wv[m] = wn[m];
        fetch(it + 1 < n ? blk + 64 : blk);
        __syncthreads();
        cpx z[16];
        fft_reg<2048, -1, false, true>(buf[c], tb, t, z);
        auto wcx = [](const unsigned w) { return cpx{__uint_as_float(w << 16), __uint_as_float(w & 0xFFFF0000u)}; };
        if (blk < 2) {
#pragma unroll
            for (int m = 0; m < 16; ++m) buf[c][pad(t + 128 * m)] = z[m];
            __syncthreads();
            if (blk == 0 && c == 0) {
#pragma unroll
                for (int m = 0; m < 16; ++m) {
                    const int n = t + 128 * m, nn = (2048 - n) & 2047;
                    const cpx zn = buf[0][pad(nn)], a = wcx(wv[m]), b = wcx(wld(0, nn)), e = wcx(wld(512, n)), f = wcx(wld(512, nn));
                    const cpx h0 = cmul(cpx{0.5f * (z[m].x + zn.x), 0.5f * (z[m].y - zn.y)}, cpx{0.5f * (a.x + b.x), 0.5f * (a.y - b.y)});
                    const cpx h5 = cmul(cpx{0.5f * (z[m].y + zn.y), -0.5f * (z[m].x - zn.x)}, cpx{0.5f * (e.x + f.x), 0.5f * (e.y - f.y)});
                    z[m] = cpx{h0.x - h5.y, h0.y + h5.x};
                    wv[m] = 0x3F80u;
                }
            }
            __syncthreads();
        }
#pragma unroll
        for (int m = 0; m < 16; ++m) z[m] = cmul(z[m], wcx(wv[m]));
        fft_reg<2048, 1, true, false>(buf[c], tb, t, z);
        const float sc = 1.0f / 2048.0f;
#pragma unroll
        for (int m = 0; m < 8; ++m) {
            const int idx = tid + 256 * m, rb = idx >> 3, pc = idx & 7;
            const cpx* d = &buf[pc >> 2][pad(rb * 8 + 2 * (pc & 3))];
            st_sc1(rsF, f_pos(rb, blk * 128 + pc * 16, tw), __builtin_bit_cast(v4u, f32x4v{d[0].x * sc, d[0].y * sc, d[1].x * sc, d[1].y * sc}));
        }
    }
}

__device__ __forceinline__ void fft_rows_inv(rsrc_t rsF, const unsigned tw, u16* __restrict__ out, int rb, const int step, const int n, char* smem, int tid) {
    cpx (*buf)[PB1] = (cpx (*)[PB1])smem;
    int f = tid >> 6, t = tid & 63;
    f32x4v v[8];
    auto fetch = [&](const int r) {
#pragma unroll
        for (int m = 0; m < 8; ++m) v[m] = __builtin_bit_cast(f32x4v, __builtin_amdgcn_raw_buffer_load_b128(rsF, f_pos(r, (tid + 256 * m) * 16, tw), 0, 0));
    };
    fetch(rb);
    const TwBase tb = make_tw<1024>(t);
    for (int it = 0; it < n; ++it, rb += step) {
        asm volatile("" : "+v"(tid));
        f = tid >> 6; t = tid & 63;
        __syncthreads();
        cpx* bf = buf[tid & 3];
#pragma unroll
        for (int m = 0; m < 8; ++m) {
            const int k = (tid >> 2) + 64 * m;
            if (m == 0 && k == 0) { bf[0] = cpx{v[0][0], v[0][2]}; bf[pad(512)] = cpx{v[0][1], v[0][3]}; }
            else { bf[pad(k)] = cpx{v[m][0] - v[m][3], v[m][1] + v[m][2]}; bf[pad(1024 - k)] = cpx{v[m][0] + v[m][3], v[m][2] - v[m][1]}; }
        }
        fetch(it + 1 < n ? rb + step : rb);
        __syncthreads();
        cpx z[16];
        fft_reg<1024, 1, false, false>(buf[f], tb, t, z);
        const float sc = 1.0f / 1024.0f;
        const cpx* d = &buf[f][pad(16 * t)];
        u32x4v a[2], b[2];
#pragma unroll
        for (int h = 0; h < 2; ++h)
#pragma unroll
            for (int e = 0; e < 4; ++e) {
                cpx p = d[8 * h + 2 * e], q = d[8 * h + 2 * e + 1];
                a[h][e] = cvt_pk_bf16(p.x * sc, q.x * sc);
                b[h][e] = cvt_pk_bf16(p.y * sc, q.y * sc);
            }
        u16* o0 = out + (size_t)(rb * 8 + 2 * f) * OUTW + 1024 + 16 * t;
        *(u32x4v*)o0 = a[0]; *(u32x4v*)(o0 + 8) = a[1];
        *(u32x4v*)(o0 + OUTW) = b[0]; *(u32x4v*)(o0 + OUTW + 8) = b[1];
    }
}

__device__ __forceinline__ void glds16s(unsigned voff, const void* sbase, unsigned lds_dst) {
    unsigned keep;
    asm volatile("s_mov_b32 %0, m0\n\ts_mov_b32 m0, %3\n\ts_nop 0\n\tglobal_load_lds_dwordx4 %1, %2\n\ts_mov_b32 m0, %0" : "=&s"(keep) : "v"(voff), "s"(sbase), "s"(lds_dst) : "memory");
}
#define K8_BAR do { asm volatile("" ::: "memory"); __builtin_amdgcn_s_barrier(); asm volatile("" ::: "memory"); } while (0)
#define K8_WV(n) asm volatile("s_waitcnt vmcnt(" #n ")" ::: "memory")
#define K8_WL(n) asm volatile("s_waitcnt lgkmcnt(" #n ")" ::: "memory")
#define K8_SCHED __builtin_amdgcn_sched_barrier(0)
template <int BN>
__device__ __forceinline__ int bswz2(int row) {
    if (BN >= 128) return (row & 3) | (((row >> 3) & 1) << 2);
    else return ((row >> 1) & 1) | (((row >> 3) & 1) << 1);
}
template <int BM>
__device__ __forceinline__ void gemm_tile(u16* sm, const char* A, const int s0, const unsigned tau, const u16* B, u16* C) {
    constexpr int K = HD, LDB = DM, LDC = OUTW;
    constexpr int BN = 128, BK = 64, ST = BM == 128 ? 4 : 3, WN = BM == 128 ? 4 : 2;
    constexpr int ATILE = BM * BK, BTILE = BK * BN;
    constexpr int NT = 8 / WN, GA = BM / 64, GB = 2, GS = GA + GB;
    const int tid = opaque_tid(), w = tid >> 6, l = tid & 63, i = l & 15, g = l >> 4;
    const int wr = w / WN, wc = w % WN;
    const unsigned wu = __builtin_amdgcn_readfirstlane(w);
    unsigned vb;
    { const int row = l >> 4, c = l & 15; vb = (unsigned)(row * LDB + 8 * (c ^ ((row | ((wu & 1) << 2)) << 1))) * 2u; }
    const int sg = s0 + (BM == 128 ? wu >> 1 : wu);
    const char* ab = A + (BM == 128 ? (wu & 1) * 4096 : 0);
    const char* bb = (const char*)(B + (size_t)wu * 8 * LDB);
    const unsigned lds0 = (unsigned)(uintptr_t)(__attribute__((address_space(3))) u16*)sm;
    const unsigned va = (unsigned)l * 16u, va2 = va + 2048u, vb2 = vb + 4u * LDB * 2u - 1024u;
    auto req = [&](const int buf, const int kt) {
        const unsigned d = lds0 + buf * (ATILE + BTILE) * 2;
        const int wh = kt >> 1;
        const char* pa = ab + ((blk_off(sg, (wh & 7) * 4 + (wh >> 3), tau) ^ 8192) + (kt & 1) * 2048);
        const char* pb = bb + (size_t)kt * BK * LDB * 2;
        unsigned keep;
        if (BM == 128) asm volatile("s_mov_b32 %0, m0\n\ts_mov_b32 m0, %1\n\ts_nop 0\n\tglobal_load_lds_dwordx4 %3, %5\n\tglobal_load_lds_dwordx4 %3, %5 offset:1024\n\t"
                     "s_mov_b32 m0, %2\n\ts_nop 0\n\tglobal_load_lds_dwordx4 %6, %8\n\tglobal_load_lds_dwordx4 %7, %8 offset:1024\n\ts_mov_b32 m0, %0"
                     : "=&s"(keep) : "s"(d + wu * GA * 1024), "s"(d + ATILE * 2 + wu * GB * 1024), "v"(va), "v"(va2), "s"(pa), "v"(vb), "v"(vb2), "s"(pb) : "memory");
        else asm volatile("s_mov_b32 %0, m0\n\ts_mov_b32 m0, %1\n\ts_nop 0\n\tglobal_load_lds_dwordx4 %3, %5\n\tglobal_load_lds_dwordx4 %3, %5 offset:1024\n\t"
                     "global_load_lds_dwordx4 %4, %5 offset:2048\n\tglobal_load_lds_dwordx4 %4, %5 offset:3072\n\t"
                     "s_mov_b32 m0, %2\n\ts_nop 0\n\tglobal_load_lds_dwordx4 %6, %8\n\tglobal_load_lds_dwordx4 %7, %8 offset:1024\n\ts_mov_b32 m0, %0"
                     : "=&s"(keep) : "s"(d + wu * GA * 1024), "s"(d + ATILE * 2 + wu * GB * 1024), "v"(va), "v"(va2), "s"(pa), "v"(vb), "v"(vb2), "s"(pb) : "memory");
    };
    f32x4 acc[4][NT] = {};
    constexpr int nt = K / BK;
#pragma unroll
    for (int s = 0; s < ST - 1; ++s) req(s, s);
    int boff[NT];
    const int aoff = wr * 4096 + l * 8;
#pragma unroll
    for (int n = 0; n < NT; ++n) { const int row = 8 * g + (i >> 2); boff[n] = row * BN + 16 * ((wc * NT + n) ^ bswz2<BN>(row)) + 4 * (i & 3); }
    asm volatile("s_waitcnt vmcnt(%0)" :: "i"((ST - 2) * GS) : "memory");
    K8_BAR;
    if (wu >= 4) K8_BAR;
    for (int t0 = 0; t0 < nt + ST - 1 - (nt + ST - 1) % ST; t0 += ST) {
#pragma unroll
        for (int rb = 0; rb < ST; ++rb) {
            const int t = t0 + rb;
            if (nt % ST != 0 && t >= nt) break;
            const int kk = min(t + ST - 1, nt - 1);
            const u16* as = sm + rb * (ATILE + BTILE);
            const u16* bs = as + ATILE;
            bf16x8 af[2][4], bf[2][NT];
#pragma unroll
            for (int ks = 0; ks < 2; ++ks) {
#pragma unroll
                for (int n = 0; n < NT; ++n) bf[ks][n] = tr_frag(bs + boff[n] + ks * 32 * BN, bs + boff[n] + ks * 32 * BN + 4 * BN);
#pragma unroll
                for (int m = 0; m < 4; ++m) af[ks][m] = *(const bf16x8*)(as + aoff + m * 1024 + ks * 512);
            }
            K8_SCHED;
            req((rb + ST - 1) % ST, kk);
            K8_WL(0);
            if (wu >= 4) asm volatile("s_waitcnt vmcnt(%0)" :: "i"((ST - 2) * GS) : "memory");
            K8_BAR;
            __builtin_amdgcn_s_setprio(1);
#pragma unroll
            for (int q = 0; q < 8; ++q)
#pragma unroll
                for (int n = 0; n < NT; ++n) acc[q & 3][n] = __builtin_amdgcn_mfma_f32_16x16x32_bf16(bf[q >> 2][n], af[q >> 2][q & 3], acc[q & 3][n], 0, 0, 0);
            __builtin_amdgcn_s_setprio(0);
            K8_SCHED;
            asm volatile("s_waitcnt vmcnt(%0)" :: "i"((ST - 2) * GS) : "memory");
            K8_BAR;
        }
    }
    if (wu < 4) K8_BAR;
    asm volatile("s_waitcnt vmcnt(0)" ::: "memory");
#pragma unroll
    for (int m = 0; m < 4; ++m)
#pragma unroll
        for (int n = 0; n < NT; n += 2) {
            auto r0 = __builtin_amdgcn_permlane16_swap(cvt_pk_bf16(acc[m][n][0], acc[m][n][1]), cvt_pk_bf16(acc[m][n + 1][0], acc[m][n + 1][1]), false, false);
            auto r1 = __builtin_amdgcn_permlane16_swap(cvt_pk_bf16(acc[m][n][2], acc[m][n][3]), cvt_pk_bf16(acc[m][n + 1][2], acc[m][n + 1][3]), false, false);
            *(v4u*)(C + (wr * 64 + m * 16 + i) * LDC + (wc * NT + n + (g & 1)) * 16 + 8 * (g >> 1)) = v4u{r0[0], r1[0], r0[1], r1[1]};
        }
}

__device__ __forceinline__ void xattn_unit(const ChainArgs& a, rsrc_t rsQ, rsrc_t rsK, rsrc_t rsV, rsrc_t rsS, const int s, const int sn, const int h, u16 (*Vs)[VP], unsigned* mbox, const unsigned tau, int& pend, bool& ready) {
    const int l = opaque_tid() & 63, i = l & 15, g = l >> 4;
    const unsigned known = __builtin_amdgcn_readfirstlane(*(volatile unsigned*)mbox);
    const bool early = known > (unsigned)(s + 1);
    const int bq = km_off(s, h, tau), bb = v_off(s, h, tau);
    auto count = [&](const int t) { arrive(a.sync, t < SSEG ? SY_OX + (t >> 2) * 8 : SY_OXS + (t - SSEG) * 8, t < SSEG ? 128u : 32u, 8); };
    const int xk = blk_off(s, h, tau), xq = xk ^ 8192;
    v4u vv[8], vm[2], k0f[4], k1f[4], kmf[4], q0f[4], q1f[4];
#pragma unroll
    for (int e = 0; e < 8; ++e) vv[e] = ld_sc1s(rsV, l * 16 + e * 1024, xk);
#pragma unroll
    for (int ks = 0; ks < 4; ++ks) {
        k0f[ks] = ld_sc1s(rsK, l * 16 + ks * 1024, xk);
        k1f[ks] = ld_sc1s(rsK, l * 16 + ks * 1024, xk + 4096);
        q0f[ks] = ld_sc1s(rsQ, l * 16 + ks * 1024, xq);
        q1f[ks] = ld_sc1s(rsQ, l * 16 + ks * 1024, xq + 4096);
    }
#pragma unroll
    for (int e = 0; e < 2; ++e) vm[e] = ld_sc1s(rsS, l * 16 + e * 1024, early ? bb : 1 << 30);
#pragma unroll
    for (int ks = 0; ks < 4; ++ks) kmf[ks] = ld_sc1s(rsS, ks * 512 + g * 128 + (i & 7) * 16, early ? bq : 1 << 30);
    const unsigned fl = ld_flag(a.sync + (l >= 8 ? SY_PROG * 32 : (((l & 4) ? SY_Q : SY_KV) + (min(sn, NSEG - 1) >> 3) * 8) * 32 + (l & 3)) + (h >> 2) * 32);
    if (pend >= 0) { asm volatile("s_waitcnt vmcnt(31)" ::: "memory"); count(pend); }
#pragma unroll
    for (int e = 0; e < 8; ++e) *(v4u*)&Vs[16 * (e >> 2) + i][32 * (e & 3) + 8 * g] = vv[e];
    const float scale = 0.08838834764831845f;
    const float L2E = 1.4426950408889634f;
    auto round2 = [&](const f32x4 c, float* o) {
#pragma unroll
        for (int p = 0; p < 2; ++p) {
            unsigned u = cvt_pk_bf16(c[2 * p], c[2 * p + 1]);
            float lo = __uint_as_float(u << 16) * scale, hi = __uint_as_float(u & 0xFFFF0000u) * scale;
            unsigned u2 = cvt_pk_bf16(lo, hi);
            o[2 * p] = __uint_as_float(u2 << 16);
            o[2 * p + 1] = __uint_as_float(u2 & 0xFFFF0000u);
        }
    };
    float sv[2][12], mxx[2];
    {
        f32x4 sx[3] = {};
#pragma unroll
        for (int ks = 0; ks < 4; ++ks) {
            sx[0] = __builtin_amdgcn_mfma_f32_16x16x32_bf16(as_frag(k0f[ks]), as_frag(q0f[ks]), sx[0], 0, 0, 0);
            sx[1] = __builtin_amdgcn_mfma_f32_16x16x32_bf16(as_frag(k0f[ks]), as_frag(q1f[ks]), sx[1], 0, 0, 0);
            sx[2] = __builtin_amdgcn_mfma_f32_16x16x32_bf16(as_frag(k1f[ks]), as_frag(q1f[ks]), sx[2], 0, 0, 0);
        }
        round2(sx[0], &sv[0][0]); round2(sx[1], &sv[1][0]); round2(sx[2], &sv[1][4]);
        mxx[0] = mxx[1] = -3.0e38f;
#pragma unroll
        for (int r = 0; r < 4; ++r) {
            if (4 * g + r > i) { sv[0][r] = -3.0e38f; sv[1][4 + r] = -3.0e38f; }
            mxx[0] = fmaxf(mxx[0], sv[0][r]);
            mxx[1] = fmaxf(mxx[1], fmaxf(sv[1][r], sv[1][4 + r]));
        }
    }
    const u16* vb = &Vs[4 * g + (i >> 2)][4 * (i & 3)];
    bf16x8 vfx[8];
#pragma unroll
    for (int t = 0; t < 8; ++t) vfx[t] = tr_frag(vb + 16 * t, vb + 16 * VP + 16 * t);
#pragma unroll
    for (int t = 0; t < 8; ++t) asm volatile("" : "+v"(vfx[t]));
#pragma unroll
    for (int r = 0; r < 4; ++r) asm volatile("" : "+v"(sv[0][r]), "+v"(sv[1][r]), "+v"(sv[1][4 + r]));
    asm volatile("" : "+v"(mxx[0]), "+v"(mxx[1]));
    bool have = false;
    unsigned seen = __builtin_amdgcn_readlane(fl, 8);
    ready = (sn >> 3) == (s >> 3) || (__ballot(fl != 0u) & 255ull) == 255ull;
    if (early) {
        if ((h & 3) == 0 && l == 0) atomicMax(mbox, seen);
        unsigned m = mx4(mx4(0u, vm[0]), vm[1]);
#pragma unroll
        for (int ks = 0; ks < 4; ++ks) m = mx4(m, kmf[ks]);
        have = __all(m != SENT);
    } else if ((h & 3) == 0) {
        const unsigned* pw = a.sync + (SY_PROG + (h >> 2)) * 32;
        unsigned v = seen;
        if (v <= (unsigned)(s + 1)) {
            if (s < NSEG - XA_FAST) { wait_ge<32>(pw, (unsigned)(s + 1), a.tmo, 4000u); __builtin_amdgcn_s_sleep(24); }
            else { wait_ge<6>(pw, (unsigned)(s + 1), a.tmo, 4000u); __builtin_amdgcn_s_sleep(XA_D); }
            v = (unsigned)(s + 1);
        }
        if (l == 0) atomicMax(mbox, v);
    } else {
#pragma nounroll
        for (int spin = 0; spin < 4000000 && *(volatile unsigned*)mbox < (unsigned)(s + 1); ++spin) __builtin_amdgcn_s_sleep(2);
        asm volatile("" ::: "memory");
    }
#pragma nounroll
    for (int spin = 0; !have; ++spin) {
        asm volatile("" ::: "memory");
#pragma unroll
        for (int e = 0; e < 2; ++e) vm[e] = ld_sc1s(rsS, l * 16 + e * 1024, bb);
#pragma unroll
        for (int ks = 0; ks < 4; ++ks) kmf[ks] = ld_sc1s(rsS, ks * 512 + g * 128 + (i & 7) * 16, bq);
        unsigned m = mx4(mx4(0u, vm[0]), vm[1]);
#pragma unroll
        for (int ks = 0; ks < 4; ++ks) m = mx4(m, kmf[ks]);
        if (__all(m != SENT) || spin > 20000) break;
        __builtin_amdgcn_s_sleep(2);
    }
#pragma unroll
    for (int e = 0; e < 8; ++e) *(unsigned*)&Vs[32 + (i >> 2) + 4 * ((i >> 1) & 1)][64 * (e >> 2) + 16 * g + 4 * (e & 3) + 2 * (i & 1)] = vm[e >> 2][e & 3];
    f32x4 sm_[2] = {};
#pragma unroll
    for (int ks = 0; ks < 4; ++ks) {
        sm_[0] = __builtin_amdgcn_mfma_f32_16x16x32_bf16(as_frag(kmf[ks]), as_frag(q0f[ks]), sm_[0], 0, 0, 0);
        sm_[1] = __builtin_amdgcn_mfma_f32_16x16x32_bf16(as_frag(kmf[ks]), as_frag(q1f[ks]), sm_[1], 0, 0, 0);
    }
    v4u pf[2][2];
#pragma unroll
    for (int qt = 0; qt < 2; ++qt) {
        round2(sm_[qt], &sv[qt][8]);
        float mx = fmaxf(fmaxf(mxx[qt], fmaxf(sv[qt][8], sv[qt][9])), fmaxf(sv[qt][10], sv[qt][11]));
        mx = xor16_max(mx);
        mx = xor32_max(mx);
        const float nmx = -mx * L2E;
        float sum = 0.f;
#pragma unroll
        for (int e = 0; e < 12; ++e) {
            if (qt == 0 && e >= 4 && e < 8) { sv[qt][e] = 0.f; continue; }
            float ev = __builtin_amdgcn_exp2f(fmaf(sv[qt][e], L2E, nmx));
            if (e >= 8) ev = g < 2 ? ev : 0.f;
            sv[qt][e] = ev; sum += ev;
        }
        sum = xor16_sum(sum);
        sum = xor32_sum(sum);
        const float inv = 1.0f / sum;
        pf[qt][0][0] = cvt_pk_bf16(sv[qt][0] * inv, sv[qt][1] * inv);
        pf[qt][0][1] = cvt_pk_bf16(sv[qt][2] * inv, sv[qt][3] * inv);
        pf[qt][0][2] = qt ? cvt_pk_bf16(sv[qt][4] * inv, sv[qt][5] * inv) : 0u;
        pf[qt][0][3] = qt ? cvt_pk_bf16(sv[qt][6] * inv, sv[qt][7] * inv) : 0u;
        pf[qt][1][0] = cvt_pk_bf16(sv[qt][8] * inv, sv[qt][9] * inv);
        pf[qt][1][1] = cvt_pk_bf16(sv[qt][10] * inv, sv[qt][11] * inv);
        pf[qt][1][2] = 0u; pf[qt][1][3] = 0u;
    }
    v2u vmf[8];
#pragma unroll
    for (int t = 0; t < 8; ++t) vmf[t] = tr4(&Vs[32 + 4 * (g & 1) + (i >> 2)][16 * t + 4 * (i & 3)]);
#pragma unroll
    for (int t = 0; t < 8; t += 2) {
        unsigned pk[2][2][2];
#pragma unroll
        for (int tt = 0; tt < 2; ++tt) {
            const bf16x8 vf0 = vfx[t + tt];
            const bf16x8 vf1 = as_frag(v4u{vmf[t + tt][0], vmf[t + tt][1], 0u, 0u});
#pragma unroll
            for (int qt = 0; qt < 2; ++qt) {
                f32x4 o = {};
                o = __builtin_amdgcn_mfma_f32_16x16x32_bf16(vf0, as_frag(pf[qt][0]), o, 0, 0, 0);
                o = __builtin_amdgcn_mfma_f32_16x16x32_bf16(vf1, as_frag(pf[qt][1]), o, 0, 0, 0);
                pk[qt][tt][0] = cvt_pk_bf16(o[0], o[1]);
                pk[qt][tt][1] = cvt_pk_bf16(o[2], o[3]);
            }
        }
#pragma unroll
        for (int qt = 0; qt < 2; ++qt) {
            auto r0 = __builtin_amdgcn_permlane16_swap(pk[qt][0][0], pk[qt][1][0], false, false);
            auto r1 = __builtin_amdgcn_permlane16_swap(pk[qt][0][1], pk[qt][1][1], false, false);
            v4u o = {r0[0], r1[0], r0[1], r1[1]};
            st_sc1s(rsV, (t >> 1) * 1024 + ((g & 1) * 2 + (g >> 1)) * 256 + i * 16, xq + qt * 4096, o);
        }
    }
    pend = seen > (unsigned)(sn + 1) && ready ? s : -1;
    if (pend < 0) { asm volatile("s_waitcnt vmcnt(0)" ::: "memory"); count(s); }
}

__device__ __forceinline__ void tile_stream(const ChainArgs& a, const u16* X, unsigned long long todo, const int xc, unsigned& tau, const bool early, const int nw, u16* sm8 WEVP) {
    constexpr int K = DM, LDB = HD;
    const int tid = opaque_tid(), w = tid >> 6, l = tid & 63, i = l & 15, g = l >> 4;
    const int wr = w >> 2, wc = w & 3;
    const unsigned lds0 = (unsigned)(uintptr_t)(__attribute__((address_space(3))) u16*)sm8;
    const unsigned wu = __builtin_amdgcn_readfirstlane(w);
    const unsigned ldsw = lds0 + wu * 2048;
    unsigned voffA, voffB;
    { int row = 16 * w + (l >> 3), c = l & 7; voffA = (unsigned)(row * K + 8 * (c ^ (row & 7))) * 2u; }
    { int row = 8 * w + g, c = i; voffB = (unsigned)(row * LDB + 8 * (c ^ (bswz2<128>(row) << 1))) * 2u; }
    const unsigned voffA2 = voffA + 8u * K * 2u - 1024u, voffB2 = voffB + 4u * LDB * 2u - 1024u;
    auto st2 = [&](const unsigned v0, const unsigned v1, const char* p, const unsigned d) {
        unsigned keep;
        asm volatile("s_mov_b32 %0, m0\n\ts_mov_b32 m0, %1\n\ts_nop 0\n\tglobal_load_lds_dwordx4 %2, %4\n\tglobal_load_lds_dwordx4 %3, %4 offset:1024\n\ts_mov_b32 m0, %0"
                     : "=&s"(keep) : "s"(d), "v"(v0), "v"(v1), "s"(p) : "memory");
    };
    auto stA = [&](int b, int h, const char* p) {
        st2(voffA, voffA2, p + (size_t)(h * 128) * K * 2, ldsw + (2 * b + h) * 16384);
    };
    auto stB = [&](int b, int h, const char* p) {
        st2(voffB, voffB2, p + h * 2048, ldsw + 65536 + (2 * b + h) * 16384);
    };
    constexpr int SA = 128, SB = 64 * LDB * 2;
    auto a_of = [&](const unsigned t) { return (const char*)(X + (size_t)((t >> 4) & 7) * 256 * DM); };
    auto b_of = [&](const unsigned t) { const int kind = t & 7; return (const char*)((kind < 2 ? a.Wk : (kind < 4 ? a.Wv : a.Wq)) + hcol(4 * xc + (kind & 1) * 2)); };
    auto c_of = [&](const unsigned t) { const int kind = t & 7; return ((kind & 6) == 2 ? VX_OFF - KX_OFF : 0) + (int)((t >> 4) & 7) * 4194304 + blk_off(0, 4 * xc + (kind & 1) * 2, kind < 4 ? tau : tau ^ 1u) + (int)((t >> 3) & 1u); };
    auto publish = [&](const unsigned t) {
        const int kind = t & 7, ln = lane_now();
        if (wu == 0 && ln < (kind < 4 ? 1 : 2))
            __hip_atomic_store((gu32*)(a.sync + ((kind < 4 ? SY_KV : SY_Q) + ((t >> 4) & 7) * 8 + xc) * 32 + (kind < 4 ? kind : 2 * kind - 8 + ln)), 16u, __ATOMIC_RELAXED, __HIP_MEMORY_SCOPE_AGENT);
    };
    const char* smc = (const char*)sm8;
    int aoff[2], boff[2];
#pragma unroll
    for (int ks = 0; ks < 2; ++ks) aoff[ks] = (wr * 64 + i) * 128 + (((ks * 4 + g) ^ (i & 7)) << 4);
#pragma unroll
    for (int n = 0; n < 2; ++n) { int row = 8 * g + (i >> 2); boff[n] = 65536 + row * 256 + (((wc * 2 + n) ^ bswz2<128>(row)) << 5) + 8 * (i & 3); }
    f32x4 acc[2][2][4][2] = {};
    bf16x8 At[4][2], B0[2][2], B1[2][2];
#define K8_LDA(b, h) _Pragma("unroll") for (int m = 0; m < 4; ++m) _Pragma("unroll") for (int ks = 0; ks < 2; ++ks) \
        At[m][ks] = *(const bf16x8*)(smc + aoff[ks] + (2 * (b) + (h)) * 16384 + m * 2048)
#define K8_LDB(dst, b, h) _Pragma("unroll") for (int n = 0; n < 2; ++n) _Pragma("unroll") for (int ks = 0; ks < 2; ++ks) { \
        const char* p_ = smc + boff[n] + (2 * (b) + (h)) * 16384 + ks * 8192; \
        v4i16 lo_ = __builtin_amdgcn_ds_read_tr16_b64_v4i16((__attribute__((address_space(3))) v4i16*)p_); \
        v4i16 hi_ = __builtin_amdgcn_ds_read_tr16_b64_v4i16((__attribute__((address_space(3))) v4i16*)(p_ + 1024)); \
        dst[n][ks] = bf16x8{lo_[0], lo_[1], lo_[2], lo_[3], hi_[0], hi_[1], hi_[2], hi_[3]}; }
#define K8_MMA(ai, bj, Bt) do { __builtin_amdgcn_s_setprio(1); \
        _Pragma("unroll") for (int m = 0; m < 4; ++m) _Pragma("unroll") for (int n = 0; n < 2; ++n) _Pragma("unroll") for (int ks = 0; ks < 2; ++ks) \
            acc[ai][bj][m][n] = __builtin_amdgcn_mfma_f32_16x16x32_bf16(Bt[n][ks], At[m][ks], acc[ai][bj][m][n], 0, 0, 0); \
        __builtin_amdgcn_s_setprio(0); } while (0)
    const rsrc_t rsC = __builtin_amdgcn_make_buffer_rsrc((void*)(arena(a.slots) + KX_OFF), 0, FX_OFF - KX_OFF, 0x00020000);
#define K8_EPI(ai, bj, cb) do { const int ln_ = lane_now(); \
        const int vo_ = (int)(wu & 3) * 1024 + (((ln_ >> 4) & 1) * 2 + (ln_ >> 5)) * 256 + (ln_ & 15) * 16; \
        _Pragma("unroll") for (int m = 0; m < 4; ++m) { \
            const f32x4 c0 = acc[ai][bj][m][0], c1 = acc[ai][bj][m][1]; \
            auto r0 = __builtin_amdgcn_permlane16_swap(cvt_pk_bf16(c0[0], c0[1]), cvt_pk_bf16(c1[0], c1[1]), false, false); \
            auto r1 = __builtin_amdgcn_permlane16_swap(cvt_pk_bf16(c0[2], c0[3]), cvt_pk_bf16(c1[2], c1[3]), false, false); \
            const int sg_ = 4 * ai + 2 * (int)(wu >> 2) + (m >> 1); \
            st_sc1s(rsC, vo_, ((ai ? ((cb) & ~1) ^ 8192 : (cb) & 1 ? 1 << 30 : (cb)) ^ ((sg_ & SWZ) << 15)) + sg_ * 524288 + bj * 16384 + (m & 1) * 4096, v4u{r0[0], r1[0], r0[1], r1[1]}); \
            acc[ai][bj][m][0] = ZERO4; acc[ai][bj][m][1] = ZERO4; } } while (0)
    constexpr int nt = K / 64;
    unsigned cur = (unsigned)todo & 255u, prev = 0u;
    const char* pA = a_of(cur); const char* pB = b_of(cur);
    if (TX0 && early) touch((const char*)X + 524288 + (size_t)(nw * 128 + (tid >> 2)) * 128, lds0 + 163840 - 256);
    stB(0, 0, pB); stB(0, 1, pB); stA(0, 0, pA); stA(0, 1, pA);
    stB(1, 0, pB + SB); stB(1, 1, pB + SB); stA(1, 0, pA + SA);
    K8_WV(8); K8_BAR;
    if (wu >= 4) K8_BAR;
#define K8_GO(ai) __builtin_amdgcn_s_waitcnt(0xC07F); K8_BAR; K8_MMA(ai, 0, B0); K8_MMA(ai, 1, B1); K8_SCHED; K8_BAR
    int cbp = 0;
    for (;;) {
        todo >>= 8;
        const unsigned nxt = todo != 0ull ? (unsigned)todo & 255u : cur;
        int cb = c_of(cur);
#pragma nounroll
        for (int t = 0; t < nt; t += 2) {
            if (!(TC0 & 1) && __builtin_expect(early && prev == 0u && t == TXT, 0))
                touch((const char*)X + 3 * 524288 + (size_t)(nw * 320 + min(TIDX, 319)) * 128, lds0 + 163840 - 256);
            if (__builtin_expect(early && prev == 0u && t == nt - 4, 0)) {
                tau = __builtin_amdgcn_readfirstlane((unsigned)boot_wait(a.boot) & 255u);
                cb = c_of(cur);
            }
            const bool first = t == 0 && prev != 0u, last = t == nt - 2;
            if (t == 2 && prev != 0u) { publish(prev); WEV(3) }
            K8_LDB(B0, 0, 0); K8_LDA(0, 0); K8_LDB(B1, 0, 1); K8_SCHED; stA(1, 1, pA + SA);
            pA = last ? a_of(nxt) : pA + 2 * SA; pB = last ? b_of(nxt) : pB + 2 * SB;
            if (first) { K8_EPI(1, 0, cbp); K8_EPI(1, 1, cbp); K8_WV(24); } else K8_WV(8);
            K8_GO(0);
            K8_LDA(0, 1); K8_SCHED; stB(0, 0, pB); stB(0, 1, pB); stA(0, 0, pA);
            if (first) K8_WV(24); else K8_WV(8);
            K8_GO(1);
            K8_LDB(B0, 1, 0); K8_LDA(1, 0); K8_LDB(B1, 1, 1); K8_SCHED; stA(0, 1, pA);
            if (first) K8_WV(16); else K8_WV(8);
            K8_GO(0);
            K8_LDA(1, 1); K8_SCHED; stB(1, 0, pB + SB); stB(1, 1, pB + SB); stA(1, 0, pA + SA);
            if (last) { K8_EPI(0, 0, cb); K8_EPI(0, 1, cb); K8_WV(16); } else K8_WV(8);
            K8_GO(1);
        }
        WEV(2)
        prev = cur; cbp = cb; cur = nxt;
        if (todo == 0ull) break;
    }
    K8_EPI(1, 0, cbp); K8_EPI(1, 1, cbp);
    K8_WV(0);
    if (wu < 4) K8_BAR;
    K8_BAR;
    publish(prev);
    WEV(3)
}

__device__ __forceinline__ void oproj_squad(const ChainArgs& a, u16* out, const int ct, const int rh, const int xc, const unsigned tau, char* smem) {
    const int tid = opaque_tid(), w = tid >> 6, l = tid & 63, i = l & 15, g = l >> 4;
    const int wu = __builtin_amdgcn_readfirstlane(w);
    bf16x8 wf[2][16];
    {
        u16* stg = (u16*)(smem + wu * 10240);
#pragma unroll
        for (int ch = 0; ch < 4; ++ch) {
            const u16* src = a.Wo + (size_t)hcol(4 * wu + ch) * DM + 32 * ct;
            v4u raw[8];
#pragma unroll
            for (int e = 0; e < 8; ++e) { int idx = l + 64 * e, r = idx >> 2, q4 = idx & 3; raw[e] = *(const v4u*)(src + (size_t)r * DM + 8 * q4); }
#pragma unroll
            for (int e = 0; e < 8; ++e) { int idx = l + 64 * e, r = idx >> 2, q4 = idx & 3; *(v4u*)(stg + r * 40 + 8 * q4) = raw[e]; }
            __builtin_amdgcn_s_waitcnt(0xC07F);
#pragma unroll
            for (int jj = 0; jj < 2; ++jj)
#pragma unroll
                for (int ks = 0; ks < 4; ++ks) {
                    const u16* p = stg + (32 * ks + 8 * g + (i >> 2)) * 40 + 16 * jj + 4 * (i & 3);
                    wf[jj][4 * ch + ks] = tr_frag(p, p + 4 * 40);
                }
            __builtin_amdgcn_s_waitcnt(0xC07F);
        }
    }
    const rsrc_t rsO = __builtin_amdgcn_make_buffer_rsrc((void*)(arena(a.slots) + VX_OFF), 0, 32 << 20, 0x00020000);
    float* red = (float*)(smem + 81920);
    const int voff = rh * 4096 + l * 16;
    volatile unsigned* rdy = (volatile unsigned*)(smem + 81920 + 2 * 8 * 16 * 36 * 4);
    unsigned ready = 0;
    bool pre = false;
    v4u xf[16];
    for (int s = SSEG; s < NSEG - 1; ++s) {
        if (!(ready & 1u)) {
            if (wu == 0) {
                const unsigned* q = a.sync + (SY_OXS + (s - SSEG) * 8 + xc) * 32;
#pragma nounroll
                for (int spin = 0;; ++spin) {
                    asm volatile("" ::: "memory");
                    if (ld_flag(q) != 0u) break;
                    if (spin > 20000) { __hip_atomic_store((gu32*)a.tmo, 7000u, __ATOMIC_RELAXED, __HIP_MEMORY_SCOPE_AGENT); break; }
                    if (s == SSEG) __builtin_amdgcn_s_sleep(64); else if (s < NSEG - 5) __builtin_amdgcn_s_sleep(16); else __builtin_amdgcn_s_sleep(SQ_SF);
                }
            }
            asm volatile("" ::: "memory");
            __builtin_amdgcn_s_barrier();
            asm volatile("" ::: "memory");
        }
        if (!pre) {
#pragma unroll
            for (int ks = 0; ks < 16; ++ks) xf[ks] = ld_sc1s(rsO, voff + (ks & 3) * 1024, blk_off(s, 4 * wu + (ks >> 2), tau) ^ 8192);
        }
        unsigned nf = 0;
        if (wu == 0) nf = ld_flag(a.sync + (SY_OXS + (s + 1 - SSEG + (l & 1)) * 8 + xc) * 32);
        pre = (ready & 2u) != 0u;
        f32x4 acc[2] = {};
        if (pre) {
#pragma unroll
            for (int ks = 0; ks < 16; ++ks) {
#pragma unroll
                for (int jj = 0; jj < 2; ++jj) acc[jj] = __builtin_amdgcn_mfma_f32_16x16x32_bf16(wf[jj][ks], as_frag(xf[ks]), acc[jj], 0, 0, 0);
                K8_SCHED;
                xf[ks] = ld_sc1s(rsO, voff + (ks & 3) * 1024, blk_off(s + 1, 4 * wu + (ks >> 2), tau) ^ 8192);
                K8_SCHED;
            }
        } else {
#pragma unroll
            for (int ks = 0; ks < 16; ++ks)
#pragma unroll
                for (int jj = 0; jj < 2; ++jj) acc[jj] = __builtin_amdgcn_mfma_f32_16x16x32_bf16(wf[jj][ks], as_frag(xf[ks]), acc[jj], 0, 0, 0);
        }
        float* rp = red + (s & 1) * (8 * 16 * 36);
#pragma unroll
        for (int jj = 0; jj < 2; ++jj) *(f32x4*)&rp[(wu * 16 + i) * 36 + 16 * jj + 4 * g] = acc[jj];
        if (wu == 0) rdy[s & 1] = (unsigned)__ballot(nf != 0u) & 3u;
        K8_WL(0); K8_BAR;
        ready = __builtin_amdgcn_readfirstlane(rdy[s & 1]) | (pre ? 1u : 0u);
        if (tid >= 64 && tid < 192) {
            const int t = tid - 64, row = t >> 3, q4 = t & 7;
            f32x4 sv_[8];
#pragma unroll
            for (int e = 0; e < 8; ++e) sv_[e] = *(const f32x4*)&rp[(e * 16 + row) * 36 + 4 * q4];
            const f32x4 s0 = ((sv_[0] + sv_[1]) + (sv_[2] + sv_[3])) + ((sv_[4] + sv_[5]) + (sv_[6] + sv_[7]));
            v2u o; o[0] = cvt_pk_bf16(s0[0], s0[1]); o[1] = cvt_pk_bf16(s0[2], s0[3]);
            out8(out + (size_t)(s * SEG + 16 * rh + row) * OUTW + 32 * ct + 4 * q4, o);
        }
    }
}

__device__ __forceinline__ void kv_stream(const ChainArgs& a, const u16* __restrict__ X, const u16* __restrict__ B, char* C, const int head, const int xc, const int wk, unsigned& tau, char* smem) {
    const int tid = opaque_tid(), w = tid >> 6, l = tid & 63, i = l & 15, g = l >> 4;
    const int wu = __builtin_amdgcn_readfirstlane(w), kh = wu >> 2, cq = wu & 3;
    const unsigned lds0 = (unsigned)(uintptr_t)(__attribute__((address_space(3))) char*)smem;
    unsigned voA, voB;
    { const int row = (wu & 1) * 8 + (l >> 3), c = l & 7; voA = (unsigned)(row * DM + 8 * (c ^ (row & 7))) * 2u + (wu >> 1) * 512; }
    { const int row = tid >> 4, c = tid & 15; voB = (unsigned)(row * HD + 8 * (c ^ (bswz2<128>(row) << 1))) * 2u; }
    auto stB = [&](int ch, int slot) {
#pragma unroll
        for (int e = 0; e < 4; ++e) glds16s(voB, (const char*)B + (size_t)(4 * ch + e) * 32 * HD * 2, lds0 + 65536 + slot * 32768 + e * 8192 + wu * 1024);
    };
    const unsigned ldA = lds0 + (wu >> 1) * 8192 + (wu & 1) * 1024;
    auto stP = [&](const char* p, const unsigned d) {
        unsigned keep;
        asm volatile("s_mov_b32 %0, m0\n\ts_mov_b32 m0, %1\n\ts_nop 0\n\tglobal_load_lds_dwordx4 %3, %2 offset:-4096\n\tglobal_load_lds_dwordx4 %4, %2 offset:-2048\n\t"
                     "global_load_lds_dwordx4 %5, %2\n\tglobal_load_lds_dwordx4 %6, %2 offset:2048\n\ts_mov_b32 m0, %0"
                     : "=&s"(keep) : "s"(d), "s"(p), "v"(voA + 8192u), "v"(voA + 6272u), "v"(voA + 4352u), "v"(voA + 2432u) : "memory");
    };
    auto srcA = [&](int h) { return (const char*)X + (size_t)min(h, 2 * KVS - 1) * 32768 - 4096; };
    auto stA = [&](int h) { stP(srcA(h), ldA + (h & 3) * 32768 + 4096); };
    stB(0, 0); stA(0); stA(1); stB(1, 1);
    touch((const char*)X + 65536 + min(wk * 512 + tid, (KVS - 1) * 512 - 1) * 128, lds0 + 131072 + wu * 1024);
    if (KVS > 9) touch((const char*)X + 65536 + min(4096 + wk * 512 + tid, (KVS - 1) * 512 - 1) * 128, lds0 + 131072 + wu * 1024);
    stB(2, 2);
    bf16x8 bf[16][2], af[8];
    f32x4 acc[2][2] = {}, keep[2];
    const int rb = 64 * kh + 8 * g + (i >> 2), sw = bswz2<128>(rb);
    const char* bp = smem + 65536 + rb * 256 + 8 * (i & 3);
    const char* sa0 = smem + kh * 2048 + i * 128 + 16 * (g ^ (i & 7));
    const char* sa1 = smem + kh * 2048 + i * 128 + 16 * ((4 + g) ^ (i & 7));
#define KV_AF(slot, q) *(const bf16x8*)((((q) & 1) ? sa1 : sa0) + (slot) * 32768 + ((q) >> 1) * 4096)
#pragma unroll
    for (int ch = 0; ch < 8; ++ch) {
        if (ch == 0) { if (KVS > 9) K8_WV(10); else K8_WV(9); } else if (ch < 6) K8_WV(8); else if (ch == 6) K8_WV(4); else K8_WV(0);
        K8_BAR;
#pragma unroll
        for (int e = 0; e < 4; ++e) { const char* p_ = bp + (ch % 3) * 32768 + (e >> 1) * 8192 + 32 * ((2 * cq + (e & 1)) ^ sw); bf[2 * ch + (e >> 1)][e & 1] = tr_frag(p_, p_ + 1024); }
#pragma unroll
        for (int e = 0; e < 4; ++e) af[e] = KV_AF(e >> 1, 2 * ch + (e & 1));
        K8_WL(0); K8_SCHED; K8_BAR;
        if (ch < 5) stB(ch + 3, ch % 3);
        if (ch == 7) { stA(2); stA(3); }
#pragma unroll
        for (int e = 0; e < 8; ++e) acc[e >> 2][e & 1] = __builtin_amdgcn_mfma_f32_16x16x32_bf16(bf[2 * ch + ((e >> 1) & 1)][e & 1], af[e >> 1], acc[e >> 2][e & 1], 0, 0, 0);
    }
    float* red = (float*)(smem + 131072) + l * 4;
#pragma unroll
    for (int p = 0; p < 2; ++p) { *(f32x4*)(red + (p * 8 + wu) * 256) = kh ? acc[p][0] : acc[p][1]; keep[p] = kh ? acc[p][1] : acc[p][0]; }
    unsigned long long tag = 0;
    if (wu == 4) { tag = boot_wait(a.boot); *(volatile unsigned*)(smem + 163840 - 512) = (unsigned)tag & 255u; }
    const rsrc_t rc = __builtin_amdgcn_make_buffer_rsrc((void*)C, 0, KVS * 524288, 0x00020000);
    const int vo = cq * 1024 + (2 * kh + (g >> 1)) * 256 + i * 16 + (g & 1) * 8;
    auto put = [&](const f32x4 o, const int so) {
        const v2u pk = {cvt_pk_bf16(o[0], o[1]), cvt_pk_bf16(o[2], o[3])};
        asm volatile("buffer_store_dwordx2 %0, %1, %2, %3 offen sc1" :: "v"(pk), "v"(vo), "s"(rc), "s"(so) : "memory");
    };
    K8_WL(0); K8_WV(0); K8_BAR;
    tau = __builtin_amdgcn_readfirstlane(*(volatile unsigned*)(smem + 163840 - 512));
#pragma unroll
    for (int p = 0; p < 2; ++p) put(keep[p] + *(const f32x4*)(red + (p * 8 + (wu ^ 4)) * 256), blk_off(0, head, tau) + p * 4096);
    stA(4);
    K8_WV(4); K8_BAR;
    if (tid == 256) __hip_atomic_store((gu64*)(a.boot + 256 + 16 * xc + wk), tag, __ATOMIC_RELAXED, __HIP_MEMORY_SCOPE_AGENT);
    auto stages = [&](auto kc) {
        constexpr int KH = decltype(kc)::value;
        if (KH) K8_BAR;
        f32x4 ac[2] = {ZERO4, ZERO4}, kp = ZERO4;
        int h_first = 2, so = OOB;
        asm volatile("" : "+s"(h_first));
        const char* pn = srcA(5);
        unsigned dn = ldA + 32768 + 4096;
        int r0 = (int)(sa0 - smem) + 2 * 32768, r1 = (int)(sa1 - smem) + 2 * 32768;
        for (int h = h_first; h < 2 * KVS + 4; ++h) {
            const f32x4 oth = *(const f32x4*)(red + (wu ^ 4) * 256);
            *(f32x4*)(red + wu * 256) = ac[KH ^ 1];
            bf16x8 ax[16];
#pragma unroll
            for (int q = 0; q < 16; ++q) ax[q] = *(const bf16x8*)(smem + ((q & 1) ? r1 : r0) + (q >> 1) * 4096);
            K8_SCHED;
            stP(pn, dn);
            put((KH ? ac[1] : kp) + oth, so);
            kp = ac[0];
            __builtin_amdgcn_s_waitcnt(0xC07F);
            K8_WV(10);
            if (__builtin_expect((STRICT >> h) & 1u, 0)) K8_WV(0);
            K8_BAR;
            if (KH && ((STRICT | (0xAAA80u & ~(STRICT << 2))) >> h) & 1u && tid == 256)
                __hip_atomic_store((gu32*)(a.sync + (SY_KV0 + (h / 2 - ((STRICT >> h) & 1u ? 1 : 2)) * 8 + xc) * 32 + wk), 16u, __ATOMIC_RELAXED, __HIP_MEMORY_SCOPE_AGENT);
            if (__builtin_expect(h < 2 * KVS, 1)) {
                __builtin_amdgcn_s_setprio(1);
#pragma unroll
                for (int q = 0; q < 16; ++q)
#pragma unroll
                    for (int n = 0; n < 2; ++n) ac[n] = __builtin_amdgcn_mfma_f32_16x16x32_bf16(bf[q][n], ax[q], q ? ac[n] : ZERO4, 0, 0, 0);
                __builtin_amdgcn_s_setprio(0);
            }
            {
                const int hs = h - 1 + KH;
                so = blk_off(hs >> 1, head, tau) + (hs & 1) * 4096;
                asm volatile("" : "+s"(so));
                so = hs > 1 && hs < 2 * KVS ? so : OOB;
                pn = srcA(h + 4); dn = ldA + (h & 3) * 32768 + 4096;
                r0 = (r0 + 32768) & 131071; r1 = (r1 + 32768) & 131071;
                asm volatile("" : "+s"(so), "+s"(pn), "+s"(dn), "+v"(r0), "+v"(r1));
            }
            K8_BAR;
        }
        if (!KH) K8_BAR;
    };
    if (kh) stages(std::integral_constant<int, 1>{}); else stages(std::integral_constant<int, 0>{});
    K8_WV(0);
}

struct MegaArgs {
    ChainArgs ch;
    const u16* x; u16* out;
    const u16* bx; const u16* wr; const u16* wi;
};
constexpr int MEGA_LDS = 163840;
#define WSTAMP(n)
__global__ __launch_bounds__(512, 2) void mega_kernel(MegaArgs m) {
    extern __shared__ __attribute__((aligned(256))) char chsm[];
    const int b = BIDX, xc = (b & 3) + 4 * (b >> 7), sl = (b >> 3) & 15;
    if ((b & 4) == CHS) { chain_body(m.ch, xc, sl, chsm); return; }
    const ChainArgs& a = m.ch;
    const int wk = sl, tid = TIDX;
    char* ar = arena(a.slots);
    unsigned tauw = 0;
    WSTAMP(0)
    if (wk < 8) kv_stream(a, m.x, (wk < 4 ? a.Wk : a.Wv) + hcol(4 * xc + (wk & 3)), ar + (wk < 4 ? KX_OFF : VX_OFF), 4 * xc + (wk & 3), xc, wk, tauw, chsm);
    if (TCH && wk < 8) {
        const int t = opaque_tid(), n = xc * 8 + wk;
        const unsigned sp = (unsigned)(uintptr_t)(__attribute__((address_space(3))) char*)chsm + 163840 - 256;
        touch((const char*)m.x + 3 * 524288 + (n * 320 + min(t, 319)) * 128, sp);
        touch((const char*)m.bx + (n * 512 + t) * 128, sp);
        touch((const char*)(t < 256 ? m.wr : m.wi) + (n * 256 + (t & 255)) * 128, sp);
    }
    WSTAMP(8) WEV(1)
    {
        const int p = wk & 1, q = wk & 3;
        const unsigned long long todo = wk < 2 ? 0x84u + p : wk < 4 ? TD23 + 0x01010101u * p : wk < 8 ? 0ull
                                      : wk < 12 ? (wk < 10 ? TD89 : 0x7060503010ull) + (wk < 10 ? TM89 : 0x0101010101ull) * q : (wk < 14 ? 0x54341420u : 0x64442422u) + 0x01010101u * p;
        __syncthreads();
        if (wk >= 12) {
            const int n = (xc * 4 + q) * 512 + opaque_tid();
            const unsigned sp = (unsigned)(uintptr_t)(__attribute__((address_space(3))) char*)chsm + 163840 - 256;
            if (TC0 & 1) { touch((const char*)m.x + 3 * 524288 + n * 128, sp); touch((const char*)m.x + 3 * 524288 + min(16384 + n, 20479) * 128, sp); }
            if (TC0 & 2) { touch((const char*)m.bx + n * 128, sp); touch((const char*)m.bx + (16384 + n) * 128, sp); }
            if (TC0 & 4) { touch((const char*)m.wr + n * 128, sp); touch((const char*)m.wi + n * 128, sp); }
        }
        if (todo != 0ull) tile_stream(a, m.x, todo, xc, tauw, wk >= 8, xc * 8 + (wk & 7), (u16*)chsm WEVA);
    }
    WSTAMP(2)
    if (wk < 2) {
        unsigned* mbox = (unsigned*)(chsm + 131072);
        if (tid < 5) mbox[tid] = tid ? 2u : 0u;
        __syncthreads();
        const int w = __builtin_amdgcn_readfirstlane(tid >> 6), h = 4 * xc + (w & 3);
        const rsrc_t rsQ = __builtin_amdgcn_make_buffer_rsrc((void*)(ar + KX_OFF), 0, S_LEN * HD * 4, 0x00020000);
        const rsrc_t rsK = __builtin_amdgcn_make_buffer_rsrc((void*)(ar + KX_OFF), 0, S_LEN * HD * 4, 0x00020000);
        const rsrc_t rsV = __builtin_amdgcn_make_buffer_rsrc((void*)(ar + VX_OFF), 0, S_LEN * HD * 4, 0x00020000);
        const rsrc_t rsS = slot_rsrc(a.slots);
        u16 (*Vs)[VP] = (u16 (*)[VP])(chsm + w * (40 * VP * 2));
        const unsigned tau = tauw;
        int pend = -1;
        bool ready = false;
        for (int s = 2 * wk + (w >> 2), sn; s < NSEG - 1; s = sn) {
            const int band = s >> 3;
            unsigned n = 0;
            if (lane_now() == 0) n = atomicAdd(mbox + 1 + (w & 3), 1u);
            n = __builtin_amdgcn_readfirstlane(n);
            sn = 2 * wk + (int)(n & 1u) + 4 * (int)(n >> 1);
            if (sn - s > 4) __builtin_amdgcn_s_setprio(2); else __builtin_amdgcn_s_setprio(0);
            if (s < 4) wait_ge<32>(a.sync + (SY_KV0 + (KVS - 1) * 8 + xc) * 32 + (tid & 7), 16u, a.tmo, 4100u);
            if (!ready) {
                if (band > 0) wait_ge<32>(a.sync + (SY_KV + band * 8 + xc) * 32 + (tid & 3), 1u, a.tmo, 4100u);
                wait_ge<32>(a.sync + (SY_Q + band * 8 + xc) * 32 + (tid & 3), 1u, a.tmo, 4200u);
            }
            xattn_unit(a, rsQ, rsK, rsV, rsS, s, sn, h, Vs, mbox, tau, pend, ready);
        }
        return;
    }
    if (wk >= 4 && wk < 8) {
        const int fw = (wk - 4) * 8 + xc;
        const rsrc_t rsF = __builtin_amdgcn_make_buffer_rsrc((void*)(ar + FX_OFF), 0, 16 << 20, 0x00020000);
        const rsrc_t rsW = __builtin_amdgcn_make_buffer_rsrc((void*)(ar + WT_OFF), 0, 10 << 20, 0x00020000);
        const unsigned tw = tauw ^ FNEAR;
        {
            const int tq = opaque_tid();
            for (int z = 0; z < FSR; ++z) if (TIDX >= 256) __builtin_amdgcn_s_barrier();
            fft_rows_fwd(m.bx, rsF, tw, 2 * fw + (tq >> 8), chsm + (tq >> 8) * 36864, tq & 255, (const char*)m.wr, (const char*)m.wi, (unsigned)(uintptr_t)(__attribute__((address_space(3))) char*)chsm + 163840 - 256);
            for (int z = 0; z < FSR; ++z) if (TIDX < 256) __builtin_amdgcn_s_barrier();
        }
        for (int t = fw; t < 64; t += 32) {
            __syncthreads();
            filt_transpose(m.wr, m.wi, rsW, tw, t, chsm, opaque_tid());
        }
        wg_publish(a.sync, SY_DONE, 32u, 8);
        WSTAMP(3) WEV(4)
        wg_wait_acquire<16>(a.sync + (SY_DONE + xc) * 32, a.tmo, 6000u);
        {
            const int tq = opaque_tid();
            for (int z = 0; z < fw; ++z) __builtin_amdgcn_s_sleep(FST);
            for (int z = 0; z < FSG; ++z) if (TIDX >= 256) __builtin_amdgcn_s_barrier();
            fft_cols(rsF, rsW, tw, 2 * fw + (tq >> 8), 4, chsm + (tq >> 8) * 36864, tq & 255);
            for (int z = 0; z < FSG; ++z) if (TIDX < 256) __builtin_amdgcn_s_barrier();
        }
        wg_publish(a.sync, SY_DONE + 8, 32u, 8);
        WSTAMP(4) WEV(5)
    }
    if (wk >= 4 && wk < 10) {
        const rsrc_t rsF = __builtin_amdgcn_make_buffer_rsrc((void*)(ar + FX_OFF), 0, 16 << 20, 0x00020000);
        wg_wait_acquire<16>(a.sync + (SY_DONE + 8 + xc) * 32, a.tmo, 6001u);
        const int tq = opaque_tid();
        fft_rows_inv(rsF, tauw ^ FNEAR, m.out, 2 * ((wk & 3) * 8 + xc) + (tq >> 8) + (wk < 8 ? 0 : 192), wk < 8 ? 64 : 32, wk < 8 ? 3 : 2, chsm + (tq >> 8) * 36864, tq & 255);
        WSTAMP(5) WEV(6)
    }
    if (wk >= 12) {
        const int r = 2 * (wk - 12);
        wg_wait_acquire<16>(a.sync + (SY_OX + r * 8 + xc) * 32, a.tmo, 5000u);
        wg_wait_acquire<16>(a.sync + (SY_OX + (r + 1) * 8 + xc) * 32, a.tmo, 5000u);
        WEV(7)
        gemm_tile<256>((u16*)chsm, ar + VX_OFF, 4 * r, tauw, a.Wo + xc * 128, m.out + (size_t)r * 128 * OUTW + xc * 128);
        WEV(8)
    }
    if (wk < 4) {
        const int r = wk + 6;
        wg_wait_acquire<16>(a.sync + (SY_OX + r * 8 + xc) * 32, a.tmo, 5000u);
        WEV(7)
        gemm_tile<128>((u16*)chsm, ar + VX_OFF, 4 * r, tauw, a.Wo + xc * 128, m.out + (size_t)r * 128 * OUTW + xc * 128);
        WEV(8)
        WSTAMP(9)
    }
    if (wk >= 4 && wk < 12) {
        __syncthreads();
        oproj_squad(a, m.out, (wk - 4) * 4 + (xc & 3), xc >> 2, xc, tauw, chsm);
        WSTAMP(7)
    }
}

#if defined(__HIP_DEVICE_COMPILE__)
#pragma clang attribute pop
#endif
extern "C" void kernel_launch(void* const* d_in, const int* in_sizes, int n_in,
                              void* d_out, int out_size, void* d_ws, size_t ws_size,
                              hipStream_t stream) {
    char* ws = (char*)d_ws;
    size_t off = 0;
    auto alloc = [&](size_t bytes) { void* p = ws + off; off += (bytes + 255) & ~(size_t)255; return p; };
    char* slots = (char*)alloc((size_t)ARENA_BYTES + (2u << 20));
    static_assert((SY_WORDS + 2) * 128 <= 131072, "sync block");
    unsigned* sync = (unsigned*)alloc(131072);
    unsigned long long* boot = (unsigned long long*)alloc(4096);

    MegaArgs m;
    m.ch.mem0 = (const u16*)d_in[1]; m.ch.Wq = (const u16*)d_in[2]; m.ch.Wk = (const u16*)d_in[3]; m.ch.Wv = (const u16*)d_in[4]; m.ch.Wo = (const u16*)d_in[5];
    m.ch.out = (u16*)d_out; m.ch.slots = slots; m.ch.tmo = sync + SY_WORDS * 32;
    m.ch.stamps = (unsigned long long*)(ws + (240ull << 20));
    m.ch.sync = sync; m.ch.boot = boot;
    m.x = (const u16*)d_in[0]; m.out = (u16*)d_out;
    m.bx = (const u16*)d_in[7]; m.wr = (const u16*)d_in[8]; m.wi = (const u16*)d_in[9];
    (void)hipFuncSetAttribute((const void*)mega_kernel, hipFuncAttributeMaxDynamicSharedMemorySize, MEGA_LDS);
    mega_kernel<<<256, 512, MEGA_LDS, stream>>>(m);
}
```

```cpp
#if defined(__HIP_DEVICE_COMPILE__)
#pragma clang attribute push (__attribute__((target("no-packed-fp32-ops,no-load-store-opt"))), apply_to = function)
#endif
#include <hip/hip_runtime.h>
#include <hip/hip_bf16.h>
#include <stdint.h>
#include <type_traits>

typedef unsigned short u16;
#define TIDX ((int)__builtin_amdgcn_workitem_id_x())
#define BIDX ((int)__builtin_amdgcn_workgroup_id_x())
__device__ __forceinline__ int opaque_tid() { int t = TIDX; asm volatile("" : "+v"(t)); return t; }
__device__ __forceinline__ int lane_now() { int t; asm volatile("v_mbcnt_lo_u32_b32 %0, -1, 0\n\tv_mbcnt_hi_u32_b32 %0, -1, %0" : "=v"(t)); return t; }
typedef __attribute__((ext_vector_type(8))) short bf16x8;
typedef __attribute__((ext_vector_type(4))) float f32x4;

constexpr int S_LEN = 2048, DM = 1024, NH = 32, DH = 128, MM = 8, SEG = 32;
constexpr int NSEG = S_LEN / SEG;
constexpr int HD = NH * DH;
constexpr int OUTW = 2048;

typedef unsigned v4u __attribute__((ext_vector_type(4)));
typedef unsigned v2u __attribute__((ext_vector_type(2)));
typedef short v4i16 __attribute__((ext_vector_type(4)));
typedef __amdgpu_buffer_rsrc_t rsrc_t;
#define AUX_SC1 16
__device__ __forceinline__ v4u ld_sc1s(rsrc_t rs, int voff, int soff) { return __builtin_amdgcn_raw_buffer_load_b128(rs, voff, soff, AUX_SC1); }
__device__ __forceinline__ void st_sc1s(rsrc_t rs, int voff, int soff, v4u v) { __builtin_amdgcn_raw_buffer_store_b128(v, rs, voff, soff, AUX_SC1); asm volatile("s_nop 1" :: "v"(v) : "memory"); }
__device__ __forceinline__ void st8_sc1s(rsrc_t rs, int voff, int soff, v2u v) { __builtin_amdgcn_raw_buffer_store_b64(v, rs, voff, soff, AUX_SC1); }
__device__ __forceinline__ void st_sc1(rsrc_t rs, int off, v4u v) { __builtin_amdgcn_raw_buffer_store_b128(v, rs, off, 0, AUX_SC1); }
__device__ __forceinline__ void out8(void* p, v2u v) { asm volatile("global_store_dwordx2 %0, %1, off sc1" :: "v"(p), "v"(v) : "memory"); }
__device__ __forceinline__ void out16(void* p, v4u v) { asm volatile("global_store_dwordx4 %0, %1, off sc1\n\ts_nop 1" :: "v"(p), "v"(v) : "memory"); }
typedef __bf16 bf16x2_t __attribute__((ext_vector_type(2)));
typedef float f32x2_t __attribute__((ext_vector_type(2)));
__device__ __forceinline__ unsigned cvt_pk_bf16(float lo, float hi) {
    f32x2_t f = {lo, hi};
    return __builtin_bit_cast(unsigned, __builtin_convertvector(f, bf16x2_t));
}
#define SENT 0xFFBFFFBF
__device__ __forceinline__ unsigned umax3(unsigned a, unsigned b, unsigned c) { return max(max(a, b), c); }
__device__ __forceinline__ unsigned mx4(unsigned acc, v4u v) { return umax3(umax3(acc, v[0], v[1]), v[2], v[3]); }
__device__ __forceinline__ bf16x8 as_frag(v4u v) { return __builtin_bit_cast(bf16x8, v); }
__device__ __forceinline__ float xor16_sum(float x) {
    auto r = __builtin_amdgcn_permlane16_swap(__float_as_uint(x), __float_as_uint(x), false, false);
    return __uint_as_float(r[0]) + __uint_as_float(r[1]);
}
__device__ __forceinline__ float xor32_sum(float x) {
    auto r = __builtin_amdgcn_permlane32_swap(__float_as_uint(x), __float_as_uint(x), false, false);
    return __uint_as_float(r[0]) + __uint_as_float(r[1]);
}
__device__ __forceinline__ float xor16_max(float x) {
    auto r = __builtin_amdgcn_permlane16_swap(__float_as_uint(x), __float_as_uint(x), false, false);
    return fmaxf(__uint_as_float(r[0]), __uint_as_float(r[1]));
}
__device__ __forceinline__ float xor32_max(float x) {
    auto r = __builtin_amdgcn_permlane32_swap(__float_as_uint(x), __float_as_uint(x), false, false);
    return fmaxf(__uint_as_float(r[0]), __uint_as_float(r[1]));
}
constexpr int SPIN_LIMIT = 400000;
constexpr int OOB = 0x7FFFFF00;
#define XST 32
#define SQ_SL 16
#define PLL 0
#define SE 6
#define TDE 72
#define WT 1
#define BWC 6
#define PB 14
#define TD 60
#define PS 46
#define TE0 10
#define PVA 8
#define PV 1
#define PV1 2
#define PV2 11
#define PL 20
#define PF 48
#define PC 31
#define PP 15
#define PKA 28
#define PG0 2
#define PG1 32
#define DLY_U 4
#define DLY_C 4
#define XA_FAST 6
#define XA_D 12
#define SQ_SF 4
#define LW 0
constexpr int NPROBE = 2;
constexpr int M_OFF = 32 * 16384, F_OFF = M_OFF + 2 * 16384, C_OFF = F_OFF + 2 * 16384, REC = C_OFF + 16 * 16384, PERWG = REC / 128, SLOT_BYTES = 34 * 2097152;
static_assert(PERWG == 6656, "arming shares");
__device__ __forceinline__ int rec_base(int s) { return (s >> 1) * 2097152 + (s & 1) * REC; }
__device__ __forceinline__ char* arena(char* slots) { return (char*)(((uintptr_t)slots + 2097151u) & ~(uintptr_t)2097151u); }
constexpr int KX_OFF = SLOT_BYTES, VX_OFF = KX_OFF + (32 << 20), FX_OFF = VX_OFF + (32 << 20), WT_OFF = FX_OFF + (16 << 20), ARENA_BYTES = WT_OFF + (10 << 20);
__device__ __forceinline__ rsrc_t slot_rsrc(char* slots) { return __builtin_amdgcn_make_buffer_rsrc((void*)arena(slots), 0, FX_OFF, 0x00020000); }
__device__ __forceinline__ int blk_off(int seg, int h, unsigned tau) { return seg * 524288 + (((h & 3) >> 1) * 16 + (h >> 2) * 2 + (h & 1)) * 16384 + (int)((tau ^ (unsigned)(seg >> 2)) & 1u) * 8192; }
constexpr int TM_OFF = 124 * 16384;
constexpr int CAL_OFF = 32 * 2097152;
__device__ __forceinline__ int near_half(int s, unsigned tau) { return (int)((tau ^ (unsigned)(s >> 1)) & 1u) * 8192; }
__device__ __forceinline__ int chunk_off(int s, int g, int j, unsigned tau) { return rec_base(s) + (j < 2 ? (g & 3) * 2 + j : 6 + 3 * g + j) * 16384 + near_half(s, tau); }
__device__ __forceinline__ int x_off(int s, int h, unsigned tau) { return chunk_off(s, h >> 2, 2, tau) + (h & 3) * 2048; }
__device__ __forceinline__ int km_off(int s, int h, unsigned tau) { return chunk_off(s, h >> 2, 3, tau) + (h & 3) * 2048; }
__device__ __forceinline__ int m_off(int s, int h, unsigned tau) { return rec_base(s) + M_OFF + (h >> 4) * 16384 + near_half(s, tau) + (h & 15) * 512; }
__device__ __forceinline__ int v_off(int s, int h, unsigned tau) { return chunk_off(s, h >> 2, 4, tau) + (h & 3) * 2048; }
__device__ __forceinline__ int f_off(int s, int g, unsigned tau) { return rec_base(s) + F_OFF + (g >> 2) * 16384 + near_half(s, tau) + (g & 3) * 2048; }
__device__ __forceinline__ int hcol(int h) { return ((h & 3) * 8 + (h >> 2)) * 128; }
constexpr int VP = 144;

struct ChainArgs {
    u16* out;
    const u16* mem0; const u16* Wq; const u16* Wk; const u16* Wv; const u16* Wo;
    char* slots;
    unsigned* tmo;
    unsigned long long* stamps;
    unsigned* sync;
    unsigned long long* boot;
};
constexpr int SY_KV = 0;
constexpr int SY_Q = 64;
constexpr int SY_PROG = 128;
constexpr int SY_OX = 136;
constexpr int SY_DONE = 264;
constexpr int SY_OXS = 472;
constexpr int SY_KV0 = 408;
constexpr int KVS = 8;
constexpr int SY_FLAGS = 680;
constexpr int SY_WORDS = SY_FLAGS + SY_FLAGS / 8;
constexpr int SSEG = 44;
typedef __attribute__((address_space(1))) unsigned gu32;
typedef __attribute__((address_space(1))) unsigned long long gu64;
__device__ __forceinline__ unsigned ld_flag(const unsigned* p) { return __hip_atomic_load((gu32*)p, __ATOMIC_RELAXED, __HIP_MEMORY_SCOPE_AGENT); }
__device__ __forceinline__ unsigned long long boot_tag(const unsigned long long* boot) {
    return __hip_atomic_load((gu64*)(boot + 144), __ATOMIC_RELAXED, __HIP_MEMORY_SCOPE_AGENT) + 1ull;
}
__device__ __forceinline__ unsigned long long boot_wait(const unsigned long long* boot) {
    const int l = TIDX & 63;
    unsigned long long tag = 0, x = 0, y = 0;
#pragma nounroll
    for (int spin = 0; spin < 100000; ++spin) {
        asm volatile("" ::: "memory");
        x = __hip_atomic_load((gu64*)(boot + l), __ATOMIC_RELAXED, __HIP_MEMORY_SCOPE_AGENT);
        y = __hip_atomic_load((gu64*)(boot + 64 + l), __ATOMIC_RELAXED, __HIP_MEMORY_SCOPE_AGENT);
        tag = boot_tag(boot);
        if (__all((x | 3u) == ((tag << 2) | 3u) && (y | 3u) == ((tag << 2) | 3u))) break;
        __builtin_amdgcn_s_sleep(16);
    }
    asm volatile("" ::: "memory");
    const unsigned m = ((unsigned)__ballot(x & 1u) ^ (__popcll(__ballot(x & 2u)) + __popcll(__ballot(y & 2u)) > 64 ? 1u : 0u)) & 1u;
    return (tag << 8) | m;
}
template <int SL>
__device__ __forceinline__ void wait_ge(const unsigned* p, unsigned need, unsigned* tmo, unsigned code) {
#pragma nounroll
    for (int spin = 0;; ++spin) {
        asm volatile("" ::: "memory");
        if (__all(ld_flag(p) >= need)) break;
        if (spin > 400000 / SL) { __hip_atomic_store((gu32*)tmo, code, __ATOMIC_RELAXED, __HIP_MEMORY_SCOPE_AGENT); break; }
        __builtin_amdgcn_s_sleep(SL);
    }
    asm volatile("" ::: "memory");
}
template <int SL>
__device__ __forceinline__ void wg_wait_acquire(const unsigned* p, unsigned* tmo, unsigned code) {
    if (TIDX < 64) {
        wait_ge<SL>(p, 1u, tmo, code);
        __builtin_amdgcn_fence(__ATOMIC_ACQUIRE, "agent");
        asm volatile("s_waitcnt vmcnt(0)" ::: "memory");
    }
    __syncthreads();
}
__device__ __forceinline__ void arrive(unsigned* sync, const int f, const unsigned need, const int n) {
    const int l = lane_now();
    unsigned old = 0;
    if (l == 0) old = __hip_atomic_fetch_add((gu32*)(sync + (SY_FLAGS + (f >> 3)) * 32), 1u, __ATOMIC_RELAXED, __HIP_MEMORY_SCOPE_AGENT);
    if (__builtin_amdgcn_readfirstlane(old) == need - 1u && l < n) __hip_atomic_store((gu32*)(sync + (f + l) * 32), 16u, __ATOMIC_RELAXED, __HIP_MEMORY_SCOPE_AGENT);
}
__device__ __forceinline__ void wg_publish(unsigned* sync, const int f, const unsigned need, const int n) {
    asm volatile("s_waitcnt vmcnt(0)" ::: "memory");
    __syncthreads();
    if (TIDX < 64) arrive(sync, f, need, n);
}
#define TMO_SET(code) { dead = true; if (l == 0) __hip_atomic_store(a.tmo, (code) + s, __ATOMIC_RELAXED, __HIP_MEMORY_SCOPE_AGENT); }

__device__ __forceinline__ void glds16h(unsigned voff, const void* sbase, unsigned lds_dst) {
    unsigned keep;
    asm volatile("s_mov_b32 %0, m0\n\ts_mov_b32 m0, %3\n\ts_nop 0\n\tglobal_load_lds_dwordx4 %1, %2 sc1\n\ts_mov_b32 m0, %0" : "=&s"(keep) : "v"(voff), "s"(sbase), "s"(lds_dst) : "memory");
}
__device__ __forceinline__ void touch(const void* p, unsigned lds_dst) {
    unsigned keep;
    asm volatile("s_mov_b32 %0, m0\n\ts_mov_b32 m0, %2\n\ts_nop 0\n\tglobal_load_lds_dword %1, off\n\ts_mov_b32 m0, %0" : "=&s"(keep) : "v"(p), "s"(lds_dst) : "memory");
}
__device__ __forceinline__ void touch_sc1(const void* p, unsigned lds_dst) {
    unsigned keep;
    asm volatile("s_mov_b32 %0, m0\n\ts_mov_b32 m0, %2\n\ts_nop 0\n\tglobal_load_lds_dword %1, off sc1\n\ts_mov_b32 m0, %0" : "=&s"(keep) : "v"(p), "s"(lds_dst) : "memory");
}
#define WEV(code)
#define WEVP
#define WEVA
constexpr int C2_K = 32768, C2_V = 65536;
constexpr int C2_R1 = 0, R1S = 3200;
constexpr int C2_R3 = C2_R1 + 4 * R1S;
constexpr int C2_MB = 24576;
#define PST(n)
#define LT(n)
#define LTA(n)
#define LTOUT0
#define LTOUT
#define CN(n)
#define CNS
#define CNOUT
#define CLOB16(a, b, c, d, e, f, g, h, i, j, k, l, m, n, o, p) "v" #a, "v" #b, "v" #c, "v" #d, "v" #e, "v" #f, "v" #g, "v" #h, "v" #i, "v" #j, "v" #k, "v" #l, "v" #m, "v" #n, "v" #o, "v" #p
#define LDS3(p) ((__attribute__((address_space(3))) v4i16*)(p))
__device__ __forceinline__ bf16x8 tr_frag(const void* p0, const void* p1) {
    v4i16 lo = __builtin_amdgcn_ds_read_tr16_b64_v4i16(LDS3(p0));
    v4i16 hi = __builtin_amdgcn_ds_read_tr16_b64_v4i16(LDS3(p1));
    return bf16x8{lo[0], lo[1], lo[2], lo[3], hi[0], hi[1], hi[2], hi[3]};
}
typedef __bf16 bf16x8v __attribute__((ext_vector_type(8)));
typedef __bf16 bf16x16v __attribute__((ext_vector_type(16)));
typedef unsigned v8u __attribute__((ext_vector_type(8)));
typedef unsigned v32u __attribute__((ext_vector_type(32)));
#define PUT8(v, o, x) { const v8u x_ = (x); _Pragma("unroll") for (int e_ = 0; e_ < 8; ++e_) (v)[(o) + e_] = x_[e_]; }
#define SUB4(v, o) v4u{(v)[(o)], (v)[(o) + 1], (v)[(o) + 2], (v)[(o) + 3]}
#define PUT4(v, o, x) { const v4u x_ = (x); (v)[(o)] = x_[0]; (v)[(o) + 1] = x_[1]; (v)[(o) + 2] = x_[2]; (v)[(o) + 3] = x_[3]; }
__device__ __forceinline__ v2u tr4(const u16* p) { return __builtin_bit_cast(v2u, __builtin_amdgcn_ds_read_tr16_b64_v4i16(LDS3(p))); }
__device__ __forceinline__ f32x4 smm(v4u a, v8u b, f32x4 c, int idx) {
    return __builtin_amdgcn_smfmac_f32_16x16x64_bf16(__builtin_bit_cast(bf16x8v, a), __builtin_bit_cast(bf16x16v, b), c, idx, 0, 0);
}
__device__ __forceinline__ v8u tr_frag64(const u16* p, const int pitch) {
    const v2u r0 = tr4(p), r1 = tr4(p + 8 * pitch), r2 = tr4(p + 32 * pitch), r3 = tr4(p + 40 * pitch);
    return v8u{r0[0], r1[0], r0[1], r1[1], r2[0], r3[0], r2[1], r3[1]};
}
#define MM(a_, b_, c_) __builtin_amdgcn_mfma_f32_16x16x32_bf16(as_frag(a_), as_frag(b_), c_, 0, 0, 0)
#define STR2(x) #x
#define STR(x) STR2(x)
#define PAUSE(n) ".rept (" STR(n) ") / 4\n\ts_nop 15\n\t.endr\n\t.rept (" STR(n) ") %% 4\n\ts_nop 3\n\t.endr\n\t"
#define ZERO4 f32x4{0.f, 0.f, 0.f, 0.f}
constexpr int XB_OFF = CAL_OFF + 262144, XP = 68;
__device__ __forceinline__ void last_rows(const char* chsm, rsrc_t rsS, const int wu, const int grp, const int c, const unsigned tau) {
    const int l = lane_now();
    __builtin_amdgcn_s_barrier();
    asm volatile("" ::: "memory");
    const float* rp = (const float*)(chsm + C2_K) + (4 * wu + (l >> 4)) * XP + 4 * (l & 15);
    const f32x4 s0 = (*(const f32x4*)rp + *(const f32x4*)(rp + 32 * XP)) + (*(const f32x4*)(rp + 64 * XP) + *(const f32x4*)(rp + 96 * XP));
    v2u o;
    o[0] = cvt_pk_bf16(s0[0], s0[1]); o[1] = cvt_pk_bf16(s0[2], s0[3]);
    st8_sc1s(rsS, l * 8, XB_OFF + ((c >> 1) * 8 + wu) * 16384 + (int)(tau & 1u) * 8192 + (c & 1) * 4096 + grp * 512, o);
}
__device__ __forceinline__ const unsigned* kv_flag(const unsigned* sync, const int sq, const int grp, const int l) { return sync + (sq < KVS ? (SY_KV0 + sq * 8 + grp) * 32 + (l & 7) : (SY_KV + 8 + grp) * 32 + (l & 3)); }
__device__ __forceinline__ void chain_body(const ChainArgs& a, const int grp, const int c, char* chsm) {
    const int wu = __builtin_amdgcn_readfirstlane(TIDX >> 6);
    const int widx = grp * 16 + c;
    const int hs = 4 * grp + (c >> 2), jq = c & 3;
    const rsrc_t rsS = slot_rsrc(a.slots);
    const unsigned l0 = (unsigned)(uintptr_t)(__attribute__((address_space(3))) char*)chsm;
    bool dead = false;

    const int cal_off = CAL_OFF + grp * 32768 + (c >> 3) * 16384 + (c & 7) * 1024;
        if (wu == 0) {
            const int l = lane_now();
            const v4u ff = {SENT, SENT, SENT, SENT};
#pragma unroll
            for (int e = 0; e < 13; ++e) st_sc1s(rsS, l * 16, widx * 2 * PERWG + min(e * 1024, 2 * PERWG - 1024), ff);
            const rsrc_t rsY = __builtin_amdgcn_make_buffer_rsrc((void*)a.sync, 0, 131072, 0x00020000);
            st_sc1s(rsY, l * 16, widx * 1024, v4u{0u, 0u, 0u, 0u});
            __builtin_amdgcn_raw_buffer_store_b32(0u, rsS, (l & 3) * 256 + ((l >> 2) & 1) * 8192, cal_off, AUX_SC1);
            if (c < 2) __builtin_amdgcn_raw_buffer_store_b32(0u, rsS, (l >> 5) * 8192 + (l & 31) * 4, TM_OFF + c * 16384 + grp * 256, AUX_SC1);
        }
    asm volatile("" ::: "memory");
    if (wu >= 4) {
        __builtin_amdgcn_s_sleep(DLY_U);
        const int l = opaque_tid() & 63, i = l & 15, g = l >> 4;
        const int sidx = (i & 1) ? 0xEEEE : 0x4444;
        const int u = wu - 4;
        const int k0 = 256 * u;
        u16* stg = (u16*)(chsm + 40960 + u * 20480);
        v4u af[4];
        v32u wS[6];
        {
            v4u raw[3][16];
            {
                const size_t wo = (size_t)k0 * HD + hcol(hs) + 32 * jq;
                const rsrc_t rq = __builtin_amdgcn_make_buffer_rsrc((void*)(a.Wq + wo), 0, 256 * HD * 2, 0x00020000);
                const rsrc_t rk = __builtin_amdgcn_make_buffer_rsrc((void*)(a.Wk + wo), 0, 256 * HD * 2, 0x00020000);
                const rsrc_t rv = __builtin_amdgcn_make_buffer_rsrc((void*)(a.Wv + wo), 0, 256 * HD * 2, 0x00020000);
                const int vw = (l >> 2) * HD * 2 + (l & 3) * 16;
#pragma unroll
                for (int p = 0; p < 4; ++p) af[p] = *(const v4u*)(a.mem0 + (size_t)(i >> 1) * DM + k0 + 64 * p + 16 * g + 8 * (i & 1));
#pragma unroll
                for (int e = 0; e < 16; ++e) raw[0][e] = __builtin_amdgcn_raw_buffer_load_b128(rq, vw, e * 16 * HD * 2, 0);
#pragma unroll
                for (int e = 0; e < 16; ++e) raw[1][e] = __builtin_amdgcn_raw_buffer_load_b128(rk, vw, e * 16 * HD * 2, 0);
#pragma unroll
                for (int e = 0; e < 12; ++e) raw[2][e] = __builtin_amdgcn_raw_buffer_load_b128(rv, vw, e * 16 * HD * 2, 0);
                __builtin_amdgcn_sched_barrier(0);
#pragma unroll
                for (int e = 0; e < 16; ++e) *(v4u*)(stg + (16 * e + (l >> 2)) * 40 + 8 * (l & 3)) = raw[0][e];
                __builtin_amdgcn_sched_barrier(0);
#pragma unroll
                for (int e = 12; e < 16; ++e) raw[2][e] = __builtin_amdgcn_raw_buffer_load_b128(rv, vw, e * 16 * HD * 2, 0);
            }
            const int fr = 16 * (g >> 1) + 4 * (g & 1) + (i >> 2);
            __builtin_amdgcn_sched_barrier(0);
#pragma unroll
            for (int hx = 0; hx < 3; ++hx) {
                if (hx) {
#pragma unroll
                    for (int e = 0; e < 16; ++e) *(v4u*)(stg + (16 * e + (l >> 2)) * 40 + 8 * (l & 3)) = raw[hx][e];
                }
                __builtin_amdgcn_s_waitcnt(0xC07F);
#pragma unroll
                for (int jj = 0; jj < 2; ++jj)
#pragma unroll
                    for (int p = 0; p < 4; ++p) PUT8(wS[2 * hx + jj], 8 * p, tr_frag64(stg + (64 * p + fr) * 40 + 16 * jj + 4 * (i & 3), 40))
                __builtin_amdgcn_s_waitcnt(0xC07F);
                __builtin_amdgcn_sched_barrier(0);
            }
        }
        PST(u)
        if (u == 0) { PST(4) }
        const unsigned r1w = l0 + C2_R1 + u * R1S + (2 * g * 16 + i) * 4, r1v = l0 + C2_R1 + u * R1S + 2176 + (2 * g * 16 + ((i & 9) | ((i & 2) << 1) | ((i & 4) >> 1))) * 4;
#define LD4(d, va, so, o) "buffer_load_dwordx4 v[" d "], " va ", %[rs], " so " offen offset:" o " sc1\n\t"
#define CHK(a, b, c, d) "v_max3_u32 v244, v244, v" a ", v" b "\n\tv_max3_u32 v244, v244, v" c ", v" d "\n\t"
#define CHK0(a, b, c, d) "v_max_u32 v244, v" a ", v" b "\n\tv_max3_u32 v244, v244, v" c ", v" d "\n\t"
#define SMF(d, a, w) "v_smfmac_f32_16x16x64_bf16 v[" d "], v[" a "], v[" w "], %[sx]\n\t"
#define ADD(d, a, b) "v_add_f32 v" d ", v" a ", v" b "\n\t"
#define Z2(r) "v_mov_b64 v[" r "], 0\n\t"
#define DW2(ad, x, y, o0, o1) "ds_write2_b32 " ad ", v" x ", v" y " offset0:" o0 " offset1:" o1 "\n\t"
#define A0 "224:227"
#define A1 "228:231"
#define A2 "232:235"
#define A3 "236:239"
#define Q0 "200:203"
#define Q1 "204:207"
#define Q2 "208:211"
#define Q3 "212:215"
#define V0 "216:219"
#define V1 "220:223"
#define S1_HEAD \
            Z2("216:217") Z2("218:219") Z2("220:221") Z2("222:223") \
            "s_waitcnt vmcnt(4)\n\t" ZQ \
            "s_waitcnt vmcnt(3)\n\t" SMF(V0, A0, "136:143") CHK0("224", "225", "226", "227") SMF(V1, A0, "168:175") \
            "s_waitcnt vmcnt(2)\n\t" SMF(V0, A1, "144:151") CHK("228", "229", "230", "231") SMF(V1, A1, "176:183") \
            "s_waitcnt vmcnt(1)\n\t" SMF(V0, A2, "152:159") CHK("232", "233", "234", "235") SMF(V1, A2, "184:191") \
            "s_waitcnt vmcnt(0)\n\t" SMF(V0, A3, "160:167") CHK("236", "237", "238", "239") SMF(V1, A3, "192:199") \
            "v_cmp_eq_u32 vcc, " STR(SENT) ", v244\n\t"
#define S1_TAIL(B1, B2) \
            SMF(Q0, A0, "8:15") SMF(Q0, A1, "16:23") SMF(Q0, A2, "24:31") \
            ADD("240", "216", "217") ADD("241", "218", "219") SMF(Q0, A3, "32:39") DW2("%[rv]", "240", "241", "0", "16") \
            ADD("242", "220", "221") ADD("243", "222", "223") SMF(Q1, A0, "40:47") DW2("%[rv]", "242", "243", "128", "144") \
            SMF(Q1, A1, "48:55") SMF(Q1, A2, "56:63") SMF(Q1, A3, "64:71") \
            ADD("240", "200", "201") ADD("241", "202", "203") SMF(Q2, A0, "72:79") DW2("%[rw]", "240", "241", "0", "16") \
            SMF(Q2, A1, "80:87") SMF(Q2, A2, "88:95") "s_nop 1\n\t" \
            ADD("242", "204", "205") ADD("243", "206", "207") SMF(Q2, A3, "96:103") DW2("%[rw]", "242", "243", "136", "152") \
            SMF(Q3, A0, "104:111") SMF(Q3, A1, "112:119") \
            B1        \
            LTA(0) \
            SMF(Q3, A2, "120:127") SMF(Q3, A3, "128:135") "s_nop 0\n\t" \
            ADD("240", "208", "209") ADD("241", "210", "211") DW2("v246", "240", "241", "0", "16") \
            "s_nop 3\n\t" \
            ADD("242", "212", "213") ADD("243", "214", "215") DW2("v246", "242", "243", "136", "152") \
            "s_waitcnt lgkmcnt(0)\n\t" B2 "s_barrier\n\t"
#define ZQ Z2("200:201") Z2("202:203") Z2("204:205") Z2("206:207") Z2("208:209") Z2("210:211") Z2("212:213") Z2("214:215")
#define U_W "{v[8:39]}"(wS[0]), "{v[40:71]}"(wS[1]), "{v[72:103]}"(wS[2]), "{v[104:135]}"(wS[3]), "{v[136:167]}"(wS[4]), "{v[168:199]}"(wS[5])
#define U_CLOB CLOB16(200, 201, 202, 203, 204, 205, 206, 207, 208, 209, 210, 211, 212, 213, 214, 215), CLOB16(216, 217, 218, 219, 220, 221, 222, 223, 224, 225, 226, 227, 228, 229, 230, 231), \
            CLOB16(232, 233, 234, 235, 236, 237, 238, 239, 240, 241, 242, 243, 244, 245, 246, 247)
        __builtin_amdgcn_s_setprio(2);
        {
            asm volatile(
                "v_add_u32 v246, 1088, %[rw]\n\t"
                S1_HEAD "s_nop 0\n\t" S1_TAIL("s_nop 1\n\t", "s_barrier\n\t")
                : LTOUT0
                : U_W, "{v[224:227]}"(af[0]), "{v[228:231]}"(af[1]), "{v[232:235]}"(af[2]), "{v[236:239]}"(af[3]), [sx] "v"(sidx), [rv] "v"(r1v), [rw] "v"(r1w)
                : "memory", "vcc", CLOB16(200, 201, 202, 203, 204, 205, 206, 207, 208, 209, 210, 211, 212, 213, 214, 215), CLOB16(216, 217, 218, 219, 220, 221, 222, 223, 240, 241, 242, 243, 244, 245, 246, 247));
        }
        const unsigned tau = __builtin_amdgcn_readfirstlane(*(volatile unsigned*)(chsm + C2_MB + 8));
        const unsigned long long wmask = u == 3 ? (c == 0 ? ~0ull : 0ull) : u == 1 ? 0x0020202020202000ull : u == 2 ? 1ull << (NSEG - 3) : 0ull;
        const int pvv = u == 3 ? 2 : -1, pvm = u == 3 ? -1 : 0;
        const unsigned* pfl = a.sync + (u == 3 ? SY_PROG + grp : u == 1 ? SY_KV + 16 + grp : SY_Q + 7 * 8 + grp) * 32;
        int s_first = 0;
        asm volatile("s_branch 1f\n\t.p2align 8\n1:" : "+s"(s_first));
        for (int s = s_first; s < NSEG - 1; ++s) {
            __builtin_amdgcn_s_setprio(0);
            LT(1)
            const int pv = (wmask >> s) & 1ull ? pvv + (s & pvm) : 0;
            const unsigned* pa = pfl;
            if (pv < 0) pfl += 256;
            const int cb = grp & 3;
            int soffc = rec_base(s) + C_OFF + c * 16384 + near_half(s, tau) + cb * 2048;
            int o_as = rec_base(s + 1) + ((c >> 3) + (grp >> 2) * 4) * 16384 + near_half(s + 1, tau) + ((4 * c + cb) & 31) * 256;
            int soff = chunk_off(s + 1, grp, u >> 1, tau) + (u & 1) * 4096;
            const int o_tm = (s >> 1) * 2097152 + TM_OFF + (s & 1) * 16384;
            int lim = dead ? 0 : SPIN_LIMIT, bad = 0;
            const bool rcd = u > 0 || widx < 32;
            const int pc = u > 0 ? 3 * widx + u - 1 : rcd ? 384 + widx : s * 96 + widx - 32;
            const int o_arm = (rcd ? s + 2 < NSEG : pc < 512) ? (rcd ? rec_base(s + 2) : XB_OFF) + (pc >> 3) * 16384 + near_half(rcd ? s + 2 : 0, tau) + (pc & 7) * 1024 : OOB;
            const unsigned* ck = s <= KVS ? kv_flag(a.sync, s + 2, grp, 0) : nullptr;
            const char* vb = arena(a.slots) + (s + 2 >= NSEG ? KX_OFF : VX_OFF) + (blk_off(min(s + 2, NSEG - 1), 4 * grp + 2 * u - 2, tau) ^ (s + 2 >= NSEG ? 8192 : 0));
            asm volatile("" : "+s"(soffc), "+s"(o_as), "+s"(soff), "+s"(lim), "+s"(vb), "+s"(ck));
#define GL(o) "global_load_lds_dwordx4 %[l16], s[84:85] offset:" o " sc1\n\t"
#define GL4 GL("0") GL("1024") GL("2048") GL("3072")
#define GLN(d) "s_add_u32 m0, m0, 4096\n\ts_add_u32 s84, s84, " d "\n\ts_addc_u32 s85, s85, 0\n\t"
#define PKE PAUSE(PKA)
#define C_TRY(x, x0, x1, x2, x3, y, y0, y1, y2, y3) \
                "s_waitcnt vmcnt(3)\n\t" \
                "v_mfma_f32_16x16x32_bf16 v[240:243], v[216:219], v[" x "], 0\n\t" CHK0(x0, x1, x2, x3) \
                "s_waitcnt vmcnt(2)\n\t" \
                "v_mfma_f32_16x16x32_bf16 v[240:243], v[216:219], v[" y "], v[240:243]\n\t" CHK(y0, y1, y2, y3) \
                "v_cmp_eq_u32 vcc, " STR(SENT) ", v244\n\t" \
                "s_cbranch_vccz 3b\n\t" CN(1) \
                LD4(x, "%[l16]", "%[sc]", "0") LD4(y, "%[l16]", "%[sc]", "1024")
            asm volatile(
                "v_and_b32 v221, 16, %[l16]\n\t"
                "v_mov_b32 v222, 0x3f80\n\t"
                "v_bfe_u32 v223, %[l16], 5, 2\n\t"
                "v_lshlrev_b32 v221, v221, v222\n\t"
                "v_cmp_eq_u32 vcc, 0, v223\n\tv_and_b32 v220, 0xf0, %[l16]\n\tv_bfe_u32 v222, %[l16], 8, 1\n\tv_cndmask_b32 v216, 0, v221, vcc\n\t"
                "v_cmp_eq_u32 vcc, 1, v223\n\tv_lshl_add_u32 v220, v222, 3, v220\n\tv_lshrrev_b32 v222, 9, %[l16]\n\tv_cndmask_b32 v217, 0, v221, vcc\n\t"
                "v_cmp_eq_u32 vcc, 2, v223\n\tv_lshl_add_u32 v220, v222, 30, v220\n\ts_nop 0\n\tv_cndmask_b32 v218, 0, v221, vcc\n\t"
                "v_cmp_eq_u32 vcc, 3, v223\n\ts_nop 1\n\tv_cndmask_b32 v219, 0, v221, vcc\n\t"
                "v_mul_u32_u24 v222, 0x8000, v222\n\t"
                "v_and_b32 v221, 0xff, v220\n\t"
                "v_add_u32 v221, v221, v222\n\t"
                "v_lshrrev_b32 v247, 2, %[l16]\n\t"
                PKE
                "v_mov_b32 v240, " STR(SENT) "\n\tv_mov_b32 v241, v240\n\tv_mov_b32 v242, v240\n\tv_mov_b32 v243, v240\n\t"
                "buffer_store_dwordx4 v[240:243], %[l16], %[rs], %[arm] offen sc1\n\t"
                LTA(2)
                "s_setprio 2\n\t"
                "s_barrier\n\t"
                LTA(3)
                "v_add_u32 v246, 1088, %[rw]\n\t"
                "s_cmp_lt_u32 %[uu], 1\n\t"
                "s_cbranch_scc1 7f\n\t"
                "s_cmp_eq_u32 %[uu], 3\n\t"
                "s_cbranch_scc1 20f\n\t"
                "s_memrealtime s[84:85]\n\t"
                "v_lshrrev_b32 v241, 9, %[l16]\n\t"
                "v_and_b32 v242, 0x7c, v247\n\t"
                "v_lshl_add_u32 v241, v241, 13, v242\n\t"
                "s_waitcnt lgkmcnt(0)\n\t"
                "v_mov_b32 v240, s84\n\t"
                "buffer_store_dword v240, v241, %[rs], %[ts] offen sc1\n\t"
                "s_cmp_eq_u64 %[ck], 0\n\t"
                "s_cbranch_scc1 14f\n\t"
                "v_and_b32 v241, %[cm], v247\n\t"
                "global_load_dword v240, v241, %[ck] sc1\n\t"
                "v_mov_b32 v241, %[cw]\n\t"
                "s_waitcnt vmcnt(0)\n\t"
                "v_cmp_gt_u32 vcc, 16, v240\n\t"
                "s_cmp_eq_u64 vcc, 0\n\t"
                "s_cselect_b32 s84, 1, 0\n\t"
                "v_mov_b32 v240, s84\n\t"
                "ds_write_b32 v241, v240\n\t"
                "s_cbranch_scc0 11f\n\t"
                "14:\n\t"
                "s_mov_b32 s86, m0\n\t"
                "s_mov_b64 s[84:85], %[vb]\n\t"
                "s_mov_b32 m0, %[lv]\n\t"
                PAUSE(PG0)
                "s_cmp_eq_u32 %[uu], 1\n\t"
                "s_cbranch_scc1 12f\n\t"
                PAUSE(PG1)
                "12:\n\t"
                GL4 GLN("4096") GL4 GLN("12288") GL4 GLN("4096") GL4
                "s_mov_b32 m0, s86\n\t"
                "11:\n\t"
                "s_cmp_lt_i32 %[pv], 0\n\t"
                "s_cbranch_scc0 8f\n\t"
                "v_and_b32 v241, 12, v247\n\t"
                "10:\n\t"
                "global_load_dword v240, v241, %[pa] sc1\n\t"
                "s_waitcnt vmcnt(0)\n\t"
                "v_cmp_gt_u32 vcc, 16, v240\n\t"
                "s_cbranch_vccz 8f\n\t"
                "s_sleep 6\n\t"
                "s_sub_u32 %[lim], %[lim], 1\n\t"
                "s_cbranch_scc0 10b\n\t"
                "s_mov_b32 %[bad], 3000\n\t"
                "s_mov_b32 %[lim], 0\n\t"
                "s_branch 8f\n\t"
                "20:\n\t"
                PAUSE(PS)
                "s_memrealtime s[84:85]\n\t"
                "s_load_dword s86, %[tb], 0\n\ts_load_dword s87, %[tb], 0x100\n\ts_load_dword s88, %[tb], 0x200\n\ts_load_dword s89, %[tb], 0x300\n\t"
                "s_load_dword s90, %[tb], 0x400\n\ts_load_dword s91, %[tb], 0x500\n\ts_load_dword s92, %[tb], 0x600\n\ts_load_dword s93, %[tb], 0x700\n\t"
                "s_waitcnt lgkmcnt(0)\n\t"
                "s_sub_u32 s86, s84, s86\n\ts_sub_u32 s87, s84, s87\n\ts_sub_u32 s88, s84, s88\n\ts_sub_u32 s89, s84, s89\n\t"
                "s_sub_u32 s90, s84, s90\n\ts_sub_u32 s91, s84, s91\n\ts_sub_u32 s92, s84, s92\n\ts_sub_u32 s93, s84, s93\n\t"
                "s_or_b32 s85, s86, s87\n\ts_min_u32 s86, s86, s87\n\ts_or_b32 s85, s85, s88\n\ts_min_u32 s86, s86, s88\n\ts_or_b32 s85, s85, s89\n\ts_min_u32 s86, s86, s89\n\t"
                "s_memrealtime s[88:89]\n\t"
                "s_or_b32 s85, s85, s90\n\ts_min_u32 s86, s86, s90\n\ts_or_b32 s85, s85, s91\n\ts_min_u32 s86, s86, s91\n\ts_or_b32 s85, s85, s92\n\ts_min_u32 s86, s86, s92\n\t"
                "s_or_b32 s85, s85, s93\n\ts_min_u32 s86, s86, s93\n\t"
                "s_cmp_gt_u32 s85, 127\n\t"
                CNS
                "s_cselect_b32 s85, " STR(TE0) ", 127\n\t"
                "s_min_u32 s86, s86, s85\n\t"
                "s_sub_u32 s84, s84, s86\n\t"
                "s_add_u32 s84, s84, %[td]\n\t"
                "s_cmp_lt_i32 %[pv], 1\n\t"
                "s_cbranch_scc1 4f\n\t"
                "v_mov_b32 v247, %[pv]\n\t"
                "s_mov_b64 exec, 1\n\t"
                "global_store_dword %[l16], v247, %[pa] sc1\n\t"
                "s_mov_b64 exec, -1\n\t"
                "4:\n\t"
                "s_waitcnt lgkmcnt(0)\n\t"
                "s_sub_i32 s84, s84, s88\n\t"
                "s_max_i32 s84, s84, 0\n\t"
                "s_min_i32 s84, s84, 40\n\t"
                "21:\n\t"
                "s_sub_u32 s84, s84, 1\n\t"
                "s_cbranch_scc0 21b\n\t"
                "s_branch 8f\n\t"
                "7:\n\t"
                PAUSE(PC)
                LD4(A0, "%[l16]", "%[sc]", "0") LD4(A1, "%[l16]", "%[sc]", "1024")
                PAUSE(PP)
                LD4(A2, "%[l16]", "%[sc]", "0") LD4(A3, "%[l16]", "%[sc]", "1024")
                "s_waitcnt vmcnt(3)\n\t"
                "v_mfma_f32_16x16x32_bf16 v[240:243], v[216:219], v[224:227], 0\n\t" CHK0("224", "225", "226", "227")
                "s_waitcnt vmcnt(2)\n\t"
                "v_mfma_f32_16x16x32_bf16 v[240:243], v[216:219], v[228:231], v[240:243]\n\t" CHK("228", "229", "230", "231")
                "v_cmp_eq_u32 vcc, " STR(SENT) ", v244\n\t"
                "s_cbranch_vccnz 30f\n\t"
                "3:\n\t"
                "s_nop 3\n\t"
                "v_cvt_pk_bf16_f32 v244, v240, v241\n\t"
                "v_cvt_pk_bf16_f32 v245, v242, v243\n\t"
                "buffer_store_dwordx2 v[244:245], v221, %[rs], %[oas] offen sc1\n\t"
                PAUSE(PB)
                "8:\n\t"
                LTA(4)
                "s_barrier\n\t"
                PAUSE(1)
                "2:\n\t"
                LD4(A0, "%[l16]", "%[sa]", "0") LD4(A1, "%[l16]", "%[sa]", "1024") LD4(A2, "%[l16]", "%[sa]", "2048") LD4(A3, "%[l16]", "%[sa]", "3072")
                S1_HEAD
                "s_cbranch_vccnz 6f\n\t"
                "5:\n\t"
                LTA(5)
                S1_TAIL("s_waitcnt lgkmcnt(0)\n\ts_barrier\n\t", "")
                "s_setprio 0\n\t"
                "s_branch 9f\n\t"
                "30:\n\t" CN(1)
                LD4(A0, "%[l16]", "%[sc]", "0") LD4(A1, "%[l16]", "%[sc]", "1024")
                "1:\n\t"
                C_TRY(A2, "232", "233", "234", "235", A3, "236", "237", "238", "239")
                C_TRY(A0, "224", "225", "226", "227", A1, "228", "229", "230", "231")
                "s_sub_u32 %[lim], %[lim], 1\n\t"
                "s_cbranch_scc0 1b\n\t"
                "s_mov_b32 %[bad], 2000\n\t"
                "s_mov_b32 %[lim], 0\n\t"
                "s_branch 3b\n\t"
                "6:\n\t" CN(65536)
                "v_mov_b32 v240, %[tg]\n\t"
                "v_mov_b32 v241, %[lf]\n\t"
                "ds_write_b32 v241, v240\n\t"
                "s_nop 0\n\t"
                "s_sub_u32 %[lim], %[lim], 1\n\t"
                "s_cbranch_scc0 2b\n\t"
                "s_mov_b32 %[bad], 1\n\t"
                "s_branch 5b\n\t"
                "9:\n\t"
                : [lim] "+s"(lim), [bad] "+s"(bad), [oas] "+s"(o_as) LTOUT CNOUT
                : U_W, [l16] "v"(l * 16), [rs] "s"(rsS), [sx] "v"(sidx), [rv] "v"(r1v), [rw] "v"(r1w),
                  [sc] "s"(soffc), [sa] "s"(soff), [uu] "s"(u), [pv] "s"(pv), [pa] "s"(pa), [vb] "s"(vb), [ck] "s"(ck), [cm] "s"(s + 2 < KVS ? 28 : 12), [cw] "s"(l0 + C2_MB + 32 + (s & 1) * 16 + u * 4), [lv] "s"(l0 + (s + 2 >= NSEG ? C2_V + 32768 : s & 1 ? C2_K : C2_V) + (u - 1) * 16384), [td] "s"(s < SE ? TDE : TD), [ts] "s"(c == 0 && (u == 2 || s < NSEG - 3) ? o_tm + grp * 256 + (2 - u) * 2097152 : OOB), [tb] "s"(arena(a.slots) + o_tm + near_half(s, tau)),

                  [arm] "s"(o_arm),
                  [tg] "s"(s + 1), [lf] "s"(l0 + C2_MB + 16)
                : "memory", "vcc", "scc", "s84", "s85", "s86", "s87", "s88", "s89", "s90", "s91", "s92", "s93", U_CLOB);
            if (__builtin_expect(bad != 0, 0)) TMO_SET((unsigned)bad)
        }
        last_rows(chsm, rsS, wu, grp, c, tau);
        if (u == 3 && widx == 0 && l == 0) __hip_atomic_store((gu64*)(a.boot + 144), *(volatile unsigned long long*)(chsm + C2_MB), __ATOMIC_RELAXED, __HIP_MEMORY_SCOPE_AGENT);
        return;
    }
    const int l = opaque_tid() & 63, i = l & 15, g = l >> 4;
    const int sidx = (i & 1) ? 0xEEEE : 0x4444;
    const int ha = 4 * grp + wu;
    unsigned long long tag;
    v32u wO[2];
    {
        u16* stg = (u16*)(chsm + 122880 + wu * 10240);
        tag = boot_tag(a.boot);
        asm volatile("" ::: "memory");
        v4u rawo[16];
        const u16* src = a.Wo + (size_t)(hcol(ha) + (l >> 3)) * DM + 64 * c + 8 * (l & 7);
#pragma unroll
        for (int e = 0; e < 16; ++e) rawo[e] = *(const v4u*)(src + (size_t)8 * e * DM);
        __builtin_amdgcn_sched_barrier(0);
        if (wu == 0) {
            asm volatile("s_waitcnt vmcnt(16)" ::: "memory");
            unsigned tm[2] = {~0u, ~0u};
            const char* pb = arena(a.slots) + cal_off;
#pragma unroll
            for (int k = 0; k < 2 * NPROBE; ++k) {
                unsigned long long c0, c1; unsigned d;
                asm volatile("s_memtime %0\n\ts_waitcnt lgkmcnt(0)\n\ts_load_dword %2, %3, %4\n\ts_waitcnt lgkmcnt(0)\n\ts_memtime %1\n\ts_waitcnt lgkmcnt(0)"
                             : "=&s"(c0), "=&s"(c1), "=&s"(d) : "s"(pb), "i"((k >> 1) * 256 + (k & 1) * 8192) : "memory");
                tm[k & 1] = min(tm[k & 1], (unsigned)(c1 - c0));
            }
            const unsigned side = (__builtin_amdgcn_s_getreg(20 | (3 << 11)) >> 2) & 1u;
            if (l == 0) __hip_atomic_store((gu64*)(a.boot + widx), (tag << 2) | (((tm[0] > tm[1] ? 1u : 0u) ^ side) << 1) | side, __ATOMIC_RELAXED, __HIP_MEMORY_SCOPE_AGENT);
            PST(7)
        }
#pragma unroll
        for (int m = 0; m < 2; ++m) {
#pragma unroll
            for (int e = 0; e < 8; ++e) *(v4u*)(stg + (8 * e + (l >> 3)) * 72 + 8 * (l & 7)) = rawo[8 * m + e];
            __builtin_amdgcn_s_waitcnt(0xC07F);
#pragma unroll
            for (int ct = 0; ct < 4; ++ct) {
                const u16* p = stg + (8 * g + (i >> 2)) * 72 + 16 * ct + 4 * (i & 3);
                const v2u r0 = tr4(p), r1 = tr4(p + 4 * 72), r2 = tr4(p + 32 * 72), r3 = tr4(p + 36 * 72);
                PUT4(wO[ct >> 1], (ct & 1) * 16 + m * 8, (v4u{r0[0], r0[1], r1[0], r1[1]})) PUT4(wO[ct >> 1], (ct & 1) * 16 + m * 8 + 4, (v4u{r2[0], r2[1], r3[0], r3[1]}))
            }
            __builtin_amdgcn_s_waitcnt(0xC07F);
        }
        if (wu == 0) { PST(5) }
        if (widx == 0 && wu == 0) *(volatile unsigned long long*)(chsm + C2_MB) = tag;
    }
    if (wu >= 2) touch_sc1(arena(a.slots) + (size_t)min(l + (wu - 2) * 64, FX_OFF / 2097152 - 1) * 2097152 + 2097024 - 128, l0 + C2_MB + 256);
    unsigned long long fw = 0;
#pragma nounroll
    for (int spin = 0; spin < 100000; ++spin) {
        asm volatile("" ::: "memory");
        fw = __hip_atomic_load((gu64*)(a.boot + 256 + 16 * grp + (l & 7)), __ATOMIC_RELAXED, __HIP_MEMORY_SCOPE_AGENT);
        if (__all((fw | 255u) == ((tag << 8) | 255u))) break;
        __builtin_amdgcn_s_sleep(4);
    }
    asm volatile("" ::: "memory");
    const unsigned tau = __builtin_amdgcn_readfirstlane((unsigned)fw & 255u);
    if (wu == 3) { *(volatile unsigned*)(chsm + C2_MB + 8) = tau; *(volatile unsigned*)(chsm + C2_MB + 16) = ~0u; __builtin_amdgcn_s_waitcnt(0xC07F); }
    if (wu == 1) { PST(9) }

    const unsigned r3a = l0 + C2_R3 + wu * 2048 + g * 128 + i * 4;
    v32u yb;
    v8u kx;
    v8u sel; v2u sel3;
#pragma unroll
    for (int d = 0; d < 8; ++d) sel[d] = (i >> 1) == d ? (0x3C00u << ((i & 1) * 16)) : 0u;
#pragma unroll
    for (int d = 0; d < 2; ++d) sel3[d] = (i & 3) == 0 && (i >> 3) == d ? (0x3C00u << (((i >> 2) & 1) * 16)) : 0u;
    asm volatile("s_setreg_imm32_b32 hwreg(HW_REG_MODE, 23, 1), 1\n\ts_nop 3");
#define MF(d, a, b, c) "v_mfma_f32_16x16x32_bf16 v[" d "], v[" a "], v[" b "], " c "\n\t"
#define PX4(d0, d1, d2, d3, a, w0, w1, w2, w3) MF(d0, a, w0, "v[" d0 "]") MF(d1, a, w1, "v[" d1 "]") MF(d2, a, w2, "v[" d2 "]") MF(d3, a, w3, "v[" d3 "]")
#define PXT(d0, d1, d2, d3, a0, a1, a2, a3) MF(d0, a0, "32:35", "0") MF(d1, a0, "48:51", "0") MF(d2, a0, "64:67", "0") MF(d3, a0, "80:83", "0") \
        PX4(d0, d1, d2, d3, a1, "36:39", "52:55", "68:71", "84:87") PX4(d0, d1, d2, d3, a2, "40:43", "56:59", "72:75", "88:91") PX4(d0, d1, d2, d3, a3, "44:47", "60:63", "76:79", "92:95")
#define CVP(d, a, b) "v_cvt_pk_bf16_f32 v" d ", v" a ", v" b "\n\t"
#define PROJ_X(cnt) \
        "s_waitcnt " cnt "(8)\n\t" PXT("192:195", "196:199", "200:203", "204:207", "160:163", "164:167", "168:171", "172:175") \
        "s_waitcnt " cnt "(4)\n\t" PXT("208:211", "212:215", "216:219", "220:223", "176:179", "180:183", "184:187", "188:191") \
        CVP("128", "192", "193") CVP("129", "194", "195") CVP("136", "196", "197") CVP("137", "198", "199") CVP("144", "200", "201") CVP("145", "202", "203") CVP("152", "204", "205") CVP("153", "206", "207") \
        CVP("132", "208", "209") CVP("133", "210", "211") CVP("140", "212", "213") CVP("141", "214", "215") CVP("148", "216", "217") CVP("149", "218", "219") CVP("156", "220", "221") CVP("157", "222", "223") \
        "s_waitcnt " cnt "(0)\n\t"
#define CLOB_PX CLOB16(160, 161, 162, 163, 164, 165, 166, 167, 168, 169, 170, 171, 172, 173, 174, 175), CLOB16(176, 177, 178, 179, 180, 181, 182, 183, 184, 185, 186, 187, 188, 189, 190, 191), \
        CLOB16(192, 193, 194, 195, 196, 197, 198, 199, 200, 201, 202, 203, 204, 205, 206, 207), CLOB16(208, 209, 210, 211, 212, 213, 214, 215, 216, 217, 218, 219, 220, 221, 222, 223)
#define LDX(d, va, so, o) "buffer_load_dwordx4 v[" d "], " va ", %[rs], " so " offen offset:" o " sc1\n\t"
#define DSV(d, o) "ds_read_b128 v[" d "], %[lv] offset:" o "\n\t"
    const int voffX = ((i >> 1) & 1) * 4096 + g * 256 + (2 * (i >> 2) + (i & 1)) * 16;
    const int pk = (((i >> 1) & 3) << 1) | (i >> 3);
    const unsigned hv = wu >= 2 ? l0 + C2_R1 + 2176 + ((wu & 1) * 128 + ((l >> 4) + 4 * ((l >> 3) & 1)) * 16 + 8 * ((l >> 1) & 1) + 4 * ((l >> 2) & 1) + 2 * (l & 1)) * 4
                                : l0 + C2_R1 + (i & 1) * 544 + (i >> 1) * 64 + g * 16;
    const unsigned hk = l0 + C2_R1 + 1088 + (i & 1) * 544 + pk * 64 + g * 16;
    const int vo1 = wu >= 2 ? l * 4 : wu == 1 ? (g >> 1) * 1024 + i * 16 + (g & 1) * 8 : (i & 1) ? 1 << 30 : (i >> 1) * 16 + 4 * g;
    const int vo2 = (2 * (i & 1) + (g >> 1)) * 128 + pk * 16 + (g & 1) * 8;
    const int voffK = (g >> 1) * 256 + i * 16 + (g & 1) * 8;
#define RD4(w, ad, a, b, c, d) "ds_read_" w " v[" a "], " ad "\n\tds_read_" w " v[" b "], " ad " offset:3200\n\tds_read_" w " v[" c "], " ad " offset:6400\n\tds_read_" w " v[" d "], " ad " offset:9600\n\t"
#define AD4(d0, d1, d2, d3, a0, a1, a2, a3) ADD(d0, d0, a0) ADD(d1, d1, a1) ADD(d2, d2, a2) ADD(d3, d3, a3)
#define QSC(r) "v_mul_f32 v" r ", 0x3e0293ee, v" r "\n\t"
#define FLD "buffer_load_dword v114, %[vf], %[rs], %[fl] offen sc1\n\t"
#define DUM "buffer_store_dword v168, off, %[rs], %[ob] sc1\n\t"
#define QHEAD "s_barrier\n\t" RD4("b128", "%[hv]", "160:163", "164:167", "168:171", "172:175") FLD \
        "s_waitcnt lgkmcnt(2)\n\t" AD4("160", "161", "162", "163", "164", "165", "166", "167") \
        "s_waitcnt lgkmcnt(0)\n\t" AD4("168", "169", "170", "171", "172", "173", "174", "175") \
        "s_barrier\n\t"
#define QTAIL AD4("160", "161", "162", "163", "168", "169", "170", "171") QSC("160") QSC("161") QSC("162") QSC("163")
#define CVH(d, a, b) "v_cvt_pk_f16_f32 v" d ", v" a ", v" b "\n\t"
#define SWA(d, a, b) "v_add_f32_dpp v" d ", v" a ", v" b " quad_perm:[1,0,3,2] row_mask:0xf bank_mask:0xf\n\t"
#define KLD(so) "buffer_load_dwordx2 v[96:97], %[vk], %[rs], " so " offen sc1\n\tbuffer_load_dwordx2 v[102:103], %[vk], %[rs], " so " offen offset:512 sc1\n\t" \
        "s_add_u32 " so ", " so ", 4096\n\t" \
        "buffer_load_dwordx2 v[100:101], %[vk], %[rs], " so " offen sc1\n\tbuffer_load_dwordx2 v[98:99], %[vk], %[rs], " so " offen offset:512 sc1\n\t"
    auto hand_over = [&](const int o_1, const int o_2, const int o_f, const int tg) {
        int dmy = 0;
        asm volatile(
            "s_cmp_eq_u32 %[wu], 0\n\t"
            "s_cbranch_scc1 2f\n\t"
            "s_cmp_eq_u32 %[wu], 1\n\t"
            "s_cbranch_scc1 1f\n\t"
            "s_barrier\n\t"
            RD4("b64", "%[hv]", "160:161", "162:163", "164:165", "166:167") FLD
            "s_waitcnt lgkmcnt(2)\n\t" ADD("160", "160", "162") ADD("161", "161", "163")
            "s_waitcnt lgkmcnt(0)\n\t" ADD("164", "164", "166") ADD("165", "165", "167") ADD("160", "160", "164") ADD("161", "161", "165")
            CVP("168", "160", "161")
            "buffer_store_dword v168, %[v1], %[rs], %[o1] offen sc1\n\t"
            "s_barrier\n\t"
            DUM PAUSE(PV2)
            "s_branch 3f\n\t"
            "5:\n\t"
            "v_mov_b32 v115, %[lf]\n\t"
            "ds_read_b32 v115, v115\n\t"
            "s_waitcnt lgkmcnt(0)\n\t"
            "v_cmp_eq_u32 vcc, %[tg], v115\n\t"
            "s_cbranch_vccnz 4f\n\t" CN(256)
            PAUSE(PL)
            "s_branch 4f\n\t"
            "1:\n\t"
            Z2("176:177") Z2("178:179")
            "s_waitcnt vmcnt(0)\n\t"
            QHEAD QTAIL
            "s_mov_b64 exec, %[em]\n\t"
            CVP("176", "160", "161") CVP("177", "162", "163")
            "s_not_b64 exec, exec\n\t"
            CVP("178", "160", "161") CVP("179", "162", "163")
            "s_mov_b64 exec, -1\n\t"
            "s_nop 0\n\t"
            MF("180:183", "96:99", "176:179", "0") MF("184:187", "100:103", "176:179", "0")
            "s_nop 7\n\t"
            SWA("180", "184", "180") SWA("181", "185", "181") SWA("182", "186", "182") SWA("183", "187", "183")
            CVH("188", "180", "181") CVH("189", "182", "183")
            "buffer_store_dwordx2 v[188:189], %[v1], %[rs], %[o1] offen sc1\n\t"
            DUM PAUSE(PV1)
            "s_branch 3f\n\t"
            "2:\n\t"
            QHEAD
            RD4("b128", "%[hk]", "176:179", "180:183", "184:187", "188:191")
            QTAIL CVP("164", "160", "161") CVP("165", "162", "163")
            "s_waitcnt lgkmcnt(2)\n\t" AD4("176", "177", "178", "179", "180", "181", "182", "183")
            "s_waitcnt lgkmcnt(0)\n\t" AD4("184", "185", "186", "187", "188", "189", "190", "191") AD4("176", "177", "178", "179", "184", "185", "186", "187")
            CVP("166", "176", "177") CVP("167", "178", "179")
            "s_nop 1\n\t"
            "v_mfma_f32_16x16x16_bf16 v[168:171], v[166:167], v[164:165], 0\n\t"
            "buffer_store_dwordx2 v[166:167], %[v2], %[rs], %[o2] offen sc1\n\t"
            "s_nop 6\n\t"
            SWA("172", "169", "168") SWA("173", "171", "170")
            CVH("174", "172", "173")
            "buffer_store_dword v174, %[v1], %[rs], %[o1] offen sc1\n\t"
            "3:\n\t"
            PAUSE(PVA)
            "s_waitcnt vmcnt(2)\n\t"
            "v_cmp_ne_u32 vcc, " STR(SENT) ", v114\n\t"
            "s_cbranch_vccnz 5b\n\t"
            "4:\n\t"
            : [dm] "+s"(dmy) CNOUT : "{v[96:103]}"(kx), [fl] "s"(o_f), [vf] "v"(i * 128), [ob] "s"(1 << 30), [tg] "s"(tg), [lf] "s"(l0 + C2_MB + 16), [wu] "s"(wu), [hv] "v"(hv), [hk] "v"(hk), [v1] "v"(vo1), [v2] "v"(vo2), [rs] "s"(rsS), [o1] "s"(o_1), [o2] "s"(o_2), [em] "s"(0x5555555555555555ull)
            : "memory", "scc", "vcc", "v114", "v115", CLOB16(160, 161, 162, 163, 164, 165, 166, 167, 168, 169, 170, 171, 172, 173, 174, 175), CLOB16(176, 177, 178, 179, 180, 181, 182, 183, 184, 185, 186, 187, 188, 189, 190, 191));
    };
    auto o1_of = [&](const int s) { return wu >= 2 ? v_off(s, hs, tau) + jq * 512 + (wu & 1) * 256 : wu == 1 ? x_off(s, hs, tau) + jq * 256 : m_off(s, hs, tau) + jq * 128; };

    __builtin_amdgcn_s_setprio(1);
    int o_f = f_off(0, grp, tau);
    int o_cs = rec_base(0) + C_OFF + c * 16384 + near_half(0, tau) + wu * 2048 + grp * 256;
    int o_1 = o1_of(0), o_2 = km_off(0, hs, tau) + jq * 512, bq = x_off(0, ha, tau), bm = m_off(0, ha, tau), bb = v_off(0, ha, tau);
    {
            const int b0 = blk_off(0, ha, tau);
            int k0 = KX_OFF + blk_off(0, hs, tau) + jq * 1024;
            asm volatile(
                LDX("160:163", "%[vx]", "%[vb]", "0") LDX("164:167", "%[vx]", "%[vb]", "1024") LDX("168:171", "%[vx]", "%[vb]", "2048") LDX("172:175", "%[vx]", "%[vb]", "3072")
                LDX("176:179", "%[vx]", "%[vb]", "128") LDX("180:183", "%[vx]", "%[vb]", "1152") LDX("184:187", "%[vx]", "%[vb]", "2176") LDX("188:191", "%[vx]", "%[vb]", "3200")
                KLD("%[k0]")
                "v_mov_b32 v131, 0\n\tv_mov_b32 v135, 0\n\tv_mov_b32 v139, 0\n\tv_mov_b32 v143, 0\n\tv_mov_b32 v147, 0\n\tv_mov_b32 v151, 0\n\tv_mov_b32 v155, 0\n\tv_mov_b32 v159, 0\n\t"
                PROJ_X("vmcnt")
                : "=&{v[128:159]}"(yb), "=&{v[96:103]}"(kx), [k0] "+s"(k0)
                : "{v[32:63]}"(wO[0]), "{v[64:95]}"(wO[1]), [vx] "v"(voffX), [vk] "v"(voffK), [rs] "s"(rsS), [vb] "s"(VX_OFF + b0)
                : "memory", "scc", CLOB_PX);
        if (wu == 1) { PST(10) }
    }
    int s_first = 0;
    asm volatile("s_branch 1f\n\t.p2align 8\n1:" : "+s"(s_first));
    for (int s = s_first; s < NSEG - 1; ++s) {
        hand_over(o_1, o_2, o_f, s);
        if (s == 0 && wu == 1) { PST(8) }
        LT(0)
#define LDPA "buffer_load_dwordx2 v[160:161], v254, %[rs], %[bm] offen sc1\n\t" LDX("164:167", "%[l16]", "%[bq]", "0") LDX("168:171", "%[l16]", "%[bq]", "1024")
#define SM(d, a, b, x) "v_smfmac_f32_16x16x64_bf16 v[" d "], v[" a "], v[" b "], " x "\n\t"
#define MX2(a, b) "v_max_u32 v244, v" a ", v" b "\n\t"
#define MX3(a, b) "v_max3_u32 v244, v244, v" a ", v" b "\n\t"
#define ZAM Z2("208:209") Z2("210:211") Z2("212:213") Z2("214:215") Z2("216:217") Z2("218:219") Z2("220:221") Z2("222:223")
#define MUL(d, a, b) "v_mul_f32 v" d ", v" a ", v" b "\n\t"
#define CV0(d, a) "v_cvt_pk_bf16_f32 v" d ", v" a ", 0\n\t"
#define RDC "ds_read_b64 v[224:225], %[rc]\n\tds_read_b64 v[226:227], %[rc] offset:2048\n\tds_read_b64 v[228:229], %[rc] offset:4096\n\tds_read_b64 v[230:231], %[rc] offset:6144\n\t"
#define EXD(d, r) "v_exp_f32 v" d ", v" r "\n\t"
#define SUBM(r) "v_sub_f32 v" r ", v" r ", v250\n\t"
#define PCVM CVP("241", "180", "180") CVP("243", "180", "180")
#define PCVX CVP("242", "178", "179") CVP("240", "176", "177")
#define OUT(t0, t1, a0, a1, a2, a3, ad, o0, o1) ADD(t0, a0, a1) ADD(t1, a2, a3) MUL(t0, t0, "250") MUL(t1, t1, "251") DW2(ad, t0, t1, o0, o1)
#define TS(n)
#define TSOUT
        int lim = dead ? 0 : SPIN_LIMIT, bad = 0;
        asm volatile(
            LDX("192:195", "%[l16]", "%[bb]", "0") LDX("196:199", "%[l16]", "%[bb]", "1024")
            ZAM Z2("224:225") Z2("226:227")
            Z2("16:17") Z2("18:19") Z2("20:21") Z2("22:23") Z2("24:25") Z2("26:27") Z2("28:29") Z2("30:31")
            "v_mov_b32 v200, 0x3f803f80\n\tv_mov_b32 v201, 0x3f803f80\n\tv_mov_b32 v202, 0x3f80\n\tv_mov_b32 v203, 0\n\t"
            "v_mov_b64 v[204:205], v[200:201]\n\tv_mov_b64 v[206:207], v[202:203]\n\t"
            "v_xor_b32 v252, 0xa000, %[sx]\n\tv_add_u32 v253, 1024, %[ra]\n\tv_lshrrev_b32 v254, 1, %[l16]\n\t"
            PAUSE(PV)
            TS(8)
            LDPA
            "4:\n\t"
            "s_waitcnt vmcnt(4)\n\t"
            SM("208:211", "192:195", "32:39", "%[sx]") MX2("192", "193") SM("212:215", "192:195", "48:55", "%[sx]") MX3("194", "195")
            SM("216:219", "192:195", "64:71", "%[sx]") SM("220:223", "192:195", "80:87", "%[sx]")
            "s_waitcnt vmcnt(3)\n\t"
            SM("208:211", "196:199", "40:47", "%[sx]") MX3("196", "197") SM("212:215", "196:199", "56:63", "%[sx]") MX3("198", "199")
            SM("216:219", "196:199", "72:79", "%[sx]") "v_cmp_eq_u32 vcc, " STR(SENT) ", v244\n\t" SM("220:223", "196:199", "88:95", "%[sx]")
            "s_cbranch_vccnz 5f\n\t"
            "1:\n\t" TS(11)
            "s_waitcnt vmcnt(2)\n\t"
            "v_mfma_f32_16x16x16_f16 v[236:239], v[112:113], v[160:161], 0\n\t"
            MX2("160", "161") ADD("245", "208", "209") ADD("246", "210", "211") CV0("130", "245") CV0("134", "246")
            ADD("245", "212", "213") ADD("246", "214", "215") CV0("138", "245") CV0("142", "246")
            EXD("180", "236")
            "s_waitcnt vmcnt(1)\n\t"
            "v_mfma_f32_16x16x32_f16 v[228:231], v[104:107], v[164:167], 0\n\t"
            MX3("164", "165") MX3("166", "167") ADD("245", "216", "217") ADD("246", "218", "219") CV0("146", "245") CV0("150", "246")
            ADD("245", "220", "221") ADD("246", "222", "223") CV0("154", "245") CV0("158", "246") PCVM
            "s_waitcnt vmcnt(0)\n\t"
            "v_mfma_f32_16x16x32_f16 v[228:231], v[108:111], v[168:171], v[228:231]\n\t"
            MX3("168", "169") MX3("170", "171")
            "v_cmp_eq_u32 vcc, " STR(SENT) ", v244\n\t"
            "s_cbranch_vccnz 7f\n\t"
            "2:\n\t" TS(9)
            "s_nop 3\n\t"
            EXD("179", "231") EXD("178", "230") EXD("177", "229") EXD("176", "228")
            PCVX
            "v_max3_f32 v250, |v236|, |v231|, |v230|\n\t"
            "v_max3_f32 v250, v250, |v229|, |v228|\n\t"
            SM("224:227", "240:243", "200:207", "v252")
            "v_cmp_nge_f32 vcc, 0x41c00000, v250\n\t"
            "s_nop 0\n\t"
            SM("16:19", "240:243", "128:135", "v252")
            "s_cbranch_vccnz 10f\n\t"
            "13:\n\t"
            "s_nop 0\n\t"
            SM("20:23", "240:243", "136:143", "v252") "s_nop 1\n\t"
            ADD("250", "224", "225") ADD("251", "226", "227")
            SM("24:27", "240:243", "144:151", "v252")
            "v_rcp_f32 v250, v250\n\t"
            SM("28:31", "240:243", "152:159", "v252")
            "v_rcp_f32 v251, v251\n\t"
            OUT("245", "246", "16", "17", "18", "19", "%[ra]", "0", "16")
            OUT("247", "248", "20", "21", "22", "23", "%[ra]", "128", "144")
            OUT("245", "246", "24", "25", "26", "27", "v253", "0", "16")
            OUT("247", "248", "28", "29", "30", "31", "v253", "128", "144")
            TS(10)
            "s_waitcnt lgkmcnt(0)\n\t"
            "s_barrier\n\t"
            RDC
            "v_lshrrev_b32 v247, 2, %[l16]\n\t"
            "s_waitcnt lgkmcnt(2)\n\t"
            ADD("224", "224", "226") ADD("225", "225", "227")
            "s_waitcnt lgkmcnt(0)\n\t"
            ADD("228", "228", "230") ADD("229", "229", "231") ADD("224", "224", "228") ADD("225", "225", "229")
            "v_cvt_pk_bf16_f32 v245, v224, v225\n\t"
            "buffer_store_dword v245, v247, %[rs], %[cs] offen sc1\n\t"
            "s_branch 9f\n\t"
            "10:\n\t"
            "v_max3_f32 v250, v236, v231, v230\n\t"
            "v_max3_f32 v250, v250, v229, v228\n\t"
            "s_nop 1\n\t"
            "v_max_f32_dpp v251, v250, v250 quad_perm:[1,0,3,2] row_mask:0xf bank_mask:0xf\n\t"
            "v_max_f32_dpp v244, v250, v250 quad_perm:[1,0,3,2] row_mask:0xf bank_mask:0xf\n\t"
            "s_nop 1\n\t"
            "v_permlane16_swap_b32 v251, v244\n\t"
            "v_max_f32 v250, v251, v244\n\t"
            "v_max_f32 v251, v251, v244\n\t"
            Z2("224:225") Z2("226:227")
            "v_permlane32_swap_b32 v250, v251\n\t"
            "v_max_f32 v250, v250, v251\n\t"
            Z2("16:17") Z2("18:19")
            SUBM("228") SUBM("229") SUBM("230") SUBM("231") SUBM("236")
            EXD("180", "236") EXD("179", "231") EXD("178", "230") EXD("177", "229") EXD("176", "228")
            PCVM PCVX
            "s_nop 1\n\t"
            SM("224:227", "240:243", "200:207", "v252") "s_nop 1\n\t"
            SM("16:19", "240:243", "128:135", "v252")
            "s_branch 13b\n\t"
            "5:\n\t" CN(1)
            LDX("192:195", "%[l16]", "%[bb]", "0") LDX("196:199", "%[l16]", "%[bb]", "1024")
            LDPA
            "s_sub_u32 %[lim], %[lim], 1\n\t"
            "s_nop 1\n\t"
            ZAM
            "s_cbranch_scc0 4b\n\t"
            "s_mov_b32 %[bad], 1500\n\t"
            "s_mov_b32 %[lim], 0\n\t"
            "s_branch 1b\n\t"
            "7:\n\t" CN(65536)
            LDPA
            "s_sub_u32 %[lim], %[lim], 1\n\t"
            "s_cbranch_scc0 1b\n\t"
            "s_mov_b32 %[bad], 1000\n\t"
            "s_branch 2b\n\t"
            "9:\n\t"
            : [lim] "+s"(lim), [bad] "+s"(bad), "+{v[128:159]}"(yb) TSOUT CNOUT
            : "{v[32:63]}"(wO[0]), "{v[64:95]}"(wO[1]), "{v[104:111]}"(sel), "{v[112:113]}"(sel3), [sx] "v"(sidx), [l16] "v"(l * 16),
              [ra] "v"(r3a), [rs] "s"(rsS), [bb] "s"(bb), [bq] "s"(bq), [bm] "s"(bm), [cs] "s"(o_cs), [rc] "v"(l0 + C2_R3 + wu * 512 + l * 8)
            : "memory", "vcc", "scc", CLOB_PX, CLOB16(16, 17, 18, 19, 20, 21, 22, 23, 24, 25, 26, 27, 28, 29, 30, 31),
              CLOB16(224, 225, 226, 227, 228, 229, 230, 231, 232, 233, 234, 235, 236, 237, 238, 239), CLOB16(240, 241, 242, 243, 244, 245, 246, 247, 248, 249, 250, 251, 252, 253, 254, 254));
        if (__builtin_expect(bad != 0, 0)) TMO_SET((unsigned)bad)
        __builtin_amdgcn_s_setprio(0);
        if (s == 0 && wu == 1) { PST(6) }

        if (__builtin_expect(s <= KVS + 1, 0)) {
            if (s == 0 || __builtin_amdgcn_readfirstlane(*(volatile unsigned*)(chsm + C2_MB + 32 + ((s - 1) & 1) * 16 + 4 + (wu >> 1) * 4)) == 0u) {
                wait_ge<6>(kv_flag(a.sync, s + 1, grp, l), 1u, a.tmo, 3001u);
                const char* kp = arena(a.slots) + VX_OFF + blk_off(s + 1, ha, tau);
#pragma unroll
                for (int e = 0; e < 8; ++e) glds16h((unsigned)l * 16u, kp + e * 1024, l0 + (s & 1 ? C2_V : C2_K) + wu * 8192 + e * 1024);
                asm volatile("s_waitcnt vmcnt(0)" ::: "memory");
            }
        }
        {
            int k0 = KX_OFF + blk_off(s + 1, hs, tau) + jq * 1024;
            asm volatile(
                DSV("160:163", "0") DSV("164:167", "1024") DSV("168:171", "2048") DSV("172:175", "3072") DSV("176:179", "128") DSV("180:183", "1152") DSV("184:187", "2176") DSV("188:191", "3200")
                "s_cmp_lg_u32 %[wu], 1\n\t"
                "s_cbranch_scc1 1f\n\t"
                KLD("%[k0]")
                "1:\n\t"
                "s_waitcnt lgkmcnt(4)\n\t"
                PXT("192:195", "196:199", "200:203", "204:207", "160:163", "164:167", "168:171", "172:175")
                "s_waitcnt lgkmcnt(0)\n\t"
                PXT("208:211", "212:215", "216:219", "220:223", "176:179", "180:183", "184:187", "188:191")
                CVP("128", "192", "193") CVP("129", "194", "195") CVP("136", "196", "197") CVP("137", "198", "199") CVP("144", "200", "201") CVP("145", "202", "203") CVP("152", "204", "205") CVP("153", "206", "207")
                CVP("132", "208", "209") CVP("133", "210", "211") CVP("140", "212", "213") CVP("141", "214", "215") CVP("148", "216", "217") CVP("149", "218", "219") CVP("156", "220", "221") CVP("157", "222", "223")
                : "+{v[128:159]}"(yb), "+{v[96:103]}"(kx), [k0] "+s"(k0)
                : "{v[32:63]}"(wO[0]), "{v[64:95]}"(wO[1]), [lv] "v"(l0 + (s & 1 ? C2_V : C2_K) + wu * 8192 + voffX), [vk] "v"(voffK), [rs] "s"(rsS), [wu] "s"(wu)
                : "memory", "scc", CLOB_PX);
        }
        __builtin_amdgcn_sched_barrier(0);
        o_1 = o1_of(s + 1); o_2 = km_off(s + 1, hs, tau) + jq * 512;
        bq = x_off(s + 1, ha, tau); bm = m_off(s + 1, ha, tau); bb = v_off(s + 1, ha, tau);
        o_f = f_off(s + 1, grp, tau);
        o_cs = rec_base(s + 1) + C_OFF + c * 16384 + near_half(s + 1, tau) + wu * 2048 + grp * 256;
        asm volatile("" : "+s"(o_cs), "+s"(o_f), "+s"(o_1), "+s"(o_2), "+s"(bq), "+s"(bm), "+s"(bb));
        __builtin_amdgcn_s_barrier();
        asm volatile(
            "s_cmp_lg_u32 %[wu], 3\n\t"
            "s_cbranch_scc1 1f\n\t"
            "v_mov_b32 v160, %[lf]\n\t"
            PAUSE(PF)
            "ds_read_b32 v160, v160\n\t"
            "s_waitcnt lgkmcnt(0)\n\t"
            "v_cmp_eq_u32 vcc, %[tg], v160\n\t"
            "s_cbranch_vccz 1f\n\t"
            "s_mov_b32 exec_hi, 0\n\t"
            "buffer_store_dword v160, %[l4], %[rs], %[fo] offen sc1\n\t"
            "s_mov_b64 exec, -1\n\t"
            "1:\n\t"
            :: [wu] "s"(wu), [lf] "s"(l0 + C2_MB + 16), [tg] "s"(s + 1), [l4] "v"(l * 4), [rs] "s"(rsS), [fo] "s"(o_f + c * 128)
            : "memory", "scc", "vcc", "v160");
        __builtin_amdgcn_s_setprio(1);
    }
    {
        constexpr int s = NSEG - 1;
        const int l = lane_now(), i = l & 15, g = l >> 4;
        v4u qf[8], kf[8], pv[2], px[4];
        asm volatile("s_waitcnt vmcnt(0)" ::: "memory");
        __builtin_amdgcn_s_sleep(4);
#pragma unroll
        for (int e = 0; e < 8; ++e) kf[e] = ld_sc1s(rsS, l * 16, KX_OFF + blk_off(s, ha, tau) + e * 1024);
        hand_over(o_1, o_2, o_f, s);
        const int bk = km_off(s, ha, tau);
        auto ask = [&]() {
            asm volatile("" ::: "memory");
            pv[0] = ld_sc1s(rsS, l * 16, bb); pv[1] = ld_sc1s(rsS, l * 16, bb + 1024);
#pragma unroll
            for (int ks = 0; ks < 4; ++ks) px[ks] = ld_sc1s(rsS, g * 128 + (i >> 1) * 16, bk + ks * 512);
            asm volatile("" ::: "memory");
        };
        asm volatile(PAUSE(PLL) ::: "memory");
        ask();
#pragma unroll
        for (int e = 0; e < 8; ++e) qf[e] = *(const v4u*)(chsm + C2_V + 32768 + wu * 8192 + e * 1024 + l * 16);
        f32x4 sx[3] = {ZERO4, ZERO4, ZERO4};
        float mxx[2] = {-3.0e38f, -3.0e38f};
#pragma unroll
        for (int ks = 0; ks < 4; ++ks) {
            sx[0] = MM(kf[ks], qf[ks], sx[0]);
            sx[1] = MM(kf[ks], qf[4 + ks], sx[1]);
            sx[2] = MM(kf[4 + ks], qf[4 + ks], sx[2]);
        }
#pragma unroll
        for (int r = 0; r < 4; ++r) {
            if (4 * g + r > i) { sx[0][r] = -3.0e38f; sx[2][r] = -3.0e38f; }
            mxx[0] = fmaxf(mxx[0], sx[0][r]);
            mxx[1] = fmaxf(mxx[1], fmaxf(sx[1][r], sx[2][r]));
        }
        asm volatile("" : "+v"(sx[0]), "+v"(sx[1]), "+v"(sx[2]), "+v"(mxx[0]), "+v"(mxx[1]));
#pragma nounroll
        for (int spin = 0;; ++spin) {
            if (__all(mx4(mx4(mx4(mx4(mx4(mx4(0u, pv[0]), pv[1]), px[0]), px[1]), px[2]), px[3]) != SENT)) break;
            if (dead) break;
            if (spin > SPIN_LIMIT) { TMO_SET(1700u) break; }
            ask();
        }
#define SUB8(v, o) v8u{(v)[(o)], (v)[(o) + 1], (v)[(o) + 2], (v)[(o) + 3], (v)[(o) + 4], (v)[(o) + 5], (v)[(o) + 6], (v)[(o) + 7]}
#pragma unroll
        for (int ct = 0; ct < 4; ++ct) {
            f32x4 am = smm(pv[0], SUB8(wO[ct >> 1], (ct & 1) * 16), ZERO4, sidx);
            am = smm(pv[1], SUB8(wO[ct >> 1], (ct & 1) * 16 + 8), am, sidx);
            yb[8 * ct + 2] = cvt_pk_bf16(am[0] + am[1], 0.f); yb[8 * ct + 6] = cvt_pk_bf16(am[2] + am[3], 0.f);
        }
        const v8u one = {0x3F803F80u, 0x3F803F80u, 0x3F80u, 0u, 0x3F803F80u, 0x3F803F80u, 0x3F80u, 0u};
        const float SCL2E = 0.08838834764831845f * 1.4426950408889634f;
        float* xo = (float*)(chsm + C2_K) + (wu * 32 + 4 * g) * XP + i;
#pragma unroll
        for (int qt = 0; qt < 2; ++qt) {
            f32x4 sc[3] = {sx[qt], sx[2], ZERO4};
#pragma unroll
            for (int ks = 0; ks < 4; ++ks) sc[2] = MM(px[ks], qf[4 * qt + ks], sc[2]);
            float mx = fmaxf(mxx[qt], fmaxf(sc[2][1], sc[2][3]));
            mx = xor32_max(xor16_max(mx));
            const float nmx = -mx * SCL2E;
#define PE(x) __builtin_amdgcn_exp2f(fmaf(x, SCL2E, nmx))
            const float m0 = PE(sc[2][1]), m1 = PE(sc[2][3]);
            const v4u ae = {cvt_pk_bf16(PE(sc[0][0]), PE(sc[0][1])), cvt_pk_bf16(m0, m0), cvt_pk_bf16(PE(sc[0][2]), PE(sc[0][3])), 0u};
            const v4u ao = {qt ? cvt_pk_bf16(PE(sc[1][0]), PE(sc[1][1])) : 0u, 0u, qt ? cvt_pk_bf16(PE(sc[1][2]), PE(sc[1][3])) : 0u, cvt_pk_bf16(m1, m1)};
            const f32x4 sum = smm(ao, one, smm(ae, one, ZERO4, 0xE444), 0x4EEE);
#pragma unroll
            for (int ct = 0; ct < 4; ++ct) {
                const f32x4 o = smm(ao, SUB8(yb, 8 * ct), smm(ae, SUB8(yb, 8 * ct), ZERO4, 0xE444), 0x4EEE);
#pragma unroll
                for (int r = 0; r < 4; ++r) xo[(16 * qt + r) * XP + 16 * ct] = o[r] * __builtin_amdgcn_rcpf(sum[r]);
            }
        }
        __builtin_amdgcn_s_waitcnt(0xC07F);
        last_rows(chsm, rsS, wu, grp, c, tau);
        if (wu < 2) {
            v4u sel, p0, p1;
#pragma unroll
            for (int d = 0; d < 4; ++d) sel[d] = (l & 6) == 2 * d ? (0x3F80u << ((l & 1) * 16)) : 0u;
            const int xb = XB_OFF + ((c >> 1) * 8 + grp) * 16384 + (int)(tau & 1u) * 8192 + (c & 1) * 4096 + wu * 256;
            __builtin_amdgcn_s_sleep(DLY_C);
#pragma nounroll
            for (int spin = 0;; ++spin) {
                asm volatile("" ::: "memory");
                p0 = ld_sc1s(rsS, g * 512 + i * 16, xb); p1 = ld_sc1s(rsS, g * 512 + i * 16, xb + 2048);
                if (__all(mx4(mx4(0u, p0), p1) != SENT)) break;
                if (dead) break;
                if (spin > SPIN_LIMIT) { TMO_SET(1800u) break; }
            }
            const f32x4 d = MM(sel, p1, MM(sel, p0, ZERO4));
            if (g < 2) out8(a.out + (size_t)(s * SEG + 4 * grp + 2 * wu + (i >> 3)) * OUTW + 64 * c + 8 * (i & 7) + 4 * g, v2u{cvt_pk_bf16(d[0], d[1]), cvt_pk_bf16(d[2], d[3])});
        }
    }
}

struct cpx { float x, y; };
__device__ __forceinline__ cpx cmul(cpx a, cpx b) { return {a.x * b.x - a.y * b.y, a.x * b.y + a.y * b.x}; }
__device__ __forceinline__ cpx cadd(cpx a, cpx b) { return {a.x + b.x, a.y + b.y}; }
__device__ __forceinline__ cpx csub(cpx a, cpx b) { return {a.x - b.x, a.y - b.y}; }
template <int DIR> __device__ __forceinline__ cpx mul_i(cpx a) {
    return (DIR < 0) ? cpx{a.y, -a.x} : cpx{-a.y, a.x};
}
template <int DIR> __device__ __forceinline__ void r4(cpx& a, cpx& b, cpx& c, cpx& d) {
    cpx t0 = cadd(a, c), t1 = csub(a, c), t2 = cadd(b, d), t3 = mul_i<DIR>(csub(b, d));
    a = cadd(t0, t2); b = cadd(t1, t3); c = csub(t0, t2); d = csub(t1, t3);
}
template <int DIR> __device__ __forceinline__ cpx twc(float c, float s) { return cpx{c, (float)DIR * s}; }
template <int DIR> __device__ __forceinline__ void dft16(cpx (&v)[16]) {
#pragma unroll
    for (int a = 0; a < 4; ++a) r4<DIR>(v[a], v[a + 4], v[a + 8], v[a + 12]);
    const float C1 = 0.92387953251128674f, S1 = 0.38268343236508977f, R2 = 0.70710678118654752f;
    v[1 + 4] = cmul(v[1 + 4], twc<DIR>(C1, S1));
    v[1 + 8] = cmul(v[1 + 8], twc<DIR>(R2, R2));
    v[1 + 12] = cmul(v[1 + 12], twc<DIR>(S1, C1));
    v[2 + 4] = cmul(v[2 + 4], twc<DIR>(R2, R2));
    v[2 + 8] = mul_i<DIR>(v[2 + 8]);
    v[2 + 12] = cmul(v[2 + 12], twc<DIR>(-R2, R2));
    v[3 + 4] = cmul(v[3 + 4], twc<DIR>(S1, C1));
    v[3 + 8] = cmul(v[3 + 8], twc<DIR>(-R2, R2));
    v[3 + 12] = cmul(v[3 + 12], twc<DIR>(-C1, -S1));
#pragma unroll
    for (int d = 0; d < 4; ++d) r4<DIR>(v[4 * d], v[4 * d + 1], v[4 * d + 2], v[4 * d + 3]);
}
__device__ __forceinline__ constexpr int x16(int q) { return 4 * (q & 3) + (q >> 2); }
template <int DIR> __device__ __forceinline__ void dft8(cpx (&v)[8]) {
    r4<DIR>(v[0], v[2], v[4], v[6]);
    r4<DIR>(v[1], v[3], v[5], v[7]);
    const float R2 = 0.70710678118654752f;
    v[3] = cmul(v[3], twc<DIR>(R2, R2));
    v[5] = mul_i<DIR>(v[5]);
    v[7] = cmul(v[7], twc<DIR>(-R2, R2));
#pragma unroll
    for (int d = 0; d < 4; ++d) { cpx p = v[2 * d], q = v[2 * d + 1]; v[2 * d] = cadd(p, q); v[2 * d + 1] = csub(p, q); }
}
__device__ __forceinline__ int pad(int i) { return i + (i >> 4); }
__device__ __forceinline__ cpx expi(float x) { float s, c; sincospif(x, &s, &c); return cpx{c, s}; }
template <int R, int DIR> __device__ __forceinline__ void twiddle(cpx* v, cpx w1) {
    cpx w[R];
    w[1] = w1;
#pragma unroll
    for (int r = 2; r < R; ++r) w[r] = (r & 1) ? cmul(w[r - 1], w1) : cmul(w[r / 2], w[r / 2]);
#pragma unroll
    for (int r = 1; r < R; ++r) v[r] = DIR < 0 ? cmul(v[r], w[r]) : cpx{v[r].x * w[r].x + v[r].y * w[r].y, v[r].y * w[r].x - v[r].x * w[r].y};
}
struct TwBase { cpx b; cpx c[4]; };
template <int N> __device__ __forceinline__ TwBase make_tw(int t) {
    TwBase tb;
    tb.b = expi(-(float)(t & 15) * (1.0f / 128.0f));
    constexpr int NQ = (N == 1024) ? 4 : 2, T = N / 16;
#pragma unroll
    for (int q = 0; q < NQ; ++q) tb.c[q] = expi(-(float)(t + T * q) * (2.0f / (float)N));
    return tb;
}
template <int N> __device__ __forceinline__ void fft_sync() {
    if constexpr (N == 1024) { __builtin_amdgcn_fence(__ATOMIC_ACQ_REL, "wavefront"); __builtin_amdgcn_wave_barrier(); } else __syncthreads();
}
template <int N, int DIR, bool RIN, bool ROUT>
__device__ __forceinline__ void fft_reg(cpx* buf, const TwBase& tb, int t, cpx (&v)[16]) {
    constexpr int T = N / 16, TP = T + T / 16;
    cpx* const bt = buf + pad(t);
    if constexpr (!RIN) {
#pragma unroll
        for (int r = 0; r < 16; ++r) v[r] = bt[TP * r];
        fft_sync<N>();
    }
    dft16<DIR>(v);
#pragma unroll
    for (int q = 0; q < 16; ++q) buf[17 * t + x16(q)] = v[q];
    fft_sync<N>();
    {
#pragma unroll
        for (int r = 0; r < 16; ++r) v[r] = bt[TP * r];
        fft_sync<N>();
        twiddle<16, DIR>(v, tb.b);
        dft16<DIR>(v);
        cpx* const bo = buf + (t >> 4) * 272 + (t & 15);
#pragma unroll
        for (int q = 0; q < 16; ++q) bo[17 * x16(q)] = v[q];
        fft_sync<N>();
    }
    if constexpr (N == 1024) {
#pragma unroll
        for (int q = 0; q < 4; ++q)
#pragma unroll
            for (int r = 0; r < 4; ++r) v[4 * q + r] = bt[68 * q + 272 * r];
#pragma unroll
        for (int q = 0; q < 4; ++q) {
            twiddle<4, DIR>(&v[4 * q], tb.c[q]);
            r4<DIR>(v[4 * q], v[4 * q + 1], v[4 * q + 2], v[4 * q + 3]);
#pragma unroll
            for (int r = 0; r < 4; ++r) bt[68 * q + 272 * r] = v[4 * q + r];
        }
        fft_sync<N>();
    } else {
        cpx u[2][8];
#pragma unroll
        for (int q = 0; q < 2; ++q)
#pragma unroll
            for (int r = 0; r < 8; ++r) u[q][r] = bt[136 * q + 272 * r];
        if constexpr (ROUT) fft_sync<N>();
#pragma unroll
        for (int q = 0; q < 2; ++q) {
            twiddle<8, DIR>(u[q], tb.c[q]);
            dft8<DIR>(u[q]);
#pragma unroll
            for (int d = 0; d < 4; ++d) {
                if constexpr (ROUT) { v[q + 2 * d] = u[q][2 * d]; v[q + 2 * d + 8] = u[q][2 * d + 1]; }
                else { bt[136 * q + 272 * d] = u[q][2 * d]; bt[136 * q + 272 * (d + 4)] = u[q][2 * d + 1]; }
            }
        }
        if constexpr (!ROUT) fft_sync<N>();
    }
}
__device__ __forceinline__ int f_hi(int rb, int j, unsigned tw) { j = (j + rb) & 7; return rb * 65536 + (j >> 1) * 16384 + (j & 1) * 4096 + (int)((tw ^ (unsigned)(rb >> 5)) & 1u) * 8192; }
__device__ __forceinline__ int f_lo(int rb, int x) { return (x + rb * 128) & 4095; }
__device__ __forceinline__ int w_hi(int k, int j, unsigned tw) { return k * 16384 + ((j + k) & 15) * 512 + (int)((tw ^ (unsigned)(k >> 7)) & 1u) * 8192; }
__device__ __forceinline__ int w_lo(int k, int x) { return (x + k * 128) & 511; }
constexpr int PB1 = 1024 + 64, PB2 = 2048 + 128;
typedef float f32x4v __attribute__((ext_vector_type(4)));
typedef unsigned u32x4v __attribute__((ext_vector_type(4)));

__device__ __forceinline__ void fft_rows_fwd(const u16* __restrict__ x, rsrc_t rsF, const unsigned tw, int rb, char* smem, int tid, const char* wr, const char* wi, const unsigned spare) {
    cpx (*buf)[PB1] = (cpx (*)[PB1])smem;
    int f = tid >> 6, t = tid & 63;
    const u16* p0 = x + (size_t)(rb * 8 + 2 * f) * 1024 + 16 * t;
    u32x4v a0 = *(const u32x4v*)p0, a1 = *(const u32x4v*)(p0 + 8), b0 = *(const u32x4v*)(p0 + 1024), b1 = *(const u32x4v*)(p0 + 1032);
    const TwBase tb = make_tw<1024>(t);
    for (int it = 0; it < 4; ++it, rb += 64) {
        asm volatile("" : "+v"(tid));
        f = tid >> 6; t = tid & 63;
        __syncthreads();
        cpx* d = &buf[f][17 * t];
#pragma unroll
        for (int e = 0; e < 4; ++e) {
            d[2 * e] = cpx{__uint_as_float(a0[e] << 16), __uint_as_float(b0[e] << 16)};
            d[2 * e + 1] = cpx{__uint_as_float(a0[e] & 0xFFFF0000u), __uint_as_float(b0[e] & 0xFFFF0000u)};
            d[8 + 2 * e] = cpx{__uint_as_float(a1[e] << 16), __uint_as_float(b1[e] << 16)};
            d[8 + 2 * e + 1] = cpx{__uint_as_float(a1[e] & 0xFFFF0000u), __uint_as_float(b1[e] & 0xFFFF0000u)};
        }
        if (it < 3) p0 += 512 * 1024;
        touch(((it & 1) ? wi : wr) + (size_t)((rb >> 1) & 31) * 32832 + (it >> 1) * 32 * 32832 + ((rb & 1) * 128 + (tid >> 1)) * 128, spare);
        a0 = *(const u32x4v*)p0; a1 = *(const u32x4v*)(p0 + 8); b0 = *(const u32x4v*)(p0 + 1024); b1 = *(const u32x4v*)(p0 + 1032);
        fft_sync<1024>();
        cpx z[16];
        fft_reg<1024, -1, false, false>(buf[f], tb, t, z);
        __syncthreads();
        const cpx* pk = &buf[tid & 3][pad(tid >> 2)];
        const cpx* pn = &buf[tid & 3][pad(1024 - (tid >> 2))];
        const int vo = f_lo(rb, tid * 16);
#pragma unroll
        for (int m = 0; m < 8; ++m) {
            const cpx zk = pk[68 * m], zn = m == 0 && tid < 4 ? pk[544] : pn[-68 * m];
            f32x4v o = {zk.x + zn.x, zk.y - zn.y, zk.y + zn.y, zn.x - zk.x};
            if (m == 0 && tid < 4) o = f32x4v{2.f * zk.x, 2.f * zn.x, 2.f * zk.y, 2.f * zn.y};
            st_sc1s(rsF, vo, f_hi(rb, m, tw), __builtin_bit_cast(v4u, o));
        }
    }
}
__device__ __forceinline__ void filt_transpose(const u16* __restrict__ wr, const u16* __restrict__ wi, rsrc_t rsW, const unsigned tw, const int tb, char* smem, const int tid) {
    const v4u* s0 = (const v4u*)(wr + (size_t)tb * 32 * 513);
    const v4u* s1 = (const v4u*)(wi + (size_t)tb * 32 * 513);
    v4u r0[5], r1[5];
#pragma unroll
    for (int e = 0; e < 5; ++e) {
        const int idx = tid + 512 * e, ic = idx < 2052 ? idx : 2051;
        r0[e] = s0[ic]; r1[e] = s1[ic];
    }
#pragma unroll
    for (int e = 0; e < 5; ++e) {
        const int idx = tid + 512 * e;
        if (idx < 2052) { *(v4u*)(smem + idx * 16) = r0[e]; *(v4u*)(smem + 32832 + idx * 16) = r1[e]; }
    }
    __syncthreads();
    const u16* a0 = (const u16*)smem;
    const u16* a1 = (const u16*)(smem + 32832);
    for (int idx = tid; idx < 513 * 8; idx += 512) {
        const int k = idx >> 3, n4 = (idx & 7) * 4;
        v4u v;
#pragma unroll
        for (int j = 0; j < 4; ++j) v[j] = (unsigned)a0[(n4 + j) * 513 + k] | ((unsigned)a1[(n4 + j) * 513 + k] << 16);
        st_sc1(rsW, w_hi(k, tb >> 2, tw) + w_lo(k, (tb & 3) * 128 + n4 * 4), v);
    }
}

__device__ __forceinline__ void fft_cols(rsrc_t rsF, rsrc_t rsW, const unsigned tw, int blk, const int n, char* smem, int tid) {
    cpx (*buf)[PB2] = (cpx (*)[PB2])smem;
    int c = tid >> 7, t = tid & 127;
    f32x4v v[8];
    unsigned wv[16], wn[16];
    auto wld = [&](const int k, const int n) { return __builtin_amdgcn_raw_buffer_load_b32(rsW, w_hi(k, n >> 7, tw) + w_lo(k, (n & 127) * 4), 0, 0); };
    auto f_at = [&](const int bk) { return f_hi(tid >> 3, bk >> 5, tw) + f_lo(tid >> 3, (bk & 31) * 128 + (tid & 7) * 16); };
    auto fetch = [&](const int bk) {
        const int kc = bk * 2 + __builtin_amdgcn_readfirstlane(c), vo = f_at(bk), vw = w_lo(kc, t * 4);
#pragma unroll
        for (int m = 0; m < 8; ++m) v[m] = __builtin_bit_cast(f32x4v, __builtin_amdgcn_raw_buffer_load_b128(rsF, m & 1 ? vo ^ 8192 : vo, m * 2097152, 0));
#pragma unroll
        for (int m = 0; m < 16; ++m) wn[m] = __builtin_amdgcn_raw_buffer_load_b32(rsW, vw, w_hi(kc, m, tw), 0);
    };
    fetch(blk);
    const TwBase tb = make_tw<2048>(t);
    for (int it = 0; it < n; ++it, blk += 64) {
        asm volatile("" : "+v"(tid));
        c = tid >> 7; t = tid & 127;
        __syncthreads();
        cpx* const d = &buf[(tid >> 2) & 1][pad((tid >> 3) * 8 + 2 * (tid & 3))];
#pragma unroll
        for (int m = 0; m < 8; ++m) { d[272 * m] = cpx{v[m][0], v[m][1]}; d[272 * m + 1] = cpx{v[m][2], v[m][3]}; }
#pragma unroll
        for (int m = 0; m < 16; ++m) wv[m] = wn[m];
        fetch(it + 1 < n ? blk + 64 : blk);
        __syncthreads();
        cpx z[16];
        fft_reg<2048, -1, false, true>(buf[c], tb, t, z);
        auto wcx = [](const unsigned w) { return cpx{__uint_as_float(w << 16), __uint_as_float(w & 0xFFFF0000u)}; };
        if (blk < 2) {
#pragma unroll
            for (int m = 0; m < 16; ++m) buf[c][pad(t + 128 * m)] = z[m];
            __syncthreads();
            if (blk == 0 && c == 0) {
#pragma unroll
                for (int m = 0; m < 16; ++m) {
                    const int n = t + 128 * m, nn = (2048 - n) & 2047;
                    const cpx zn = buf[0][pad(nn)], a = wcx(wv[m]), b = wcx(wld(0, nn)), e = wcx(wld(512, n)), f = wcx(wld(512, nn));
                    const cpx h0 = cmul(cpx{0.5f * (z[m].x + zn.x), 0.5f * (z[m].y - zn.y)}, cpx{0.5f * (a.x + b.x), 0.5f * (a.y - b.y)});
                    const cpx h5 = cmul(cpx{0.5f * (z[m].y + zn.y), -0.5f * (z[m].x - zn.x)}, cpx{0.5f * (e.x + f.x), 0.5f * (e.y - f.y)});
                    z[m] = cpx{h0.x - h5.y, h0.y + h5.x};
                    wv[m] = 0x3F80u;
                }
            }
            __syncthreads();
        }
#pragma unroll
        for (int m = 0; m < 16; ++m) z[m] = cmul(z[m], wcx(wv[m]));
        fft_reg<2048, 1, true, false>(buf[c], tb, t, z);
        const int vo = f_at(blk);
#pragma unroll
        for (int m = 0; m < 8; ++m)
            st_sc1s(rsF, m & 1 ? vo ^ 8192 : vo, m * 2097152, __builtin_bit_cast(v4u, f32x4v{d[272 * m].x, d[272 * m].y, d[272 * m + 1].x, d[272 * m + 1].y}));
    }
}

__device__ __forceinline__ void fft_rows_inv(rsrc_t rsF, const unsigned tw, u16* __restrict__ out, int rb, const int step, const int n, char* smem, int tid) {
    cpx (*buf)[PB1] = (cpx (*)[PB1])smem;
    int f = tid >> 6, t = tid & 63;
    f32x4v v[8];
    auto fetch = [&](const int r) {
        const int vo = f_lo(r, tid * 16);
#pragma unroll
        for (int m = 0; m < 8; ++m) v[m] = __builtin_bit_cast(f32x4v, __builtin_amdgcn_raw_buffer_load_b128(rsF, vo, f_hi(r, m, tw), 0));
    };
    fetch(rb);
    const TwBase tb = make_tw<1024>(t);
    for (int it = 0; it < n; ++it, rb += step) {
        asm volatile("" : "+v"(tid));
        f = tid >> 6; t = tid & 63;
        __syncthreads();
        cpx* pk = &buf[tid & 3][pad(tid >> 2)];
        cpx* pn = &buf[tid & 3][pad(1024 - (tid >> 2))];
#pragma unroll
        for (int m = 0; m < 8; ++m) {
            if (m == 0 && tid < 4) { pk[0] = cpx{v[0][0], v[0][2]}; pk[544] = cpx{v[0][1], v[0][3]}; }
            else { pk[68 * m] = cpx{v[m][0] - v[m][3], v[m][1] + v[m][2]}; pn[-68 * m] = cpx{v[m][0] + v[m][3], v[m][2] - v[m][1]}; }
        }
        fetch(it + 1 < n ? rb + step : rb);
        __syncthreads();
        cpx z[16];
        fft_reg<1024, 1, false, false>(buf[f], tb, t, z);
        const float sc = 0.5f / (1024.0f * 2048.0f);
        const cpx* d = &buf[f][17 * t];
        u32x4v a[2], b[2];
#pragma unroll
        for (int h = 0; h < 2; ++h)
#pragma unroll
            for (int e = 0; e < 4; ++e) {
                cpx p = d[8 * h + 2 * e], q = d[8 * h + 2 * e + 1];
                a[h][e] = cvt_pk_bf16(p.x * sc, q.x * sc);
                b[h][e] = cvt_pk_bf16(p.y * sc, q.y * sc);
            }
        u16* o0 = out + (size_t)(rb * 8 + 2 * f) * OUTW + 1024 + 16 * t;
        if (WT & 2) { out16(o0, a[0]); out16(o0 + 8, a[1]); out16(o0 + OUTW, b[0]); out16(o0 + OUTW + 8, b[1]); }
        else {
        *(u32x4v*)o0 = a[0]; *(u32x4v*)(o0 + 8) = a[1];
        *(u32x4v*)(o0 + OUTW) = b[0]; *(u32x4v*)(o0 + OUTW + 8) = b[1]; }
    }
}

__device__ __forceinline__ void glds16s(unsigned voff, const void* sbase, unsigned lds_dst) {
    unsigned keep;
    asm volatile("s_mov_b32 %0, m0\n\ts_mov_b32 m0, %3\n\ts_nop 0\n\tglobal_load_lds_dwordx4 %1, %2\n\ts_mov_b32 m0, %0" : "=&s"(keep) : "v"(voff), "s"(sbase), "s"(lds_dst) : "memory");
}
#define K8_BAR do { asm volatile("" ::: "memory"); __builtin_amdgcn_s_barrier(); asm volatile("" ::: "memory"); } while (0)
#define K8_WV(n) asm volatile("s_waitcnt vmcnt(" #n ")" ::: "memory")
#define K8_WL(n) asm volatile("s_waitcnt lgkmcnt(" #n ")" ::: "memory")
#define K8_SCHED __builtin_amdgcn_sched_barrier(0)
template <int BN>
__device__ __forceinline__ int bswz2(int row) {
    if (BN >= 128) return (row & 3) | (((row >> 3) & 1) << 2);
    else return ((row >> 1) & 1) | (((row >> 3) & 1) << 1);
}
template <int BM>
__device__ __forceinline__ void gemm_tile(u16* sm, const char* A, const int s0, const unsigned tau, const u16* B, u16* C) {
    constexpr int K = HD, LDB = DM, LDC = OUTW;
    constexpr int BN = 128, BK = 64, ST = BM == 128 ? 4 : 3, WN = BM == 128 ? 4 : 2;
    constexpr int ATILE = BM * BK, BTILE = BK * BN;
    constexpr int NT = 8 / WN, GA = BM / 64, GB = 2, GS = GA + GB;
    const int tid = opaque_tid(), w = tid >> 6, l = tid & 63, i = l & 15, g = l >> 4;
    const int wr = w / WN, wc = w % WN;
    const unsigned wu = __builtin_amdgcn_readfirstlane(w);
    unsigned vb;
    { const int row = l >> 4, c = l & 15; vb = (unsigned)(row * LDB + 8 * (c ^ ((row | ((wu & 1) << 2)) << 1))) * 2u; }
    const int sg = s0 + (BM == 128 ? wu >> 1 : wu);
    const char* ab = A + (BM == 128 ? (wu & 1) * 4096 : 0);
    const char* bb = (const char*)(B + (size_t)wu * 8 * LDB);
    const unsigned lds0 = (unsigned)(uintptr_t)(__attribute__((address_space(3))) u16*)sm;
    const unsigned va = (unsigned)l * 16u, va2 = va + 2048u, vb2 = vb + 4u * LDB * 2u - 1024u;
    auto req = [&](const int buf, const int kt) {
        const unsigned d = lds0 + buf * (ATILE + BTILE) * 2;
        const int wh = kt >> 1;
        const char* pa = ab + ((blk_off(sg, (wh & 7) * 4 + (wh >> 3), tau) ^ 8192) + (kt & 1) * 2048);
        const char* pb = bb + (size_t)kt * BK * LDB * 2;
        unsigned keep;
        if (BM == 128) asm volatile("s_mov_b32 %0, m0\n\ts_mov_b32 m0, %2\n\ts_nop 0\n\tglobal_load_lds_dwordx4 %6, %8\n\tglobal_load_lds_dwordx4 %7, %8 offset:1024\n\t"
                     "s_mov_b32 m0, %1\n\ts_nop 0\n\tglobal_load_lds_dwordx4 %3, %5\n\tglobal_load_lds_dwordx4 %3, %5 offset:1024\n\ts_mov_b32 m0, %0"
                     : "=&s"(keep) : "s"(d + wu * GA * 1024), "s"(d + ATILE * 2 + wu * GB * 1024), "v"(va), "v"(va2), "s"(pa), "v"(vb), "v"(vb2), "s"(pb) : "memory");
        else asm volatile("s_mov_b32 %0, m0\n\ts_mov_b32 m0, %2\n\ts_nop 0\n\tglobal_load_lds_dwordx4 %6, %8\n\tglobal_load_lds_dwordx4 %7, %8 offset:1024\n\t"
                     "s_mov_b32 m0, %1\n\ts_nop 0\n\tglobal_load_lds_dwordx4 %3, %5\n\tglobal_load_lds_dwordx4 %3, %5 offset:1024\n\t"
                     "global_load_lds_dwordx4 %4, %5 offset:2048\n\tglobal_load_lds_dwordx4 %4, %5 offset:3072\n\ts_mov_b32 m0, %0"
                     : "=&s"(keep) : "s"(d + wu * GA * 1024), "s"(d + ATILE * 2 + wu * GB * 1024), "v"(va), "v"(va2), "s"(pa), "v"(vb), "v"(vb2), "s"(pb) : "memory");
    };
    f32x4 acc[4][NT] = {};
    constexpr int nt = K / BK;
#pragma unroll
    for (int s = 0; s < ST - 1; ++s) req(s, s);
    int boff[NT];
    const int aoff = wr * 4096 + l * 8;
#pragma unroll
    for (int n = 0; n < NT; ++n) { const int row = 8 * g + (i >> 2); boff[n] = row * BN + 16 * ((wc * NT + n) ^ bswz2<BN>(row)) + 4 * (i & 3); }
    asm volatile("s_waitcnt vmcnt(%0)" :: "i"((ST - 2) * GS) : "memory");
    K8_BAR;
    if (wu >= 4) K8_BAR;
    for (int t0 = 0; t0 < nt + ST - 1 - (nt + ST - 1) % ST; t0 += ST) {
#pragma unroll
        for (int rb = 0; rb < ST; ++rb) {
            const int t = t0 + rb;
            if (nt % ST != 0 && t >= nt) break;
            const int kk = min(t + ST - 1, nt - 1);
            const u16* as = sm + rb * (ATILE + BTILE);
            const u16* bs = as + ATILE;
            bf16x8 af[2][4], bf[2][NT];
#pragma unroll
            for (int ks = 0; ks < 2; ++ks) {
#pragma unroll
                for (int n = 0; n < NT; ++n) bf[ks][n] = tr_frag(bs + boff[n] + ks * 32 * BN, bs + boff[n] + ks * 32 * BN + 4 * BN);
#pragma unroll
                for (int m = 0; m < 4; ++m) af[ks][m] = *(const bf16x8*)(as + aoff + m * 1024 + ks * 512);
            }
            K8_SCHED;
            req((rb + ST - 1) % ST, kk);
            K8_WL(0);
            asm volatile("s_waitcnt vmcnt(%0)" :: "i"((ST - 2) * GS + GA) : "memory");
            K8_BAR;
            __builtin_amdgcn_s_setprio(1);
#pragma unroll
            for (int q = 0; q < 8; ++q)
#pragma unroll
                for (int n = 0; n < NT; ++n) acc[q & 3][n] = __builtin_amdgcn_mfma_f32_16x16x32_bf16(bf[q >> 2][n], af[q >> 2][q & 3], acc[q & 3][n], 0, 0, 0);
            __builtin_amdgcn_s_setprio(0);
            K8_SCHED;
            asm volatile("s_waitcnt vmcnt(%0)" :: "i"((ST - 2) * GS) : "memory");
            K8_BAR;
        }
    }
    if (wu < 4) K8_BAR;
    asm volatile("s_waitcnt vmcnt(0)" ::: "memory");
#pragma unroll
    for (int m = 0; m < 4; ++m)
#pragma unroll
        for (int n = 0; n < NT; n += 2) {
            auto r0 = __builtin_amdgcn_permlane16_swap(cvt_pk_bf16(acc[m][n][0], acc[m][n][1]), cvt_pk_bf16(acc[m][n + 1][0], acc[m][n + 1][1]), false, false);
            auto r1 = __builtin_amdgcn_permlane16_swap(cvt_pk_bf16(acc[m][n][2], acc[m][n][3]), cvt_pk_bf16(acc[m][n + 1][2], acc[m][n + 1][3]), false, false);
            if (WT & 1) out16(C + (wr * 64 + m * 16 + i) * LDC + (wc * NT + n + (g & 1)) * 16 + 8 * (g >> 1), v4u{r0[0], r1[0], r0[1], r1[1]});
            else *(v4u*)(C + (wr * 64 + m * 16 + i) * LDC + (wc * NT + n + (g & 1)) * 16 + 8 * (g >> 1)) = v4u{r0[0], r1[0], r0[1], r1[1]};
        }
}

__device__ __forceinline__ void xattn_unit(const ChainArgs& a, rsrc_t rsQ, rsrc_t rsK, rsrc_t rsV, rsrc_t rsS, const int s, const int sn, const int h, u16 (*Vs)[VP], unsigned* mbox, const unsigned tau, int& pend, bool& ready) {
    const int l = opaque_tid() & 63, i = l & 15, g = l >> 4;
    const unsigned known = __builtin_amdgcn_readfirstlane(*(volatile unsigned*)mbox);
    const bool early = known > (unsigned)(s + 1);
    const int bq = km_off(s, h, tau), bb = v_off(s, h, tau);
    auto count = [&](const int t) {
        if (t < SSEG) { arrive(a.sync, SY_OX + (t >> 2) * 8, 128u, 8); return; }
        unsigned n = 0;
        if (l == 0) n = atomicAdd(mbox + 8 + t - SSEG, 1u);
        if (__builtin_amdgcn_readfirstlane(n) == 3u && l < 8) __hip_atomic_store((gu32*)(a.sync + (SY_OXS + (t - SSEG) * 8 + l) * 32 + (h >> 2)), 16u, __ATOMIC_RELAXED, __HIP_MEMORY_SCOPE_AGENT);
    };
    const int xk = blk_off(s, h, tau), xq = xk ^ 8192;
    v4u vv[8], vm[2], k0f[4], k1f[4], kmf[4], q0f[4], q1f[4];
#pragma unroll
    for (int e = 0; e < 8; ++e) vv[e] = ld_sc1s(rsV, l * 16 + e * 1024, xk);
#pragma unroll
    for (int ks = 0; ks < 4; ++ks) {
        k0f[ks] = ld_sc1s(rsK, l * 16 + ks * 1024, xk);
        k1f[ks] = ld_sc1s(rsK, l * 16 + ks * 1024, xk + 4096);
        q0f[ks] = ld_sc1s(rsQ, l * 16 + ks * 1024, xq);
        q1f[ks] = ld_sc1s(rsQ, l * 16 + ks * 1024, xq + 4096);
    }
#pragma unroll
    for (int e = 0; e < 2; ++e) vm[e] = ld_sc1s(rsS, l * 16 + e * 1024, early ? bb : 1 << 30);
#pragma unroll
    for (int ks = 0; ks < 4; ++ks) kmf[ks] = ld_sc1s(rsS, ks * 512 + g * 128 + (i & 7) * 16, early ? bq : 1 << 30);
    const unsigned fl = ld_flag(a.sync + (l >= 8 ? SY_PROG * 32 : (((l & 4) ? SY_Q : SY_KV) + (min(sn, NSEG - 1) >> 3) * 8) * 32 + (l & 3)) + (h >> 2) * 32);
    if (pend >= 0) { asm volatile("s_waitcnt vmcnt(31)" ::: "memory"); count(pend); }
#pragma unroll
    for (int e = 0; e < 8; ++e) *(v4u*)&Vs[16 * (e >> 2) + i][32 * (e & 3) + 8 * g] = vv[e];
    const float scale = 0.08838834764831845f;
    const float L2E = 1.4426950408889634f;
    auto round2 = [&](const f32x4 c, float* o) {
#pragma unroll
        for (int p = 0; p < 2; ++p) {
            unsigned u = cvt_pk_bf16(c[2 * p], c[2 * p + 1]);
            float lo = __uint_as_float(u << 16) * scale, hi = __uint_as_float(u & 0xFFFF0000u) * scale;
            unsigned u2 = cvt_pk_bf16(lo, hi);
            o[2 * p] = __uint_as_float(u2 << 16);
            o[2 * p + 1] = __uint_as_float(u2 & 0xFFFF0000u);
        }
    };
    float sv[2][12], mxx[2];
    {
        f32x4 sx[3] = {};
#pragma unroll
        for (int ks = 0; ks < 4; ++ks) {
            sx[0] = __builtin_amdgcn_mfma_f32_16x16x32_bf16(as_frag(k0f[ks]), as_frag(q0f[ks]), sx[0], 0, 0, 0);
            sx[1] = __builtin_amdgcn_mfma_f32_16x16x32_bf16(as_frag(k0f[ks]), as_frag(q1f[ks]), sx[1], 0, 0, 0);
            sx[2] = __builtin_amdgcn_mfma_f32_16x16x32_bf16(as_frag(k1f[ks]), as_frag(q1f[ks]), sx[2], 0, 0, 0);
        }
        round2(sx[0], &sv[0][0]); round2(sx[1], &sv[1][0]); round2(sx[2], &sv[1][4]);
        mxx[0] = mxx[1] = -3.0e38f;
#pragma unroll
        for (int r = 0; r < 4; ++r) {
            if (4 * g + r > i) { sv[0][r] = -3.0e38f; sv[1][4 + r] = -3.0e38f; }
            mxx[0] = fmaxf(mxx[0], sv[0][r]);
            mxx[1] = fmaxf(mxx[1], fmaxf(sv[1][r], sv[1][4 + r]));
        }
    }
    const u16* vb = &Vs[4 * g + (i >> 2)][4 * (i & 3)];
    bf16x8 vfx[8];
#pragma unroll
    for (int t = 0; t < 8; ++t) vfx[t] = tr_frag(vb + 16 * t, vb + 16 * VP + 16 * t);
#pragma unroll
    for (int t = 0; t < 8; ++t) asm volatile("" : "+v"(vfx[t]));
#pragma unroll
    for (int r = 0; r < 4; ++r) asm volatile("" : "+v"(sv[0][r]), "+v"(sv[1][r]), "+v"(sv[1][4 + r]));
    asm volatile("" : "+v"(mxx[0]), "+v"(mxx[1]));
    bool have = false;
    unsigned seen = __builtin_amdgcn_readlane(fl, 8);
    ready = (sn >> 3) == (s >> 3) || (__ballot(fl != 0u) & 255ull) == 255ull;
    if (early) {
        if ((h & 3) == 0 && l == 0) atomicMax(mbox, seen);
        unsigned m = mx4(mx4(0u, vm[0]), vm[1]);
#pragma unroll
        for (int ks = 0; ks < 4; ++ks) m = mx4(m, kmf[ks]);
        have = __all(m != SENT);
    } else if ((h & 3) == 0) {
        const unsigned* pw = a.sync + (SY_PROG + (h >> 2)) * 32;
        unsigned v = seen;
        if (v <= (unsigned)(s + 1)) {
            if (s < NSEG - XA_FAST) { wait_ge<32>(pw, (unsigned)(s + 1), a.tmo, 4000u); __builtin_amdgcn_s_sleep(24); }
            else { wait_ge<6>(pw, (unsigned)(s + 1), a.tmo, 4000u); __builtin_amdgcn_s_sleep(XA_D); }
            v = (unsigned)(s + 1);
        }
        if (l == 0) atomicMax(mbox, v);
    } else {
#pragma nounroll
        for (int spin = 0; spin < 4000000 && *(volatile unsigned*)mbox < (unsigned)(s + 1); ++spin) __builtin_amdgcn_s_sleep(2);
        asm volatile("" ::: "memory");
    }
#pragma nounroll
    for (int spin = 0; !have; ++spin) {
        asm volatile("" ::: "memory");
#pragma unroll
        for (int e = 0; e < 2; ++e) vm[e] = ld_sc1s(rsS, l * 16 + e * 1024, bb);
#pragma unroll
        for (int ks = 0; ks < 4; ++ks) kmf[ks] = ld_sc1s(rsS, ks * 512 + g * 128 + (i & 7) * 16, bq);
        unsigned m = mx4(mx4(0u, vm[0]), vm[1]);
#pragma unroll
        for (int ks = 0; ks < 4; ++ks) m = mx4(m, kmf[ks]);
        if (__all(m != SENT) || spin > 20000) break;
        __builtin_amdgcn_s_sleep(2);
    }
#pragma unroll
    for (int e = 0; e < 8; ++e) *(unsigned*)&Vs[32 + (i >> 2) + 4 * ((i >> 1) & 1)][64 * (e >> 2) + 16 * g + 4 * (e & 3) + 2 * (i & 1)] = vm[e >> 2][e & 3];
    f32x4 sm_[2] = {};
#pragma unroll
    for (int ks = 0; ks < 4; ++ks) {
        sm_[0] = __builtin_amdgcn_mfma_f32_16x16x32_bf16(as_frag(kmf[ks]), as_frag(q0f[ks]), sm_[0], 0, 0, 0);
        sm_[1] = __builtin_amdgcn_mfma_f32_16x16x32_bf16(as_frag(kmf[ks]), as_frag(q1f[ks]), sm_[1], 0, 0, 0);
    }
    v4u pf[2][2];
#pragma unroll
    for (int qt = 0; qt < 2; ++qt) {
        round2(sm_[qt], &sv[qt][8]);
        float mx = fmaxf(fmaxf(mxx[qt], fmaxf(sv[qt][8], sv[qt][9])), fmaxf(sv[qt][10], sv[qt][11]));
        mx = xor16_max(mx);
        mx = xor32_max(mx);
        const float nmx = -mx * L2E;
        float sum = 0.f;
#pragma unroll
        for (int e = 0; e < 12; ++e) {
            if (qt == 0 && e >= 4 && e < 8) { sv[qt][e] = 0.f; continue; }
            float ev = __builtin_amdgcn_exp2f(fmaf(sv[qt][e], L2E, nmx));
            if (e >= 8) ev = g < 2 ? ev : 0.f;
            sv[qt][e] = ev; sum += ev;
        }
        sum = xor16_sum(sum);
        sum = xor32_sum(sum);
        const float inv = 1.0f / sum;
        pf[qt][0][0] = cvt_pk_bf16(sv[qt][0] * inv, sv[qt][1] * inv);
        pf[qt][0][1] = cvt_pk_bf16(sv[qt][2] * inv, sv[qt][3] * inv);
        pf[qt][0][2] = qt ? cvt_pk_bf16(sv[qt][4] * inv, sv[qt][5] * inv) : 0u;
        pf[qt][0][3] = qt ? cvt_pk_bf16(sv[qt][6] * inv, sv[qt][7] * inv) : 0u;
        pf[qt][1][0] = cvt_pk_bf16(sv[qt][8] * inv, sv[qt][9] * inv);
        pf[qt][1][1] = cvt_pk_bf16(sv[qt][10] * inv, sv[qt][11] * inv);
        pf[qt][1][2] = 0u; pf[qt][1][3] = 0u;
    }
    v2u vmf[8];
#pragma unroll
    for (int t = 0; t < 8; ++t) vmf[t] = tr4(&Vs[32 + 4 * (g & 1) + (i >> 2)][16 * t + 4 * (i & 3)]);
#pragma unroll
    for (int t = 0; t < 8; t += 2) {
        unsigned pk[2][2][2];
#pragma unroll
        for (int tt = 0; tt < 2; ++tt) {
            const bf16x8 vf0 = vfx[t + tt];
            const bf16x8 vf1 = as_frag(v4u{vmf[t + tt][0], vmf[t + tt][1], 0u, 0u});
#pragma unroll
            for (int qt = 0; qt < 2; ++qt) {
                f32x4 o = {};
                o = __builtin_amdgcn_mfma_f32_16x16x32_bf16(vf0, as_frag(pf[qt][0]), o, 0, 0, 0);
                o = __builtin_amdgcn_mfma_f32_16x16x32_bf16(vf1, as_frag(pf[qt][1]), o, 0, 0, 0);
                pk[qt][tt][0] = cvt_pk_bf16(o[0], o[1]);
                pk[qt][tt][1] = cvt_pk_bf16(o[2], o[3]);
            }
        }
#pragma unroll
        for (int qt = 0; qt < 2; ++qt) {
            auto r0 = __builtin_amdgcn_permlane16_swap(pk[qt][0][0], pk[qt][1][0], false, false);
            auto r1 = __builtin_amdgcn_permlane16_swap(pk[qt][0][1], pk[qt][1][1], false, false);
            v4u o = {r0[0], r1[0], r0[1], r1[1]};
            st_sc1s(rsV, (t >> 1) * 1024 + ((g & 1) * 2 + (g >> 1)) * 256 + i * 16, xq + qt * 4096, o);
        }
    }
    pend = seen > (unsigned)(sn + 1) && ready ? s : -1;
    if (pend < 0) { asm volatile("s_waitcnt vmcnt(0)" ::: "memory"); count(s); }
}

__device__ __forceinline__ void tile_stream(const ChainArgs& a, const u16* X, unsigned long long todo, const int xc, unsigned& tau, const bool early, const int nw, u16* sm8 WEVP) {
    constexpr int K = DM, LDB = HD;
    const int tid = opaque_tid(), w = tid >> 6, l = tid & 63, i = l & 15, g = l >> 4;
    const int wr = w >> 2, wc = w & 3;
    const unsigned lds0 = (unsigned)(uintptr_t)(__attribute__((address_space(3))) u16*)sm8;
    const unsigned wu = __builtin_amdgcn_readfirstlane(w);
    const unsigned ldsw = lds0 + wu * 2048;
    unsigned voffA, voffB;
    { int row = 16 * w + (l >> 3), c = l & 7; voffA = (unsigned)(row * K + 8 * (c ^ (row & 7))) * 2u; }
    { int row = 8 * w + g, c = i; voffB = (unsigned)(row * LDB + 8 * (c ^ (bswz2<128>(row) << 1))) * 2u; }
    const unsigned voffA2 = voffA + 8u * K * 2u - 1024u, voffB2 = voffB + 4u * LDB * 2u - 1024u;
    auto st2 = [&](const unsigned v0, const unsigned v1, const char* p, const unsigned d) {
        unsigned keep;
        asm volatile("s_mov_b32 %0, m0\n\ts_mov_b32 m0, %1\n\ts_nop 0\n\tglobal_load_lds_dwordx4 %2, %4\n\tglobal_load_lds_dwordx4 %3, %4 offset:1024\n\ts_mov_b32 m0, %0"
                     : "=&s"(keep) : "s"(d), "v"(v0), "v"(v1), "s"(p) : "memory");
    };
    auto stA = [&](int b, int h, const char* p) {
        st2(voffA, voffA2, p + (size_t)(h * 128) * K * 2, ldsw + (2 * b + h) * 16384);
    };
    auto stB = [&](int b, int h, const char* p) {
        st2(voffB, voffB2, p + h * 2048, ldsw + 65536 + (2 * b + h) * 16384);
    };
    constexpr int SA = 128, SB = 64 * LDB * 2;
    auto a_of = [&](const unsigned t) { return (const char*)(X + (size_t)((t >> 4) & 7) * 256 * DM); };
    auto b_of = [&](const unsigned t) { const int kind = t & 7; return (const char*)((kind < 2 ? a.Wk : (kind < 4 ? a.Wv : a.Wq)) + hcol(4 * xc + (kind & 1) * 2)); };
    auto c_of = [&](const unsigned t) { const int kind = t & 7; return ((kind & 6) == 2 ? VX_OFF - KX_OFF : 0) + (int)((t >> 4) & 7) * 4194304 + blk_off(0, 4 * xc + (kind & 1) * 2, kind < 4 ? tau : tau ^ 1u); };
    auto publish = [&](const unsigned t) {
        const int kind = t & 7, ln = lane_now();
        if (wu == 0 && ln < (kind < 4 ? 1 : 2))
            __hip_atomic_store((gu32*)(a.sync + ((kind < 4 ? SY_KV : SY_Q) + ((t >> 4) & 7) * 8 + xc) * 32 + (kind < 4 ? kind : 2 * kind - 8 + ln)), 16u, __ATOMIC_RELAXED, __HIP_MEMORY_SCOPE_AGENT);
    };
    const char* smc = (const char*)sm8;
    int aoff[2], boff[2];
#pragma unroll
    for (int ks = 0; ks < 2; ++ks) aoff[ks] = (wr * 64 + i) * 128 + (((ks * 4 + g) ^ (i & 7)) << 4);
#pragma unroll
    for (int n = 0; n < 2; ++n) { int row = 8 * g + (i >> 2); boff[n] = 65536 + row * 256 + (((wc * 2 + n) ^ bswz2<128>(row)) << 5) + 8 * (i & 3); }
    f32x4 acc[2][2][4][2] = {};
    bf16x8 At[4][2], B0[2][2], B1[2][2];
#define K8_LDA(b, h) _Pragma("unroll") for (int m = 0; m < 4; ++m) _Pragma("unroll") for (int ks = 0; ks < 2; ++ks) \
        At[m][ks] = *(const bf16x8*)(smc + aoff[ks] + (2 * (b) + (h)) * 16384 + m * 2048)
#define K8_LDB(dst, b, h) _Pragma("unroll") for (int n = 0; n < 2; ++n) _Pragma("unroll") for (int ks = 0; ks < 2; ++ks) { \
        const char* p_ = smc + boff[n] + (2 * (b) + (h)) * 16384 + ks * 8192; \
        v4i16 lo_ = __builtin_amdgcn_ds_read_tr16_b64_v4i16((__attribute__((address_space(3))) v4i16*)p_); \
        v4i16 hi_ = __builtin_amdgcn_ds_read_tr16_b64_v4i16((__attribute__((address_space(3))) v4i16*)(p_ + 1024)); \
        dst[n][ks] = bf16x8{lo_[0], lo_[1], lo_[2], lo_[3], hi_[0], hi_[1], hi_[2], hi_[3]}; }
#define K8_MMA(ai, bj, Bt) do { __builtin_amdgcn_s_setprio(1); \
        _Pragma("unroll") for (int m = 0; m < 4; ++m) _Pragma("unroll") for (int n = 0; n < 2; ++n) _Pragma("unroll") for (int ks = 0; ks < 2; ++ks) \
            acc[ai][bj][m][n] = __builtin_amdgcn_mfma_f32_16x16x32_bf16(Bt[n][ks], At[m][ks], acc[ai][bj][m][n], 0, 0, 0); \
        __builtin_amdgcn_s_setprio(0); } while (0)
    const rsrc_t rsC = __builtin_amdgcn_make_buffer_rsrc((void*)(arena(a.slots) + KX_OFF), 0, FX_OFF - KX_OFF, 0x00020000);
#define K8_EPI(ai, bj, cb) do { const int ln_ = lane_now(); \
        const int vo_ = (int)(wu & 3) * 1024 + (((ln_ >> 4) & 1) * 2 + (ln_ >> 5)) * 256 + (ln_ & 15) * 16; \
        _Pragma("unroll") for (int m = 0; m < 4; ++m) { \
            const f32x4 c0 = acc[ai][bj][m][0], c1 = acc[ai][bj][m][1]; \
            auto r0 = __builtin_amdgcn_permlane16_swap(cvt_pk_bf16(c0[0], c0[1]), cvt_pk_bf16(c1[0], c1[1]), false, false); \
            auto r1 = __builtin_amdgcn_permlane16_swap(cvt_pk_bf16(c0[2], c0[3]), cvt_pk_bf16(c1[2], c1[3]), false, false); \
            const int sg_ = 4 * ai + 2 * (int)(wu >> 2) + (m >> 1); \
            st_sc1s(rsC, vo_, ((cb) ^ (ai * 8192)) + sg_ * 524288 + bj * 16384 + (m & 1) * 4096, v4u{r0[0], r1[0], r0[1], r1[1]}); \
            acc[ai][bj][m][0] = ZERO4; acc[ai][bj][m][1] = ZERO4; } } while (0)
    constexpr int nt = K / 64;
    unsigned cur = (unsigned)todo & 255u, prev = 0u;
    const char* pA = a_of(cur); const char* pB = b_of(cur);
    if (early) touch((const char*)X + 524288 + (size_t)(nw * 128 + (tid >> 2)) * 128, lds0 + 163840 - 256);
    stB(0, 0, pB); stB(0, 1, pB); stA(0, 0, pA); stA(0, 1, pA);
    stB(1, 0, pB + SB); stB(1, 1, pB + SB); stA(1, 0, pA + SA);
    K8_WV(8); K8_BAR;
    if (wu >= 4) K8_BAR;
#define K8_GO(ai) __builtin_amdgcn_s_waitcnt(0xC07F); K8_BAR; K8_MMA(ai, 0, B0); K8_MMA(ai, 1, B1); K8_SCHED; K8_BAR
    int cbp = 0;
    for (;;) {
        todo >>= 8;
        const unsigned nxt = todo != 0ull ? (unsigned)todo & 255u : cur;
        int cb = c_of(cur);
#pragma nounroll
        for (int t = 0; t < nt; t += 2) {
            if (__builtin_expect(early && prev == 0u && t == 10, 0))
                touch((const char*)X + 3 * 524288 + (size_t)(nw * 320 + min(TIDX, 319)) * 128, lds0 + 163840 - 256);
            if (__builtin_expect(early && prev == 0u && t == nt - 4, 0)) {
                tau = __builtin_amdgcn_readfirstlane((unsigned)boot_wait(a.boot) & 255u);
                cb = c_of(cur);
            }
            const bool first = t == 0 && prev != 0u, last = t == nt - 2;
            if (t == 2 && prev != 0u) { publish(prev); WEV(3) }
            K8_LDB(B0, 0, 0); K8_LDA(0, 0); K8_LDB(B1, 0, 1); K8_SCHED; stA(1, 1, pA + SA);
            pA = last ? a_of(nxt) : pA + 2 * SA; pB = last ? b_of(nxt) : pB + 2 * SB;
            if (first) { K8_EPI(1, 0, cbp); K8_EPI(1, 1, cbp); K8_WV(24); } else K8_WV(8);
            K8_GO(0);
            K8_LDA(0, 1); K8_SCHED; stB(0, 0, pB); stB(0, 1, pB); stA(0, 0, pA);
            if (first) K8_WV(24); else K8_WV(8);
            K8_GO(1);
            K8_LDB(B0, 1, 0); K8_LDA(1, 0); K8_LDB(B1, 1, 1); K8_SCHED; stA(0, 1, pA);
            if (first) K8_WV(16); else K8_WV(8);
            K8_GO(0);
            K8_LDA(1, 1); K8_SCHED; stB(1, 0, pB + SB); stB(1, 1, pB + SB); stA(1, 0, pA + SA);
            if (last) { K8_EPI(0, 0, cb); K8_EPI(0, 1, cb); K8_WV(16); } else K8_WV(8);
            K8_GO(1);
        }
        WEV(2)
        prev = cur; cbp = cb; cur = nxt;
        if (todo == 0ull) break;
    }
    K8_EPI(1, 0, cbp); K8_EPI(1, 1, cbp);
    K8_WV(0);
    if (wu < 4) K8_BAR;
    K8_BAR;
    publish(prev);
    WEV(3)
}

__device__ __forceinline__ void oproj_squad(const ChainArgs& a, u16* out, const int ct, const int rh, const int xc, const unsigned tau, char* smem) {
    const int tid = opaque_tid(), w = tid >> 6, l = tid & 63, i = l & 15, g = l >> 4;
    const int wu = __builtin_amdgcn_readfirstlane(w);
    const int c0 = (ct / 3) * 128 + (ct % 3) * 48, nq = ct % 3 == 2 ? 8 : 12;
    bf16x8 wf[2][16];
    char* w3 = smem + wu * 16384 + l * 16;
    {
        u16* stg = (u16*)(smem + wu * 16384);
        bf16x8 wt[16];
#pragma unroll
        for (int ch = 0; ch < 4; ++ch) {
            const u16* src = a.Wo + (size_t)hcol(4 * wu + ch) * DM;
            v4u raw[12];
#pragma unroll
            for (int e = 0; e < 12; ++e) { const int idx = l + 64 * e, r = idx / 6, q = idx % 6; raw[e] = *(const v4u*)(src + (size_t)r * DM + min(c0 + 8 * q, DM - 8)); }
#pragma unroll
            for (int e = 0; e < 12; ++e) { const int idx = l + 64 * e, r = idx / 6, q = idx % 6; *(v4u*)(stg + r * 56 + 8 * q) = raw[e]; }
            __builtin_amdgcn_s_waitcnt(0xC07F);
#pragma unroll
            for (int jj = 0; jj < 3; ++jj)
#pragma unroll
                for (int ks = 0; ks < 4; ++ks) {
                    const u16* p = stg + (32 * ks + 8 * g + (i >> 2)) * 56 + 16 * jj + 4 * (i & 3);
                    (jj < 2 ? wf[jj & 1] : wt)[4 * ch + ks] = tr_frag(p, p + 4 * 56);
                }
            __builtin_amdgcn_s_waitcnt(0xC07F);
        }
#pragma unroll
        for (int ks = 0; ks < 16; ++ks) *(bf16x8*)(w3 + ks * 1024) = wt[ks];
    }
    const rsrc_t rsO = __builtin_amdgcn_make_buffer_rsrc((void*)(arena(a.slots) + VX_OFF), 0, 32 << 20, 0x00020000);
    float* rp = (float*)(smem + 131072);
    const int voff = rh * 4096 + l * 16;
    bool pre = false;
    v4u xf[16];
    const unsigned* fq = a.sync + (SY_OXS + xc) * 32 + wu;
    for (int s = SSEG; s < NSEG - 1; ++s, fq += 256) {
        if (!pre) {
#pragma nounroll
            for (int spin = 0;; ++spin) {
                asm volatile("" ::: "memory");
                if (ld_flag(fq) != 0u) break;
                if (spin > 20000) { __hip_atomic_store((gu32*)a.tmo, 7000u, __ATOMIC_RELAXED, __HIP_MEMORY_SCOPE_AGENT); break; }
                if (s == SSEG) __builtin_amdgcn_s_sleep(64); else if (s < NSEG - 3) __builtin_amdgcn_s_sleep(SQ_SL); else __builtin_amdgcn_s_sleep(SQ_SF);
            }
            asm volatile("" ::: "memory");
        }
        const unsigned nf = ld_flag(fq + 256);
        if (!pre) {
#pragma unroll
            for (int ks = 0; ks < 16; ++ks) xf[ks] = ld_sc1s(rsO, voff + (ks & 3) * 1024, blk_off(s, 4 * wu + (ks >> 2), tau) ^ 8192);
        }
        pre = __builtin_amdgcn_readfirstlane(nf) != 0u;
        f32x4 acc[3] = {};
        if (pre) {
            bf16x8 wn = *(const bf16x8*)w3;
#pragma unroll
            for (int ks = 0; ks < 16; ++ks) {
#pragma unroll
                for (int jj = 0; jj < 2; ++jj) acc[jj] = __builtin_amdgcn_mfma_f32_16x16x32_bf16(wf[jj][ks], as_frag(xf[ks]), acc[jj], 0, 0, 0);
                acc[2] = __builtin_amdgcn_mfma_f32_16x16x32_bf16(wn, as_frag(xf[ks]), acc[2], 0, 0, 0);
                wn = *(const bf16x8*)(w3 + ((ks + 1) & 15) * 1024);
                K8_SCHED;
                xf[ks] = ld_sc1s(rsO, voff + (ks & 3) * 1024, blk_off(s + 1, 4 * wu + (ks >> 2), tau) ^ 8192);
                K8_SCHED;
            }
        } else {
#pragma unroll
            for (int ks = 0; ks < 16; ++ks) {
#pragma unroll
                for (int jj = 0; jj < 2; ++jj) acc[jj] = __builtin_amdgcn_mfma_f32_16x16x32_bf16(wf[jj][ks], as_frag(xf[ks]), acc[jj], 0, 0, 0);
                acc[2] = __builtin_amdgcn_mfma_f32_16x16x32_bf16(*(const bf16x8*)(w3 + ks * 1024), as_frag(xf[ks]), acc[2], 0, 0, 0);
            }
        }
        K8_BAR;
#pragma unroll
        for (int jj = 0; jj < 3; ++jj) *(f32x4*)&rp[(wu * 16 + i) * 52 + 16 * jj + 4 * g] = acc[jj];
        K8_WL(0); K8_BAR;
        if (tid >= 256 && (tid & 15) < nq) {
            const int row = (tid >> 4) & 15, q4 = tid & 15;
            f32x4 sv_[8];
#pragma unroll
            for (int e = 0; e < 8; ++e) sv_[e] = *(const f32x4*)&rp[(e * 16 + row) * 52 + 4 * q4];
            const f32x4 s0 = ((sv_[0] + sv_[1]) + (sv_[2] + sv_[3])) + ((sv_[4] + sv_[5]) + (sv_[6] + sv_[7]));
            v2u o; o[0] = cvt_pk_bf16(s0[0], s0[1]); o[1] = cvt_pk_bf16(s0[2], s0[3]);
            out8(out + (size_t)(s * SEG + 16 * rh + row) * OUTW + c0 + 4 * q4, o);
        }
    }
}

__device__ __forceinline__ void kv_stream(const ChainArgs& a, const u16* __restrict__ X, const u16* __restrict__ B, char* C, const int head, const int xc, const int wk, unsigned& tau, char* smem) {
    const int tid = opaque_tid(), w = tid >> 6, l = tid & 63, i = l & 15, g = l >> 4;
    const int wu = __builtin_amdgcn_readfirstlane(w), kh = wu >> 2, cq = wu & 3;
    const unsigned lds0 = (unsigned)(uintptr_t)(__attribute__((address_space(3))) char*)smem;
    unsigned voA, voB;
    { const int row = (wu & 1) * 8 + (l >> 3), c = l & 7; voA = (unsigned)(row * DM + 8 * (c ^ (row & 7))) * 2u + (wu >> 1) * 512; }
    { const int row = tid >> 4, c = tid & 15; voB = (unsigned)(row * HD + 8 * (c ^ (bswz2<128>(row) << 1))) * 2u; }
    auto stB = [&](int ch, int slot) {
#pragma unroll
        for (int e = 0; e < 4; ++e) glds16s(voB, (const char*)B + (size_t)(4 * ch + e) * 32 * HD * 2, lds0 + 65536 + slot * 32768 + e * 8192 + wu * 1024);
    };
    const unsigned ldA = lds0 + (wu >> 1) * 8192 + (wu & 1) * 1024;
    auto stP = [&](const char* p, const unsigned d) {
        unsigned keep;
        asm volatile("s_mov_b32 %0, m0\n\ts_mov_b32 m0, %1\n\ts_nop 0\n\tglobal_load_lds_dwordx4 %3, %2 offset:-4096\n\tglobal_load_lds_dwordx4 %4, %2 offset:-2048\n\t"
                     "global_load_lds_dwordx4 %5, %2\n\tglobal_load_lds_dwordx4 %6, %2 offset:2048\n\ts_mov_b32 m0, %0"
                     : "=&s"(keep) : "s"(d), "s"(p), "v"(voA + 8192u), "v"(voA + 6272u), "v"(voA + 4352u), "v"(voA + 2432u) : "memory");
    };
    auto srcA = [&](int h) { return (const char*)X + (size_t)min(h, 2 * KVS - 1) * 32768 - 4096; };
    auto stA = [&](int h) { stP(srcA(h), ldA + (h & 3) * 32768 + 4096); };
    stB(0, 0); stA(0); stA(1); stB(1, 1);
    touch((const char*)X + 65536 + min(wk * 512 + tid, (KVS - 1) * 512 - 1) * 128, lds0 + 131072 + wu * 1024);
    stB(2, 2);
    bf16x8 bf[16][2], af[8];
    f32x4 acc[2][2] = {}, keep[2];
    const int rb = 64 * kh + 8 * g + (i >> 2), sw = bswz2<128>(rb);
    const char* bp = smem + 65536 + rb * 256 + 8 * (i & 3);
    const char* sa0 = smem + kh * 2048 + i * 128 + 16 * (g ^ (i & 7));
    const char* sa1 = smem + kh * 2048 + i * 128 + 16 * ((4 + g) ^ (i & 7));
#define KV_AF(slot, q) *(const bf16x8*)((((q) & 1) ? sa1 : sa0) + (slot) * 32768 + ((q) >> 1) * 4096)
    unsigned long long tag = 0;
#pragma unroll
    for (int ch = 0; ch < 8; ++ch) {
        if (ch == 0) K8_WV(9); else if (ch < 6) K8_WV(8); else if (ch == 6) K8_WV(4); else K8_WV(0);
        K8_BAR;
#pragma unroll
        for (int e = 0; e < 4; ++e) { const char* p_ = bp + (ch % 3) * 32768 + (e >> 1) * 8192 + 32 * ((2 * cq + (e & 1)) ^ sw); bf[2 * ch + (e >> 1)][e & 1] = tr_frag(p_, p_ + 1024); }
#pragma unroll
        for (int e = 0; e < 4; ++e) af[e] = KV_AF(e >> 1, 2 * ch + (e & 1));
        K8_WL(0); K8_SCHED; K8_BAR;
        if (ch == BWC && wu == 4) tag = boot_wait(a.boot);
        if (ch < 5) stB(ch + 3, ch % 3);
        if (ch == 7) { stA(2); stA(3); }
#pragma unroll
        for (int e = 0; e < 8; ++e) acc[e >> 2][e & 1] = __builtin_amdgcn_mfma_f32_16x16x32_bf16(bf[2 * ch + ((e >> 1) & 1)][e & 1], af[e >> 1], acc[e >> 2][e & 1], 0, 0, 0);
    }
    float* red = (float*)(smem + 131072) + l * 4;
#pragma unroll
    for (int p = 0; p < 2; ++p) { *(f32x4*)(red + (p * 8 + wu) * 256) = kh ? acc[p][0] : acc[p][1]; keep[p] = kh ? acc[p][1] : acc[p][0]; }
    if (wu == 4) *(volatile unsigned*)(smem + 163840 - 512) = (unsigned)tag & 255u;
    const rsrc_t rc = __builtin_amdgcn_make_buffer_rsrc((void*)C, 0, KVS * 524288, 0x00020000);
    const int vo = cq * 1024 + (2 * kh + (g >> 1)) * 256 + i * 16 + (g & 1) * 8;
    auto put = [&](const f32x4 o, const int so) {
        const v2u pk = {cvt_pk_bf16(o[0], o[1]), cvt_pk_bf16(o[2], o[3])};
        asm volatile("buffer_store_dwordx2 %0, %1, %2, %3 offen sc1" :: "v"(pk), "v"(vo), "s"(rc), "s"(so) : "memory");
    };
    K8_WL(0); K8_BAR;
    tau = __builtin_amdgcn_readfirstlane(*(volatile unsigned*)(smem + 163840 - 512));
#pragma unroll
    for (int p = 0; p < 2; ++p) put(keep[p] + *(const f32x4*)(red + (p * 8 + (wu ^ 4)) * 256), blk_off(0, head, tau) + p * 4096);
    stA(4);
    K8_WV(4); K8_BAR;
    if (tid == 256) __hip_atomic_store((gu64*)(a.boot + 256 + 16 * xc + wk), tag, __ATOMIC_RELAXED, __HIP_MEMORY_SCOPE_AGENT);
    auto stages = [&](auto kc) {
        constexpr int KH = decltype(kc)::value;
        if (KH) K8_BAR;
        f32x4 ac[2] = {ZERO4, ZERO4}, kp = ZERO4;
        int h_first = 2, so = OOB;
        asm volatile("" : "+s"(h_first));
        const char* pn = srcA(5);
        unsigned dn = ldA + 32768 + 4096;
        int r0 = (int)(sa0 - smem) + 2 * 32768, r1 = (int)(sa1 - smem) + 2 * 32768;
        for (int h = h_first; h < 2 * KVS + 4; ++h) {
            const f32x4 oth = *(const f32x4*)(red + (wu ^ 4) * 256);
            *(f32x4*)(red + wu * 256) = ac[KH ^ 1];
            bf16x8 ax[16];
#pragma unroll
            for (int q = 0; q < 16; ++q) ax[q] = *(const bf16x8*)(smem + ((q & 1) ? r1 : r0) + (q >> 1) * 4096);
            K8_SCHED;
            stP(pn, dn);
            put((KH ? ac[1] : kp) + oth, so);
            kp = ac[0];
            __builtin_amdgcn_s_waitcnt(0xC07F);
            K8_WV(10);
            if (__builtin_expect(h == 5, 0)) K8_WV(0);
            K8_BAR;
            if (KH && (0xAAA20u >> h) & 1u && tid == 256)
                __hip_atomic_store((gu32*)(a.sync + (SY_KV0 + (h / 2 - (h == 5 ? 1 : 2)) * 8 + xc) * 32 + wk), 16u, __ATOMIC_RELAXED, __HIP_MEMORY_SCOPE_AGENT);
            if (__builtin_expect(h < 2 * KVS, 1)) {
                __builtin_amdgcn_s_setprio(1);
#pragma unroll
                for (int q = 0; q < 16; ++q)
#pragma unroll
                    for (int n = 0; n < 2; ++n) ac[n] = __builtin_amdgcn_mfma_f32_16x16x32_bf16(bf[q][n], ax[q], q ? ac[n] : ZERO4, 0, 0, 0);
                __builtin_amdgcn_s_setprio(0);
            }
            {
                const int hs = h - 1 + KH;
                so = blk_off(hs >> 1, head, tau) + (hs & 1) * 4096;
                asm volatile("" : "+s"(so));
                so = hs > 1 && hs < 2 * KVS ? so : OOB;
                pn = srcA(h + 4); dn = ldA + (h & 3) * 32768 + 4096;
                r0 = (r0 + 32768) & 131071; r1 = (r1 + 32768) & 131071;
                asm volatile("" : "+s"(so), "+s"(pn), "+s"(dn), "+v"(r0), "+v"(r1));
            }
            K8_BAR;
        }
        if (!KH) K8_BAR;
    };
    if (kh) stages(std::integral_constant<int, 1>{}); else stages(std::integral_constant<int, 0>{});
    K8_WV(0);
}

struct MegaArgs {
    ChainArgs ch;
    const u16* x; u16* out;
    const u16* bx; const u16* wr; const u16* wi;
};
constexpr int MEGA_LDS = 163840;
#define WSTAMP(n)
__global__ __launch_bounds__(512, 2) void mega_kernel(MegaArgs m) {
    extern __shared__ __attribute__((aligned(256))) char chsm[];
    const int b = BIDX, xc = (b & 3) + 4 * (b >> 7), sl = (b >> 3) & 15;
    if ((b & 4) == 0) { chain_body(m.ch, xc, sl, chsm); return; }
    const ChainArgs& a = m.ch;
    const int wk = sl, tid = TIDX;
    char* ar = arena(a.slots);
    unsigned tauw = 0;
    const int hf = __builtin_amdgcn_readfirstlane(TIDX >> 8);
    WSTAMP(0)
    if (wk < 8) kv_stream(a, m.x, (wk < 4 ? a.Wk : a.Wv) + hcol(4 * xc + (wk & 3)), ar + (wk < 4 ? KX_OFF : VX_OFF), 4 * xc + (wk & 3), xc, wk, tauw, chsm);
    WSTAMP(8) WEV(1)
    {
        const int p = wk & 1, q = wk & 3;
        const unsigned long long todo = wk < 2 ? 0x84u + p : wk < 4 ? 0x74704240u + 0x01010101u * p : wk < 8 ? 0ull : wk < 10 ? 0x60503010ull + 0x01010101ull * q
                                      : wk < 12 ? 0x7060503010ull + 0x0101010101ull * q : (wk < 14 ? 0x54341420u : 0x64442422u) + 0x01010101u * p;
        __syncthreads();
        if (todo != 0ull) tile_stream(a, m.x, todo, xc, tauw, wk >= 8, xc * 8 + (wk & 7), (u16*)chsm WEVA);
    }
    WSTAMP(2)
    if (wk < 2) {
        unsigned* mbox = (unsigned*)(chsm + 131072);
        if (tid < 40) mbox[tid] = tid >= 1 && tid < 5 ? 2u : 0u;
        __syncthreads();
        const int w = __builtin_amdgcn_readfirstlane(tid >> 6), h = 4 * xc + (w & 3);
        const rsrc_t rsQ = __builtin_amdgcn_make_buffer_rsrc((void*)(ar + KX_OFF), 0, S_LEN * HD * 4, 0x00020000);
        const rsrc_t rsK = __builtin_amdgcn_make_buffer_rsrc((void*)(ar + KX_OFF), 0, S_LEN * HD * 4, 0x00020000);
        const rsrc_t rsV = __builtin_amdgcn_make_buffer_rsrc((void*)(ar + VX_OFF), 0, S_LEN * HD * 4, 0x00020000);
        const rsrc_t rsS = slot_rsrc(a.slots);
        u16 (*Vs)[VP] = (u16 (*)[VP])(chsm + w * (40 * VP * 2));
        const unsigned tau = tauw;
        int pend = -1;
        bool ready = false;
        for (int s = 2 * wk + (w >> 2), sn; s < NSEG - 1; s = sn) {
            const int band = s >> 3;
            sn = s + 4;
            if (s < XST) {
                unsigned n = 0;
                if (lane_now() == 0) n = atomicAdd(mbox + 1 + (w & 3), 1u);
                n = __builtin_amdgcn_readfirstlane(n);
                sn = 2 * wk + (n < XST / 2 ? (int)(n & 1u) + 4 * (int)(n >> 1) : XST + (w >> 2));
            }
            if (sn - s > 4) __builtin_amdgcn_s_setprio(2); else __builtin_amdgcn_s_setprio(0);
            if (s < 4) wait_ge<32>(a.sync + (SY_KV0 + (KVS - 1) * 8 + xc) * 32 + (tid & 7), 16u, a.tmo, 4100u);
            if (!ready) {
                if (band > 0) wait_ge<32>(a.sync + (SY_KV + band * 8 + xc) * 32 + (tid & 3), 1u, a.tmo, 4100u);
                wait_ge<32>(a.sync + (SY_Q + band * 8 + xc) * 32 + (tid & 3), 1u, a.tmo, 4200u);
            }
            xattn_unit(a, rsQ, rsK, rsV, rsS, s, sn, h, Vs, mbox, tau, pend, ready);
        }
        return;
    }
    if (wk >= 4 && wk < 8) {
        const int fw = (wk - 4) * 8 + xc;
        const rsrc_t rsF = __builtin_amdgcn_make_buffer_rsrc((void*)(ar + FX_OFF), 0, 16 << 20, 0x00020000);
        const rsrc_t rsW = __builtin_amdgcn_make_buffer_rsrc((void*)(ar + WT_OFF), 0, 10 << 20, 0x00020000);
        const unsigned tw = tauw ^ 1u;
        {
            const int tq = opaque_tid();
            fft_rows_fwd(m.bx, rsF, tw, 2 * fw + hf, chsm + hf * 36864, tq & 255, (const char*)m.wr, (const char*)m.wi, (unsigned)(uintptr_t)(__attribute__((address_space(3))) char*)chsm + 163840 - 256);
        }
        for (int t = fw; t < 64; t += 32) {
            __syncthreads();
            filt_transpose(m.wr, m.wi, rsW, tw, t, chsm, opaque_tid());
        }
        wg_publish(a.sync, SY_DONE, 32u, 8);
        WSTAMP(3) WEV(4)
        wg_wait_acquire<16>(a.sync + (SY_DONE + xc) * 32, a.tmo, 6000u);
        {
            const int tq = opaque_tid();
            fft_cols(rsF, rsW, tw, 2 * fw + hf, 4, chsm + hf * 36864, tq & 255);
        }
        wg_publish(a.sync, SY_DONE + 8, 32u, 8);
        WSTAMP(4) WEV(5)
    }
    if (wk >= 4 && wk < 10) {
        const rsrc_t rsF = __builtin_amdgcn_make_buffer_rsrc((void*)(ar + FX_OFF), 0, 16 << 20, 0x00020000);
        wg_wait_acquire<16>(a.sync + (SY_DONE + 8 + xc) * 32, a.tmo, 6001u);
        const int tq = opaque_tid();
        fft_rows_inv(rsF, tauw ^ 1u, m.out, 2 * ((wk & 3) * 8 + xc) + hf + (wk < 8 ? 0 : 192), wk < 8 ? 64 : 32, wk < 8 ? 3 : 2, chsm + hf * 36864, tq & 255);
        WSTAMP(5) WEV(6)
    }
    if (wk >= 12) {
        const int r = 2 * (wk - 12);
        wg_wait_acquire<16>(a.sync + (SY_OX + r * 8 + xc) * 32, a.tmo, 5000u);
        wg_wait_acquire<16>(a.sync + (SY_OX + (r + 1) * 8 + xc) * 32, a.tmo, 5000u);
        WEV(7)
        gemm_tile<256>((u16*)chsm, ar + VX_OFF, 4 * r, tauw, a.Wo + xc * 128, m.out + (size_t)r * 128 * OUTW + xc * 128);
        WEV(8)
    }
    if (wk < 4 || wk == 10) {
        const int r = wk < 4 ? wk + 6 : 10;
        wg_wait_acquire<16>(a.sync + (SY_OX + r * 8 + xc) * 32, a.tmo, 5000u);
        WEV(7)
        gemm_tile<128>((u16*)chsm, ar + VX_OFF, 4 * r, tauw, a.Wo + xc * 128, m.out + (size_t)r * 128 * OUTW + xc * 128);
        WEV(8)
        WSTAMP(9)
    }
    if (wk >= 4 && wk < 10) {
        __syncthreads();
        oproj_squad(a, m.out, (wk - 4) * 4 + (xc & 3), xc >> 2, xc, tauw, chsm);
        WSTAMP(7)
    }
}

#if defined(__HIP_DEVICE_COMPILE__)
#pragma clang attribute pop
#endif
extern "C" void kernel_launch(void* const* d_in, const int* in_sizes, int n_in,
                              void* d_out, int out_size, void* d_ws, size_t ws_size,
                              hipStream_t stream) {
    char* ws = (char*)d_ws;
    size_t off = 0;
    auto alloc = [&](size_t bytes) { void* p = ws + off; off += (bytes + 255) & ~(size_t)255; return p; };
    char* slots = (char*)alloc((size_t)ARENA_BYTES + (2u << 20));
    static_assert((SY_WORDS + 2) * 128 <= 131072, "sync block");
    unsigned* sync = (unsigned*)alloc(131072);
    unsigned long long* boot = (unsigned long long*)alloc(4096);

    MegaArgs m;
    m.ch.mem0 = (const u16*)d_in[1]; m.ch.Wq = (const u16*)d_in[2]; m.ch.Wk = (const u16*)d_in[3]; m.ch.Wv = (const u16*)d_in[4]; m.ch.Wo = (const u16*)d_in[5];
    m.ch.out = (u16*)d_out; m.ch.slots = slots; m.ch.tmo = sync + SY_WORDS * 32;
    m.ch.stamps = (unsigned long long*)(ws + (240ull << 20));
    m.ch.sync = sync; m.ch.boot = boot;
    m.x = (const u16*)d_in[0]; m.out = (u16*)d_out;
    m.bx = (const u16*)d_in[7]; m.wr = (const u16*)d_in[8]; m.wi = (const u16*)d_in[9];
    (void)hipFuncSetAttribute((const void*)mega_kernel, hipFuncAttributeMaxDynamicSharedMemorySize, MEGA_LDS);
    mega_kernel<<<256, 512, MEGA_LDS, stream>>>(m);
}
```
